```python
import torch

def _softmax(x, axis=-1):
    return torch.softmax(x, dim=axis)
def _gelu(x):
    return torch.nn.functional.gelu(x, approximate="tanh")
def _silu(x):
    return torch.nn.functional.silu(x)
def _sigmoid(x):
    return torch.sigmoid(x)
def _layernorm(x, axis=-1, eps=1e-5):
    m = x.mean(dim=axis, keepdim=True)
    v = x.var(dim=axis, keepdim=True, unbiased=False)
    return (x - m) / torch.sqrt(v + eps)
def _scatter_add(tgt, idx, src, axis=0):
    return tgt.clone().index_add_(axis, idx, src)
def _segment_sum(src, seg, n):
    out = torch.zeros((n,) + src.shape[1:], dtype=src.dtype)
    return out.index_add_(0, seg, src)
def _pad(x, pw):
    flat = tuple(v for pair in reversed(pw) for v in pair)
    return torch.nn.functional.pad(x, flat)

kernel_name = 'concat_heads__mha_block_cached__gqa_block_cached__near_ridge'

def setup_inputs():

    _w = (lambda a: (lambda t: t.to(torch.bfloat16) if t.is_floating_point() else t)(torch.as_tensor(a)))
    def _A_setup():
        B, Sq, D = 8, 176, 1024
        H, Sk, Dh = 32, 512, 128
        return {
            "x": _w((lambda *s: torch.randn(*s, dtype=torch.bfloat16) * 0.8)(B, Sq, D)),
            "k_cache": _w((lambda *s: torch.randn(*s, dtype=torch.bfloat16) * 0.8)(B, H, Sk, Dh)),
            "v_cache": _w((lambda *s: torch.randn(*s, dtype=torch.bfloat16) * 0.05)(B, H, Sk, Dh)),
            "wq": _w((lambda *s: torch.randn(*s, dtype=torch.bfloat16) * 0.8)(D, H * Dh) * (D ** -0.5)),
            "wo": _w((lambda *s: torch.randn(*s, dtype=torch.bfloat16) * 0.05)(H * Dh, D) * ((H * Dh) ** -0.5)),
        }
    _A_in = _A_setup()

    def _B_setup():
        B, Sq, D = 8, 176, 1024
        Hq, Hkv, Sk, Dh = 32, 32, 512, 128
        assert Hq % Hkv == 0, (Hq, Hkv)
        return {
            "x": _w((lambda *s: torch.randn(*s, dtype=torch.bfloat16) * 0.8)(B, Sq, D)),
            "k_cache": _w((lambda *s: torch.randn(*s, dtype=torch.bfloat16) * 0.8)(B, Hkv, Sk, Dh)),
            "v_cache": _w((lambda *s: torch.randn(*s, dtype=torch.bfloat16) * 0.05)(B, Hkv, Sk, Dh)),
            "wq": _w((lambda *s: torch.randn(*s, dtype=torch.bfloat16) * 0.8)(D, Hq * Dh) * (D ** -0.5)),
            "wo": _w((lambda *s: torch.randn(*s, dtype=torch.bfloat16) * 0.05)(Hq * Dh, D) * ((Hq * Dh) ** -0.5)),
        }
    _B_in = _B_setup()

    return {
        "A_x": _A_in["x"],
        "A_k_cache": _A_in["k_cache"],
        "A_v_cache": _A_in["v_cache"],
        "A_wq": _A_in["wq"],
        "A_wo": _A_in["wo"],
        "B_x": _B_in["x"],
        "B_k_cache": _B_in["k_cache"],
        "B_v_cache": _B_in["v_cache"],
        "B_wq": _B_in["wq"],
        "B_wo": _B_in["wo"],
    }

def reference(A_x, A_k_cache, A_v_cache, A_wq, A_wo, B_x, B_k_cache, B_v_cache, B_wq, B_wo):
    def _A_ref(x, k_cache, v_cache, wq, wo):
        B, Sq, D = x.shape
        H, Dh = 32, 128
        q = (lambda x, *d: x.permute(*d))((x @ wq).reshape(B, Sq, H, Dh), 0, 2, 1, 3)
        scores = torch.einsum("bhqd,bhkd->bhqk", q, k_cache) * (Dh ** -0.5)
        p = _softmax(scores, axis=-1)
        o = torch.einsum("bhqk,bhkd->bhqd", p, v_cache)
        return (lambda x, *d: x.permute(*d))(o, 0, 2, 1, 3).reshape(B, Sq, H * Dh) @ wo

    def _B_ref(x, k_cache, v_cache, wq, wo):
        B, Sq, D = x.shape
        Hq, Dh = 32, 128
        Hkv = k_cache.shape[1]
        rep = Hq // Hkv
        q = (lambda x, *d: x.permute(*d))((x @ wq).reshape(B, Sq, Hq, Dh), 0, 2, 1, 3)
        k = (lambda x, n, axis: torch.repeat_interleave(x, n, dim=axis))(k_cache, rep, axis=1)
        v = (lambda x, n, axis: torch.repeat_interleave(x, n, dim=axis))(v_cache, rep, axis=1)
        scores = torch.einsum("bhqd,bhkd->bhqk", q, k) * (Dh ** -0.5)
        p = _softmax(scores, axis=-1)
        o = torch.einsum("bhqk,bhkd->bhqd", p, v)
        return (lambda x, *d: x.permute(*d))(o, 0, 2, 1, 3).reshape(B, Sq, Hq * Dh) @ wo

    a = _A_ref(A_x, A_k_cache, A_v_cache, A_wq, A_wo)
    b = _B_ref(B_x, B_k_cache, B_v_cache, B_wq, B_wo)
    return torch.concatenate([a, b], axis=-1)
```

```cpp
#include <hip/hip_runtime.h>
#include <hip/hip_bf16.h>

__global__ __launch_bounds__(256, 1) void fused_attn_block(const char* xA, const char* kA, const char* vA, const char* wqA, const char* woA,
                                                           const char* xB, const char* kB, const char* vB, const char* wqB, const char* woB,
                                                           char* out, char* ws) {
  __shared__ __attribute__((aligned(16))) char lds[163840];
  const unsigned blk = blockIdx.x, xcd = blk & 7u, idx = blk >> 3;
  const unsigned br = xcd >> 2, b = ((xcd >> 1) & 1u) * 4u + (idx >> 3), j = (idx & 7u) * 2u + (xcd & 1u);
  const char* px = (br ? xB : xA) + (size_t)b * (176 * 2048);
  const char* pw = (br ? wqB : wqA) + j * 256u;
  const char* pk = (br ? kB : kA) + (size_t)(b * 32u + j) * (512 * 256);
  const char* pv = (br ? vB : vA) + (size_t)(b * 32u + j) * (512 * 256);
  const char* wo = br ? woB : woA;
  const char* pwarm = wo;
  const unsigned t4 = (2u + (unsigned)((size_t)pw >> 8)) & 3u;
  const unsigned wline = t4 == 3u ? 65536u : ((b * 4u + (j >> 2)) * 3u + t4) * 86u;
  const char* pwo = wo + (j >> 1) * 256u;
  char* pout = out + (size_t)b * (176 * 4096) + br * 2048u + j * 128u;
  char* pf = ws + (size_t)(br * 8u + b) * (48 * 44 * 1024 + 4096);
  asm volatile("" :: "s"((unsigned)(size_t)lds));
  asm volatile(
    ".L_start_%=:\n"
    "v_lshrrev_b32 v52, 6, %7\n"
    "v_and_b32 v44, 63, %7\n"
    "v_lshrrev_b32 v45, 4, v44\n"
    "v_and_b32 v46, 15, v44\n"
    "v_bfe_u32 v47, v44, 2, 2\n"
    "v_and_b32 v48, 3, v44\n"
    "v_and_b32 v49, 1, v45\n"
    "v_lshl_or_b32 v49, v49, 2, v47\n"
    "v_lshrrev_b32 v50, 3, v44\n"
    "v_and_b32 v51, 7, v44\n"
    "v_readfirstlane_b32 s36, v52\n"
    "s_cmp_eq_u32 s36, 3\n"
    "s_cbranch_scc0 .L_pathC_%=\n"
    "s_bfe_u32 s57, %11, 0x20000\n"
    "s_mov_b32 s50, 0x69412530\n"
    "s_mov_b32 s51, 0xfebcad87\n"
    "s_cmp_eq_u32 s57, 1\n"
    "s_cselect_b32 s50, 0xa5346120, s50\n"
    "s_cselect_b32 s51, 0xfdbce987, s51\n"
    "s_cmp_eq_u32 s57, 2\n"
    "s_cselect_b32 s50, 0x65273410, s50\n"
    "s_cselect_b32 s51, 0xedbfa98c, s51\n"
    "s_cmp_eq_u32 s57, 3\n"
    "s_cselect_b32 s50, 0x65812340, s50\n"
    "s_cselect_b32 s51, 0xfedca9b7, s51\n"
    "s_and_b32 s44, s50, 15\n"
    "s_lshr_b64 s[50:51], s[50:51], 4\n"
    "s_lshl_b32 s44, s44, 7\n"
    "s_lshl_b32 s45, %5, 8\n"
    "s_sub_u32 s45, 8388608, s45\n"
    "s_mov_b64 s[12:13], %1\n"
    "s_and_b32 s13, s13, 0xffff\n"
    "s_mov_b32 s14, s45\n"
    "s_mov_b32 s15, 0x20000\n"
    "v_lshlrev_b32 v52, 1, v45\n"
    "v_xor_b32 v52, v46, v52\n"
    "v_lshlrev_b32 v52, 4, v52\n"
    "v_lshl_add_u32 v5, v45, 13, v52\n"
    "v_add_u32 v6, 31744, v5\n"
    "v_xor_b32 v53, 128, v5\n"
    "v_add_u32 v7, 63488, v53\n"
    "v_add_u32 v8, 95232, v53\n"
    "s_lshl_b32 s43, s44, 12\n"
    "s_mov_b32 s39, 16384\n"
    "s_add_u32 m0, s39, 88064\n"
    "s_add_u32 s45, s43, 0\n"
    "buffer_load_dwordx4 v5, s[12:15], s45 offen lds\n"
    "buffer_load_dwordx4 v6, s[12:15], s45 offen offset:1024 lds\n"
    "buffer_load_dwordx4 v7, s[12:15], s45 offen offset:2048 lds\n"
    "buffer_load_dwordx4 v8, s[12:15], s45 offen offset:3072 lds\n"
    "s_add_u32 m0, s39, 92160\n"
    "s_add_u32 s46, s43, 131072\n"
    "buffer_load_dwordx4 v5, s[12:15], s46 offen lds\n"
    "buffer_load_dwordx4 v6, s[12:15], s46 offen offset:1024 lds\n"
    "buffer_load_dwordx4 v7, s[12:15], s46 offen offset:2048 lds\n"
    "buffer_load_dwordx4 v8, s[12:15], s46 offen offset:3072 lds\n"
    "s_add_u32 m0, s39, 96256\n"
    "s_add_u32 s45, s43, 262144\n"
    "buffer_load_dwordx4 v5, s[12:15], s45 offen lds\n"
    "buffer_load_dwordx4 v6, s[12:15], s45 offen offset:1024 lds\n"
    "buffer_load_dwordx4 v7, s[12:15], s45 offen offset:2048 lds\n"
    "buffer_load_dwordx4 v8, s[12:15], s45 offen offset:3072 lds\n"
    "s_add_u32 m0, s39, 100352\n"
    "s_add_u32 s46, s43, 393216\n"
    "buffer_load_dwordx4 v5, s[12:15], s46 offen lds\n"
    "buffer_load_dwordx4 v6, s[12:15], s46 offen offset:1024 lds\n"
    "buffer_load_dwordx4 v7, s[12:15], s46 offen offset:2048 lds\n"
    "buffer_load_dwordx4 v8, s[12:15], s46 offen offset:3072 lds\n"
    "s_mov_b64 s[8:9], %0\n"
    "s_and_b32 s9, s9, 0xffff\n"
    "s_mov_b32 s10, 360448\n"
    "s_mov_b32 s11, 0x20000\n"
    "v_xor_b32 v52, v51, v45\n"
    "v_lshlrev_b32 v52, 4, v52\n"
    "v_lshl_add_u32 v1, v50, 11, v52\n"
    "v_xor_b32 v53, 64, v1\n"
    "v_add_u32 v2, 15360, v53\n"
    "v_add_u32 v3, 30720, v1\n"
    "v_add_u32 v4, 46080, v53\n"
    "s_mov_b32 m0, 65536\n"
    "s_add_u32 s45, s44, 0\n"
    "buffer_load_dwordx4 v1, s[8:11], s45 offen lds\n"
    "buffer_load_dwordx4 v2, s[8:11], s45 offen offset:1024 lds\n"
    "buffer_load_dwordx4 v3, s[8:11], s45 offen offset:2048 lds\n"
    "buffer_load_dwordx4 v4, s[8:11], s45 offen offset:3072 lds\n"
    "s_mov_b32 m0, 69632\n"
    "s_add_u32 s46, s44, 65536\n"
    "buffer_load_dwordx4 v1, s[8:11], s46 offen lds\n"
    "buffer_load_dwordx4 v2, s[8:11], s46 offen offset:1024 lds\n"
    "buffer_load_dwordx4 v3, s[8:11], s46 offen offset:2048 lds\n"
    "buffer_load_dwordx4 v4, s[8:11], s46 offen offset:3072 lds\n"
    "s_mov_b32 m0, 73728\n"
    "s_add_u32 s45, s44, 131072\n"
    "buffer_load_dwordx4 v1, s[8:11], s45 offen lds\n"
    "buffer_load_dwordx4 v2, s[8:11], s45 offen offset:1024 lds\n"
    "buffer_load_dwordx4 v3, s[8:11], s45 offen offset:2048 lds\n"
    "buffer_load_dwordx4 v4, s[8:11], s45 offen offset:3072 lds\n"
    "s_mov_b32 m0, 77824\n"
    "s_add_u32 s46, s44, 196608\n"
    "buffer_load_dwordx4 v1, s[8:11], s46 offen lds\n"
    "buffer_load_dwordx4 v2, s[8:11], s46 offen offset:1024 lds\n"
    "buffer_load_dwordx4 v3, s[8:11], s46 offen offset:2048 lds\n"
    "buffer_load_dwordx4 v4, s[8:11], s46 offen offset:3072 lds\n"
    "s_mov_b32 m0, 81920\n"
    "s_add_u32 s45, s44, 262144\n"
    "buffer_load_dwordx4 v1, s[8:11], s45 offen lds\n"
    "buffer_load_dwordx4 v2, s[8:11], s45 offen offset:1024 lds\n"
    "buffer_load_dwordx4 v3, s[8:11], s45 offen offset:2048 lds\n"
    "buffer_load_dwordx4 v4, s[8:11], s45 offen offset:3072 lds\n"
    "s_mov_b32 m0, 86016\n"
    "s_add_u32 s46, s44, 327680\n"
    "buffer_load_dwordx4 v1, s[8:11], s46 offen lds\n"
    "buffer_load_dwordx4 v2, s[8:11], s46 offen offset:1024 lds\n"
    "s_add_u32 s43, s43, 4096\n"
    "s_mov_b32 s39, 32768\n"
    "s_add_u32 m0, s39, 88064\n"
    "s_add_u32 s45, s43, 0\n"
    "buffer_load_dwordx4 v5, s[12:15], s45 offen lds\n"
    "buffer_load_dwordx4 v6, s[12:15], s45 offen offset:1024 lds\n"
    "buffer_load_dwordx4 v7, s[12:15], s45 offen offset:2048 lds\n"
    "buffer_load_dwordx4 v8, s[12:15], s45 offen offset:3072 lds\n"
    "s_add_u32 m0, s39, 92160\n"
    "s_add_u32 s46, s43, 131072\n"
    "buffer_load_dwordx4 v5, s[12:15], s46 offen lds\n"
    "buffer_load_dwordx4 v6, s[12:15], s46 offen offset:1024 lds\n"
    "buffer_load_dwordx4 v7, s[12:15], s46 offen offset:2048 lds\n"
    "buffer_load_dwordx4 v8, s[12:15], s46 offen offset:3072 lds\n"
    "s_add_u32 m0, s39, 96256\n"
    "s_add_u32 s45, s43, 262144\n"
    "buffer_load_dwordx4 v5, s[12:15], s45 offen lds\n"
    "buffer_load_dwordx4 v6, s[12:15], s45 offen offset:1024 lds\n"
    "buffer_load_dwordx4 v7, s[12:15], s45 offen offset:2048 lds\n"
    "buffer_load_dwordx4 v8, s[12:15], s45 offen offset:3072 lds\n"
    "s_add_u32 m0, s39, 100352\n"
    "s_add_u32 s46, s43, 393216\n"
    "buffer_load_dwordx4 v5, s[12:15], s46 offen lds\n"
    "buffer_load_dwordx4 v6, s[12:15], s46 offen offset:1024 lds\n"
    "buffer_load_dwordx4 v7, s[12:15], s46 offen offset:2048 lds\n"
    "buffer_load_dwordx4 v8, s[12:15], s46 offen offset:3072 lds\n"
    "s_load_dwordx2 s[62:63], %4, 0x100\n"
    "s_mov_b64 s[16:17], %2\n"
    "s_and_b32 s17, s17, 0xffff\n"
    "s_mov_b32 s18, 2228224\n"
    "s_mov_b32 s19, 0x20000\n"
    "s_mov_b64 s[20:21], %3\n"
    "s_and_b32 s21, s21, 0xffff\n"
    "s_mov_b32 s22, 2228224\n"
    "s_mov_b32 s23, 0x20000\n"
    "s_mov_b64 s[24:25], %4\n"
    "s_and_b32 s25, s25, 0xffff\n"
    "s_mov_b32 s26, 2162688\n"
    "s_mov_b32 s27, 0x20000\n"
    "s_add_u32 s24, s24, 4096\n"
    "s_addc_u32 s25, s25, 0\n"
    "v_lshlrev_b32 v37, 4, v44\n"
    "v_lshlrev_b32 v52, 1, v45\n"
    "v_xor_b32 v52, v46, v52\n"
    "v_lshlrev_b32 v52, 4, v52\n"
    "v_lshl_add_u32 v13, v45, 8, v52\n"
    "v_xor_b32 v14, 128, v13\n"
    "v_xor_b32 v52, v46, v45\n"
    "v_lshlrev_b32 v52, 4, v52\n"
    "v_lshl_add_u32 v9, v45, 8, v52\n"
    "v_lshl_add_u32 v33, v46, 8, v52\n"
    "v_xor_b32 v10, 64, v9\n"
    "v_xor_b32 v34, 64, v33\n"
    "v_xor_b32 v11, 128, v9\n"
    "v_xor_b32 v35, 128, v33\n"
    "v_xor_b32 v12, 192, v9\n"
    "v_xor_b32 v36, 192, v33\n"
    "s_mul_i32 s45, s36, 6144\n"
    "s_add_u32 s45, s45, 65536\n"
    "v_lshrrev_b32 v52, 1, v46\n"
    "v_xor_b32 v52, v52, v45\n"
    "v_lshlrev_b32 v52, 4, v52\n"
    "v_lshl_add_u32 v52, v46, 7, v52\n"
    "v_add_u32 v15, s45, v52\n"
    "v_xor_b32 v16, 64, v15\n"
    "v_add_u32 v38, 71680, v15\n"
    "v_add_u32 v39, 71680, v16\n"
    "v_lshl_add_u32 v54, v45, 3, v47\n"
    "v_lshlrev_b32 v54, 8, v54\n"
    "v_lshl_add_u32 v54, v48, 3, v54\n"
    "v_lshl_add_u32 v54, v49, 5, v54\n"
    "v_add_u32 v17, 104448, v54\n"
    "v_lshl_add_u32 v55, v45, 2, v47\n"
    "v_lshlrev_b32 v55, 8, v55\n"
    "v_lshl_add_u32 v55, v48, 3, v55\n"
    "v_lshl_add_u32 v55, v49, 5, v55\n"
    "v_add_u32 v25, 65536, v55\n"
    "v_xor_b32 v18, 32, v17\n"
    "v_xor_b32 v26, 32, v25\n"
    "v_xor_b32 v19, 64, v17\n"
    "v_xor_b32 v27, 64, v25\n"
    "v_xor_b32 v20, 96, v17\n"
    "v_xor_b32 v28, 96, v25\n"
    "v_xor_b32 v21, 128, v17\n"
    "v_xor_b32 v29, 128, v25\n"
    "v_xor_b32 v22, 160, v17\n"
    "v_xor_b32 v30, 160, v25\n"
    "v_xor_b32 v23, 192, v17\n"
    "v_xor_b32 v31, 192, v25\n"
    "v_xor_b32 v24, 224, v17\n"
    "v_xor_b32 v32, 224, v25\n"
    "s_mov_b32 s41, 0x4000\n"
    "s_mov_b32 s42, 0xffff8000\n"
    "s_mov_b32 s58, 0x3e0293ee\n"
    "s_mov_b32 s59, 0x3e0293ee\n"
    "s_mov_b32 s38, 0x8000\n"
    "s_mov_b32 s60, 2097152\n"
    "s_lshl_b32 s61, s36, 12\n"
    "s_add_u32 s61, s61, 147456\n"
    "v_mov_b32 v52, 0x3f803f80\n"
    "v_accvgpr_write_b32 a224, v52\n"
    "v_accvgpr_write_b32 a228, 0\n"
    "v_accvgpr_write_b32 a225, v52\n"
    "v_accvgpr_write_b32 a229, 0\n"
    "v_accvgpr_write_b32 a226, v52\n"
    "v_accvgpr_write_b32 a230, 0\n"
    "v_accvgpr_write_b32 a227, v52\n"
    "v_accvgpr_write_b32 a231, 0\n"
    "v_and_b32 v42, 15, v44\n"
    "v_lshlrev_b32 v42, 3, v42\n"
    "s_nop 1\n"
    "v_mfma_f32_32x32x16_bf16 v[64:79], a[228:231], a[228:231], 0\n"
    "v_mfma_f32_32x32x16_bf16 v[80:95], a[228:231], a[228:231], 0\n"
    "v_mfma_f32_32x32x16_bf16 v[96:111], a[228:231], a[228:231], 0\n"
    "v_mfma_f32_32x32x16_bf16 v[112:127], a[228:231], a[228:231], 0\n"
    "v_mfma_f32_32x32x16_bf16 v[128:143], a[228:231], a[228:231], 0\n"
    "v_mfma_f32_32x32x16_bf16 v[144:159], a[228:231], a[228:231], 0\n"
    "v_mfma_f32_32x32x16_bf16 v[160:175], a[228:231], a[228:231], 0\n"
    "v_mfma_f32_32x32x16_bf16 v[176:191], a[228:231], a[228:231], 0\n"
    "v_mfma_f32_32x32x16_bf16 v[192:207], a[228:231], a[228:231], 0\n"
    "v_mfma_f32_32x32x16_bf16 v[208:223], a[228:231], a[228:231], 0\n"
    "v_mfma_f32_32x32x16_bf16 v[224:239], a[228:231], a[228:231], 0\n"
    "v_mfma_f32_32x32x16_bf16 v[240:255], a[228:231], a[228:231], 0\n"
    "s_waitcnt vmcnt(16)\n"
    "s_barrier\n"
    "ds_read_b64_tr_b16 a[0:1], v17 offset:0\n"
    "ds_read_b64_tr_b16 a[2:3], v17 offset:1024\n"
    "ds_read_b64_tr_b16 a[4:5], v17 offset:8192\n"
    "ds_read_b64_tr_b16 a[6:7], v17 offset:9216\n"
    "ds_read_b64_tr_b16 a[8:9], v18 offset:0\n"
    "ds_read_b64_tr_b16 a[10:11], v18 offset:1024\n"
    "ds_read_b64_tr_b16 a[12:13], v18 offset:8192\n"
    "ds_read_b64_tr_b16 a[14:15], v18 offset:9216\n"
    "ds_read_b64_tr_b16 a[16:17], v19 offset:0\n"
    "ds_read_b64_tr_b16 a[18:19], v19 offset:1024\n"
    "ds_read_b64_tr_b16 a[20:21], v19 offset:8192\n"
    "ds_read_b64_tr_b16 a[22:23], v19 offset:9216\n"
    "ds_read_b64_tr_b16 a[24:25], v20 offset:0\n"
    "ds_read_b64_tr_b16 a[26:27], v20 offset:1024\n"
    "ds_read_b64_tr_b16 a[28:29], v20 offset:8192\n"
    "ds_read_b64_tr_b16 a[30:31], v20 offset:9216\n"
    "ds_read_b64_tr_b16 a[32:33], v21 offset:0\n"
    "ds_read_b64_tr_b16 a[34:35], v21 offset:1024\n"
    "ds_read_b64_tr_b16 a[36:37], v21 offset:8192\n"
    "ds_read_b64_tr_b16 a[38:39], v21 offset:9216\n"
    "ds_read_b64_tr_b16 a[40:41], v22 offset:0\n"
    "ds_read_b64_tr_b16 a[42:43], v22 offset:1024\n"
    "ds_read_b64_tr_b16 a[44:45], v22 offset:8192\n"
    "ds_read_b64_tr_b16 a[46:47], v22 offset:9216\n"
    "ds_read_b64_tr_b16 a[48:49], v23 offset:0\n"
    "ds_read_b64_tr_b16 a[50:51], v23 offset:1024\n"
    "ds_read_b64_tr_b16 a[52:53], v23 offset:8192\n"
    "ds_read_b64_tr_b16 a[54:55], v23 offset:9216\n"
    "ds_read_b64_tr_b16 a[56:57], v24 offset:0\n"
    "ds_read_b64_tr_b16 a[58:59], v24 offset:1024\n"
    "ds_read_b64_tr_b16 a[60:61], v24 offset:8192\n"
    "ds_read_b64_tr_b16 a[62:63], v24 offset:9216\n"
    "ds_read_b128 a[128:131], v15 offset:0\n"
    "ds_read_b128 a[132:135], v16 offset:0\n"
    "ds_read_b128 a[136:139], v15 offset:2048\n"
    "ds_read_b128 a[140:143], v16 offset:2048\n"
    "s_and_b32 s44, s50, 15\n"
    "s_lshr_b64 s[50:51], s[50:51], 4\n"
    "s_lshl_b32 s44, s44, 7\n"
    "s_lshl_b32 s43, s44, 12\n"
    "s_mov_b32 s39, 0\n"
    "s_add_u32 m0, s39, 88064\n"
    "s_add_u32 s45, s43, 0\n"
    "buffer_load_dwordx4 v5, s[12:15], s45 offen lds\n"
    "buffer_load_dwordx4 v6, s[12:15], s45 offen offset:1024 lds\n"
    "buffer_load_dwordx4 v7, s[12:15], s45 offen offset:2048 lds\n"
    "buffer_load_dwordx4 v8, s[12:15], s45 offen offset:3072 lds\n"
    "s_add_u32 m0, s39, 92160\n"
    "s_add_u32 s46, s43, 131072\n"
    "buffer_load_dwordx4 v5, s[12:15], s46 offen lds\n"
    "buffer_load_dwordx4 v6, s[12:15], s46 offen offset:1024 lds\n"
    "buffer_load_dwordx4 v7, s[12:15], s46 offen offset:2048 lds\n"
    "buffer_load_dwordx4 v8, s[12:15], s46 offen offset:3072 lds\n"
    "s_add_u32 m0, s39, 96256\n"
    "s_add_u32 s45, s43, 262144\n"
    "buffer_load_dwordx4 v5, s[12:15], s45 offen lds\n"
    "buffer_load_dwordx4 v6, s[12:15], s45 offen offset:1024 lds\n"
    "buffer_load_dwordx4 v7, s[12:15], s45 offen offset:2048 lds\n"
    "buffer_load_dwordx4 v8, s[12:15], s45 offen offset:3072 lds\n"
    "s_add_u32 m0, s39, 100352\n"
    "s_add_u32 s46, s43, 393216\n"
    "buffer_load_dwordx4 v5, s[12:15], s46 offen lds\n"
    "buffer_load_dwordx4 v6, s[12:15], s46 offen offset:1024 lds\n"
    "buffer_load_dwordx4 v7, s[12:15], s46 offen offset:2048 lds\n"
    "buffer_load_dwordx4 v8, s[12:15], s46 offen offset:3072 lds\n"
    "s_mov_b32 m0, 137216\n"
    "s_add_u32 s45, s44, 0\n"
    "buffer_load_dwordx4 v1, s[8:11], s45 offen lds\n"
    "buffer_load_dwordx4 v2, s[8:11], s45 offen offset:1024 lds\n"
    "buffer_load_dwordx4 v3, s[8:11], s45 offen offset:2048 lds\n"
    "buffer_load_dwordx4 v4, s[8:11], s45 offen offset:3072 lds\n"
    "s_mov_b32 m0, 141312\n"
    "s_add_u32 s46, s44, 65536\n"
    "buffer_load_dwordx4 v1, s[8:11], s46 offen lds\n"
    "buffer_load_dwordx4 v2, s[8:11], s46 offen offset:1024 lds\n"
    "buffer_load_dwordx4 v3, s[8:11], s46 offen offset:2048 lds\n"
    "buffer_load_dwordx4 v4, s[8:11], s46 offen offset:3072 lds\n"
    "s_mov_b32 m0, 145408\n"
    "s_add_u32 s45, s44, 131072\n"
    "buffer_load_dwordx4 v1, s[8:11], s45 offen lds\n"
    "buffer_load_dwordx4 v2, s[8:11], s45 offen offset:1024 lds\n"
    "buffer_load_dwordx4 v3, s[8:11], s45 offen offset:2048 lds\n"
    "buffer_load_dwordx4 v4, s[8:11], s45 offen offset:3072 lds\n"
    "s_mov_b32 m0, 149504\n"
    "s_add_u32 s46, s44, 196608\n"
    "buffer_load_dwordx4 v1, s[8:11], s46 offen lds\n"
    "buffer_load_dwordx4 v2, s[8:11], s46 offen offset:1024 lds\n"
    "buffer_load_dwordx4 v3, s[8:11], s46 offen offset:2048 lds\n"
    "buffer_load_dwordx4 v4, s[8:11], s46 offen offset:3072 lds\n"
    "s_mov_b32 m0, 153600\n"
    "s_add_u32 s45, s44, 262144\n"
    "buffer_load_dwordx4 v1, s[8:11], s45 offen lds\n"
    "buffer_load_dwordx4 v2, s[8:11], s45 offen offset:1024 lds\n"
    "buffer_load_dwordx4 v3, s[8:11], s45 offen offset:2048 lds\n"
    "buffer_load_dwordx4 v4, s[8:11], s45 offen offset:3072 lds\n"
    "s_mov_b32 m0, 157696\n"
    "s_add_u32 s46, s44, 327680\n"
    "buffer_load_dwordx4 v1, s[8:11], s46 offen lds\n"
    "buffer_load_dwordx4 v2, s[8:11], s46 offen offset:1024 lds\n"
    "s_add_u32 s43, s43, 4096\n"
    "s_mov_b32 s39, 0x4000\n"
    "s_and_b32 s44, s50, 15\n"
    "s_lshr_b64 s[50:51], s[50:51], 4\n"
    "s_lshl_b32 s44, s44, 7\n"
    "v_add_u32 v17, s41, v17\n"
    "v_add_u32 v18, s41, v18\n"
    "v_add_u32 v19, s41, v19\n"
    "v_add_u32 v20, s41, v20\n"
    "v_add_u32 v21, s41, v21\n"
    "v_add_u32 v22, s41, v22\n"
    "v_add_u32 v23, s41, v23\n"
    "v_add_u32 v24, s41, v24\n"
    "s_mov_b32 s37, 7\n"
    ".L_qD_%=:\n"
    "s_waitcnt vmcnt(30) lgkmcnt(0)\n"
    "s_barrier\n"
    "v_mfma_f32_16x16x32_bf16 v[160:163], a[0:3], a[128:131], v[160:163]\n"
    "s_cmp_eq_u32 s38, 0x8000\n"
    "ds_read_b64_tr_b16 a[64:65], v17 offset:0\n"
    "ds_read_b64_tr_b16 a[66:67], v17 offset:1024\n"
    "v_mfma_f32_16x16x32_bf16 v[160:163], a[4:7], a[132:135], v[160:163]\n"
    "s_cselect_b32 s40, s42, s41\n"
    "ds_read_b64_tr_b16 a[68:69], v17 offset:8192\n"
    "s_add_u32 m0, s39, 88064\n"
    "s_add_u32 s45, s43, 0\n"
    "buffer_load_dwordx4 v5, s[12:15], s45 offen lds\n"
    "buffer_load_dwordx4 v6, s[12:15], s45 offen offset:1024 lds\n"
    "v_mfma_f32_16x16x32_bf16 v[164:167], a[4:7], a[140:143], v[164:167]\n"
    "ds_read_b64_tr_b16 a[70:71], v17 offset:9216\n"
    "v_add_u32 v17, s40, v17\n"
    "buffer_load_dwordx4 v7, s[12:15], s45 offen offset:2048 lds\n"
    "v_mfma_f32_16x16x32_bf16 v[164:167], a[0:3], a[136:139], v[164:167]\n"
    "ds_read_b64_tr_b16 a[72:73], v18 offset:0\n"
    "buffer_load_dwordx4 v8, s[12:15], s45 offen offset:3072 lds\n"
    "v_mfma_f32_16x16x32_bf16 v[176:179], a[8:11], a[136:139], v[176:179]\n"
    "ds_read_b64_tr_b16 a[74:75], v18 offset:1024\n"
    "s_add_u32 m0, s39, 92160\n"
    "s_add_u32 s46, s43, 131072\n"
    "buffer_load_dwordx4 v5, s[12:15], s46 offen lds\n"
    "v_mfma_f32_16x16x32_bf16 v[176:179], a[12:15], a[140:143], v[176:179]\n"
    "ds_read_b64_tr_b16 a[76:77], v18 offset:8192\n"
    "buffer_load_dwordx4 v6, s[12:15], s46 offen offset:1024 lds\n"
    "v_mfma_f32_16x16x32_bf16 v[172:175], a[12:15], a[132:135], v[172:175]\n"
    "ds_read_b64_tr_b16 a[78:79], v18 offset:9216\n"
    "v_add_u32 v18, s40, v18\n"
    "buffer_load_dwordx4 v7, s[12:15], s46 offen offset:2048 lds\n"
    "v_mfma_f32_16x16x32_bf16 v[172:175], a[8:11], a[128:131], v[172:175]\n"
    "ds_read_b64_tr_b16 a[80:81], v19 offset:0\n"
    "buffer_load_dwordx4 v8, s[12:15], s46 offen offset:3072 lds\n"
    "v_mfma_f32_16x16x32_bf16 v[184:187], a[16:19], a[128:131], v[184:187]\n"
    "ds_read_b64_tr_b16 a[82:83], v19 offset:1024\n"
    "s_add_u32 m0, s39, 96256\n"
    "s_add_u32 s45, s43, 262144\n"
    "buffer_load_dwordx4 v5, s[12:15], s45 offen lds\n"
    "v_mfma_f32_16x16x32_bf16 v[184:187], a[20:23], a[132:135], v[184:187]\n"
    "ds_read_b64_tr_b16 a[84:85], v19 offset:8192\n"
    "buffer_load_dwordx4 v6, s[12:15], s45 offen offset:1024 lds\n"
    "v_mfma_f32_16x16x32_bf16 v[188:191], a[20:23], a[140:143], v[188:191]\n"
    "ds_read_b64_tr_b16 a[86:87], v19 offset:9216\n"
    "v_add_u32 v19, s40, v19\n"
    "buffer_load_dwordx4 v7, s[12:15], s45 offen offset:2048 lds\n"
    "v_mfma_f32_16x16x32_bf16 v[188:191], a[16:19], a[136:139], v[188:191]\n"
    "ds_read_b64_tr_b16 a[88:89], v20 offset:0\n"
    "buffer_load_dwordx4 v8, s[12:15], s45 offen offset:3072 lds\n"
    "v_mfma_f32_16x16x32_bf16 v[200:203], a[24:27], a[136:139], v[200:203]\n"
    "ds_read_b64_tr_b16 a[90:91], v20 offset:1024\n"
    "s_add_u32 m0, s39, 100352\n"
    "s_add_u32 s46, s43, 393216\n"
    "buffer_load_dwordx4 v5, s[12:15], s46 offen lds\n"
    "v_mfma_f32_16x16x32_bf16 v[200:203], a[28:31], a[140:143], v[200:203]\n"
    "ds_read_b64_tr_b16 a[92:93], v20 offset:8192\n"
    "buffer_load_dwordx4 v6, s[12:15], s46 offen offset:1024 lds\n"
    "v_mfma_f32_16x16x32_bf16 v[196:199], a[28:31], a[132:135], v[196:199]\n"
    "ds_read_b64_tr_b16 a[94:95], v20 offset:9216\n"
    "v_add_u32 v20, s40, v20\n"
    "buffer_load_dwordx4 v7, s[12:15], s46 offen offset:2048 lds\n"
    "v_mfma_f32_16x16x32_bf16 v[196:199], a[24:27], a[128:131], v[196:199]\n"
    "ds_read_b64_tr_b16 a[96:97], v21 offset:0\n"
    "ds_read_b64_tr_b16 a[98:99], v21 offset:1024\n"
    "buffer_load_dwordx4 v8, s[12:15], s46 offen offset:3072 lds\n"
    "v_mfma_f32_16x16x32_bf16 v[208:211], a[32:35], a[128:131], v[208:211]\n"
    "ds_read_b64_tr_b16 a[100:101], v21 offset:8192\n"
    "s_mov_b32 m0, 65536\n"
    "s_add_u32 s45, s44, 0\n"
    "buffer_load_dwordx4 v1, s[8:11], s45 offen lds\n"
    "buffer_load_dwordx4 v2, s[8:11], s45 offen offset:1024 lds\n"
    "v_mfma_f32_16x16x32_bf16 v[208:211], a[36:39], a[132:135], v[208:211]\n"
    "ds_read_b64_tr_b16 a[102:103], v21 offset:9216\n"
    "v_add_u32 v21, s40, v21\n"
    "buffer_load_dwordx4 v3, s[8:11], s45 offen offset:2048 lds\n"
    "v_mfma_f32_16x16x32_bf16 v[212:215], a[36:39], a[140:143], v[212:215]\n"
    "ds_read_b64_tr_b16 a[104:105], v22 offset:0\n"
    "buffer_load_dwordx4 v4, s[8:11], s45 offen offset:3072 lds\n"
    "v_mfma_f32_16x16x32_bf16 v[212:215], a[32:35], a[136:139], v[212:215]\n"
    "ds_read_b64_tr_b16 a[106:107], v22 offset:1024\n"
    "s_mov_b32 m0, 69632\n"
    "s_add_u32 s46, s44, 65536\n"
    "buffer_load_dwordx4 v1, s[8:11], s46 offen lds\n"
    "v_mfma_f32_16x16x32_bf16 v[224:227], a[40:43], a[136:139], v[224:227]\n"
    "ds_read_b64_tr_b16 a[108:109], v22 offset:8192\n"
    "buffer_load_dwordx4 v2, s[8:11], s46 offen offset:1024 lds\n"
    "v_mfma_f32_16x16x32_bf16 v[224:227], a[44:47], a[140:143], v[224:227]\n"
    "ds_read_b64_tr_b16 a[110:111], v22 offset:9216\n"
    "v_add_u32 v22, s40, v22\n"
    "buffer_load_dwordx4 v3, s[8:11], s46 offen offset:2048 lds\n"
    "v_mfma_f32_16x16x32_bf16 v[220:223], a[44:47], a[132:135], v[220:223]\n"
    "ds_read_b64_tr_b16 a[112:113], v23 offset:0\n"
    "buffer_load_dwordx4 v4, s[8:11], s46 offen offset:3072 lds\n"
    "v_mfma_f32_16x16x32_bf16 v[220:223], a[40:43], a[128:131], v[220:223]\n"
    "ds_read_b64_tr_b16 a[114:115], v23 offset:1024\n"
    "s_mov_b32 m0, 73728\n"
    "s_add_u32 s45, s44, 131072\n"
    "buffer_load_dwordx4 v1, s[8:11], s45 offen lds\n"
    "v_mfma_f32_16x16x32_bf16 v[232:235], a[48:51], a[128:131], v[232:235]\n"
    "ds_read_b64_tr_b16 a[116:117], v23 offset:8192\n"
    "buffer_load_dwordx4 v2, s[8:11], s45 offen offset:1024 lds\n"
    "v_mfma_f32_16x16x32_bf16 v[232:235], a[52:55], a[132:135], v[232:235]\n"
    "ds_read_b64_tr_b16 a[118:119], v23 offset:9216\n"
    "v_add_u32 v23, s40, v23\n"
    "buffer_load_dwordx4 v3, s[8:11], s45 offen offset:2048 lds\n"
    "v_mfma_f32_16x16x32_bf16 v[236:239], a[52:55], a[140:143], v[236:239]\n"
    "ds_read_b64_tr_b16 a[120:121], v24 offset:0\n"
    "buffer_load_dwordx4 v4, s[8:11], s45 offen offset:3072 lds\n"
    "v_mfma_f32_16x16x32_bf16 v[236:239], a[48:51], a[136:139], v[236:239]\n"
    "ds_read_b64_tr_b16 a[122:123], v24 offset:1024\n"
    "s_mov_b32 m0, 77824\n"
    "s_add_u32 s46, s44, 196608\n"
    "buffer_load_dwordx4 v1, s[8:11], s46 offen lds\n"
    "v_mfma_f32_16x16x32_bf16 v[248:251], a[56:59], a[136:139], v[248:251]\n"
    "ds_read_b64_tr_b16 a[124:125], v24 offset:8192\n"
    "buffer_load_dwordx4 v2, s[8:11], s46 offen offset:1024 lds\n"
    "v_mfma_f32_16x16x32_bf16 v[248:251], a[60:63], a[140:143], v[248:251]\n"
    "ds_read_b64_tr_b16 a[126:127], v24 offset:9216\n"
    "v_add_u32 v24, s40, v24\n"
    "buffer_load_dwordx4 v3, s[8:11], s46 offen offset:2048 lds\n"
    "v_mfma_f32_16x16x32_bf16 v[244:247], a[60:63], a[132:135], v[244:247]\n"
    "buffer_load_dwordx4 v4, s[8:11], s46 offen offset:3072 lds\n"
    "v_mfma_f32_16x16x32_bf16 v[244:247], a[56:59], a[128:131], v[244:247]\n"
    "s_lshl_b32 s43, s44, 12\n"
    "s_mov_b32 s39, s38\n"
    "s_add_u32 s38, s38, s40\n"
    "s_waitcnt vmcnt(32) lgkmcnt(0)\n"
    "s_barrier\n"
    "v_mfma_f32_16x16x32_bf16 v[64:67], a[64:67], a[128:131], v[64:67]\n"
    "s_cmp_eq_u32 s38, 0x8000\n"
    "ds_read_b128 a[152:155], v38 offset:0\n"
    "ds_read_b128 a[156:159], v39 offset:0\n"
    "v_mfma_f32_16x16x32_bf16 v[64:67], a[68:71], a[132:135], v[64:67]\n"
    "s_cselect_b32 s40, s42, s41\n"
    "ds_read_b128 a[160:163], v38 offset:2048\n"
    "v_mfma_f32_16x16x32_bf16 v[68:71], a[68:71], a[140:143], v[68:71]\n"
    "s_add_u32 s45, s43, 0\n"
    "ds_read_b128 a[164:167], v39 offset:2048\n"
    "v_mfma_f32_16x16x32_bf16 v[68:71], a[64:67], a[136:139], v[68:71]\n"
    "s_add_u32 m0, s39, 88064\n"
    "ds_read_b64_tr_b16 a[0:1], v17 offset:0\n"
    "buffer_load_dwordx4 v5, s[12:15], s45 offen lds\n"
    "v_mfma_f32_16x16x32_bf16 v[80:83], a[72:75], a[136:139], v[80:83]\n"
    "ds_read_b64_tr_b16 a[2:3], v17 offset:1024\n"
    "buffer_load_dwordx4 v6, s[12:15], s45 offen offset:1024 lds\n"
    "v_mfma_f32_16x16x32_bf16 v[80:83], a[76:79], a[140:143], v[80:83]\n"
    "ds_read_b64_tr_b16 a[4:5], v17 offset:8192\n"
    "ds_read_b64_tr_b16 a[6:7], v17 offset:9216\n"
    "v_add_u32 v17, s40, v17\n"
    "buffer_load_dwordx4 v7, s[12:15], s45 offen offset:2048 lds\n"
    "v_mfma_f32_16x16x32_bf16 v[76:79], a[76:79], a[132:135], v[76:79]\n"
    "s_add_u32 s46, s43, 131072\n"
    "ds_read_b64_tr_b16 a[8:9], v18 offset:0\n"
    "buffer_load_dwordx4 v8, s[12:15], s45 offen offset:3072 lds\n"
    "v_mfma_f32_16x16x32_bf16 v[76:79], a[72:75], a[128:131], v[76:79]\n"
    "s_add_u32 m0, s39, 92160\n"
    "ds_read_b64_tr_b16 a[10:11], v18 offset:1024\n"
    "buffer_load_dwordx4 v5, s[12:15], s46 offen lds\n"
    "v_mfma_f32_16x16x32_bf16 v[88:91], a[80:83], a[128:131], v[88:91]\n"
    "ds_read_b64_tr_b16 a[12:13], v18 offset:8192\n"
    "buffer_load_dwordx4 v6, s[12:15], s46 offen offset:1024 lds\n"
    "v_mfma_f32_16x16x32_bf16 v[88:91], a[84:87], a[132:135], v[88:91]\n"
    "ds_read_b64_tr_b16 a[14:15], v18 offset:9216\n"
    "v_add_u32 v18, s40, v18\n"
    "buffer_load_dwordx4 v7, s[12:15], s46 offen offset:2048 lds\n"
    "v_mfma_f32_16x16x32_bf16 v[92:95], a[84:87], a[140:143], v[92:95]\n"
    "s_add_u32 s45, s43, 262144\n"
    "ds_read_b64_tr_b16 a[16:17], v19 offset:0\n"
    "ds_read_b64_tr_b16 a[18:19], v19 offset:1024\n"
    "buffer_load_dwordx4 v8, s[12:15], s46 offen offset:3072 lds\n"
    "v_mfma_f32_16x16x32_bf16 v[92:95], a[80:83], a[136:139], v[92:95]\n"
    "s_add_u32 m0, s39, 96256\n"
    "ds_read_b64_tr_b16 a[20:21], v19 offset:8192\n"
    "buffer_load_dwordx4 v5, s[12:15], s45 offen lds\n"
    "v_mfma_f32_16x16x32_bf16 v[104:107], a[88:91], a[136:139], v[104:107]\n"
    "ds_read_b64_tr_b16 a[22:23], v19 offset:9216\n"
    "v_add_u32 v19, s40, v19\n"
    "buffer_load_dwordx4 v6, s[12:15], s45 offen offset:1024 lds\n"
    "v_mfma_f32_16x16x32_bf16 v[104:107], a[92:95], a[140:143], v[104:107]\n"
    "ds_read_b64_tr_b16 a[24:25], v20 offset:0\n"
    "buffer_load_dwordx4 v7, s[12:15], s45 offen offset:2048 lds\n"
    "v_mfma_f32_16x16x32_bf16 v[100:103], a[92:95], a[132:135], v[100:103]\n"
    "s_add_u32 s46, s43, 393216\n"
    "ds_read_b64_tr_b16 a[26:27], v20 offset:1024\n"
    "buffer_load_dwordx4 v8, s[12:15], s45 offen offset:3072 lds\n"
    "v_mfma_f32_16x16x32_bf16 v[100:103], a[88:91], a[128:131], v[100:103]\n"
    "s_add_u32 m0, s39, 100352\n"
    "ds_read_b64_tr_b16 a[28:29], v20 offset:8192\n"
    "ds_read_b64_tr_b16 a[30:31], v20 offset:9216\n"
    "v_add_u32 v20, s40, v20\n"
    "buffer_load_dwordx4 v5, s[12:15], s46 offen lds\n"
    "v_mfma_f32_16x16x32_bf16 v[112:115], a[96:99], a[128:131], v[112:115]\n"
    "ds_read_b64_tr_b16 a[32:33], v21 offset:0\n"
    "buffer_load_dwordx4 v6, s[12:15], s46 offen offset:1024 lds\n"
    "v_mfma_f32_16x16x32_bf16 v[112:115], a[100:103], a[132:135], v[112:115]\n"
    "ds_read_b64_tr_b16 a[34:35], v21 offset:1024\n"
    "buffer_load_dwordx4 v7, s[12:15], s46 offen offset:2048 lds\n"
    "v_mfma_f32_16x16x32_bf16 v[116:119], a[100:103], a[140:143], v[116:119]\n"
    "s_add_u32 s45, s44, 262144\n"
    "ds_read_b64_tr_b16 a[36:37], v21 offset:8192\n"
    "buffer_load_dwordx4 v8, s[12:15], s46 offen offset:3072 lds\n"
    "v_mfma_f32_16x16x32_bf16 v[116:119], a[96:99], a[136:139], v[116:119]\n"
    "s_mov_b32 m0, 81920\n"
    "ds_read_b64_tr_b16 a[38:39], v21 offset:9216\n"
    "v_add_u32 v21, s40, v21\n"
    "buffer_load_dwordx4 v1, s[8:11], s45 offen lds\n"
    "v_mfma_f32_16x16x32_bf16 v[128:131], a[104:107], a[136:139], v[128:131]\n"
    "ds_read_b64_tr_b16 a[40:41], v22 offset:0\n"
    "ds_read_b64_tr_b16 a[42:43], v22 offset:1024\n"
    "buffer_load_dwordx4 v2, s[8:11], s45 offen offset:1024 lds\n"
    "v_mfma_f32_16x16x32_bf16 v[128:131], a[108:111], a[140:143], v[128:131]\n"
    "ds_read_b64_tr_b16 a[44:45], v22 offset:8192\n"
    "buffer_load_dwordx4 v3, s[8:11], s45 offen offset:2048 lds\n"
    "v_mfma_f32_16x16x32_bf16 v[124:127], a[108:111], a[132:135], v[124:127]\n"
    "s_add_u32 s46, s44, 327680\n"
    "ds_read_b64_tr_b16 a[46:47], v22 offset:9216\n"
    "v_add_u32 v22, s40, v22\n"
    "buffer_load_dwordx4 v4, s[8:11], s45 offen offset:3072 lds\n"
    "v_mfma_f32_16x16x32_bf16 v[124:127], a[104:107], a[128:131], v[124:127]\n"
    "s_mov_b32 m0, 86016\n"
    "ds_read_b64_tr_b16 a[48:49], v23 offset:0\n"
    "buffer_load_dwordx4 v1, s[8:11], s46 offen lds\n"
    "v_mfma_f32_16x16x32_bf16 v[136:139], a[112:115], a[128:131], v[136:139]\n"
    "ds_read_b64_tr_b16 a[50:51], v23 offset:1024\n"
    "buffer_load_dwordx4 v2, s[8:11], s46 offen offset:1024 lds\n"
    "v_mfma_f32_16x16x32_bf16 v[136:139], a[116:119], a[132:135], v[136:139]\n"
    "ds_read_b64_tr_b16 a[52:53], v23 offset:8192\n"
    "ds_read_b64_tr_b16 a[54:55], v23 offset:9216\n"
    "v_add_u32 v23, s40, v23\n"
    "v_mfma_f32_16x16x32_bf16 v[140:143], a[116:119], a[140:143], v[140:143]\n"
    "ds_read_b64_tr_b16 a[56:57], v24 offset:0\n"
    "v_mfma_f32_16x16x32_bf16 v[140:143], a[112:115], a[136:139], v[140:143]\n"
    "ds_read_b64_tr_b16 a[58:59], v24 offset:1024\n"
    "v_mfma_f32_16x16x32_bf16 v[152:155], a[120:123], a[136:139], v[152:155]\n"
    "ds_read_b64_tr_b16 a[60:61], v24 offset:8192\n"
    "v_mfma_f32_16x16x32_bf16 v[152:155], a[124:127], a[140:143], v[152:155]\n"
    "ds_read_b64_tr_b16 a[62:63], v24 offset:9216\n"
    "v_add_u32 v24, s40, v24\n"
    "v_mfma_f32_16x16x32_bf16 v[148:151], a[124:127], a[132:135], v[148:151]\n"
    "v_mfma_f32_16x16x32_bf16 v[148:151], a[120:123], a[128:131], v[148:151]\n"
    "s_add_u32 s43, s43, 4096\n"
    "s_and_b32 s44, s50, 15\n"
    "s_lshr_b64 s[50:51], s[50:51], 4\n"
    "s_lshl_b32 s44, s44, 7\n"
    "s_mov_b32 s39, s38\n"
    "s_add_u32 s38, s38, s40\n"
    "s_waitcnt vmcnt(30) lgkmcnt(0)\n"
    "s_barrier\n"
    "v_mfma_f32_16x16x32_bf16 v[160:163], a[0:3], a[152:155], v[160:163]\n"
    "s_cmp_eq_u32 s38, 0x8000\n"
    "ds_read_b64_tr_b16 a[64:65], v17 offset:0\n"
    "ds_read_b64_tr_b16 a[66:67], v17 offset:1024\n"
    "v_mfma_f32_16x16x32_bf16 v[160:163], a[4:7], a[156:159], v[160:163]\n"
    "s_cselect_b32 s40, s42, s41\n"
    "ds_read_b64_tr_b16 a[68:69], v17 offset:8192\n"
    "s_add_u32 m0, s39, 88064\n"
    "s_add_u32 s45, s43, 0\n"
    "buffer_load_dwordx4 v5, s[12:15], s45 offen lds\n"
    "buffer_load_dwordx4 v6, s[12:15], s45 offen offset:1024 lds\n"
    "v_mfma_f32_16x16x32_bf16 v[164:167], a[4:7], a[164:167], v[164:167]\n"
    "ds_read_b64_tr_b16 a[70:71], v17 offset:9216\n"
    "v_add_u32 v17, s40, v17\n"
    "buffer_load_dwordx4 v7, s[12:15], s45 offen offset:2048 lds\n"
    "v_mfma_f32_16x16x32_bf16 v[164:167], a[0:3], a[160:163], v[164:167]\n"
    "ds_read_b64_tr_b16 a[72:73], v18 offset:0\n"
    "buffer_load_dwordx4 v8, s[12:15], s45 offen offset:3072 lds\n"
    "v_mfma_f32_16x16x32_bf16 v[176:179], a[8:11], a[160:163], v[176:179]\n"
    "ds_read_b64_tr_b16 a[74:75], v18 offset:1024\n"
    "s_add_u32 m0, s39, 92160\n"
    "s_add_u32 s46, s43, 131072\n"
    "buffer_load_dwordx4 v5, s[12:15], s46 offen lds\n"
    "v_mfma_f32_16x16x32_bf16 v[176:179], a[12:15], a[164:167], v[176:179]\n"
    "ds_read_b64_tr_b16 a[76:77], v18 offset:8192\n"
    "buffer_load_dwordx4 v6, s[12:15], s46 offen offset:1024 lds\n"
    "v_mfma_f32_16x16x32_bf16 v[172:175], a[12:15], a[156:159], v[172:175]\n"
    "ds_read_b64_tr_b16 a[78:79], v18 offset:9216\n"
    "v_add_u32 v18, s40, v18\n"
    "buffer_load_dwordx4 v7, s[12:15], s46 offen offset:2048 lds\n"
    "v_mfma_f32_16x16x32_bf16 v[172:175], a[8:11], a[152:155], v[172:175]\n"
    "ds_read_b64_tr_b16 a[80:81], v19 offset:0\n"
    "buffer_load_dwordx4 v8, s[12:15], s46 offen offset:3072 lds\n"
    "v_mfma_f32_16x16x32_bf16 v[184:187], a[16:19], a[152:155], v[184:187]\n"
    "ds_read_b64_tr_b16 a[82:83], v19 offset:1024\n"
    "s_add_u32 m0, s39, 96256\n"
    "s_add_u32 s45, s43, 262144\n"
    "buffer_load_dwordx4 v5, s[12:15], s45 offen lds\n"
    "v_mfma_f32_16x16x32_bf16 v[184:187], a[20:23], a[156:159], v[184:187]\n"
    "ds_read_b64_tr_b16 a[84:85], v19 offset:8192\n"
    "buffer_load_dwordx4 v6, s[12:15], s45 offen offset:1024 lds\n"
    "v_mfma_f32_16x16x32_bf16 v[188:191], a[20:23], a[164:167], v[188:191]\n"
    "ds_read_b64_tr_b16 a[86:87], v19 offset:9216\n"
    "v_add_u32 v19, s40, v19\n"
    "buffer_load_dwordx4 v7, s[12:15], s45 offen offset:2048 lds\n"
    "v_mfma_f32_16x16x32_bf16 v[188:191], a[16:19], a[160:163], v[188:191]\n"
    "ds_read_b64_tr_b16 a[88:89], v20 offset:0\n"
    "buffer_load_dwordx4 v8, s[12:15], s45 offen offset:3072 lds\n"
    "v_mfma_f32_16x16x32_bf16 v[200:203], a[24:27], a[160:163], v[200:203]\n"
    "ds_read_b64_tr_b16 a[90:91], v20 offset:1024\n"
    "s_add_u32 m0, s39, 100352\n"
    "s_add_u32 s46, s43, 393216\n"
    "buffer_load_dwordx4 v5, s[12:15], s46 offen lds\n"
    "v_mfma_f32_16x16x32_bf16 v[200:203], a[28:31], a[164:167], v[200:203]\n"
    "ds_read_b64_tr_b16 a[92:93], v20 offset:8192\n"
    "buffer_load_dwordx4 v6, s[12:15], s46 offen offset:1024 lds\n"
    "v_mfma_f32_16x16x32_bf16 v[196:199], a[28:31], a[156:159], v[196:199]\n"
    "ds_read_b64_tr_b16 a[94:95], v20 offset:9216\n"
    "v_add_u32 v20, s40, v20\n"
    "buffer_load_dwordx4 v7, s[12:15], s46 offen offset:2048 lds\n"
    "v_mfma_f32_16x16x32_bf16 v[196:199], a[24:27], a[152:155], v[196:199]\n"
    "ds_read_b64_tr_b16 a[96:97], v21 offset:0\n"
    "ds_read_b64_tr_b16 a[98:99], v21 offset:1024\n"
    "buffer_load_dwordx4 v8, s[12:15], s46 offen offset:3072 lds\n"
    "v_mfma_f32_16x16x32_bf16 v[208:211], a[32:35], a[152:155], v[208:211]\n"
    "ds_read_b64_tr_b16 a[100:101], v21 offset:8192\n"
    "s_mov_b32 m0, 137216\n"
    "s_add_u32 s45, s44, 0\n"
    "buffer_load_dwordx4 v1, s[8:11], s45 offen lds\n"
    "buffer_load_dwordx4 v2, s[8:11], s45 offen offset:1024 lds\n"
    "v_mfma_f32_16x16x32_bf16 v[208:211], a[36:39], a[156:159], v[208:211]\n"
    "ds_read_b64_tr_b16 a[102:103], v21 offset:9216\n"
    "v_add_u32 v21, s40, v21\n"
    "buffer_load_dwordx4 v3, s[8:11], s45 offen offset:2048 lds\n"
    "v_mfma_f32_16x16x32_bf16 v[212:215], a[36:39], a[164:167], v[212:215]\n"
    "ds_read_b64_tr_b16 a[104:105], v22 offset:0\n"
    "buffer_load_dwordx4 v4, s[8:11], s45 offen offset:3072 lds\n"
    "v_mfma_f32_16x16x32_bf16 v[212:215], a[32:35], a[160:163], v[212:215]\n"
    "ds_read_b64_tr_b16 a[106:107], v22 offset:1024\n"
    "s_mov_b32 m0, 141312\n"
    "s_add_u32 s46, s44, 65536\n"
    "buffer_load_dwordx4 v1, s[8:11], s46 offen lds\n"
    "v_mfma_f32_16x16x32_bf16 v[224:227], a[40:43], a[160:163], v[224:227]\n"
    "ds_read_b64_tr_b16 a[108:109], v22 offset:8192\n"
    "buffer_load_dwordx4 v2, s[8:11], s46 offen offset:1024 lds\n"
    "v_mfma_f32_16x16x32_bf16 v[224:227], a[44:47], a[164:167], v[224:227]\n"
    "ds_read_b64_tr_b16 a[110:111], v22 offset:9216\n"
    "v_add_u32 v22, s40, v22\n"
    "buffer_load_dwordx4 v3, s[8:11], s46 offen offset:2048 lds\n"
    "v_mfma_f32_16x16x32_bf16 v[220:223], a[44:47], a[156:159], v[220:223]\n"
    "ds_read_b64_tr_b16 a[112:113], v23 offset:0\n"
    "buffer_load_dwordx4 v4, s[8:11], s46 offen offset:3072 lds\n"
    "v_mfma_f32_16x16x32_bf16 v[220:223], a[40:43], a[152:155], v[220:223]\n"
    "ds_read_b64_tr_b16 a[114:115], v23 offset:1024\n"
    "s_mov_b32 m0, 145408\n"
    "s_add_u32 s45, s44, 131072\n"
    "buffer_load_dwordx4 v1, s[8:11], s45 offen lds\n"
    "v_mfma_f32_16x16x32_bf16 v[232:235], a[48:51], a[152:155], v[232:235]\n"
    "ds_read_b64_tr_b16 a[116:117], v23 offset:8192\n"
    "buffer_load_dwordx4 v2, s[8:11], s45 offen offset:1024 lds\n"
    "v_mfma_f32_16x16x32_bf16 v[232:235], a[52:55], a[156:159], v[232:235]\n"
    "ds_read_b64_tr_b16 a[118:119], v23 offset:9216\n"
    "v_add_u32 v23, s40, v23\n"
    "buffer_load_dwordx4 v3, s[8:11], s45 offen offset:2048 lds\n"
    "v_mfma_f32_16x16x32_bf16 v[236:239], a[52:55], a[164:167], v[236:239]\n"
    "ds_read_b64_tr_b16 a[120:121], v24 offset:0\n"
    "buffer_load_dwordx4 v4, s[8:11], s45 offen offset:3072 lds\n"
    "v_mfma_f32_16x16x32_bf16 v[236:239], a[48:51], a[160:163], v[236:239]\n"
    "ds_read_b64_tr_b16 a[122:123], v24 offset:1024\n"
    "s_mov_b32 m0, 149504\n"
    "s_add_u32 s46, s44, 196608\n"
    "buffer_load_dwordx4 v1, s[8:11], s46 offen lds\n"
    "v_mfma_f32_16x16x32_bf16 v[248:251], a[56:59], a[160:163], v[248:251]\n"
    "ds_read_b64_tr_b16 a[124:125], v24 offset:8192\n"
    "buffer_load_dwordx4 v2, s[8:11], s46 offen offset:1024 lds\n"
    "v_mfma_f32_16x16x32_bf16 v[248:251], a[60:63], a[164:167], v[248:251]\n"
    "ds_read_b64_tr_b16 a[126:127], v24 offset:9216\n"
    "v_add_u32 v24, s40, v24\n"
    "buffer_load_dwordx4 v3, s[8:11], s46 offen offset:2048 lds\n"
    "v_mfma_f32_16x16x32_bf16 v[244:247], a[60:63], a[156:159], v[244:247]\n"
    "buffer_load_dwordx4 v4, s[8:11], s46 offen offset:3072 lds\n"
    "v_mfma_f32_16x16x32_bf16 v[244:247], a[56:59], a[152:155], v[244:247]\n"
    "s_lshl_b32 s43, s44, 12\n"
    "s_mov_b32 s39, s38\n"
    "s_add_u32 s38, s38, s40\n"
    "s_waitcnt vmcnt(32) lgkmcnt(0)\n"
    "s_barrier\n"
    "v_mfma_f32_16x16x32_bf16 v[64:67], a[64:67], a[152:155], v[64:67]\n"
    "s_cmp_eq_u32 s38, 0x8000\n"
    "ds_read_b128 a[128:131], v15 offset:0\n"
    "ds_read_b128 a[132:135], v16 offset:0\n"
    "v_mfma_f32_16x16x32_bf16 v[64:67], a[68:71], a[156:159], v[64:67]\n"
    "s_cselect_b32 s40, s42, s41\n"
    "ds_read_b128 a[136:139], v15 offset:2048\n"
    "v_mfma_f32_16x16x32_bf16 v[68:71], a[68:71], a[164:167], v[68:71]\n"
    "s_add_u32 s45, s43, 0\n"
    "ds_read_b128 a[140:143], v16 offset:2048\n"
    "v_mfma_f32_16x16x32_bf16 v[68:71], a[64:67], a[160:163], v[68:71]\n"
    "s_add_u32 m0, s39, 88064\n"
    "ds_read_b64_tr_b16 a[0:1], v17 offset:0\n"
    "buffer_load_dwordx4 v5, s[12:15], s45 offen lds\n"
    "v_mfma_f32_16x16x32_bf16 v[80:83], a[72:75], a[160:163], v[80:83]\n"
    "ds_read_b64_tr_b16 a[2:3], v17 offset:1024\n"
    "buffer_load_dwordx4 v6, s[12:15], s45 offen offset:1024 lds\n"
    "v_mfma_f32_16x16x32_bf16 v[80:83], a[76:79], a[164:167], v[80:83]\n"
    "ds_read_b64_tr_b16 a[4:5], v17 offset:8192\n"
    "ds_read_b64_tr_b16 a[6:7], v17 offset:9216\n"
    "v_add_u32 v17, s40, v17\n"
    "buffer_load_dwordx4 v7, s[12:15], s45 offen offset:2048 lds\n"
    "v_mfma_f32_16x16x32_bf16 v[76:79], a[76:79], a[156:159], v[76:79]\n"
    "s_add_u32 s46, s43, 131072\n"
    "ds_read_b64_tr_b16 a[8:9], v18 offset:0\n"
    "buffer_load_dwordx4 v8, s[12:15], s45 offen offset:3072 lds\n"
    "v_mfma_f32_16x16x32_bf16 v[76:79], a[72:75], a[152:155], v[76:79]\n"
    "s_add_u32 m0, s39, 92160\n"
    "ds_read_b64_tr_b16 a[10:11], v18 offset:1024\n"
    "buffer_load_dwordx4 v5, s[12:15], s46 offen lds\n"
    "v_mfma_f32_16x16x32_bf16 v[88:91], a[80:83], a[152:155], v[88:91]\n"
    "ds_read_b64_tr_b16 a[12:13], v18 offset:8192\n"
    "buffer_load_dwordx4 v6, s[12:15], s46 offen offset:1024 lds\n"
    "v_mfma_f32_16x16x32_bf16 v[88:91], a[84:87], a[156:159], v[88:91]\n"
    "ds_read_b64_tr_b16 a[14:15], v18 offset:9216\n"
    "v_add_u32 v18, s40, v18\n"
    "buffer_load_dwordx4 v7, s[12:15], s46 offen offset:2048 lds\n"
    "v_mfma_f32_16x16x32_bf16 v[92:95], a[84:87], a[164:167], v[92:95]\n"
    "s_add_u32 s45, s43, 262144\n"
    "ds_read_b64_tr_b16 a[16:17], v19 offset:0\n"
    "ds_read_b64_tr_b16 a[18:19], v19 offset:1024\n"
    "buffer_load_dwordx4 v8, s[12:15], s46 offen offset:3072 lds\n"
    "v_mfma_f32_16x16x32_bf16 v[92:95], a[80:83], a[160:163], v[92:95]\n"
    "s_add_u32 m0, s39, 96256\n"
    "ds_read_b64_tr_b16 a[20:21], v19 offset:8192\n"
    "buffer_load_dwordx4 v5, s[12:15], s45 offen lds\n"
    "v_mfma_f32_16x16x32_bf16 v[104:107], a[88:91], a[160:163], v[104:107]\n"
    "ds_read_b64_tr_b16 a[22:23], v19 offset:9216\n"
    "v_add_u32 v19, s40, v19\n"
    "buffer_load_dwordx4 v6, s[12:15], s45 offen offset:1024 lds\n"
    "v_mfma_f32_16x16x32_bf16 v[104:107], a[92:95], a[164:167], v[104:107]\n"
    "ds_read_b64_tr_b16 a[24:25], v20 offset:0\n"
    "buffer_load_dwordx4 v7, s[12:15], s45 offen offset:2048 lds\n"
    "v_mfma_f32_16x16x32_bf16 v[100:103], a[92:95], a[156:159], v[100:103]\n"
    "s_add_u32 s46, s43, 393216\n"
    "ds_read_b64_tr_b16 a[26:27], v20 offset:1024\n"
    "buffer_load_dwordx4 v8, s[12:15], s45 offen offset:3072 lds\n"
    "v_mfma_f32_16x16x32_bf16 v[100:103], a[88:91], a[152:155], v[100:103]\n"
    "s_add_u32 m0, s39, 100352\n"
    "ds_read_b64_tr_b16 a[28:29], v20 offset:8192\n"
    "ds_read_b64_tr_b16 a[30:31], v20 offset:9216\n"
    "v_add_u32 v20, s40, v20\n"
    "buffer_load_dwordx4 v5, s[12:15], s46 offen lds\n"
    "v_mfma_f32_16x16x32_bf16 v[112:115], a[96:99], a[152:155], v[112:115]\n"
    "ds_read_b64_tr_b16 a[32:33], v21 offset:0\n"
    "buffer_load_dwordx4 v6, s[12:15], s46 offen offset:1024 lds\n"
    "v_mfma_f32_16x16x32_bf16 v[112:115], a[100:103], a[156:159], v[112:115]\n"
    "ds_read_b64_tr_b16 a[34:35], v21 offset:1024\n"
    "buffer_load_dwordx4 v7, s[12:15], s46 offen offset:2048 lds\n"
    "v_mfma_f32_16x16x32_bf16 v[116:119], a[100:103], a[164:167], v[116:119]\n"
    "s_add_u32 s45, s44, 262144\n"
    "ds_read_b64_tr_b16 a[36:37], v21 offset:8192\n"
    "buffer_load_dwordx4 v8, s[12:15], s46 offen offset:3072 lds\n"
    "v_mfma_f32_16x16x32_bf16 v[116:119], a[96:99], a[160:163], v[116:119]\n"
    "s_mov_b32 m0, 153600\n"
    "ds_read_b64_tr_b16 a[38:39], v21 offset:9216\n"
    "v_add_u32 v21, s40, v21\n"
    "buffer_load_dwordx4 v1, s[8:11], s45 offen lds\n"
    "v_mfma_f32_16x16x32_bf16 v[128:131], a[104:107], a[160:163], v[128:131]\n"
    "ds_read_b64_tr_b16 a[40:41], v22 offset:0\n"
    "ds_read_b64_tr_b16 a[42:43], v22 offset:1024\n"
    "buffer_load_dwordx4 v2, s[8:11], s45 offen offset:1024 lds\n"
    "v_mfma_f32_16x16x32_bf16 v[128:131], a[108:111], a[164:167], v[128:131]\n"
    "ds_read_b64_tr_b16 a[44:45], v22 offset:8192\n"
    "buffer_load_dwordx4 v3, s[8:11], s45 offen offset:2048 lds\n"
    "v_mfma_f32_16x16x32_bf16 v[124:127], a[108:111], a[156:159], v[124:127]\n"
    "s_add_u32 s46, s44, 327680\n"
    "ds_read_b64_tr_b16 a[46:47], v22 offset:9216\n"
    "v_add_u32 v22, s40, v22\n"
    "buffer_load_dwordx4 v4, s[8:11], s45 offen offset:3072 lds\n"
    "v_mfma_f32_16x16x32_bf16 v[124:127], a[104:107], a[152:155], v[124:127]\n"
    "s_mov_b32 m0, 157696\n"
    "ds_read_b64_tr_b16 a[48:49], v23 offset:0\n"
    "buffer_load_dwordx4 v1, s[8:11], s46 offen lds\n"
    "v_mfma_f32_16x16x32_bf16 v[136:139], a[112:115], a[152:155], v[136:139]\n"
    "ds_read_b64_tr_b16 a[50:51], v23 offset:1024\n"
    "buffer_load_dwordx4 v2, s[8:11], s46 offen offset:1024 lds\n"
    "v_mfma_f32_16x16x32_bf16 v[136:139], a[116:119], a[156:159], v[136:139]\n"
    "ds_read_b64_tr_b16 a[52:53], v23 offset:8192\n"
    "ds_read_b64_tr_b16 a[54:55], v23 offset:9216\n"
    "v_add_u32 v23, s40, v23\n"
    "v_mfma_f32_16x16x32_bf16 v[140:143], a[116:119], a[164:167], v[140:143]\n"
    "ds_read_b64_tr_b16 a[56:57], v24 offset:0\n"
    "v_mfma_f32_16x16x32_bf16 v[140:143], a[112:115], a[160:163], v[140:143]\n"
    "ds_read_b64_tr_b16 a[58:59], v24 offset:1024\n"
    "v_mfma_f32_16x16x32_bf16 v[152:155], a[120:123], a[160:163], v[152:155]\n"
    "ds_read_b64_tr_b16 a[60:61], v24 offset:8192\n"
    "v_mfma_f32_16x16x32_bf16 v[152:155], a[124:127], a[164:167], v[152:155]\n"
    "ds_read_b64_tr_b16 a[62:63], v24 offset:9216\n"
    "v_add_u32 v24, s40, v24\n"
    "v_mfma_f32_16x16x32_bf16 v[148:151], a[124:127], a[156:159], v[148:151]\n"
    "v_mfma_f32_16x16x32_bf16 v[148:151], a[120:123], a[152:155], v[148:151]\n"
    "s_add_u32 s43, s43, 4096\n"
    "s_and_b32 s44, s50, 15\n"
    "s_lshr_b64 s[50:51], s[50:51], 4\n"
    "s_lshl_b32 s44, s44, 7\n"
    "s_mov_b32 s39, s38\n"
    "s_add_u32 s38, s38, s40\n"
    "s_sub_u32 s37, s37, 1\n"
    "s_cmp_lg_u32 s37, 0\n"
    "s_cbranch_scc1 .L_qD_%=\n"
    "s_waitcnt vmcnt(30) lgkmcnt(0)\n"
    "s_barrier\n"
    "v_mfma_f32_16x16x32_bf16 v[160:163], a[0:3], a[128:131], v[160:163]\n"
    "s_cmp_eq_u32 s38, 0x8000\n"
    "ds_read_b64_tr_b16 a[64:65], v17 offset:0\n"
    "ds_read_b64_tr_b16 a[66:67], v17 offset:1024\n"
    "v_mfma_f32_16x16x32_bf16 v[160:163], a[4:7], a[132:135], v[160:163]\n"
    "s_cselect_b32 s40, s42, s41\n"
    "ds_read_b64_tr_b16 a[68:69], v17 offset:8192\n"
    "v_mfma_f32_16x16x32_bf16 v[164:167], a[4:7], a[140:143], v[164:167]\n"
    "s_add_u32 s45, s43, 0\n"
    "ds_read_b64_tr_b16 a[70:71], v17 offset:9216\n"
    "v_add_u32 v17, s40, v17\n"
    "v_mfma_f32_16x16x32_bf16 v[164:167], a[0:3], a[136:139], v[164:167]\n"
    "s_add_u32 m0, s39, 88064\n"
    "ds_read_b64_tr_b16 a[72:73], v18 offset:0\n"
    "buffer_load_dwordx4 v5, s[12:15], s45 offen lds\n"
    "v_mfma_f32_16x16x32_bf16 v[176:179], a[8:11], a[136:139], v[176:179]\n"
    "ds_read_b64_tr_b16 a[74:75], v18 offset:1024\n"
    "buffer_load_dwordx4 v6, s[12:15], s45 offen offset:1024 lds\n"
    "v_mfma_f32_16x16x32_bf16 v[176:179], a[12:15], a[140:143], v[176:179]\n"
    "ds_read_b64_tr_b16 a[76:77], v18 offset:8192\n"
    "buffer_load_dwordx4 v7, s[12:15], s45 offen offset:2048 lds\n"
    "v_mfma_f32_16x16x32_bf16 v[172:175], a[12:15], a[132:135], v[172:175]\n"
    "s_add_u32 s46, s43, 131072\n"
    "ds_read_b64_tr_b16 a[78:79], v18 offset:9216\n"
    "v_add_u32 v18, s40, v18\n"
    "buffer_load_dwordx4 v8, s[12:15], s45 offen offset:3072 lds\n"
    "v_mfma_f32_16x16x32_bf16 v[172:175], a[8:11], a[128:131], v[172:175]\n"
    "s_add_u32 m0, s39, 92160\n"
    "ds_read_b64_tr_b16 a[80:81], v19 offset:0\n"
    "buffer_load_dwordx4 v5, s[12:15], s46 offen lds\n"
    "v_mfma_f32_16x16x32_bf16 v[184:187], a[16:19], a[128:131], v[184:187]\n"
    "ds_read_b64_tr_b16 a[82:83], v19 offset:1024\n"
    "buffer_load_dwordx4 v6, s[12:15], s46 offen offset:1024 lds\n"
    "v_mfma_f32_16x16x32_bf16 v[184:187], a[20:23], a[132:135], v[184:187]\n"
    "ds_read_b64_tr_b16 a[84:85], v19 offset:8192\n"
    "buffer_load_dwordx4 v7, s[12:15], s46 offen offset:2048 lds\n"
    "v_mfma_f32_16x16x32_bf16 v[188:191], a[20:23], a[140:143], v[188:191]\n"
    "s_add_u32 s45, s43, 262144\n"
    "ds_read_b64_tr_b16 a[86:87], v19 offset:9216\n"
    "v_add_u32 v19, s40, v19\n"
    "buffer_load_dwordx4 v8, s[12:15], s46 offen offset:3072 lds\n"
    "v_mfma_f32_16x16x32_bf16 v[188:191], a[16:19], a[136:139], v[188:191]\n"
    "s_add_u32 m0, s39, 96256\n"
    "ds_read_b64_tr_b16 a[88:89], v20 offset:0\n"
    "buffer_load_dwordx4 v5, s[12:15], s45 offen lds\n"
    "v_mfma_f32_16x16x32_bf16 v[200:203], a[24:27], a[136:139], v[200:203]\n"
    "ds_read_b64_tr_b16 a[90:91], v20 offset:1024\n"
    "buffer_load_dwordx4 v6, s[12:15], s45 offen offset:1024 lds\n"
    "v_mfma_f32_16x16x32_bf16 v[200:203], a[28:31], a[140:143], v[200:203]\n"
    "ds_read_b64_tr_b16 a[92:93], v20 offset:8192\n"
    "buffer_load_dwordx4 v7, s[12:15], s45 offen offset:2048 lds\n"
    "v_mfma_f32_16x16x32_bf16 v[196:199], a[28:31], a[132:135], v[196:199]\n"
    "s_add_u32 s46, s43, 393216\n"
    "ds_read_b64_tr_b16 a[94:95], v20 offset:9216\n"
    "v_add_u32 v20, s40, v20\n"
    "buffer_load_dwordx4 v8, s[12:15], s45 offen offset:3072 lds\n"
    "v_mfma_f32_16x16x32_bf16 v[196:199], a[24:27], a[128:131], v[196:199]\n"
    "s_add_u32 m0, s39, 100352\n"
    "ds_read_b64_tr_b16 a[96:97], v21 offset:0\n"
    "ds_read_b64_tr_b16 a[98:99], v21 offset:1024\n"
    "buffer_load_dwordx4 v5, s[12:15], s46 offen lds\n"
    "v_mfma_f32_16x16x32_bf16 v[208:211], a[32:35], a[128:131], v[208:211]\n"
    "ds_read_b64_tr_b16 a[100:101], v21 offset:8192\n"
    "buffer_load_dwordx4 v6, s[12:15], s46 offen offset:1024 lds\n"
    "v_mfma_f32_16x16x32_bf16 v[208:211], a[36:39], a[132:135], v[208:211]\n"
    "ds_read_b64_tr_b16 a[102:103], v21 offset:9216\n"
    "v_add_u32 v21, s40, v21\n"
    "buffer_load_dwordx4 v7, s[12:15], s46 offen offset:2048 lds\n"
    "v_mfma_f32_16x16x32_bf16 v[212:215], a[36:39], a[140:143], v[212:215]\n"
    "ds_read_b64_tr_b16 a[104:105], v22 offset:0\n"
    "buffer_load_dwordx4 v8, s[12:15], s46 offen offset:3072 lds\n"
    "v_mfma_f32_16x16x32_bf16 v[212:215], a[32:35], a[136:139], v[212:215]\n"
    "ds_read_b64_tr_b16 a[106:107], v22 offset:1024\n"
    "v_mfma_f32_16x16x32_bf16 v[224:227], a[40:43], a[136:139], v[224:227]\n"
    "ds_read_b64_tr_b16 a[108:109], v22 offset:8192\n"
    "s_mov_b32 m0, 65536\n"
    "s_mov_b32 s45, 0\n"
    "s_waitcnt vmcnt(12)\n"
    "buffer_load_dwordx4 v13, s[20:23], s45 offen nt lds\n"
    "v_mfma_f32_16x16x32_bf16 v[224:227], a[44:47], a[140:143], v[224:227]\n"
    "ds_read_b64_tr_b16 a[110:111], v22 offset:9216\n"
    "v_add_u32 v22, s40, v22\n"
    "v_mfma_f32_16x16x32_bf16 v[220:223], a[44:47], a[132:135], v[220:223]\n"
    "ds_read_b64_tr_b16 a[112:113], v23 offset:0\n"
    "v_mfma_f32_16x16x32_bf16 v[220:223], a[40:43], a[128:131], v[220:223]\n"
    "ds_read_b64_tr_b16 a[114:115], v23 offset:1024\n"
    "buffer_load_dwordx4 v14, s[20:23], s45 offen offset:1024 nt lds\n"
    "v_mfma_f32_16x16x32_bf16 v[232:235], a[48:51], a[128:131], v[232:235]\n"
    "ds_read_b64_tr_b16 a[116:117], v23 offset:8192\n"
    "v_mfma_f32_16x16x32_bf16 v[232:235], a[52:55], a[132:135], v[232:235]\n"
    "ds_read_b64_tr_b16 a[118:119], v23 offset:9216\n"
    "v_add_u32 v23, s40, v23\n"
    "v_mfma_f32_16x16x32_bf16 v[236:239], a[52:55], a[140:143], v[236:239]\n"
    "ds_read_b64_tr_b16 a[120:121], v24 offset:0\n"
    "s_waitcnt vmcnt(12)\n"
    "buffer_load_dwordx4 v13, s[20:23], s45 offen offset:2048 nt lds\n"
    "v_mfma_f32_16x16x32_bf16 v[236:239], a[48:51], a[136:139], v[236:239]\n"
    "ds_read_b64_tr_b16 a[122:123], v24 offset:1024\n"
    "v_mfma_f32_16x16x32_bf16 v[248:251], a[56:59], a[136:139], v[248:251]\n"
    "ds_read_b64_tr_b16 a[124:125], v24 offset:8192\n"
    "v_mfma_f32_16x16x32_bf16 v[248:251], a[60:63], a[140:143], v[248:251]\n"
    "ds_read_b64_tr_b16 a[126:127], v24 offset:9216\n"
    "v_add_u32 v24, s40, v24\n"
    "buffer_load_dwordx4 v14, s[20:23], s45 offen offset:3072 nt lds\n"
    "v_mfma_f32_16x16x32_bf16 v[244:247], a[60:63], a[132:135], v[244:247]\n"
    "v_mfma_f32_16x16x32_bf16 v[244:247], a[56:59], a[128:131], v[244:247]\n"
    "s_mov_b32 s39, s38\n"
    "s_add_u32 s38, s38, s40\n"
    "s_waitcnt vmcnt(20) lgkmcnt(0)\n"
    "s_barrier\n"
    "v_mfma_f32_16x16x32_bf16 v[64:67], a[64:67], a[128:131], v[64:67]\n"
    "s_cmp_eq_u32 s38, 0x8000\n"
    "ds_read_b128 a[152:155], v38 offset:0\n"
    "ds_read_b128 a[156:159], v39 offset:0\n"
    "s_mov_b32 m0, 69632\n"
    "s_mov_b32 s45, 4096\n"
    "s_waitcnt vmcnt(12)\n"
    "buffer_load_dwordx4 v13, s[20:23], s45 offen nt lds\n"
    "v_mfma_f32_16x16x32_bf16 v[64:67], a[68:71], a[132:135], v[64:67]\n"
    "s_cselect_b32 s40, s42, s41\n"
    "ds_read_b128 a[160:163], v38 offset:2048\n"
    "v_mfma_f32_16x16x32_bf16 v[68:71], a[68:71], a[140:143], v[68:71]\n"
    "ds_read_b128 a[164:167], v39 offset:2048\n"
    "buffer_load_dwordx4 v14, s[20:23], s45 offen offset:1024 nt lds\n"
    "v_mfma_f32_16x16x32_bf16 v[68:71], a[64:67], a[136:139], v[68:71]\n"
    "ds_read_b64_tr_b16 a[0:1], v17 offset:0\n"
    "v_mfma_f32_16x16x32_bf16 v[80:83], a[72:75], a[136:139], v[80:83]\n"
    "ds_read_b64_tr_b16 a[2:3], v17 offset:1024\n"
    "v_mfma_f32_16x16x32_bf16 v[80:83], a[76:79], a[140:143], v[80:83]\n"
    "ds_read_b64_tr_b16 a[4:5], v17 offset:8192\n"
    "ds_read_b64_tr_b16 a[6:7], v17 offset:9216\n"
    "v_add_u32 v17, s40, v17\n"
    "s_waitcnt vmcnt(12)\n"
    "buffer_load_dwordx4 v13, s[20:23], s45 offen offset:2048 nt lds\n"
    "v_mfma_f32_16x16x32_bf16 v[76:79], a[76:79], a[132:135], v[76:79]\n"
    "ds_read_b64_tr_b16 a[8:9], v18 offset:0\n"
    "v_mfma_f32_16x16x32_bf16 v[76:79], a[72:75], a[128:131], v[76:79]\n"
    "ds_read_b64_tr_b16 a[10:11], v18 offset:1024\n"
    "v_mfma_f32_16x16x32_bf16 v[88:91], a[80:83], a[128:131], v[88:91]\n"
    "ds_read_b64_tr_b16 a[12:13], v18 offset:8192\n"
    "buffer_load_dwordx4 v14, s[20:23], s45 offen offset:3072 nt lds\n"
    "v_mfma_f32_16x16x32_bf16 v[88:91], a[84:87], a[132:135], v[88:91]\n"
    "ds_read_b64_tr_b16 a[14:15], v18 offset:9216\n"
    "v_add_u32 v18, s40, v18\n"
    "v_mfma_f32_16x16x32_bf16 v[92:95], a[84:87], a[140:143], v[92:95]\n"
    "ds_read_b64_tr_b16 a[16:17], v19 offset:0\n"
    "ds_read_b64_tr_b16 a[18:19], v19 offset:1024\n"
    "s_mov_b32 m0, 73728\n"
    "s_mov_b32 s45, 8192\n"
    "s_waitcnt vmcnt(12)\n"
    "buffer_load_dwordx4 v13, s[20:23], s45 offen nt lds\n"
    "v_mfma_f32_16x16x32_bf16 v[92:95], a[80:83], a[136:139], v[92:95]\n"
    "ds_read_b64_tr_b16 a[20:21], v19 offset:8192\n"
    "v_mfma_f32_16x16x32_bf16 v[104:107], a[88:91], a[136:139], v[104:107]\n"
    "ds_read_b64_tr_b16 a[22:23], v19 offset:9216\n"
    "v_add_u32 v19, s40, v19\n"
    "v_mfma_f32_16x16x32_bf16 v[104:107], a[92:95], a[140:143], v[104:107]\n"
    "ds_read_b64_tr_b16 a[24:25], v20 offset:0\n"
    "buffer_load_dwordx4 v14, s[20:23], s45 offen offset:1024 nt lds\n"
    "v_mfma_f32_16x16x32_bf16 v[100:103], a[92:95], a[132:135], v[100:103]\n"
    "ds_read_b64_tr_b16 a[26:27], v20 offset:1024\n"
    "v_mfma_f32_16x16x32_bf16 v[100:103], a[88:91], a[128:131], v[100:103]\n"
    "ds_read_b64_tr_b16 a[28:29], v20 offset:8192\n"
    "ds_read_b64_tr_b16 a[30:31], v20 offset:9216\n"
    "v_add_u32 v20, s40, v20\n"
    "v_mfma_f32_16x16x32_bf16 v[112:115], a[96:99], a[128:131], v[112:115]\n"
    "ds_read_b64_tr_b16 a[32:33], v21 offset:0\n"
    "s_waitcnt vmcnt(12)\n"
    "buffer_load_dwordx4 v13, s[20:23], s45 offen offset:2048 nt lds\n"
    "v_mfma_f32_16x16x32_bf16 v[112:115], a[100:103], a[132:135], v[112:115]\n"
    "ds_read_b64_tr_b16 a[34:35], v21 offset:1024\n"
    "v_mfma_f32_16x16x32_bf16 v[116:119], a[100:103], a[140:143], v[116:119]\n"
    "ds_read_b64_tr_b16 a[36:37], v21 offset:8192\n"
    "buffer_load_dwordx4 v14, s[20:23], s45 offen offset:3072 nt lds\n"
    "v_mfma_f32_16x16x32_bf16 v[116:119], a[96:99], a[136:139], v[116:119]\n"
    "ds_read_b64_tr_b16 a[38:39], v21 offset:9216\n"
    "v_add_u32 v21, s40, v21\n"
    "v_mfma_f32_16x16x32_bf16 v[128:131], a[104:107], a[136:139], v[128:131]\n"
    "ds_read_b64_tr_b16 a[40:41], v22 offset:0\n"
    "ds_read_b64_tr_b16 a[42:43], v22 offset:1024\n"
    "v_mfma_f32_16x16x32_bf16 v[128:131], a[108:111], a[140:143], v[128:131]\n"
    "ds_read_b64_tr_b16 a[44:45], v22 offset:8192\n"
    "s_mov_b32 m0, 77824\n"
    "s_mov_b32 s45, 12288\n"
    "s_waitcnt vmcnt(12)\n"
    "buffer_load_dwordx4 v13, s[20:23], s45 offen nt lds\n"
    "v_mfma_f32_16x16x32_bf16 v[124:127], a[108:111], a[132:135], v[124:127]\n"
    "ds_read_b64_tr_b16 a[46:47], v22 offset:9216\n"
    "v_add_u32 v22, s40, v22\n"
    "v_mfma_f32_16x16x32_bf16 v[124:127], a[104:107], a[128:131], v[124:127]\n"
    "ds_read_b64_tr_b16 a[48:49], v23 offset:0\n"
    "v_mfma_f32_16x16x32_bf16 v[136:139], a[112:115], a[128:131], v[136:139]\n"
    "ds_read_b64_tr_b16 a[50:51], v23 offset:1024\n"
    "buffer_load_dwordx4 v14, s[20:23], s45 offen offset:1024 nt lds\n"
    "v_mfma_f32_16x16x32_bf16 v[136:139], a[116:119], a[132:135], v[136:139]\n"
    "ds_read_b64_tr_b16 a[52:53], v23 offset:8192\n"
    "ds_read_b64_tr_b16 a[54:55], v23 offset:9216\n"
    "v_add_u32 v23, s40, v23\n"
    "v_mfma_f32_16x16x32_bf16 v[140:143], a[116:119], a[140:143], v[140:143]\n"
    "ds_read_b64_tr_b16 a[56:57], v24 offset:0\n"
    "s_waitcnt vmcnt(12)\n"
    "buffer_load_dwordx4 v13, s[20:23], s45 offen offset:2048 nt lds\n"
    "v_mfma_f32_16x16x32_bf16 v[140:143], a[112:115], a[136:139], v[140:143]\n"
    "ds_read_b64_tr_b16 a[58:59], v24 offset:1024\n"
    "v_mfma_f32_16x16x32_bf16 v[152:155], a[120:123], a[136:139], v[152:155]\n"
    "ds_read_b64_tr_b16 a[60:61], v24 offset:8192\n"
    "v_mfma_f32_16x16x32_bf16 v[152:155], a[124:127], a[140:143], v[152:155]\n"
    "ds_read_b64_tr_b16 a[62:63], v24 offset:9216\n"
    "v_add_u32 v24, s40, v24\n"
    "buffer_load_dwordx4 v14, s[20:23], s45 offen offset:3072 nt lds\n"
    "v_mfma_f32_16x16x32_bf16 v[148:151], a[124:127], a[132:135], v[148:151]\n"
    "v_mfma_f32_16x16x32_bf16 v[148:151], a[120:123], a[128:131], v[148:151]\n"
    "s_mov_b32 s39, s38\n"
    "s_add_u32 s38, s38, s40\n"
    "s_waitcnt vmcnt(16) lgkmcnt(0)\n"
    "s_barrier\n"
    "v_mfma_f32_16x16x32_bf16 v[160:163], a[0:3], a[152:155], v[160:163]\n"
    "s_cmp_eq_u32 s38, 0x8000\n"
    "ds_read_b64_tr_b16 a[64:65], v17 offset:0\n"
    "ds_read_b64_tr_b16 a[66:67], v17 offset:1024\n"
    "s_mov_b32 m0, 81920\n"
    "s_mov_b32 s45, 16384\n"
    "s_waitcnt vmcnt(12)\n"
    "buffer_load_dwordx4 v13, s[20:23], s45 offen nt lds\n"
    "v_mfma_f32_16x16x32_bf16 v[160:163], a[4:7], a[156:159], v[160:163]\n"
    "s_cselect_b32 s40, s42, s41\n"
    "ds_read_b64_tr_b16 a[68:69], v17 offset:8192\n"
    "v_mfma_f32_16x16x32_bf16 v[164:167], a[4:7], a[164:167], v[164:167]\n"
    "ds_read_b64_tr_b16 a[70:71], v17 offset:9216\n"
    "v_add_u32 v17, s40, v17\n"
    "buffer_load_dwordx4 v14, s[20:23], s45 offen offset:1024 nt lds\n"
    "v_mfma_f32_16x16x32_bf16 v[164:167], a[0:3], a[160:163], v[164:167]\n"
    "ds_read_b64_tr_b16 a[72:73], v18 offset:0\n"
    "v_mfma_f32_16x16x32_bf16 v[176:179], a[8:11], a[160:163], v[176:179]\n"
    "ds_read_b64_tr_b16 a[74:75], v18 offset:1024\n"
    "v_mfma_f32_16x16x32_bf16 v[176:179], a[12:15], a[164:167], v[176:179]\n"
    "ds_read_b64_tr_b16 a[76:77], v18 offset:8192\n"
    "s_waitcnt vmcnt(12)\n"
    "buffer_load_dwordx4 v13, s[20:23], s45 offen offset:2048 nt lds\n"
    "v_mfma_f32_16x16x32_bf16 v[172:175], a[12:15], a[156:159], v[172:175]\n"
    "ds_read_b64_tr_b16 a[78:79], v18 offset:9216\n"
    "v_add_u32 v18, s40, v18\n"
    "v_mfma_f32_16x16x32_bf16 v[172:175], a[8:11], a[152:155], v[172:175]\n"
    "ds_read_b64_tr_b16 a[80:81], v19 offset:0\n"
    "v_mfma_f32_16x16x32_bf16 v[184:187], a[16:19], a[152:155], v[184:187]\n"
    "ds_read_b64_tr_b16 a[82:83], v19 offset:1024\n"
    "buffer_load_dwordx4 v14, s[20:23], s45 offen offset:3072 nt lds\n"
    "v_mfma_f32_16x16x32_bf16 v[184:187], a[20:23], a[156:159], v[184:187]\n"
    "ds_read_b64_tr_b16 a[84:85], v19 offset:8192\n"
    "v_mfma_f32_16x16x32_bf16 v[188:191], a[20:23], a[164:167], v[188:191]\n"
    "ds_read_b64_tr_b16 a[86:87], v19 offset:9216\n"
    "v_add_u32 v19, s40, v19\n"
    "s_mov_b32 m0, 86016\n"
    "s_mov_b32 s45, 20480\n"
    "s_waitcnt vmcnt(12)\n"
    "buffer_load_dwordx4 v13, s[20:23], s45 offen nt lds\n"
    "v_mfma_f32_16x16x32_bf16 v[188:191], a[16:19], a[160:163], v[188:191]\n"
    "ds_read_b64_tr_b16 a[88:89], v20 offset:0\n"
    "v_mfma_f32_16x16x32_bf16 v[200:203], a[24:27], a[160:163], v[200:203]\n"
    "ds_read_b64_tr_b16 a[90:91], v20 offset:1024\n"
    "v_mfma_f32_16x16x32_bf16 v[200:203], a[28:31], a[164:167], v[200:203]\n"
    "ds_read_b64_tr_b16 a[92:93], v20 offset:8192\n"
    "buffer_load_dwordx4 v14, s[20:23], s45 offen offset:1024 nt lds\n"
    "v_mfma_f32_16x16x32_bf16 v[196:199], a[28:31], a[156:159], v[196:199]\n"
    "ds_read_b64_tr_b16 a[94:95], v20 offset:9216\n"
    "v_add_u32 v20, s40, v20\n"
    "v_mfma_f32_16x16x32_bf16 v[196:199], a[24:27], a[152:155], v[196:199]\n"
    "ds_read_b64_tr_b16 a[96:97], v21 offset:0\n"
    "ds_read_b64_tr_b16 a[98:99], v21 offset:1024\n"
    "v_mfma_f32_16x16x32_bf16 v[208:211], a[32:35], a[152:155], v[208:211]\n"
    "ds_read_b64_tr_b16 a[100:101], v21 offset:8192\n"
    "s_waitcnt vmcnt(12)\n"
    "buffer_load_dwordx4 v13, s[20:23], s45 offen offset:2048 nt lds\n"
    "v_mfma_f32_16x16x32_bf16 v[208:211], a[36:39], a[156:159], v[208:211]\n"
    "ds_read_b64_tr_b16 a[102:103], v21 offset:9216\n"
    "v_add_u32 v21, s40, v21\n"
    "v_mfma_f32_16x16x32_bf16 v[212:215], a[36:39], a[164:167], v[212:215]\n"
    "ds_read_b64_tr_b16 a[104:105], v22 offset:0\n"
    "buffer_load_dwordx4 v14, s[20:23], s45 offen offset:3072 nt lds\n"
    "v_mfma_f32_16x16x32_bf16 v[212:215], a[32:35], a[160:163], v[212:215]\n"
    "ds_read_b64_tr_b16 a[106:107], v22 offset:1024\n"
    "v_mfma_f32_16x16x32_bf16 v[224:227], a[40:43], a[160:163], v[224:227]\n"
    "ds_read_b64_tr_b16 a[108:109], v22 offset:8192\n"
    "v_mfma_f32_16x16x32_bf16 v[224:227], a[44:47], a[164:167], v[224:227]\n"
    "ds_read_b64_tr_b16 a[110:111], v22 offset:9216\n"
    "v_add_u32 v22, s40, v22\n"
    "s_mov_b32 m0, 90112\n"
    "s_mov_b32 s45, 24576\n"
    "s_waitcnt vmcnt(12)\n"
    "buffer_load_dwordx4 v13, s[20:23], s45 offen nt lds\n"
    "v_mfma_f32_16x16x32_bf16 v[220:223], a[44:47], a[156:159], v[220:223]\n"
    "ds_read_b64_tr_b16 a[112:113], v23 offset:0\n"
    "v_mfma_f32_16x16x32_bf16 v[220:223], a[40:43], a[152:155], v[220:223]\n"
    "ds_read_b64_tr_b16 a[114:115], v23 offset:1024\n"
    "v_mfma_f32_16x16x32_bf16 v[232:235], a[48:51], a[152:155], v[232:235]\n"
    "ds_read_b64_tr_b16 a[116:117], v23 offset:8192\n"
    "buffer_load_dwordx4 v14, s[20:23], s45 offen offset:1024 nt lds\n"
    "v_mfma_f32_16x16x32_bf16 v[232:235], a[52:55], a[156:159], v[232:235]\n"
    "ds_read_b64_tr_b16 a[118:119], v23 offset:9216\n"
    "v_add_u32 v23, s40, v23\n"
    "v_mfma_f32_16x16x32_bf16 v[236:239], a[52:55], a[164:167], v[236:239]\n"
    "ds_read_b64_tr_b16 a[120:121], v24 offset:0\n"
    "s_waitcnt vmcnt(12)\n"
    "buffer_load_dwordx4 v13, s[20:23], s45 offen offset:2048 nt lds\n"
    "v_mfma_f32_16x16x32_bf16 v[236:239], a[48:51], a[160:163], v[236:239]\n"
    "ds_read_b64_tr_b16 a[122:123], v24 offset:1024\n"
    "v_mfma_f32_16x16x32_bf16 v[248:251], a[56:59], a[160:163], v[248:251]\n"
    "ds_read_b64_tr_b16 a[124:125], v24 offset:8192\n"
    "v_mfma_f32_16x16x32_bf16 v[248:251], a[60:63], a[164:167], v[248:251]\n"
    "ds_read_b64_tr_b16 a[126:127], v24 offset:9216\n"
    "v_add_u32 v24, s40, v24\n"
    "buffer_load_dwordx4 v14, s[20:23], s45 offen offset:3072 nt lds\n"
    "v_mfma_f32_16x16x32_bf16 v[244:247], a[60:63], a[156:159], v[244:247]\n"
    "v_mfma_f32_16x16x32_bf16 v[244:247], a[56:59], a[152:155], v[244:247]\n"
    "s_mov_b32 s39, s38\n"
    "s_add_u32 s38, s38, s40\n"
    "s_waitcnt vmcnt(40) lgkmcnt(0)\n"
    "s_barrier\n"
    "v_mfma_f32_16x16x32_bf16 v[64:67], a[64:67], a[152:155], v[64:67]\n"
    "ds_read_b128 a[0:3], v33 offset:0\n"
    "s_mov_b32 m0, 94208\n"
    "s_mov_b32 s45, 28672\n"
    "s_waitcnt vmcnt(12)\n"
    "buffer_load_dwordx4 v13, s[20:23], s45 offen nt lds\n"
    "v_mfma_f32_16x16x32_bf16 v[64:67], a[68:71], a[156:159], v[64:67]\n"
    "ds_read_b128 a[4:7], v34 offset:0\n"
    "v_lshrrev_b32 v52, 1, v45\n"
    "v_mfma_f32_16x16x32_bf16 v[68:71], a[68:71], a[164:167], v[68:71]\n"
    "ds_read_b128 a[8:11], v35 offset:0\n"
    "buffer_load_dwordx4 v14, s[20:23], s45 offen offset:1024 nt lds\n"
    "v_mfma_f32_16x16x32_bf16 v[68:71], a[64:67], a[160:163], v[68:71]\n"
    "ds_read_b128 a[12:15], v36 offset:0\n"
    "v_xor_b32 v52, v52, v46\n"
    "v_mfma_f32_16x16x32_bf16 v[80:83], a[72:75], a[160:163], v[80:83]\n"
    "ds_read_b128 a[16:19], v33 offset:4096\n"
    "v_lshlrev_b32 v52, 4, v52\n"
    "v_mfma_f32_16x16x32_bf16 v[80:83], a[76:79], a[164:167], v[80:83]\n"
    "ds_read_b128 a[20:23], v34 offset:4096\n"
    "s_waitcnt vmcnt(12)\n"
    "buffer_load_dwordx4 v13, s[20:23], s45 offen offset:2048 nt lds\n"
    "v_mfma_f32_16x16x32_bf16 v[76:79], a[76:79], a[156:159], v[76:79]\n"
    "ds_read_b128 a[24:27], v35 offset:4096\n"
    "v_lshl_add_u32 v52, v46, 8, v52\n"
    "v_mfma_f32_16x16x32_bf16 v[76:79], a[72:75], a[152:155], v[76:79]\n"
    "ds_read_b128 a[28:31], v36 offset:4096\n"
    "v_and_b32 v53, 1, v45\n"
    "v_mfma_f32_16x16x32_bf16 v[88:91], a[80:83], a[152:155], v[88:91]\n"
    "ds_read_b128 a[32:35], v33 offset:8192\n"
    "buffer_load_dwordx4 v14, s[20:23], s45 offen offset:3072 nt lds\n"
    "v_mfma_f32_16x16x32_bf16 v[88:91], a[84:87], a[156:159], v[88:91]\n"
    "ds_read_b128 a[36:39], v34 offset:8192\n"
    "v_lshl_add_u32 v52, v53, 3, v52\n"
    "v_mfma_f32_16x16x32_bf16 v[92:95], a[84:87], a[164:167], v[92:95]\n"
    "ds_read_b128 a[40:43], v35 offset:8192\n"
    "s_mov_b32 m0, 98304\n"
    "s_mov_b32 s45, 32768\n"
    "s_waitcnt vmcnt(12)\n"
    "buffer_load_dwordx4 v13, s[20:23], s45 offen nt lds\n"
    "v_mfma_f32_16x16x32_bf16 v[92:95], a[80:83], a[160:163], v[92:95]\n"
    "ds_read_b128 a[44:47], v36 offset:8192\n"
    "v_add_u32 v17, s61, v52\n"
    "v_mfma_f32_16x16x32_bf16 v[104:107], a[88:91], a[160:163], v[104:107]\n"
    "ds_read_b128 a[48:51], v33 offset:12288\n"
    "v_xor_b32 v18, 32, v17\n"
    "v_mfma_f32_16x16x32_bf16 v[104:107], a[92:95], a[164:167], v[104:107]\n"
    "ds_read_b128 a[52:55], v34 offset:12288\n"
    "buffer_load_dwordx4 v14, s[20:23], s45 offen offset:1024 nt lds\n"
    "v_mfma_f32_16x16x32_bf16 v[100:103], a[92:95], a[156:159], v[100:103]\n"
    "ds_read_b128 a[56:59], v35 offset:12288\n"
    "v_xor_b32 v19, 64, v17\n"
    "v_mfma_f32_16x16x32_bf16 v[100:103], a[88:91], a[152:155], v[100:103]\n"
    "ds_read_b128 a[60:63], v36 offset:12288\n"
    "v_xor_b32 v20, 96, v17\n"
    "v_mfma_f32_16x16x32_bf16 v[112:115], a[96:99], a[152:155], v[112:115]\n"
    "s_waitcnt vmcnt(12)\n"
    "buffer_load_dwordx4 v13, s[20:23], s45 offen offset:2048 nt lds\n"
    "v_xor_b32 v21, 128, v17\n"
    "v_mfma_f32_16x16x32_bf16 v[112:115], a[100:103], a[156:159], v[112:115]\n"
    "v_xor_b32 v22, 160, v17\n"
    "v_xor_b32 v23, 192, v17\n"
    "v_mfma_f32_16x16x32_bf16 v[116:119], a[100:103], a[164:167], v[116:119]\n"
    "buffer_load_dwordx4 v14, s[20:23], s45 offen offset:3072 nt lds\n"
    "v_xor_b32 v24, 224, v17\n"
    "v_mfma_f32_16x16x32_bf16 v[116:119], a[96:99], a[160:163], v[116:119]\n"
    "v_add_u32 v52, s61, v33\n"
    "v_add_u32 v53, s61, v34\n"
    "v_mfma_f32_16x16x32_bf16 v[128:131], a[104:107], a[160:163], v[128:131]\n"
    "v_add_u32 v54, s61, v35\n"
    "v_add_u32 v55, s61, v36\n"
    "v_mfma_f32_16x16x32_bf16 v[128:131], a[108:111], a[164:167], v[128:131]\n"
    "s_mov_b32 m0, 102400\n"
    "s_mov_b32 s45, 36864\n"
    "s_waitcnt vmcnt(12)\n"
    "buffer_load_dwordx4 v13, s[20:23], s45 offen nt lds\n"
    "v_mul_f32 v160, s58, v160\n"
    "v_mfma_f32_16x16x32_bf16 v[124:127], a[108:111], a[156:159], v[124:127]\n"
    "v_mul_f32 v161, s58, v161\n"
    "v_mul_f32 v162, s58, v162\n"
    "v_mfma_f32_16x16x32_bf16 v[124:127], a[104:107], a[152:155], v[124:127]\n"
    "v_mul_f32 v163, s58, v163\n"
    "v_cvt_pk_bf16_f32 v56, v160, v161\n"
    "v_mfma_f32_16x16x32_bf16 v[136:139], a[112:115], a[152:155], v[136:139]\n"
    "buffer_load_dwordx4 v14, s[20:23], s45 offen offset:1024 nt lds\n"
    "v_cvt_pk_bf16_f32 v57, v162, v163\n"
    "v_mfma_f32_16x16x32_bf16 v[136:139], a[116:119], a[156:159], v[136:139]\n"
    "ds_write_b64 v17, v[56:57]\n"
    "v_mul_f32 v172, s58, v172\n"
    "v_mfma_f32_16x16x32_bf16 v[140:143], a[116:119], a[164:167], v[140:143]\n"
    "s_waitcnt vmcnt(12)\n"
    "buffer_load_dwordx4 v13, s[20:23], s45 offen offset:2048 nt lds\n"
    "v_mul_f32 v173, s58, v173\n"
    "v_mfma_f32_16x16x32_bf16 v[140:143], a[112:115], a[160:163], v[140:143]\n"
    "v_mul_f32 v174, s58, v174\n"
    "v_mul_f32 v175, s58, v175\n"
    "v_mfma_f32_16x16x32_bf16 v[152:155], a[120:123], a[160:163], v[152:155]\n"
    "v_cvt_pk_bf16_f32 v58, v172, v173\n"
    "v_cvt_pk_bf16_f32 v59, v174, v175\n"
    "v_mfma_f32_16x16x32_bf16 v[152:155], a[124:127], a[164:167], v[152:155]\n"
    "buffer_load_dwordx4 v14, s[20:23], s45 offen offset:3072 nt lds\n"
    "ds_write_b64 v18, v[58:59]\n"
    "v_mfma_f32_16x16x32_bf16 v[148:151], a[124:127], a[156:159], v[148:151]\n"
    "v_mul_f32 v184, s58, v184\n"
    "v_mul_f32 v185, s58, v185\n"
    "v_mfma_f32_16x16x32_bf16 v[148:151], a[120:123], a[152:155], v[148:151]\n"
    "v_mul_f32 v186, s58, v186\n"
    "v_mul_f32 v187, s58, v187\n"
    "v_cvt_pk_bf16_f32 v60, v184, v185\n"
    "s_mov_b32 m0, 106496\n"
    "s_mov_b32 s45, 40960\n"
    "s_waitcnt vmcnt(18)\n"
    "buffer_load_dwordx4 v13, s[20:23], s45 offen nt lds\n"
    "v_cvt_pk_bf16_f32 v61, v186, v187\n"
    "ds_write_b64 v19, v[60:61]\n"
    "v_pk_mul_f32 v[196:197], s[58:59], v[196:197]\n"
    "v_pk_mul_f32 v[198:199], s[58:59], v[198:199]\n"
    "v_cvt_pk_bf16_f32 v62, v196, v197\n"
    "v_cvt_pk_bf16_f32 v63, v198, v199\n"
    "buffer_load_dwordx4 v14, s[20:23], s45 offen offset:1024 nt lds\n"
    "ds_write_b64 v20, v[62:63]\n"
    "v_pk_mul_f32 v[208:209], s[58:59], v[208:209]\n"
    "v_pk_mul_f32 v[210:211], s[58:59], v[210:211]\n"
    "v_cvt_pk_bf16_f32 v56, v208, v209\n"
    "v_cvt_pk_bf16_f32 v57, v210, v211\n"
    "ds_write_b64 v21, v[56:57]\n"
    "s_waitcnt vmcnt(18)\n"
    "buffer_load_dwordx4 v13, s[20:23], s45 offen offset:2048 nt lds\n"
    "v_pk_mul_f32 v[220:221], s[58:59], v[220:221]\n"
    "v_pk_mul_f32 v[222:223], s[58:59], v[222:223]\n"
    "v_cvt_pk_bf16_f32 v58, v220, v221\n"
    "v_cvt_pk_bf16_f32 v59, v222, v223\n"
    "ds_write_b64 v22, v[58:59]\n"
    "v_pk_mul_f32 v[232:233], s[58:59], v[232:233]\n"
    "v_pk_mul_f32 v[234:235], s[58:59], v[234:235]\n"
    "buffer_load_dwordx4 v14, s[20:23], s45 offen offset:3072 nt lds\n"
    "v_cvt_pk_bf16_f32 v60, v232, v233\n"
    "v_cvt_pk_bf16_f32 v61, v234, v235\n"
    "ds_write_b64 v23, v[60:61]\n"
    "v_pk_mul_f32 v[244:245], s[58:59], v[244:245]\n"
    "v_pk_mul_f32 v[246:247], s[58:59], v[246:247]\n"
    "v_cvt_pk_bf16_f32 v62, v244, v245\n"
    "s_mov_b32 m0, 110592\n"
    "s_mov_b32 s45, 45056\n"
    "s_waitcnt vmcnt(18)\n"
    "buffer_load_dwordx4 v13, s[20:23], s45 offen nt lds\n"
    "v_cvt_pk_bf16_f32 v63, v246, v247\n"
    "ds_write_b64 v24, v[62:63]\n"
    "ds_read_b128 a[176:179], v52\n"
    "ds_read_b128 a[180:183], v53\n"
    "ds_read_b128 a[184:187], v54\n"
    "ds_read_b128 a[188:191], v55\n"
    "buffer_load_dwordx4 v14, s[20:23], s45 offen offset:1024 nt lds\n"
    "v_pk_mul_f32 v[164:165], s[58:59], v[164:165]\n"
    "v_pk_mul_f32 v[166:167], s[58:59], v[166:167]\n"
    "v_cvt_pk_bf16_f32 v56, v164, v165\n"
    "v_cvt_pk_bf16_f32 v57, v166, v167\n"
    "ds_write_b64 v17, v[56:57]\n"
    "v_pk_mul_f32 v[176:177], s[58:59], v[176:177]\n"
    "v_pk_mul_f32 v[178:179], s[58:59], v[178:179]\n"
    "s_waitcnt vmcnt(18)\n"
    "buffer_load_dwordx4 v13, s[20:23], s45 offen offset:2048 nt lds\n"
    "v_cvt_pk_bf16_f32 v58, v176, v177\n"
    "v_cvt_pk_bf16_f32 v59, v178, v179\n"
    "ds_write_b64 v18, v[58:59]\n"
    "v_pk_mul_f32 v[188:189], s[58:59], v[188:189]\n"
    "v_pk_mul_f32 v[190:191], s[58:59], v[190:191]\n"
    "v_cvt_pk_bf16_f32 v60, v188, v189\n"
    "buffer_load_dwordx4 v14, s[20:23], s45 offen offset:3072 nt lds\n"
    "v_cvt_pk_bf16_f32 v61, v190, v191\n"
    "ds_write_b64 v19, v[60:61]\n"
    "v_pk_mul_f32 v[200:201], s[58:59], v[200:201]\n"
    "v_pk_mul_f32 v[202:203], s[58:59], v[202:203]\n"
    "v_cvt_pk_bf16_f32 v62, v200, v201\n"
    "v_cvt_pk_bf16_f32 v63, v202, v203\n"
    "s_mov_b32 m0, 114688\n"
    "s_mov_b32 s45, 49152\n"
    "s_waitcnt vmcnt(18)\n"
    "buffer_load_dwordx4 v13, s[20:23], s45 offen nt lds\n"
    "ds_write_b64 v20, v[62:63]\n"
    "v_pk_mul_f32 v[212:213], s[58:59], v[212:213]\n"
    "v_pk_mul_f32 v[214:215], s[58:59], v[214:215]\n"
    "v_cvt_pk_bf16_f32 v56, v212, v213\n"
    "v_cvt_pk_bf16_f32 v57, v214, v215\n"
    "ds_write_b64 v21, v[56:57]\n"
    "v_pk_mul_f32 v[224:225], s[58:59], v[224:225]\n"
    "buffer_load_dwordx4 v14, s[20:23], s45 offen offset:1024 nt lds\n"
    "v_pk_mul_f32 v[226:227], s[58:59], v[226:227]\n"
    "v_cvt_pk_bf16_f32 v58, v224, v225\n"
    "v_cvt_pk_bf16_f32 v59, v226, v227\n"
    "ds_write_b64 v22, v[58:59]\n"
    "v_pk_mul_f32 v[236:237], s[58:59], v[236:237]\n"
    "v_pk_mul_f32 v[238:239], s[58:59], v[238:239]\n"
    "s_waitcnt vmcnt(18)\n"
    "buffer_load_dwordx4 v13, s[20:23], s45 offen offset:2048 nt lds\n"
    "v_cvt_pk_bf16_f32 v60, v236, v237\n"
    "v_cvt_pk_bf16_f32 v61, v238, v239\n"
    "ds_write_b64 v23, v[60:61]\n"
    "v_pk_mul_f32 v[248:249], s[58:59], v[248:249]\n"
    "v_pk_mul_f32 v[250:251], s[58:59], v[250:251]\n"
    "v_cvt_pk_bf16_f32 v62, v248, v249\n"
    "buffer_load_dwordx4 v14, s[20:23], s45 offen offset:3072 nt lds\n"
    "v_cvt_pk_bf16_f32 v63, v250, v251\n"
    "ds_write_b64 v24, v[62:63]\n"
    "ds_read_b128 a[192:195], v52\n"
    "ds_read_b128 a[196:199], v53\n"
    "ds_read_b128 a[200:203], v54\n"
    "ds_read_b128 a[204:207], v55\n"
    "s_mul_i32 s56, %5, 45056\n"
    "s_mul_i32 s46, s36, 3072\n"
    "s_add_u32 s56, s56, s46\n"
    "s_mov_b32 s55, 0\n"
    "s_mov_b32 s53, 0\n"
    "s_mov_b64 s[32:33], %4\n"
    "s_and_b32 s33, s33, 0xffff\n"
    "s_mov_b32 s34, 1024\n"
    "s_mov_b32 s35, 0x20000\n"
    "s_add_u32 s62, s62, 1\n"
    "s_addc_u32 s63, s63, 0\n"
    "s_waitcnt lgkmcnt(0)\n"
    "s_barrier\n"
    "v_mfma_f32_16x16x32_bf16 v[160:163], a[0:3], a[176:179], 0\n"
    "s_mov_b32 m0, 0\n"
    "s_mov_b32 s45, 65536\n"
    "s_waitcnt vmcnt(18)\n"
    "buffer_load_dwordx4 v9, s[16:19], s45 offen nt lds\n"
    "v_mfma_f32_16x16x32_bf16 v[160:163], a[4:7], a[180:183], v[160:163]\n"
    "v_mfma_f32_16x16x32_bf16 v[160:163], a[8:11], a[184:187], v[160:163]\n"
    "v_mul_f32 v64, s58, v64\n"
    "v_mul_f32 v65, s58, v65\n"
    "v_mfma_f32_16x16x32_bf16 v[160:163], a[12:15], a[188:191], v[160:163]\n"
    "v_mul_f32 v66, s58, v66\n"
    "v_mul_f32 v67, s58, v67\n"
    "buffer_load_dwordx4 v10, s[16:19], s45 offen offset:1024 nt lds\n"
    "v_mfma_f32_16x16x32_bf16 v[184:187], a[12:15], a[204:207], 0\n"
    "v_cvt_pk_bf16_f32 v56, v64, v65\n"
    "v_cvt_pk_bf16_f32 v57, v66, v67\n"
    "v_mfma_f32_16x16x32_bf16 v[184:187], a[8:11], a[200:203], v[184:187]\n"
    "ds_write_b64 v17, v[56:57]\n"
    "v_mul_f32 v76, s58, v76\n"
    "v_mfma_f32_16x16x32_bf16 v[184:187], a[4:7], a[196:199], v[184:187]\n"
    "v_mul_f32 v77, s58, v77\n"
    "s_waitcnt vmcnt(18)\n"
    "buffer_load_dwordx4 v11, s[16:19], s45 offen offset:2048 nt lds\n"
    "v_mul_f32 v78, s58, v78\n"
    "v_mfma_f32_16x16x32_bf16 v[184:187], a[0:3], a[192:195], v[184:187]\n"
    "v_mul_f32 v79, s58, v79\n"
    "v_cvt_pk_bf16_f32 v58, v76, v77\n"
    "v_mfma_f32_16x16x32_bf16 v[200:203], a[16:19], a[192:195], 0\n"
    "ds_read_b128 a[0:3], v33 offset:16384\n"
    "v_cvt_pk_bf16_f32 v59, v78, v79\n"
    "buffer_load_dwordx4 v12, s[16:19], s45 offen offset:3072 nt lds\n"
    "v_mfma_f32_16x16x32_bf16 v[200:203], a[20:23], a[196:199], v[200:203]\n"
    "ds_read_b128 a[4:7], v34 offset:16384\n"
    "v_mfma_f32_16x16x32_bf16 v[200:203], a[24:27], a[200:203], v[200:203]\n"
    "ds_read_b128 a[8:11], v35 offset:16384\n"
    "v_mfma_f32_16x16x32_bf16 v[200:203], a[28:31], a[204:207], v[200:203]\n"
    "ds_read_b128 a[12:15], v36 offset:16384\n"
    "v_mfma_f32_16x16x32_bf16 v[176:179], a[28:31], a[188:191], 0\n"
    "ds_write_b64 v18, v[58:59]\n"
    "s_mov_b32 m0, 4096\n"
    "s_mov_b32 s45, 69632\n"
    "s_waitcnt vmcnt(18)\n"
    "buffer_load_dwordx4 v9, s[16:19], s45 offen nt lds\n"
    "v_mul_f32 v88, s58, v88\n"
    "v_mfma_f32_16x16x32_bf16 v[176:179], a[24:27], a[184:187], v[176:179]\n"
    "v_mul_f32 v89, s58, v89\n"
    "v_mul_f32 v90, s58, v90\n"
    "v_mfma_f32_16x16x32_bf16 v[176:179], a[20:23], a[180:183], v[176:179]\n"
    "v_mul_f32 v91, s58, v91\n"
    "v_cvt_pk_bf16_f32 v60, v88, v89\n"
    "v_mfma_f32_16x16x32_bf16 v[176:179], a[16:19], a[176:179], v[176:179]\n"
    "buffer_load_dwordx4 v10, s[16:19], s45 offen offset:1024 nt lds\n"
    "v_cvt_pk_bf16_f32 v61, v90, v91\n"
    "ds_write_b64 v19, v[60:61]\n"
    "v_mfma_f32_16x16x32_bf16 v[168:171], a[32:35], a[176:179], 0\n"
    "ds_read_b128 a[16:19], v33 offset:20480\n"
    "v_mul_f32 v100, s58, v100\n"
    "v_mfma_f32_16x16x32_bf16 v[168:171], a[36:39], a[180:183], v[168:171]\n"
    "ds_read_b128 a[20:23], v34 offset:20480\n"
    "s_waitcnt vmcnt(18)\n"
    "buffer_load_dwordx4 v11, s[16:19], s45 offen offset:2048 nt lds\n"
    "v_mul_f32 v101, s58, v101\n"
    "v_mfma_f32_16x16x32_bf16 v[168:171], a[40:43], a[184:187], v[168:171]\n"
    "ds_read_b128 a[24:27], v35 offset:20480\n"
    "v_mul_f32 v102, s58, v102\n"
    "v_mfma_f32_16x16x32_bf16 v[168:171], a[44:47], a[188:191], v[168:171]\n"
    "ds_read_b128 a[28:31], v36 offset:20480\n"
    "v_mul_f32 v103, s58, v103\n"
    "v_mfma_f32_16x16x32_bf16 v[192:195], a[44:47], a[204:207], 0\n"
    "buffer_load_dwordx4 v12, s[16:19], s45 offen offset:3072 nt lds\n"
    "v_cvt_pk_bf16_f32 v62, v100, v101\n"
    "v_cvt_pk_bf16_f32 v63, v102, v103\n"
    "v_mfma_f32_16x16x32_bf16 v[192:195], a[40:43], a[200:203], v[192:195]\n"
    "ds_write_b64 v20, v[62:63]\n"
    "v_mul_f32 v112, s58, v112\n"
    "v_mfma_f32_16x16x32_bf16 v[192:195], a[36:39], a[196:199], v[192:195]\n"
    "v_mul_f32 v113, s58, v113\n"
    "s_mov_b32 m0, 8192\n"
    "s_mov_b32 s45, 73728\n"
    "s_waitcnt vmcnt(18)\n"
    "buffer_load_dwordx4 v9, s[16:19], s45 offen nt lds\n"
    "v_mul_f32 v114, s58, v114\n"
    "v_mfma_f32_16x16x32_bf16 v[192:195], a[32:35], a[192:195], v[192:195]\n"
    "v_mul_f32 v115, s58, v115\n"
    "v_cvt_pk_bf16_f32 v56, v112, v113\n"
    "v_mfma_f32_16x16x32_bf16 v[204:207], a[48:51], a[192:195], 0\n"
    "ds_read_b128 a[32:35], v33 offset:24576\n"
    "v_cvt_pk_bf16_f32 v57, v114, v115\n"
    "v_mfma_f32_16x16x32_bf16 v[204:207], a[52:55], a[196:199], v[204:207]\n"
    "buffer_load_dwordx4 v10, s[16:19], s45 offen offset:1024 nt lds\n"
    "ds_read_b128 a[36:39], v34 offset:24576\n"
    "v_mfma_f32_16x16x32_bf16 v[204:207], a[56:59], a[200:203], v[204:207]\n"
    "ds_read_b128 a[40:43], v35 offset:24576\n"
    "v_mfma_f32_16x16x32_bf16 v[204:207], a[60:63], a[204:207], v[204:207]\n"
    "ds_read_b128 a[44:47], v36 offset:24576\n"
    "v_mfma_f32_16x16x32_bf16 v[180:183], a[60:63], a[188:191], 0\n"
    "ds_write_b64 v21, v[56:57]\n"
    "v_mul_f32 v124, s58, v124\n"
    "s_waitcnt vmcnt(18)\n"
    "buffer_load_dwordx4 v11, s[16:19], s45 offen offset:2048 nt lds\n"
    "v_mfma_f32_16x16x32_bf16 v[180:183], a[56:59], a[184:187], v[180:183]\n"
    "v_mul_f32 v125, s58, v125\n"
    "v_mul_f32 v126, s58, v126\n"
    "v_mfma_f32_16x16x32_bf16 v[180:183], a[52:55], a[180:183], v[180:183]\n"
    "v_mul_f32 v127, s58, v127\n"
    "v_cvt_pk_bf16_f32 v58, v124, v125\n"
    "v_mfma_f32_16x16x32_bf16 v[180:183], a[48:51], a[176:179], v[180:183]\n"
    "buffer_load_dwordx4 v12, s[16:19], s45 offen offset:3072 nt lds\n"
    "v_cvt_pk_bf16_f32 v59, v126, v127\n"
    "ds_write_b64 v22, v[58:59]\n"
    "ds_read_b128 a[48:51], v33 offset:28672\n"
    "ds_read_b128 a[52:55], v34 offset:28672\n"
    "ds_read_b128 a[56:59], v35 offset:28672\n"
    "ds_read_b128 a[60:63], v36 offset:28672\n"
    "v_pk_mul_f32 v[136:137], s[58:59], v[136:137]\n"
    "v_pk_mul_f32 v[138:139], s[58:59], v[138:139]\n"
    "s_mov_b32 m0, 12288\n"
    "s_mov_b32 s45, 77824\n"
    "s_waitcnt vmcnt(18)\n"
    "buffer_load_dwordx4 v9, s[16:19], s45 offen nt lds\n"
    "v_cvt_pk_bf16_f32 v60, v136, v137\n"
    "v_cvt_pk_bf16_f32 v61, v138, v139\n"
    "ds_write_b64 v23, v[60:61]\n"
    "v_pk_mul_f32 v[148:149], s[58:59], v[148:149]\n"
    "v_pk_mul_f32 v[150:151], s[58:59], v[150:151]\n"
    "v_cvt_pk_bf16_f32 v62, v148, v149\n"
    "v_cvt_pk_bf16_f32 v63, v150, v151\n"
    "buffer_load_dwordx4 v10, s[16:19], s45 offen offset:1024 nt lds\n"
    "ds_write_b64 v24, v[62:63]\n"
    "ds_read_b128 a[128:131], v52\n"
    "ds_read_b128 a[132:135], v53\n"
    "ds_read_b128 a[136:139], v54\n"
    "ds_read_b128 a[140:143], v55\n"
    "v_pk_mul_f32 v[68:69], s[58:59], v[68:69]\n"
    "v_pk_mul_f32 v[70:71], s[58:59], v[70:71]\n"
    "v_cvt_pk_bf16_f32 v56, v68, v69\n"
    "s_waitcnt vmcnt(18)\n"
    "buffer_load_dwordx4 v11, s[16:19], s45 offen offset:2048 nt lds\n"
    "v_cvt_pk_bf16_f32 v57, v70, v71\n"
    "ds_write_b64 v17, v[56:57]\n"
    "v_pk_mul_f32 v[80:81], s[58:59], v[80:81]\n"
    "v_pk_mul_f32 v[82:83], s[58:59], v[82:83]\n"
    "v_cvt_pk_bf16_f32 v58, v80, v81\n"
    "v_cvt_pk_bf16_f32 v59, v82, v83\n"
    "ds_write_b64 v18, v[58:59]\n"
    "v_pk_mul_f32 v[92:93], s[58:59], v[92:93]\n"
    "buffer_load_dwordx4 v12, s[16:19], s45 offen offset:3072 nt lds\n"
    "v_pk_mul_f32 v[94:95], s[58:59], v[94:95]\n"
    "v_cvt_pk_bf16_f32 v60, v92, v93\n"
    "v_cvt_pk_bf16_f32 v61, v94, v95\n"
    "ds_write_b64 v19, v[60:61]\n"
    "v_pk_mul_f32 v[104:105], s[58:59], v[104:105]\n"
    "v_pk_mul_f32 v[106:107], s[58:59], v[106:107]\n"
    "v_cvt_pk_bf16_f32 v62, v104, v105\n"
    "s_mov_b32 m0, 118784\n"
    "s_mov_b32 s45, 53248\n"
    "s_waitcnt vmcnt(18)\n"
    "buffer_load_dwordx4 v13, s[20:23], s45 offen nt lds\n"
    "v_cvt_pk_bf16_f32 v63, v106, v107\n"
    "ds_write_b64 v20, v[62:63]\n"
    "v_pk_mul_f32 v[116:117], s[58:59], v[116:117]\n"
    "v_pk_mul_f32 v[118:119], s[58:59], v[118:119]\n"
    "v_cvt_pk_bf16_f32 v56, v116, v117\n"
    "v_cvt_pk_bf16_f32 v57, v118, v119\n"
    "ds_write_b64 v21, v[56:57]\n"
    "v_pk_mul_f32 v[128:129], s[58:59], v[128:129]\n"
    "buffer_load_dwordx4 v14, s[20:23], s45 offen offset:1024 nt lds\n"
    "v_pk_mul_f32 v[130:131], s[58:59], v[130:131]\n"
    "v_cvt_pk_bf16_f32 v58, v128, v129\n"
    "v_cvt_pk_bf16_f32 v59, v130, v131\n"
    "ds_write_b64 v22, v[58:59]\n"
    "v_pk_mul_f32 v[140:141], s[58:59], v[140:141]\n"
    "v_pk_mul_f32 v[142:143], s[58:59], v[142:143]\n"
    "v_cvt_pk_bf16_f32 v60, v140, v141\n"
    "v_cvt_pk_bf16_f32 v61, v142, v143\n"
    "s_waitcnt vmcnt(18)\n"
    "buffer_load_dwordx4 v13, s[20:23], s45 offen offset:2048 nt lds\n"
    "ds_write_b64 v23, v[60:61]\n"
    "v_pk_mul_f32 v[152:153], s[58:59], v[152:153]\n"
    "v_pk_mul_f32 v[154:155], s[58:59], v[154:155]\n"
    "v_cvt_pk_bf16_f32 v62, v152, v153\n"
    "v_cvt_pk_bf16_f32 v63, v154, v155\n"
    "ds_write_b64 v24, v[62:63]\n"
    "ds_read_b128 a[144:147], v52\n"
    "buffer_load_dwordx4 v14, s[20:23], s45 offen offset:3072 nt lds\n"
    "ds_read_b128 a[148:151], v53\n"
    "ds_read_b128 a[152:155], v54\n"
    "ds_read_b128 a[156:159], v55\n"
    "v_mfma_f32_16x16x32_bf16 v[64:67], a[228:231], a[228:231], 0\n"
    "v_mfma_f32_16x16x32_bf16 v[68:71], a[228:231], a[228:231], 0\n"
    "v_mfma_f32_16x16x32_bf16 v[76:79], a[228:231], a[228:231], 0\n"
    "v_mfma_f32_16x16x32_bf16 v[80:83], a[228:231], a[228:231], 0\n"
    "v_mfma_f32_16x16x32_bf16 v[88:91], a[228:231], a[228:231], 0\n"
    "s_mov_b32 m0, 122880\n"
    "s_mov_b32 s45, 57344\n"
    "s_waitcnt vmcnt(18)\n"
    "buffer_load_dwordx4 v13, s[20:23], s45 offen nt lds\n"
    "v_mfma_f32_16x16x32_bf16 v[92:95], a[228:231], a[228:231], 0\n"
    "v_mfma_f32_16x16x32_bf16 v[100:103], a[228:231], a[228:231], 0\n"
    "v_mfma_f32_16x16x32_bf16 v[104:107], a[228:231], a[228:231], 0\n"
    "v_mfma_f32_16x16x32_bf16 v[112:115], a[228:231], a[228:231], 0\n"
    "v_mfma_f32_16x16x32_bf16 v[116:119], a[228:231], a[228:231], 0\n"
    "v_mfma_f32_16x16x32_bf16 v[124:127], a[228:231], a[228:231], 0\n"
    "v_mfma_f32_16x16x32_bf16 v[128:131], a[228:231], a[228:231], 0\n"
    "buffer_load_dwordx4 v14, s[20:23], s45 offen offset:1024 nt lds\n"
    "v_mfma_f32_16x16x32_bf16 v[136:139], a[228:231], a[228:231], 0\n"
    "v_mfma_f32_16x16x32_bf16 v[140:143], a[228:231], a[228:231], 0\n"
    "v_mfma_f32_16x16x32_bf16 v[148:151], a[228:231], a[228:231], 0\n"
    "v_mfma_f32_16x16x32_bf16 v[152:155], a[228:231], a[228:231], 0\n"
    "v_mov_b32 v244, 0\n"
    "v_mov_b32 v245, 0\n"
    "v_mov_b32 v246, 0\n"
    "v_mov_b32 v247, 0\n"
    "s_waitcnt vmcnt(18)\n"
    "buffer_load_dwordx4 v13, s[20:23], s45 offen offset:2048 nt lds\n"
    "v_mov_b32 v248, 0\n"
    "v_mov_b32 v249, 0\n"
    "v_mov_b32 v250, 0\n"
    "v_mov_b32 v251, 0\n"
    "v_max3_f32 v44, v160, v161, v162\n"
    "v_max3_f32 v44, v44, v163, v176\n"
    "v_max3_f32 v44, v44, v177, v178\n"
    "v_max3_f32 v44, v44, v179, v168\n"
    "buffer_load_dwordx4 v14, s[20:23], s45 offen offset:3072 nt lds\n"
    "v_max3_f32 v44, v44, v169, v170\n"
    "v_max3_f32 v44, v44, v171, v180\n"
    "v_max3_f32 v44, v44, v181, v182\n"
    "v_max_f32 v44, v44, v183\n"
    "v_max3_f32 v46, v184, v185, v186\n"
    "v_max3_f32 v46, v46, v187, v200\n"
    "v_max3_f32 v46, v46, v201, v202\n"
    "s_mov_b32 m0, 126976\n"
    "s_mov_b32 s45, 61440\n"
    "s_waitcnt vmcnt(18)\n"
    "buffer_load_dwordx4 v13, s[20:23], s45 offen nt lds\n"
    "v_max3_f32 v46, v46, v203, v192\n"
    "v_max3_f32 v46, v46, v193, v194\n"
    "v_max3_f32 v46, v46, v195, v204\n"
    "v_max3_f32 v46, v46, v205, v206\n"
    "v_max_f32 v46, v46, v207\n"
    "v_mov_b32 v45, v44\n"
    "v_mov_b32 v47, v46\n"
    "s_nop 1\n"
    "buffer_load_dwordx4 v14, s[20:23], s45 offen offset:1024 nt lds\n"
    "v_permlane32_swap_b32 v44, v45\n"
    "v_permlane32_swap_b32 v46, v47\n"
    "v_max_f32 v44, v44, v45\n"
    "v_max_f32 v46, v46, v47\n"
    "v_mov_b32 v45, v44\n"
    "v_mov_b32 v47, v46\n"
    "s_nop 1\n"
    "s_waitcnt vmcnt(18)\n"
    "buffer_load_dwordx4 v13, s[20:23], s45 offen offset:2048 nt lds\n"
    "v_permlane16_swap_b32 v44, v45\n"
    "v_permlane16_swap_b32 v46, v47\n"
    "v_max_f32 v44, v44, v45\n"
    "v_max_f32 v46, v46, v47\n"
    "v_sub_f32 v232, 0, v44\n"
    "v_sub_f32 v233, 0, v44\n"
    "v_sub_f32 v234, 0, v44\n"
    "v_sub_f32 v235, 0, v44\n"
    "buffer_load_dwordx4 v14, s[20:23], s45 offen offset:3072 nt lds\n"
    "v_sub_f32 v236, 0, v46\n"
    "v_sub_f32 v237, 0, v46\n"
    "v_sub_f32 v238, 0, v46\n"
    "v_sub_f32 v239, 0, v46\n"
    "v_pk_add_f32 v[160:161], v[232:233], v[160:161]\n"
    "v_pk_add_f32 v[162:163], v[232:233], v[162:163]\n"
    "v_pk_add_f32 v[184:185], v[236:237], v[184:185]\n"
    "v_pk_add_f32 v[186:187], v[236:237], v[186:187]\n"
    "s_mov_b32 m0, 131072\n"
    "s_mov_b32 s45, 65536\n"
    "s_waitcnt vmcnt(18)\n"
    "buffer_load_dwordx4 v13, s[20:23], s45 offen nt lds\n"
    "v_pk_add_f32 v[176:177], v[232:233], v[176:177]\n"
    "v_pk_add_f32 v[178:179], v[232:233], v[178:179]\n"
    "v_pk_add_f32 v[200:201], v[236:237], v[200:201]\n"
    "v_pk_add_f32 v[202:203], v[236:237], v[202:203]\n"
    "v_pk_add_f32 v[168:169], v[232:233], v[168:169]\n"
    "v_pk_add_f32 v[170:171], v[232:233], v[170:171]\n"
    "v_pk_add_f32 v[192:193], v[236:237], v[192:193]\n"
    "buffer_load_dwordx4 v14, s[20:23], s45 offen offset:1024 nt lds\n"
    "v_pk_add_f32 v[194:195], v[236:237], v[194:195]\n"
    "v_pk_add_f32 v[180:181], v[232:233], v[180:181]\n"
    "v_pk_add_f32 v[182:183], v[232:233], v[182:183]\n"
    "v_pk_add_f32 v[204:205], v[236:237], v[204:205]\n"
    "v_pk_add_f32 v[206:207], v[236:237], v[206:207]\n"
    "v_exp_f32 v160, v160\n"
    "v_exp_f32 v161, v161\n"
    "v_exp_f32 v162, v162\n"
    "s_waitcnt vmcnt(18)\n"
    "buffer_load_dwordx4 v13, s[20:23], s45 offen offset:2048 nt lds\n"
    "v_exp_f32 v163, v163\n"
    "v_exp_f32 v184, v184\n"
    "v_exp_f32 v185, v185\n"
    "v_exp_f32 v186, v186\n"
    "v_exp_f32 v187, v187\n"
    "v_exp_f32 v176, v176\n"
    "v_exp_f32 v177, v177\n"
    "buffer_load_dwordx4 v14, s[20:23], s45 offen offset:3072 nt lds\n"
    "v_exp_f32 v178, v178\n"
    "v_exp_f32 v179, v179\n"
    "v_exp_f32 v200, v200\n"
    "v_exp_f32 v201, v201\n"
    "v_exp_f32 v202, v202\n"
    "v_exp_f32 v203, v203\n"
    "v_exp_f32 v168, v168\n"
    "v_exp_f32 v169, v169\n"
    "s_mov_b32 m0, 135168\n"
    "s_mov_b32 s45, 69632\n"
    "s_waitcnt vmcnt(18)\n"
    "buffer_load_dwordx4 v13, s[20:23], s45 offen nt lds\n"
    "v_exp_f32 v170, v170\n"
    "v_exp_f32 v171, v171\n"
    "v_exp_f32 v192, v192\n"
    "v_exp_f32 v193, v193\n"
    "v_exp_f32 v194, v194\n"
    "v_exp_f32 v195, v195\n"
    "v_exp_f32 v180, v180\n"
    "v_exp_f32 v181, v181\n"
    "buffer_load_dwordx4 v14, s[20:23], s45 offen offset:1024 nt lds\n"
    "v_exp_f32 v182, v182\n"
    "v_exp_f32 v183, v183\n"
    "v_exp_f32 v204, v204\n"
    "v_exp_f32 v205, v205\n"
    "v_exp_f32 v206, v206\n"
    "v_exp_f32 v207, v207\n"
    "v_cvt_pk_bf16_f32 v160, v160, v161\n"
    "s_waitcnt vmcnt(18)\n"
    "buffer_load_dwordx4 v13, s[20:23], s45 offen offset:2048 nt lds\n"
    "v_cvt_pk_bf16_f32 v161, v162, v163\n"
    "v_cvt_pk_bf16_f32 v184, v184, v185\n"
    "v_cvt_pk_bf16_f32 v185, v186, v187\n"
    "v_cvt_pk_bf16_f32 v162, v176, v177\n"
    "v_cvt_pk_bf16_f32 v163, v178, v179\n"
    "v_cvt_pk_bf16_f32 v186, v200, v201\n"
    "v_cvt_pk_bf16_f32 v187, v202, v203\n"
    "v_cvt_pk_bf16_f32 v168, v168, v169\n"
    "buffer_load_dwordx4 v14, s[20:23], s45 offen offset:3072 nt lds\n"
    "v_cvt_pk_bf16_f32 v169, v170, v171\n"
    "v_cvt_pk_bf16_f32 v192, v192, v193\n"
    "v_cvt_pk_bf16_f32 v193, v194, v195\n"
    "v_cvt_pk_bf16_f32 v170, v180, v181\n"
    "v_cvt_pk_bf16_f32 v171, v182, v183\n"
    "v_cvt_pk_bf16_f32 v194, v204, v205\n"
    "v_cvt_pk_bf16_f32 v195, v206, v207\n"
    "s_waitcnt vmcnt(62) lgkmcnt(0)\n"
    "s_barrier\n"
    "v_mfma_f32_16x16x32_bf16 v[164:167], a[0:3], a[176:179], v[232:235]\n"
    "ds_read_b64_tr_b16 a[64:65], v25 offset:0\n"
    "s_mov_b32 m0, 16384\n"
    "s_mov_b32 s45, 81920\n"
    "s_waitcnt vmcnt(12)\n"
    "buffer_load_dwordx4 v9, s[16:19], s45 offen nt lds\n"
    "v_mfma_f32_16x16x32_bf16 v[164:167], a[4:7], a[180:183], v[164:167]\n"
    "ds_read_b64_tr_b16 a[66:67], v25 offset:4096\n"
    "v_mfma_f32_16x16x32_bf16 v[164:167], a[8:11], a[184:187], v[164:167]\n"
    "ds_read_b64_tr_b16 a[68:69], v25 offset:8192\n"
    "buffer_load_dwordx4 v10, s[16:19], s45 offen offset:1024 nt lds\n"
    "v_mfma_f32_16x16x32_bf16 v[164:167], a[12:15], a[188:191], v[164:167]\n"
    "ds_read_b64_tr_b16 a[70:71], v25 offset:12288\n"
    "v_add_u32 v25, 0x4000, v25\n"
    "v_mfma_f32_16x16x32_bf16 v[188:191], a[12:15], a[204:207], v[236:239]\n"
    "ds_read_b64_tr_b16 a[72:73], v26 offset:0\n"
    "v_mfma_f32_16x16x32_bf16 v[188:191], a[8:11], a[200:203], v[188:191]\n"
    "ds_read_b64_tr_b16 a[74:75], v26 offset:4096\n"
    "s_waitcnt vmcnt(12)\n"
    "buffer_load_dwordx4 v11, s[16:19], s45 offen offset:2048 nt lds\n"
    "v_mfma_f32_16x16x32_bf16 v[188:191], a[4:7], a[196:199], v[188:191]\n"
    "ds_read_b64_tr_b16 a[76:77], v26 offset:8192\n"
    "v_mfma_f32_16x16x32_bf16 v[188:191], a[0:3], a[192:195], v[188:191]\n"
    "ds_read_b64_tr_b16 a[78:79], v26 offset:12288\n"
    "v_add_u32 v26, 0x4000, v26\n"
    "v_mfma_f32_16x16x32_bf16 v[200:203], a[16:19], a[192:195], v[236:239]\n"
    "ds_read_b64_tr_b16 a[80:81], v27 offset:0\n"
    "buffer_load_dwordx4 v12, s[16:19], s45 offen offset:3072 nt lds\n"
    "v_mfma_f32_16x16x32_bf16 v[200:203], a[20:23], a[196:199], v[200:203]\n"
    "ds_read_b64_tr_b16 a[82:83], v27 offset:4096\n"
    "v_mfma_f32_16x16x32_bf16 v[200:203], a[24:27], a[200:203], v[200:203]\n"
    "ds_read_b64_tr_b16 a[84:85], v27 offset:8192\n"
    "v_mfma_f32_16x16x32_bf16 v[200:203], a[28:31], a[204:207], v[200:203]\n"
    "ds_read_b64_tr_b16 a[86:87], v27 offset:12288\n"
    "v_add_u32 v27, 0x4000, v27\n"
    "s_mov_b32 m0, 20480\n"
    "s_mov_b32 s45, 86016\n"
    "s_waitcnt vmcnt(12)\n"
    "buffer_load_dwordx4 v9, s[16:19], s45 offen nt lds\n"
    "v_mfma_f32_16x16x32_bf16 v[176:179], a[28:31], a[188:191], v[232:235]\n"
    "ds_read_b64_tr_b16 a[88:89], v28 offset:0\n"
    "v_xor_b32 v33, 0x8000, v33\n"
    "v_mfma_f32_16x16x32_bf16 v[176:179], a[24:27], a[184:187], v[176:179]\n"
    "ds_read_b64_tr_b16 a[90:91], v28 offset:4096\n"
    "v_mfma_f32_16x16x32_bf16 v[176:179], a[20:23], a[180:183], v[176:179]\n"
    "ds_read_b64_tr_b16 a[92:93], v28 offset:8192\n"
    "v_xor_b32 v34, 0x8000, v34\n"
    "buffer_load_dwordx4 v10, s[16:19], s45 offen offset:1024 nt lds\n"
    "v_mfma_f32_16x16x32_bf16 v[176:179], a[16:19], a[176:179], v[176:179]\n"
    "ds_read_b64_tr_b16 a[94:95], v28 offset:12288\n"
    "v_add_u32 v28, 0x4000, v28\n"
    "v_mfma_f32_16x16x32_bf16 v[172:175], a[32:35], a[176:179], v[232:235]\n"
    "ds_read_b64_tr_b16 a[96:97], v29 offset:0\n"
    "v_xor_b32 v35, 0x8000, v35\n"
    "v_mfma_f32_16x16x32_bf16 v[172:175], a[36:39], a[180:183], v[172:175]\n"
    "ds_read_b64_tr_b16 a[98:99], v29 offset:4096\n"
    "s_waitcnt vmcnt(12)\n"
    "buffer_load_dwordx4 v11, s[16:19], s45 offen offset:2048 nt lds\n"
    "v_mfma_f32_16x16x32_bf16 v[172:175], a[40:43], a[184:187], v[172:175]\n"
    "ds_read_b64_tr_b16 a[100:101], v29 offset:8192\n"
    "v_xor_b32 v36, 0x8000, v36\n"
    "v_mfma_f32_16x16x32_bf16 v[172:175], a[44:47], a[188:191], v[172:175]\n"
    "ds_read_b64_tr_b16 a[102:103], v29 offset:12288\n"
    "v_add_u32 v29, 0x4000, v29\n"
    "buffer_load_dwordx4 v12, s[16:19], s45 offen offset:3072 nt lds\n"
    "v_mfma_f32_16x16x32_bf16 v[196:199], a[44:47], a[204:207], v[236:239]\n"
    "ds_read_b64_tr_b16 a[104:105], v30 offset:0\n"
    "v_mfma_f32_16x16x32_bf16 v[196:199], a[40:43], a[200:203], v[196:199]\n"
    "ds_read_b64_tr_b16 a[106:107], v30 offset:4096\n"
    "v_mfma_f32_16x16x32_bf16 v[196:199], a[36:39], a[196:199], v[196:199]\n"
    "ds_read_b64_tr_b16 a[108:109], v30 offset:8192\n"
    "s_mov_b32 m0, 24576\n"
    "s_mov_b32 s45, 90112\n"
    "s_waitcnt vmcnt(12)\n"
    "buffer_load_dwordx4 v9, s[16:19], s45 offen nt lds\n"
    "v_mfma_f32_16x16x32_bf16 v[196:199], a[32:35], a[192:195], v[196:199]\n"
    "ds_read_b64_tr_b16 a[110:111], v30 offset:12288\n"
    "v_add_u32 v30, 0x4000, v30\n"
    "v_mfma_f32_16x16x32_bf16 v[204:207], a[48:51], a[192:195], v[236:239]\n"
    "ds_read_b64_tr_b16 a[112:113], v31 offset:0\n"
    "v_mfma_f32_16x16x32_bf16 v[204:207], a[52:55], a[196:199], v[204:207]\n"
    "ds_read_b64_tr_b16 a[114:115], v31 offset:4096\n"
    "buffer_load_dwordx4 v10, s[16:19], s45 offen offset:1024 nt lds\n"
    "v_mfma_f32_16x16x32_bf16 v[204:207], a[56:59], a[200:203], v[204:207]\n"
    "ds_read_b64_tr_b16 a[116:117], v31 offset:8192\n"
    "v_mfma_f32_16x16x32_bf16 v[204:207], a[60:63], a[204:207], v[204:207]\n"
    "ds_read_b64_tr_b16 a[118:119], v31 offset:12288\n"
    "v_add_u32 v31, 0x4000, v31\n"
    "v_mfma_f32_16x16x32_bf16 v[180:183], a[60:63], a[188:191], v[232:235]\n"
    "ds_read_b64_tr_b16 a[120:121], v32 offset:0\n"
    "s_waitcnt vmcnt(12)\n"
    "buffer_load_dwordx4 v11, s[16:19], s45 offen offset:2048 nt lds\n"
    "v_mfma_f32_16x16x32_bf16 v[180:183], a[56:59], a[184:187], v[180:183]\n"
    "ds_read_b64_tr_b16 a[122:123], v32 offset:4096\n"
    "v_mfma_f32_16x16x32_bf16 v[180:183], a[52:55], a[180:183], v[180:183]\n"
    "ds_read_b64_tr_b16 a[124:125], v32 offset:8192\n"
    "v_mfma_f32_16x16x32_bf16 v[180:183], a[48:51], a[176:179], v[180:183]\n"
    "ds_read_b64_tr_b16 a[126:127], v32 offset:12288\n"
    "v_add_u32 v32, 0x4000, v32\n"
    "buffer_load_dwordx4 v12, s[16:19], s45 offen offset:3072 nt lds\n"
    "s_waitcnt lgkmcnt(0)\n"
    "v_mfma_f32_16x16x32_bf16 v[64:67], a[64:67], v[160:163], v[64:67]\n"
    "ds_read_b128 a[0:3], v33 offset:0\n"
    "v_exp_f32 v164, v164\n"
    "v_mfma_f32_16x16x32_bf16 v[64:67], a[68:71], v[168:171], v[64:67]\n"
    "v_exp_f32 v165, v165\n"
    "v_mfma_f32_16x16x32_bf16 v[68:71], a[68:71], v[192:195], v[68:71]\n"
    "s_mov_b32 m0, 28672\n"
    "s_mov_b32 s45, 94208\n"
    "s_waitcnt vmcnt(12)\n"
    "buffer_load_dwordx4 v9, s[16:19], s45 offen nt lds\n"
    "ds_read_b128 a[4:7], v34 offset:0\n"
    "v_exp_f32 v166, v166\n"
    "v_cvt_pk_bf16_f32 v164, v164, v165\n"
    "v_mfma_f32_16x16x32_bf16 v[68:71], a[64:67], v[184:187], v[68:71]\n"
    "v_exp_f32 v167, v167\n"
    "v_mfma_f32_16x16x32_bf16 v[80:83], a[72:75], v[184:187], v[80:83]\n"
    "buffer_load_dwordx4 v10, s[16:19], s45 offen offset:1024 nt lds\n"
    "ds_read_b128 a[8:11], v35 offset:0\n"
    "v_exp_f32 v188, v188\n"
    "v_cvt_pk_bf16_f32 v165, v166, v167\n"
    "v_mfma_f32_16x16x32_bf16 v[80:83], a[76:79], v[192:195], v[80:83]\n"
    "v_exp_f32 v189, v189\n"
    "v_mfma_f32_16x16x32_bf16 v[76:79], a[76:79], v[168:171], v[76:79]\n"
    "ds_read_b128 a[12:15], v36 offset:0\n"
    "v_exp_f32 v190, v190\n"
    "v_cvt_pk_bf16_f32 v188, v188, v189\n"
    "v_mfma_f32_16x16x32_bf16 v[76:79], a[72:75], v[160:163], v[76:79]\n"
    "s_waitcnt vmcnt(12)\n"
    "buffer_load_dwordx4 v11, s[16:19], s45 offen offset:2048 nt lds\n"
    "v_exp_f32 v191, v191\n"
    "v_mfma_f32_16x16x32_bf16 v[88:91], a[80:83], v[160:163], v[88:91]\n"
    "ds_read_b128 a[16:19], v33 offset:4096\n"
    "v_exp_f32 v176, v176\n"
    "v_cvt_pk_bf16_f32 v189, v190, v191\n"
    "v_mfma_f32_16x16x32_bf16 v[88:91], a[84:87], v[168:171], v[88:91]\n"
    "v_exp_f32 v177, v177\n"
    "v_mfma_f32_16x16x32_bf16 v[92:95], a[84:87], v[192:195], v[92:95]\n"
    "buffer_load_dwordx4 v12, s[16:19], s45 offen offset:3072 nt lds\n"
    "ds_read_b128 a[20:23], v34 offset:4096\n"
    "v_exp_f32 v178, v178\n"
    "v_cvt_pk_bf16_f32 v166, v176, v177\n"
    "v_mfma_f32_16x16x32_bf16 v[92:95], a[80:83], v[184:187], v[92:95]\n"
    "v_exp_f32 v179, v179\n"
    "v_mfma_f32_16x16x32_bf16 v[104:107], a[88:91], v[184:187], v[104:107]\n"
    "ds_read_b128 a[24:27], v35 offset:4096\n"
    "v_exp_f32 v200, v200\n"
    "v_cvt_pk_bf16_f32 v167, v178, v179\n"
    "v_mfma_f32_16x16x32_bf16 v[104:107], a[92:95], v[192:195], v[104:107]\n"
    "s_mov_b32 m0, 139264\n"
    "s_mov_b32 s45, 73728\n"
    "s_waitcnt vmcnt(12)\n"
    "buffer_load_dwordx4 v13, s[20:23], s45 offen nt lds\n"
    "v_exp_f32 v201, v201\n"
    "v_mfma_f32_16x16x32_bf16 v[100:103], a[92:95], v[168:171], v[100:103]\n"
    "ds_read_b128 a[28:31], v36 offset:4096\n"
    "v_exp_f32 v202, v202\n"
    "v_cvt_pk_bf16_f32 v190, v200, v201\n"
    "v_mfma_f32_16x16x32_bf16 v[100:103], a[88:91], v[160:163], v[100:103]\n"
    "v_exp_f32 v203, v203\n"
    "v_mfma_f32_16x16x32_bf16 v[112:115], a[96:99], v[160:163], v[112:115]\n"
    "buffer_load_dwordx4 v14, s[20:23], s45 offen offset:1024 nt lds\n"
    "ds_read_b128 a[32:35], v33 offset:8192\n"
    "v_exp_f32 v172, v172\n"
    "v_cvt_pk_bf16_f32 v191, v202, v203\n"
    "v_mfma_f32_16x16x32_bf16 v[112:115], a[100:103], v[168:171], v[112:115]\n"
    "v_exp_f32 v173, v173\n"
    "v_mfma_f32_16x16x32_bf16 v[116:119], a[100:103], v[192:195], v[116:119]\n"
    "ds_read_b128 a[36:39], v34 offset:8192\n"
    "v_exp_f32 v174, v174\n"
    "v_cvt_pk_bf16_f32 v172, v172, v173\n"
    "v_mfma_f32_16x16x32_bf16 v[116:119], a[96:99], v[184:187], v[116:119]\n"
    "s_waitcnt vmcnt(12)\n"
    "buffer_load_dwordx4 v13, s[20:23], s45 offen offset:2048 nt lds\n"
    "v_exp_f32 v175, v175\n"
    "v_mfma_f32_16x16x32_bf16 v[128:131], a[104:107], v[184:187], v[128:131]\n"
    "ds_read_b128 a[40:43], v35 offset:8192\n"
    "v_exp_f32 v196, v196\n"
    "v_cvt_pk_bf16_f32 v173, v174, v175\n"
    "v_mfma_f32_16x16x32_bf16 v[128:131], a[108:111], v[192:195], v[128:131]\n"
    "buffer_load_dwordx4 v14, s[20:23], s45 offen offset:3072 nt lds\n"
    "v_exp_f32 v197, v197\n"
    "v_mfma_f32_16x16x32_bf16 v[124:127], a[108:111], v[168:171], v[124:127]\n"
    "ds_read_b128 a[44:47], v36 offset:8192\n"
    "v_exp_f32 v198, v198\n"
    "v_cvt_pk_bf16_f32 v196, v196, v197\n"
    "v_mfma_f32_16x16x32_bf16 v[124:127], a[104:107], v[160:163], v[124:127]\n"
    "v_exp_f32 v199, v199\n"
    "v_mfma_f32_16x16x32_bf16 v[136:139], a[112:115], v[160:163], v[136:139]\n"
    "s_mov_b32 m0, 143360\n"
    "s_mov_b32 s45, 77824\n"
    "s_waitcnt vmcnt(12)\n"
    "buffer_load_dwordx4 v13, s[20:23], s45 offen nt lds\n"
    "ds_read_b128 a[48:51], v33 offset:12288\n"
    "v_exp_f32 v180, v180\n"
    "v_cvt_pk_bf16_f32 v197, v198, v199\n"
    "v_mfma_f32_16x16x32_bf16 v[136:139], a[116:119], v[168:171], v[136:139]\n"
    "v_exp_f32 v181, v181\n"
    "v_mfma_f32_16x16x32_bf16 v[140:143], a[116:119], v[192:195], v[140:143]\n"
    "ds_read_b128 a[52:55], v34 offset:12288\n"
    "v_exp_f32 v182, v182\n"
    "v_cvt_pk_bf16_f32 v174, v180, v181\n"
    "v_mfma_f32_16x16x32_bf16 v[140:143], a[112:115], v[184:187], v[140:143]\n"
    "buffer_load_dwordx4 v14, s[20:23], s45 offen offset:1024 nt lds\n"
    "v_exp_f32 v183, v183\n"
    "v_mfma_f32_16x16x32_bf16 v[152:155], a[120:123], v[184:187], v[152:155]\n"
    "ds_read_b128 a[56:59], v35 offset:12288\n"
    "v_exp_f32 v204, v204\n"
    "v_cvt_pk_bf16_f32 v175, v182, v183\n"
    "v_mfma_f32_16x16x32_bf16 v[152:155], a[124:127], v[192:195], v[152:155]\n"
    "v_exp_f32 v205, v205\n"
    "v_mfma_f32_16x16x32_bf16 v[148:151], a[124:127], v[168:171], v[148:151]\n"
    "s_waitcnt vmcnt(12)\n"
    "buffer_load_dwordx4 v13, s[20:23], s45 offen offset:2048 nt lds\n"
    "ds_read_b128 a[60:63], v36 offset:12288\n"
    "v_exp_f32 v206, v206\n"
    "v_cvt_pk_bf16_f32 v198, v204, v205\n"
    "v_mfma_f32_16x16x32_bf16 v[148:151], a[120:123], v[160:163], v[148:151]\n"
    "v_exp_f32 v207, v207\n"
    "v_mfma_f32_16x16x32_bf16 v[244:247], a[224:227], v[160:163], v[244:247]\n"
    "v_mfma_f32_16x16x32_bf16 v[244:247], a[224:227], v[168:171], v[244:247]\n"
    "buffer_load_dwordx4 v14, s[20:23], s45 offen offset:3072 nt lds\n"
    "v_cvt_pk_bf16_f32 v199, v206, v207\n"
    "v_mfma_f32_16x16x32_bf16 v[248:251], a[224:227], v[184:187], v[248:251]\n"
    "v_mfma_f32_16x16x32_bf16 v[248:251], a[224:227], v[192:195], v[248:251]\n"
    "s_waitcnt vmcnt(62) lgkmcnt(0)\n"
    "s_barrier\n"
    "v_mfma_f32_16x16x32_bf16 v[160:163], a[0:3], a[176:179], v[232:235]\n"
    "ds_read_b64_tr_b16 a[64:65], v25 offset:0\n"
    "s_mov_b32 m0, 32768\n"
    "s_mov_b32 s45, 98304\n"
    "s_waitcnt vmcnt(12)\n"
    "buffer_load_dwordx4 v9, s[16:19], s45 offen nt lds\n"
    "v_mfma_f32_16x16x32_bf16 v[160:163], a[4:7], a[180:183], v[160:163]\n"
    "ds_read_b64_tr_b16 a[66:67], v25 offset:4096\n"
    "v_mfma_f32_16x16x32_bf16 v[160:163], a[8:11], a[184:187], v[160:163]\n"
    "ds_read_b64_tr_b16 a[68:69], v25 offset:8192\n"
    "buffer_load_dwordx4 v10, s[16:19], s45 offen offset:1024 nt lds\n"
    "v_mfma_f32_16x16x32_bf16 v[160:163], a[12:15], a[188:191], v[160:163]\n"
    "ds_read_b64_tr_b16 a[70:71], v25 offset:12288\n"
    "v_add_u32 v25, 0x4000, v25\n"
    "v_mfma_f32_16x16x32_bf16 v[184:187], a[12:15], a[204:207], v[236:239]\n"
    "ds_read_b64_tr_b16 a[72:73], v26 offset:0\n"
    "v_mfma_f32_16x16x32_bf16 v[184:187], a[8:11], a[200:203], v[184:187]\n"
    "ds_read_b64_tr_b16 a[74:75], v26 offset:4096\n"
    "s_waitcnt vmcnt(12)\n"
    "buffer_load_dwordx4 v11, s[16:19], s45 offen offset:2048 nt lds\n"
    "v_mfma_f32_16x16x32_bf16 v[184:187], a[4:7], a[196:199], v[184:187]\n"
    "ds_read_b64_tr_b16 a[76:77], v26 offset:8192\n"
    "v_mfma_f32_16x16x32_bf16 v[184:187], a[0:3], a[192:195], v[184:187]\n"
    "ds_read_b64_tr_b16 a[78:79], v26 offset:12288\n"
    "v_add_u32 v26, 0x4000, v26\n"
    "v_mfma_f32_16x16x32_bf16 v[200:203], a[16:19], a[192:195], v[236:239]\n"
    "ds_read_b64_tr_b16 a[80:81], v27 offset:0\n"
    "buffer_load_dwordx4 v12, s[16:19], s45 offen offset:3072 nt lds\n"
    "v_mfma_f32_16x16x32_bf16 v[200:203], a[20:23], a[196:199], v[200:203]\n"
    "ds_read_b64_tr_b16 a[82:83], v27 offset:4096\n"
    "v_mfma_f32_16x16x32_bf16 v[200:203], a[24:27], a[200:203], v[200:203]\n"
    "ds_read_b64_tr_b16 a[84:85], v27 offset:8192\n"
    "v_mfma_f32_16x16x32_bf16 v[200:203], a[28:31], a[204:207], v[200:203]\n"
    "ds_read_b64_tr_b16 a[86:87], v27 offset:12288\n"
    "v_add_u32 v27, 0x4000, v27\n"
    "s_mov_b32 m0, 36864\n"
    "s_mov_b32 s45, 102400\n"
    "s_waitcnt vmcnt(12)\n"
    "buffer_load_dwordx4 v9, s[16:19], s45 offen nt lds\n"
    "v_mfma_f32_16x16x32_bf16 v[176:179], a[28:31], a[188:191], v[232:235]\n"
    "ds_read_b64_tr_b16 a[88:89], v28 offset:0\n"
    "v_mfma_f32_16x16x32_bf16 v[176:179], a[24:27], a[184:187], v[176:179]\n"
    "ds_read_b64_tr_b16 a[90:91], v28 offset:4096\n"
    "v_mfma_f32_16x16x32_bf16 v[176:179], a[20:23], a[180:183], v[176:179]\n"
    "ds_read_b64_tr_b16 a[92:93], v28 offset:8192\n"
    "buffer_load_dwordx4 v10, s[16:19], s45 offen offset:1024 nt lds\n"
    "v_mfma_f32_16x16x32_bf16 v[176:179], a[16:19], a[176:179], v[176:179]\n"
    "ds_read_b64_tr_b16 a[94:95], v28 offset:12288\n"
    "v_add_u32 v28, 0x4000, v28\n"
    "v_mfma_f32_16x16x32_bf16 v[168:171], a[32:35], a[176:179], v[232:235]\n"
    "ds_read_b64_tr_b16 a[96:97], v29 offset:0\n"
    "v_mfma_f32_16x16x32_bf16 v[168:171], a[36:39], a[180:183], v[168:171]\n"
    "ds_read_b64_tr_b16 a[98:99], v29 offset:4096\n"
    "s_waitcnt vmcnt(12)\n"
    "buffer_load_dwordx4 v11, s[16:19], s45 offen offset:2048 nt lds\n"
    "v_mfma_f32_16x16x32_bf16 v[168:171], a[40:43], a[184:187], v[168:171]\n"
    "ds_read_b64_tr_b16 a[100:101], v29 offset:8192\n"
    "v_mfma_f32_16x16x32_bf16 v[168:171], a[44:47], a[188:191], v[168:171]\n"
    "ds_read_b64_tr_b16 a[102:103], v29 offset:12288\n"
    "v_add_u32 v29, 0x4000, v29\n"
    "buffer_load_dwordx4 v12, s[16:19], s45 offen offset:3072 nt lds\n"
    "v_mfma_f32_16x16x32_bf16 v[192:195], a[44:47], a[204:207], v[236:239]\n"
    "ds_read_b64_tr_b16 a[104:105], v30 offset:0\n"
    "v_mfma_f32_16x16x32_bf16 v[192:195], a[40:43], a[200:203], v[192:195]\n"
    "ds_read_b64_tr_b16 a[106:107], v30 offset:4096\n"
    "v_mfma_f32_16x16x32_bf16 v[192:195], a[36:39], a[196:199], v[192:195]\n"
    "ds_read_b64_tr_b16 a[108:109], v30 offset:8192\n"
    "s_mov_b32 m0, 40960\n"
    "s_mov_b32 s45, 106496\n"
    "s_waitcnt vmcnt(12)\n"
    "buffer_load_dwordx4 v9, s[16:19], s45 offen nt lds\n"
    "v_mfma_f32_16x16x32_bf16 v[192:195], a[32:35], a[192:195], v[192:195]\n"
    "ds_read_b64_tr_b16 a[110:111], v30 offset:12288\n"
    "v_add_u32 v30, 0x4000, v30\n"
    "v_mfma_f32_16x16x32_bf16 v[204:207], a[48:51], a[192:195], v[236:239]\n"
    "ds_read_b64_tr_b16 a[112:113], v31 offset:0\n"
    "v_mfma_f32_16x16x32_bf16 v[204:207], a[52:55], a[196:199], v[204:207]\n"
    "ds_read_b64_tr_b16 a[114:115], v31 offset:4096\n"
    "buffer_load_dwordx4 v10, s[16:19], s45 offen offset:1024 nt lds\n"
    "v_mfma_f32_16x16x32_bf16 v[204:207], a[56:59], a[200:203], v[204:207]\n"
    "ds_read_b64_tr_b16 a[116:117], v31 offset:8192\n"
    "v_mfma_f32_16x16x32_bf16 v[204:207], a[60:63], a[204:207], v[204:207]\n"
    "ds_read_b64_tr_b16 a[118:119], v31 offset:12288\n"
    "v_add_u32 v31, 0x4000, v31\n"
    "v_mfma_f32_16x16x32_bf16 v[180:183], a[60:63], a[188:191], v[232:235]\n"
    "ds_read_b64_tr_b16 a[120:121], v32 offset:0\n"
    "s_waitcnt vmcnt(12)\n"
    "buffer_load_dwordx4 v11, s[16:19], s45 offen offset:2048 nt lds\n"
    "v_mfma_f32_16x16x32_bf16 v[180:183], a[56:59], a[184:187], v[180:183]\n"
    "ds_read_b64_tr_b16 a[122:123], v32 offset:4096\n"
    "v_mfma_f32_16x16x32_bf16 v[180:183], a[52:55], a[180:183], v[180:183]\n"
    "ds_read_b64_tr_b16 a[124:125], v32 offset:8192\n"
    "v_mfma_f32_16x16x32_bf16 v[180:183], a[48:51], a[176:179], v[180:183]\n"
    "ds_read_b64_tr_b16 a[126:127], v32 offset:12288\n"
    "v_add_u32 v32, 0x4000, v32\n"
    "buffer_load_dwordx4 v12, s[16:19], s45 offen offset:3072 nt lds\n"
    "s_waitcnt lgkmcnt(0)\n"
    "v_mfma_f32_16x16x32_bf16 v[64:67], a[64:67], v[164:167], v[64:67]\n"
    "ds_read_b128 a[0:3], v33 offset:16384\n"
    "v_exp_f32 v160, v160\n"
    "v_mfma_f32_16x16x32_bf16 v[64:67], a[68:71], v[172:175], v[64:67]\n"
    "v_exp_f32 v161, v161\n"
    "v_mfma_f32_16x16x32_bf16 v[68:71], a[68:71], v[196:199], v[68:71]\n"
    "s_mov_b32 m0, 45056\n"
    "s_mov_b32 s45, 110592\n"
    "s_waitcnt vmcnt(12)\n"
    "buffer_load_dwordx4 v9, s[16:19], s45 offen nt lds\n"
    "ds_read_b128 a[4:7], v34 offset:16384\n"
    "v_exp_f32 v162, v162\n"
    "v_cvt_pk_bf16_f32 v160, v160, v161\n"
    "v_mfma_f32_16x16x32_bf16 v[68:71], a[64:67], v[188:191], v[68:71]\n"
    "v_exp_f32 v163, v163\n"
    "v_mfma_f32_16x16x32_bf16 v[80:83], a[72:75], v[188:191], v[80:83]\n"
    "buffer_load_dwordx4 v10, s[16:19], s45 offen offset:1024 nt lds\n"
    "ds_read_b128 a[8:11], v35 offset:16384\n"
    "v_exp_f32 v184, v184\n"
    "v_cvt_pk_bf16_f32 v161, v162, v163\n"
    "v_mfma_f32_16x16x32_bf16 v[80:83], a[76:79], v[196:199], v[80:83]\n"
    "v_exp_f32 v185, v185\n"
    "v_mfma_f32_16x16x32_bf16 v[76:79], a[76:79], v[172:175], v[76:79]\n"
    "ds_read_b128 a[12:15], v36 offset:16384\n"
    "v_exp_f32 v186, v186\n"
    "v_cvt_pk_bf16_f32 v184, v184, v185\n"
    "v_mfma_f32_16x16x32_bf16 v[76:79], a[72:75], v[164:167], v[76:79]\n"
    "s_waitcnt vmcnt(12)\n"
    "buffer_load_dwordx4 v11, s[16:19], s45 offen offset:2048 nt lds\n"
    "v_exp_f32 v187, v187\n"
    "v_mfma_f32_16x16x32_bf16 v[88:91], a[80:83], v[164:167], v[88:91]\n"
    "ds_read_b128 a[16:19], v33 offset:20480\n"
    "v_exp_f32 v176, v176\n"
    "v_cvt_pk_bf16_f32 v185, v186, v187\n"
    "v_mfma_f32_16x16x32_bf16 v[88:91], a[84:87], v[172:175], v[88:91]\n"
    "v_exp_f32 v177, v177\n"
    "v_mfma_f32_16x16x32_bf16 v[92:95], a[84:87], v[196:199], v[92:95]\n"
    "buffer_load_dwordx4 v12, s[16:19], s45 offen offset:3072 nt lds\n"
    "ds_read_b128 a[20:23], v34 offset:20480\n"
    "v_exp_f32 v178, v178\n"
    "v_cvt_pk_bf16_f32 v162, v176, v177\n"
    "v_mfma_f32_16x16x32_bf16 v[92:95], a[80:83], v[188:191], v[92:95]\n"
    "v_exp_f32 v179, v179\n"
    "v_mfma_f32_16x16x32_bf16 v[104:107], a[88:91], v[188:191], v[104:107]\n"
    "ds_read_b128 a[24:27], v35 offset:20480\n"
    "v_exp_f32 v200, v200\n"
    "v_cvt_pk_bf16_f32 v163, v178, v179\n"
    "v_mfma_f32_16x16x32_bf16 v[104:107], a[92:95], v[196:199], v[104:107]\n"
    "s_mov_b32 m0, 65536\n"
    "s_mov_b32 s45, 81920\n"
    "s_waitcnt vmcnt(12)\n"
    "buffer_load_dwordx4 v13, s[20:23], s45 offen nt lds\n"
    "v_exp_f32 v201, v201\n"
    "v_mfma_f32_16x16x32_bf16 v[100:103], a[92:95], v[172:175], v[100:103]\n"
    "ds_read_b128 a[28:31], v36 offset:20480\n"
    "v_exp_f32 v202, v202\n"
    "v_cvt_pk_bf16_f32 v186, v200, v201\n"
    "v_mfma_f32_16x16x32_bf16 v[100:103], a[88:91], v[164:167], v[100:103]\n"
    "v_exp_f32 v203, v203\n"
    "v_mfma_f32_16x16x32_bf16 v[112:115], a[96:99], v[164:167], v[112:115]\n"
    "buffer_load_dwordx4 v14, s[20:23], s45 offen offset:1024 nt lds\n"
    "ds_read_b128 a[32:35], v33 offset:24576\n"
    "v_exp_f32 v168, v168\n"
    "v_cvt_pk_bf16_f32 v187, v202, v203\n"
    "v_mfma_f32_16x16x32_bf16 v[112:115], a[100:103], v[172:175], v[112:115]\n"
    "v_exp_f32 v169, v169\n"
    "v_mfma_f32_16x16x32_bf16 v[116:119], a[100:103], v[196:199], v[116:119]\n"
    "ds_read_b128 a[36:39], v34 offset:24576\n"
    "v_exp_f32 v170, v170\n"
    "v_cvt_pk_bf16_f32 v168, v168, v169\n"
    "v_mfma_f32_16x16x32_bf16 v[116:119], a[96:99], v[188:191], v[116:119]\n"
    "s_waitcnt vmcnt(12)\n"
    "buffer_load_dwordx4 v13, s[20:23], s45 offen offset:2048 nt lds\n"
    "v_exp_f32 v171, v171\n"
    "v_mfma_f32_16x16x32_bf16 v[128:131], a[104:107], v[188:191], v[128:131]\n"
    "ds_read_b128 a[40:43], v35 offset:24576\n"
    "v_exp_f32 v192, v192\n"
    "v_cvt_pk_bf16_f32 v169, v170, v171\n"
    "v_mfma_f32_16x16x32_bf16 v[128:131], a[108:111], v[196:199], v[128:131]\n"
    "buffer_load_dwordx4 v14, s[20:23], s45 offen offset:3072 nt lds\n"
    "v_exp_f32 v193, v193\n"
    "v_mfma_f32_16x16x32_bf16 v[124:127], a[108:111], v[172:175], v[124:127]\n"
    "ds_read_b128 a[44:47], v36 offset:24576\n"
    "v_exp_f32 v194, v194\n"
    "v_cvt_pk_bf16_f32 v192, v192, v193\n"
    "v_mfma_f32_16x16x32_bf16 v[124:127], a[104:107], v[164:167], v[124:127]\n"
    "v_exp_f32 v195, v195\n"
    "v_mfma_f32_16x16x32_bf16 v[136:139], a[112:115], v[164:167], v[136:139]\n"
    "s_mov_b32 m0, 69632\n"
    "s_mov_b32 s45, 86016\n"
    "s_waitcnt vmcnt(12)\n"
    "buffer_load_dwordx4 v13, s[20:23], s45 offen nt lds\n"
    "ds_read_b128 a[48:51], v33 offset:28672\n"
    "v_exp_f32 v180, v180\n"
    "v_cvt_pk_bf16_f32 v193, v194, v195\n"
    "v_mfma_f32_16x16x32_bf16 v[136:139], a[116:119], v[172:175], v[136:139]\n"
    "v_exp_f32 v181, v181\n"
    "v_mfma_f32_16x16x32_bf16 v[140:143], a[116:119], v[196:199], v[140:143]\n"
    "ds_read_b128 a[52:55], v34 offset:28672\n"
    "v_exp_f32 v182, v182\n"
    "v_cvt_pk_bf16_f32 v170, v180, v181\n"
    "v_mfma_f32_16x16x32_bf16 v[140:143], a[112:115], v[188:191], v[140:143]\n"
    "buffer_load_dwordx4 v14, s[20:23], s45 offen offset:1024 nt lds\n"
    "v_exp_f32 v183, v183\n"
    "v_mfma_f32_16x16x32_bf16 v[152:155], a[120:123], v[188:191], v[152:155]\n"
    "ds_read_b128 a[56:59], v35 offset:28672\n"
    "v_exp_f32 v204, v204\n"
    "v_cvt_pk_bf16_f32 v171, v182, v183\n"
    "v_mfma_f32_16x16x32_bf16 v[152:155], a[124:127], v[196:199], v[152:155]\n"
    "v_exp_f32 v205, v205\n"
    "v_mfma_f32_16x16x32_bf16 v[148:151], a[124:127], v[172:175], v[148:151]\n"
    "s_waitcnt vmcnt(12)\n"
    "buffer_load_dwordx4 v13, s[20:23], s45 offen offset:2048 nt lds\n"
    "ds_read_b128 a[60:63], v36 offset:28672\n"
    "v_exp_f32 v206, v206\n"
    "v_cvt_pk_bf16_f32 v194, v204, v205\n"
    "v_mfma_f32_16x16x32_bf16 v[148:151], a[120:123], v[164:167], v[148:151]\n"
    "v_exp_f32 v207, v207\n"
    "v_mfma_f32_16x16x32_bf16 v[244:247], a[224:227], v[164:167], v[244:247]\n"
    "v_mfma_f32_16x16x32_bf16 v[244:247], a[224:227], v[172:175], v[244:247]\n"
    "buffer_load_dwordx4 v14, s[20:23], s45 offen offset:3072 nt lds\n"
    "v_cvt_pk_bf16_f32 v195, v206, v207\n"
    "v_mfma_f32_16x16x32_bf16 v[248:251], a[224:227], v[188:191], v[248:251]\n"
    "v_mfma_f32_16x16x32_bf16 v[248:251], a[224:227], v[196:199], v[248:251]\n"
    "s_waitcnt vmcnt(62) lgkmcnt(0)\n"
    "s_barrier\n"
    "v_mfma_f32_16x16x32_bf16 v[164:167], a[0:3], a[176:179], v[232:235]\n"
    "ds_read_b64_tr_b16 a[64:65], v25 offset:0\n"
    "s_mov_b32 m0, 49152\n"
    "s_mov_b32 s45, 114688\n"
    "s_waitcnt vmcnt(12)\n"
    "buffer_load_dwordx4 v9, s[16:19], s45 offen nt lds\n"
    "v_mfma_f32_16x16x32_bf16 v[164:167], a[4:7], a[180:183], v[164:167]\n"
    "ds_read_b64_tr_b16 a[66:67], v25 offset:4096\n"
    "v_mfma_f32_16x16x32_bf16 v[164:167], a[8:11], a[184:187], v[164:167]\n"
    "ds_read_b64_tr_b16 a[68:69], v25 offset:8192\n"
    "buffer_load_dwordx4 v10, s[16:19], s45 offen offset:1024 nt lds\n"
    "v_mfma_f32_16x16x32_bf16 v[164:167], a[12:15], a[188:191], v[164:167]\n"
    "ds_read_b64_tr_b16 a[70:71], v25 offset:12288\n"
    "v_add_u32 v25, 0x4000, v25\n"
    "v_mfma_f32_16x16x32_bf16 v[188:191], a[12:15], a[204:207], v[236:239]\n"
    "ds_read_b64_tr_b16 a[72:73], v26 offset:0\n"
    "v_mfma_f32_16x16x32_bf16 v[188:191], a[8:11], a[200:203], v[188:191]\n"
    "ds_read_b64_tr_b16 a[74:75], v26 offset:4096\n"
    "s_waitcnt vmcnt(12)\n"
    "buffer_load_dwordx4 v11, s[16:19], s45 offen offset:2048 nt lds\n"
    "v_mfma_f32_16x16x32_bf16 v[188:191], a[4:7], a[196:199], v[188:191]\n"
    "ds_read_b64_tr_b16 a[76:77], v26 offset:8192\n"
    "v_mfma_f32_16x16x32_bf16 v[188:191], a[0:3], a[192:195], v[188:191]\n"
    "ds_read_b64_tr_b16 a[78:79], v26 offset:12288\n"
    "v_add_u32 v26, 0x4000, v26\n"
    "v_mfma_f32_16x16x32_bf16 v[200:203], a[16:19], a[192:195], v[236:239]\n"
    "ds_read_b64_tr_b16 a[80:81], v27 offset:0\n"
    "buffer_load_dwordx4 v12, s[16:19], s45 offen offset:3072 nt lds\n"
    "v_mfma_f32_16x16x32_bf16 v[200:203], a[20:23], a[196:199], v[200:203]\n"
    "ds_read_b64_tr_b16 a[82:83], v27 offset:4096\n"
    "v_mfma_f32_16x16x32_bf16 v[200:203], a[24:27], a[200:203], v[200:203]\n"
    "ds_read_b64_tr_b16 a[84:85], v27 offset:8192\n"
    "v_mfma_f32_16x16x32_bf16 v[200:203], a[28:31], a[204:207], v[200:203]\n"
    "ds_read_b64_tr_b16 a[86:87], v27 offset:12288\n"
    "v_add_u32 v27, 0x4000, v27\n"
    "s_mov_b32 m0, 53248\n"
    "s_mov_b32 s45, 118784\n"
    "s_waitcnt vmcnt(12)\n"
    "buffer_load_dwordx4 v9, s[16:19], s45 offen nt lds\n"
    "v_mfma_f32_16x16x32_bf16 v[176:179], a[28:31], a[188:191], v[232:235]\n"
    "ds_read_b64_tr_b16 a[88:89], v28 offset:0\n"
    "v_xor_b32 v33, 0x8000, v33\n"
    "v_mfma_f32_16x16x32_bf16 v[176:179], a[24:27], a[184:187], v[176:179]\n"
    "ds_read_b64_tr_b16 a[90:91], v28 offset:4096\n"
    "v_mfma_f32_16x16x32_bf16 v[176:179], a[20:23], a[180:183], v[176:179]\n"
    "ds_read_b64_tr_b16 a[92:93], v28 offset:8192\n"
    "v_xor_b32 v34, 0x8000, v34\n"
    "buffer_load_dwordx4 v10, s[16:19], s45 offen offset:1024 nt lds\n"
    "v_mfma_f32_16x16x32_bf16 v[176:179], a[16:19], a[176:179], v[176:179]\n"
    "ds_read_b64_tr_b16 a[94:95], v28 offset:12288\n"
    "v_add_u32 v28, 0x4000, v28\n"
    "v_mfma_f32_16x16x32_bf16 v[172:175], a[32:35], a[176:179], v[232:235]\n"
    "ds_read_b64_tr_b16 a[96:97], v29 offset:0\n"
    "v_xor_b32 v35, 0x8000, v35\n"
    "v_mfma_f32_16x16x32_bf16 v[172:175], a[36:39], a[180:183], v[172:175]\n"
    "ds_read_b64_tr_b16 a[98:99], v29 offset:4096\n"
    "s_waitcnt vmcnt(12)\n"
    "buffer_load_dwordx4 v11, s[16:19], s45 offen offset:2048 nt lds\n"
    "v_mfma_f32_16x16x32_bf16 v[172:175], a[40:43], a[184:187], v[172:175]\n"
    "ds_read_b64_tr_b16 a[100:101], v29 offset:8192\n"
    "v_xor_b32 v36, 0x8000, v36\n"
    "v_mfma_f32_16x16x32_bf16 v[172:175], a[44:47], a[188:191], v[172:175]\n"
    "ds_read_b64_tr_b16 a[102:103], v29 offset:12288\n"
    "v_add_u32 v29, 0x4000, v29\n"
    "buffer_load_dwordx4 v12, s[16:19], s45 offen offset:3072 nt lds\n"
    "v_mfma_f32_16x16x32_bf16 v[196:199], a[44:47], a[204:207], v[236:239]\n"
    "ds_read_b64_tr_b16 a[104:105], v30 offset:0\n"
    "v_mfma_f32_16x16x32_bf16 v[196:199], a[40:43], a[200:203], v[196:199]\n"
    "ds_read_b64_tr_b16 a[106:107], v30 offset:4096\n"
    "v_mfma_f32_16x16x32_bf16 v[196:199], a[36:39], a[196:199], v[196:199]\n"
    "ds_read_b64_tr_b16 a[108:109], v30 offset:8192\n"
    "s_mov_b32 m0, 57344\n"
    "s_mov_b32 s45, 122880\n"
    "s_waitcnt vmcnt(12)\n"
    "buffer_load_dwordx4 v9, s[16:19], s45 offen nt lds\n"
    "v_mfma_f32_16x16x32_bf16 v[196:199], a[32:35], a[192:195], v[196:199]\n"
    "ds_read_b64_tr_b16 a[110:111], v30 offset:12288\n"
    "v_add_u32 v30, 0x4000, v30\n"
    "v_mfma_f32_16x16x32_bf16 v[204:207], a[48:51], a[192:195], v[236:239]\n"
    "ds_read_b64_tr_b16 a[112:113], v31 offset:0\n"
    "v_mfma_f32_16x16x32_bf16 v[204:207], a[52:55], a[196:199], v[204:207]\n"
    "ds_read_b64_tr_b16 a[114:115], v31 offset:4096\n"
    "buffer_load_dwordx4 v10, s[16:19], s45 offen offset:1024 nt lds\n"
    "v_mfma_f32_16x16x32_bf16 v[204:207], a[56:59], a[200:203], v[204:207]\n"
    "ds_read_b64_tr_b16 a[116:117], v31 offset:8192\n"
    "v_mfma_f32_16x16x32_bf16 v[204:207], a[60:63], a[204:207], v[204:207]\n"
    "ds_read_b64_tr_b16 a[118:119], v31 offset:12288\n"
    "v_add_u32 v31, 0x4000, v31\n"
    "v_mfma_f32_16x16x32_bf16 v[180:183], a[60:63], a[188:191], v[232:235]\n"
    "ds_read_b64_tr_b16 a[120:121], v32 offset:0\n"
    "s_waitcnt vmcnt(12)\n"
    "buffer_load_dwordx4 v11, s[16:19], s45 offen offset:2048 nt lds\n"
    "v_mfma_f32_16x16x32_bf16 v[180:183], a[56:59], a[184:187], v[180:183]\n"
    "ds_read_b64_tr_b16 a[122:123], v32 offset:4096\n"
    "v_mfma_f32_16x16x32_bf16 v[180:183], a[52:55], a[180:183], v[180:183]\n"
    "ds_read_b64_tr_b16 a[124:125], v32 offset:8192\n"
    "v_mfma_f32_16x16x32_bf16 v[180:183], a[48:51], a[176:179], v[180:183]\n"
    "ds_read_b64_tr_b16 a[126:127], v32 offset:12288\n"
    "v_add_u32 v32, 0x4000, v32\n"
    "buffer_load_dwordx4 v12, s[16:19], s45 offen offset:3072 nt lds\n"
    "s_waitcnt lgkmcnt(0)\n"
    "v_mfma_f32_16x16x32_bf16 v[64:67], a[64:67], v[160:163], v[64:67]\n"
    "ds_read_b128 a[0:3], v33 offset:0\n"
    "v_exp_f32 v164, v164\n"
    "v_mfma_f32_16x16x32_bf16 v[64:67], a[68:71], v[168:171], v[64:67]\n"
    "v_exp_f32 v165, v165\n"
    "v_mfma_f32_16x16x32_bf16 v[68:71], a[68:71], v[192:195], v[68:71]\n"
    "s_mov_b32 m0, 73728\n"
    "s_mov_b32 s45, 90112\n"
    "s_waitcnt vmcnt(12)\n"
    "buffer_load_dwordx4 v13, s[20:23], s45 offen nt lds\n"
    "ds_read_b128 a[4:7], v34 offset:0\n"
    "v_exp_f32 v166, v166\n"
    "v_cvt_pk_bf16_f32 v164, v164, v165\n"
    "v_mfma_f32_16x16x32_bf16 v[68:71], a[64:67], v[184:187], v[68:71]\n"
    "v_exp_f32 v167, v167\n"
    "v_mfma_f32_16x16x32_bf16 v[80:83], a[72:75], v[184:187], v[80:83]\n"
    "buffer_load_dwordx4 v14, s[20:23], s45 offen offset:1024 nt lds\n"
    "ds_read_b128 a[8:11], v35 offset:0\n"
    "v_exp_f32 v188, v188\n"
    "v_cvt_pk_bf16_f32 v165, v166, v167\n"
    "v_mfma_f32_16x16x32_bf16 v[80:83], a[76:79], v[192:195], v[80:83]\n"
    "v_exp_f32 v189, v189\n"
    "v_mfma_f32_16x16x32_bf16 v[76:79], a[76:79], v[168:171], v[76:79]\n"
    "ds_read_b128 a[12:15], v36 offset:0\n"
    "v_exp_f32 v190, v190\n"
    "v_cvt_pk_bf16_f32 v188, v188, v189\n"
    "v_mfma_f32_16x16x32_bf16 v[76:79], a[72:75], v[160:163], v[76:79]\n"
    "s_waitcnt vmcnt(12)\n"
    "buffer_load_dwordx4 v13, s[20:23], s45 offen offset:2048 nt lds\n"
    "v_exp_f32 v191, v191\n"
    "v_mfma_f32_16x16x32_bf16 v[88:91], a[80:83], v[160:163], v[88:91]\n"
    "ds_read_b128 a[16:19], v33 offset:4096\n"
    "v_exp_f32 v176, v176\n"
    "v_cvt_pk_bf16_f32 v189, v190, v191\n"
    "v_mfma_f32_16x16x32_bf16 v[88:91], a[84:87], v[168:171], v[88:91]\n"
    "v_exp_f32 v177, v177\n"
    "v_mfma_f32_16x16x32_bf16 v[92:95], a[84:87], v[192:195], v[92:95]\n"
    "buffer_load_dwordx4 v14, s[20:23], s45 offen offset:3072 nt lds\n"
    "ds_read_b128 a[20:23], v34 offset:4096\n"
    "v_exp_f32 v178, v178\n"
    "v_cvt_pk_bf16_f32 v166, v176, v177\n"
    "v_mfma_f32_16x16x32_bf16 v[92:95], a[80:83], v[184:187], v[92:95]\n"
    "v_exp_f32 v179, v179\n"
    "v_mfma_f32_16x16x32_bf16 v[104:107], a[88:91], v[184:187], v[104:107]\n"
    "ds_read_b128 a[24:27], v35 offset:4096\n"
    "v_exp_f32 v200, v200\n"
    "v_cvt_pk_bf16_f32 v167, v178, v179\n"
    "v_mfma_f32_16x16x32_bf16 v[104:107], a[92:95], v[192:195], v[104:107]\n"
    "s_mov_b32 m0, 61440\n"
    "s_mov_b32 s45, 126976\n"
    "s_waitcnt vmcnt(12)\n"
    "buffer_load_dwordx4 v9, s[16:19], s45 offen nt lds\n"
    "v_exp_f32 v201, v201\n"
    "v_mfma_f32_16x16x32_bf16 v[100:103], a[92:95], v[168:171], v[100:103]\n"
    "ds_read_b128 a[28:31], v36 offset:4096\n"
    "v_exp_f32 v202, v202\n"
    "v_cvt_pk_bf16_f32 v190, v200, v201\n"
    "v_mfma_f32_16x16x32_bf16 v[100:103], a[88:91], v[160:163], v[100:103]\n"
    "v_exp_f32 v203, v203\n"
    "v_mfma_f32_16x16x32_bf16 v[112:115], a[96:99], v[160:163], v[112:115]\n"
    "buffer_load_dwordx4 v10, s[16:19], s45 offen offset:1024 nt lds\n"
    "ds_read_b128 a[32:35], v33 offset:8192\n"
    "v_exp_f32 v172, v172\n"
    "v_cvt_pk_bf16_f32 v191, v202, v203\n"
    "v_mfma_f32_16x16x32_bf16 v[112:115], a[100:103], v[168:171], v[112:115]\n"
    "v_exp_f32 v173, v173\n"
    "v_mfma_f32_16x16x32_bf16 v[116:119], a[100:103], v[192:195], v[116:119]\n"
    "ds_read_b128 a[36:39], v34 offset:8192\n"
    "v_exp_f32 v174, v174\n"
    "v_cvt_pk_bf16_f32 v172, v172, v173\n"
    "v_mfma_f32_16x16x32_bf16 v[116:119], a[96:99], v[184:187], v[116:119]\n"
    "s_waitcnt vmcnt(12)\n"
    "buffer_load_dwordx4 v11, s[16:19], s45 offen offset:2048 nt lds\n"
    "v_exp_f32 v175, v175\n"
    "v_mfma_f32_16x16x32_bf16 v[128:131], a[104:107], v[184:187], v[128:131]\n"
    "ds_read_b128 a[40:43], v35 offset:8192\n"
    "v_exp_f32 v196, v196\n"
    "v_cvt_pk_bf16_f32 v173, v174, v175\n"
    "v_mfma_f32_16x16x32_bf16 v[128:131], a[108:111], v[192:195], v[128:131]\n"
    "buffer_load_dwordx4 v12, s[16:19], s45 offen offset:3072 nt lds\n"
    "v_exp_f32 v197, v197\n"
    "v_mfma_f32_16x16x32_bf16 v[124:127], a[108:111], v[168:171], v[124:127]\n"
    "ds_read_b128 a[44:47], v36 offset:8192\n"
    "v_exp_f32 v198, v198\n"
    "v_cvt_pk_bf16_f32 v196, v196, v197\n"
    "v_mfma_f32_16x16x32_bf16 v[124:127], a[104:107], v[160:163], v[124:127]\n"
    "v_exp_f32 v199, v199\n"
    "v_mfma_f32_16x16x32_bf16 v[136:139], a[112:115], v[160:163], v[136:139]\n"
    "s_mov_b32 m0, 77824\n"
    "s_mov_b32 s45, 94208\n"
    "s_waitcnt vmcnt(12)\n"
    "buffer_load_dwordx4 v13, s[20:23], s45 offen nt lds\n"
    "ds_read_b128 a[48:51], v33 offset:12288\n"
    "v_exp_f32 v180, v180\n"
    "v_cvt_pk_bf16_f32 v197, v198, v199\n"
    "v_mfma_f32_16x16x32_bf16 v[136:139], a[116:119], v[168:171], v[136:139]\n"
    "v_exp_f32 v181, v181\n"
    "v_mfma_f32_16x16x32_bf16 v[140:143], a[116:119], v[192:195], v[140:143]\n"
    "ds_read_b128 a[52:55], v34 offset:12288\n"
    "v_exp_f32 v182, v182\n"
    "v_cvt_pk_bf16_f32 v174, v180, v181\n"
    "v_mfma_f32_16x16x32_bf16 v[140:143], a[112:115], v[184:187], v[140:143]\n"
    "buffer_load_dwordx4 v14, s[20:23], s45 offen offset:1024 nt lds\n"
    "v_exp_f32 v183, v183\n"
    "v_mfma_f32_16x16x32_bf16 v[152:155], a[120:123], v[184:187], v[152:155]\n"
    "ds_read_b128 a[56:59], v35 offset:12288\n"
    "v_exp_f32 v204, v204\n"
    "v_cvt_pk_bf16_f32 v175, v182, v183\n"
    "v_mfma_f32_16x16x32_bf16 v[152:155], a[124:127], v[192:195], v[152:155]\n"
    "v_exp_f32 v205, v205\n"
    "v_mfma_f32_16x16x32_bf16 v[148:151], a[124:127], v[168:171], v[148:151]\n"
    "s_waitcnt vmcnt(12)\n"
    "buffer_load_dwordx4 v13, s[20:23], s45 offen offset:2048 nt lds\n"
    "ds_read_b128 a[60:63], v36 offset:12288\n"
    "v_exp_f32 v206, v206\n"
    "v_cvt_pk_bf16_f32 v198, v204, v205\n"
    "v_mfma_f32_16x16x32_bf16 v[148:151], a[120:123], v[160:163], v[148:151]\n"
    "v_exp_f32 v207, v207\n"
    "v_mfma_f32_16x16x32_bf16 v[244:247], a[224:227], v[160:163], v[244:247]\n"
    "v_mfma_f32_16x16x32_bf16 v[244:247], a[224:227], v[168:171], v[244:247]\n"
    "buffer_load_dwordx4 v14, s[20:23], s45 offen offset:3072 nt lds\n"
    "v_cvt_pk_bf16_f32 v199, v206, v207\n"
    "v_mfma_f32_16x16x32_bf16 v[248:251], a[224:227], v[184:187], v[248:251]\n"
    "v_mfma_f32_16x16x32_bf16 v[248:251], a[224:227], v[192:195], v[248:251]\n"
    "s_waitcnt vmcnt(56) lgkmcnt(0)\n"
    "s_barrier\n"
    "v_mfma_f32_16x16x32_bf16 v[160:163], a[0:3], a[176:179], v[232:235]\n"
    "ds_read_b64_tr_b16 a[64:65], v25 offset:0\n"
    "s_mov_b32 m0, 0\n"
    "s_mov_b32 s45, 2097152\n"
    "s_waitcnt vmcnt(12)\n"
    "buffer_load_dwordx4 v9, s[16:19], s45 offen nt lds\n"
    "v_mfma_f32_16x16x32_bf16 v[160:163], a[4:7], a[180:183], v[160:163]\n"
    "ds_read_b64_tr_b16 a[66:67], v25 offset:4096\n"
    "v_mfma_f32_16x16x32_bf16 v[160:163], a[8:11], a[184:187], v[160:163]\n"
    "ds_read_b64_tr_b16 a[68:69], v25 offset:8192\n"
    "buffer_load_dwordx4 v10, s[16:19], s45 offen offset:1024 nt lds\n"
    "v_mfma_f32_16x16x32_bf16 v[160:163], a[12:15], a[188:191], v[160:163]\n"
    "ds_read_b64_tr_b16 a[70:71], v25 offset:12288\n"
    "v_add_u32 v25, 0x4000, v25\n"
    "v_mfma_f32_16x16x32_bf16 v[184:187], a[12:15], a[204:207], v[236:239]\n"
    "ds_read_b64_tr_b16 a[72:73], v26 offset:0\n"
    "v_mfma_f32_16x16x32_bf16 v[184:187], a[8:11], a[200:203], v[184:187]\n"
    "ds_read_b64_tr_b16 a[74:75], v26 offset:4096\n"
    "s_waitcnt vmcnt(12)\n"
    "buffer_load_dwordx4 v11, s[16:19], s45 offen offset:2048 nt lds\n"
    "v_mfma_f32_16x16x32_bf16 v[184:187], a[4:7], a[196:199], v[184:187]\n"
    "ds_read_b64_tr_b16 a[76:77], v26 offset:8192\n"
    "v_mfma_f32_16x16x32_bf16 v[184:187], a[0:3], a[192:195], v[184:187]\n"
    "ds_read_b64_tr_b16 a[78:79], v26 offset:12288\n"
    "v_add_u32 v26, 0x4000, v26\n"
    "v_mfma_f32_16x16x32_bf16 v[200:203], a[16:19], a[192:195], v[236:239]\n"
    "ds_read_b64_tr_b16 a[80:81], v27 offset:0\n"
    "buffer_load_dwordx4 v12, s[16:19], s45 offen offset:3072 nt lds\n"
    "v_mfma_f32_16x16x32_bf16 v[200:203], a[20:23], a[196:199], v[200:203]\n"
    "ds_read_b64_tr_b16 a[82:83], v27 offset:4096\n"
    "v_mfma_f32_16x16x32_bf16 v[200:203], a[24:27], a[200:203], v[200:203]\n"
    "ds_read_b64_tr_b16 a[84:85], v27 offset:8192\n"
    "v_mfma_f32_16x16x32_bf16 v[200:203], a[28:31], a[204:207], v[200:203]\n"
    "ds_read_b64_tr_b16 a[86:87], v27 offset:12288\n"
    "v_add_u32 v27, 0x4000, v27\n"
    "s_mov_b32 m0, 81920\n"
    "s_mov_b32 s45, 98304\n"
    "s_waitcnt vmcnt(12)\n"
    "buffer_load_dwordx4 v13, s[20:23], s45 offen nt lds\n"
    "v_mfma_f32_16x16x32_bf16 v[176:179], a[28:31], a[188:191], v[232:235]\n"
    "ds_read_b64_tr_b16 a[88:89], v28 offset:0\n"
    "v_mfma_f32_16x16x32_bf16 v[176:179], a[24:27], a[184:187], v[176:179]\n"
    "ds_read_b64_tr_b16 a[90:91], v28 offset:4096\n"
    "v_mfma_f32_16x16x32_bf16 v[176:179], a[20:23], a[180:183], v[176:179]\n"
    "ds_read_b64_tr_b16 a[92:93], v28 offset:8192\n"
    "buffer_load_dwordx4 v14, s[20:23], s45 offen offset:1024 nt lds\n"
    "v_mfma_f32_16x16x32_bf16 v[176:179], a[16:19], a[176:179], v[176:179]\n"
    "ds_read_b64_tr_b16 a[94:95], v28 offset:12288\n"
    "v_add_u32 v28, 0x4000, v28\n"
    "v_mfma_f32_16x16x32_bf16 v[168:171], a[32:35], a[176:179], v[232:235]\n"
    "ds_read_b64_tr_b16 a[96:97], v29 offset:0\n"
    "v_mfma_f32_16x16x32_bf16 v[168:171], a[36:39], a[180:183], v[168:171]\n"
    "ds_read_b64_tr_b16 a[98:99], v29 offset:4096\n"
    "s_waitcnt vmcnt(12)\n"
    "buffer_load_dwordx4 v13, s[20:23], s45 offen offset:2048 nt lds\n"
    "v_mfma_f32_16x16x32_bf16 v[168:171], a[40:43], a[184:187], v[168:171]\n"
    "ds_read_b64_tr_b16 a[100:101], v29 offset:8192\n"
    "v_mfma_f32_16x16x32_bf16 v[168:171], a[44:47], a[188:191], v[168:171]\n"
    "ds_read_b64_tr_b16 a[102:103], v29 offset:12288\n"
    "v_add_u32 v29, 0x4000, v29\n"
    "buffer_load_dwordx4 v14, s[20:23], s45 offen offset:3072 nt lds\n"
    "v_mfma_f32_16x16x32_bf16 v[192:195], a[44:47], a[204:207], v[236:239]\n"
    "ds_read_b64_tr_b16 a[104:105], v30 offset:0\n"
    "v_mfma_f32_16x16x32_bf16 v[192:195], a[40:43], a[200:203], v[192:195]\n"
    "ds_read_b64_tr_b16 a[106:107], v30 offset:4096\n"
    "v_mfma_f32_16x16x32_bf16 v[192:195], a[36:39], a[196:199], v[192:195]\n"
    "ds_read_b64_tr_b16 a[108:109], v30 offset:8192\n"
    "s_mov_b32 m0, 4096\n"
    "s_mov_b32 s45, 2101248\n"
    "s_waitcnt vmcnt(12)\n"
    "buffer_load_dwordx4 v9, s[16:19], s45 offen nt lds\n"
    "v_mfma_f32_16x16x32_bf16 v[192:195], a[32:35], a[192:195], v[192:195]\n"
    "ds_read_b64_tr_b16 a[110:111], v30 offset:12288\n"
    "v_add_u32 v30, 0x4000, v30\n"
    "v_mfma_f32_16x16x32_bf16 v[204:207], a[48:51], a[192:195], v[236:239]\n"
    "ds_read_b64_tr_b16 a[112:113], v31 offset:0\n"
    "v_mfma_f32_16x16x32_bf16 v[204:207], a[52:55], a[196:199], v[204:207]\n"
    "ds_read_b64_tr_b16 a[114:115], v31 offset:4096\n"
    "buffer_load_dwordx4 v10, s[16:19], s45 offen offset:1024 nt lds\n"
    "v_mfma_f32_16x16x32_bf16 v[204:207], a[56:59], a[200:203], v[204:207]\n"
    "ds_read_b64_tr_b16 a[116:117], v31 offset:8192\n"
    "v_mfma_f32_16x16x32_bf16 v[204:207], a[60:63], a[204:207], v[204:207]\n"
    "ds_read_b64_tr_b16 a[118:119], v31 offset:12288\n"
    "v_add_u32 v31, 0x4000, v31\n"
    "v_mfma_f32_16x16x32_bf16 v[180:183], a[60:63], a[188:191], v[232:235]\n"
    "ds_read_b64_tr_b16 a[120:121], v32 offset:0\n"
    "s_waitcnt vmcnt(12)\n"
    "buffer_load_dwordx4 v11, s[16:19], s45 offen offset:2048 nt lds\n"
    "v_mfma_f32_16x16x32_bf16 v[180:183], a[56:59], a[184:187], v[180:183]\n"
    "ds_read_b64_tr_b16 a[122:123], v32 offset:4096\n"
    "v_mfma_f32_16x16x32_bf16 v[180:183], a[52:55], a[180:183], v[180:183]\n"
    "ds_read_b64_tr_b16 a[124:125], v32 offset:8192\n"
    "v_mfma_f32_16x16x32_bf16 v[180:183], a[48:51], a[176:179], v[180:183]\n"
    "ds_read_b64_tr_b16 a[126:127], v32 offset:12288\n"
    "v_add_u32 v32, 0x4000, v32\n"
    "buffer_load_dwordx4 v12, s[16:19], s45 offen offset:3072 nt lds\n"
    "s_waitcnt lgkmcnt(0)\n"
    "v_mfma_f32_16x16x32_bf16 v[64:67], a[64:67], v[164:167], v[64:67]\n"
    "ds_read_b128 a[0:3], v33 offset:16384\n"
    "v_exp_f32 v160, v160\n"
    "v_mfma_f32_16x16x32_bf16 v[64:67], a[68:71], v[172:175], v[64:67]\n"
    "v_exp_f32 v161, v161\n"
    "v_mfma_f32_16x16x32_bf16 v[68:71], a[68:71], v[196:199], v[68:71]\n"
    "s_mov_b32 m0, 86016\n"
    "s_mov_b32 s45, 102400\n"
    "s_waitcnt vmcnt(12)\n"
    "buffer_load_dwordx4 v13, s[20:23], s45 offen nt lds\n"
    "ds_read_b128 a[4:7], v34 offset:16384\n"
    "v_exp_f32 v162, v162\n"
    "v_cvt_pk_bf16_f32 v160, v160, v161\n"
    "v_mfma_f32_16x16x32_bf16 v[68:71], a[64:67], v[188:191], v[68:71]\n"
    "v_exp_f32 v163, v163\n"
    "v_mfma_f32_16x16x32_bf16 v[80:83], a[72:75], v[188:191], v[80:83]\n"
    "buffer_load_dwordx4 v14, s[20:23], s45 offen offset:1024 nt lds\n"
    "ds_read_b128 a[8:11], v35 offset:16384\n"
    "v_exp_f32 v184, v184\n"
    "v_cvt_pk_bf16_f32 v161, v162, v163\n"
    "v_mfma_f32_16x16x32_bf16 v[80:83], a[76:79], v[196:199], v[80:83]\n"
    "v_exp_f32 v185, v185\n"
    "v_mfma_f32_16x16x32_bf16 v[76:79], a[76:79], v[172:175], v[76:79]\n"
    "ds_read_b128 a[12:15], v36 offset:16384\n"
    "v_exp_f32 v186, v186\n"
    "v_cvt_pk_bf16_f32 v184, v184, v185\n"
    "v_mfma_f32_16x16x32_bf16 v[76:79], a[72:75], v[164:167], v[76:79]\n"
    "s_waitcnt vmcnt(12)\n"
    "buffer_load_dwordx4 v13, s[20:23], s45 offen offset:2048 nt lds\n"
    "v_exp_f32 v187, v187\n"
    "v_mfma_f32_16x16x32_bf16 v[88:91], a[80:83], v[164:167], v[88:91]\n"
    "ds_read_b128 a[16:19], v33 offset:20480\n"
    "v_exp_f32 v176, v176\n"
    "v_cvt_pk_bf16_f32 v185, v186, v187\n"
    "v_mfma_f32_16x16x32_bf16 v[88:91], a[84:87], v[172:175], v[88:91]\n"
    "v_exp_f32 v177, v177\n"
    "v_mfma_f32_16x16x32_bf16 v[92:95], a[84:87], v[196:199], v[92:95]\n"
    "buffer_load_dwordx4 v14, s[20:23], s45 offen offset:3072 nt lds\n"
    "ds_read_b128 a[20:23], v34 offset:20480\n"
    "v_exp_f32 v178, v178\n"
    "v_cvt_pk_bf16_f32 v162, v176, v177\n"
    "v_mfma_f32_16x16x32_bf16 v[92:95], a[80:83], v[188:191], v[92:95]\n"
    "v_exp_f32 v179, v179\n"
    "v_mfma_f32_16x16x32_bf16 v[104:107], a[88:91], v[188:191], v[104:107]\n"
    "ds_read_b128 a[24:27], v35 offset:20480\n"
    "v_exp_f32 v200, v200\n"
    "v_cvt_pk_bf16_f32 v163, v178, v179\n"
    "v_mfma_f32_16x16x32_bf16 v[104:107], a[92:95], v[196:199], v[104:107]\n"
    "s_mov_b32 m0, 8192\n"
    "s_mov_b32 s45, 2105344\n"
    "s_waitcnt vmcnt(12)\n"
    "buffer_load_dwordx4 v9, s[16:19], s45 offen nt lds\n"
    "v_exp_f32 v201, v201\n"
    "v_mfma_f32_16x16x32_bf16 v[100:103], a[92:95], v[172:175], v[100:103]\n"
    "ds_read_b128 a[28:31], v36 offset:20480\n"
    "v_exp_f32 v202, v202\n"
    "v_cvt_pk_bf16_f32 v186, v200, v201\n"
    "v_mfma_f32_16x16x32_bf16 v[100:103], a[88:91], v[164:167], v[100:103]\n"
    "v_exp_f32 v203, v203\n"
    "v_mfma_f32_16x16x32_bf16 v[112:115], a[96:99], v[164:167], v[112:115]\n"
    "buffer_load_dwordx4 v10, s[16:19], s45 offen offset:1024 nt lds\n"
    "ds_read_b128 a[32:35], v33 offset:24576\n"
    "v_exp_f32 v168, v168\n"
    "v_cvt_pk_bf16_f32 v187, v202, v203\n"
    "v_mfma_f32_16x16x32_bf16 v[112:115], a[100:103], v[172:175], v[112:115]\n"
    "v_exp_f32 v169, v169\n"
    "v_mfma_f32_16x16x32_bf16 v[116:119], a[100:103], v[196:199], v[116:119]\n"
    "ds_read_b128 a[36:39], v34 offset:24576\n"
    "v_exp_f32 v170, v170\n"
    "v_cvt_pk_bf16_f32 v168, v168, v169\n"
    "v_mfma_f32_16x16x32_bf16 v[116:119], a[96:99], v[188:191], v[116:119]\n"
    "s_waitcnt vmcnt(12)\n"
    "buffer_load_dwordx4 v11, s[16:19], s45 offen offset:2048 nt lds\n"
    "v_exp_f32 v171, v171\n"
    "v_mfma_f32_16x16x32_bf16 v[128:131], a[104:107], v[188:191], v[128:131]\n"
    "ds_read_b128 a[40:43], v35 offset:24576\n"
    "v_exp_f32 v192, v192\n"
    "v_cvt_pk_bf16_f32 v169, v170, v171\n"
    "v_mfma_f32_16x16x32_bf16 v[128:131], a[108:111], v[196:199], v[128:131]\n"
    "buffer_load_dwordx4 v12, s[16:19], s45 offen offset:3072 nt lds\n"
    "v_exp_f32 v193, v193\n"
    "v_mfma_f32_16x16x32_bf16 v[124:127], a[108:111], v[172:175], v[124:127]\n"
    "ds_read_b128 a[44:47], v36 offset:24576\n"
    "v_exp_f32 v194, v194\n"
    "v_cvt_pk_bf16_f32 v192, v192, v193\n"
    "v_mfma_f32_16x16x32_bf16 v[124:127], a[104:107], v[164:167], v[124:127]\n"
    "v_exp_f32 v195, v195\n"
    "v_mfma_f32_16x16x32_bf16 v[136:139], a[112:115], v[164:167], v[136:139]\n"
    "s_mov_b32 m0, 90112\n"
    "s_mov_b32 s45, 106496\n"
    "s_waitcnt vmcnt(12)\n"
    "buffer_load_dwordx4 v13, s[20:23], s45 offen nt lds\n"
    "ds_read_b128 a[48:51], v33 offset:28672\n"
    "v_exp_f32 v180, v180\n"
    "v_cvt_pk_bf16_f32 v193, v194, v195\n"
    "v_mfma_f32_16x16x32_bf16 v[136:139], a[116:119], v[172:175], v[136:139]\n"
    "v_exp_f32 v181, v181\n"
    "v_mfma_f32_16x16x32_bf16 v[140:143], a[116:119], v[196:199], v[140:143]\n"
    "ds_read_b128 a[52:55], v34 offset:28672\n"
    "v_exp_f32 v182, v182\n"
    "v_cvt_pk_bf16_f32 v170, v180, v181\n"
    "v_mfma_f32_16x16x32_bf16 v[140:143], a[112:115], v[188:191], v[140:143]\n"
    "buffer_load_dwordx4 v14, s[20:23], s45 offen offset:1024 nt lds\n"
    "v_exp_f32 v183, v183\n"
    "v_mfma_f32_16x16x32_bf16 v[152:155], a[120:123], v[188:191], v[152:155]\n"
    "ds_read_b128 a[56:59], v35 offset:28672\n"
    "v_exp_f32 v204, v204\n"
    "v_cvt_pk_bf16_f32 v171, v182, v183\n"
    "v_mfma_f32_16x16x32_bf16 v[152:155], a[124:127], v[196:199], v[152:155]\n"
    "v_exp_f32 v205, v205\n"
    "v_mfma_f32_16x16x32_bf16 v[148:151], a[124:127], v[172:175], v[148:151]\n"
    "s_waitcnt vmcnt(12)\n"
    "buffer_load_dwordx4 v13, s[20:23], s45 offen offset:2048 nt lds\n"
    "ds_read_b128 a[60:63], v36 offset:28672\n"
    "v_exp_f32 v206, v206\n"
    "v_cvt_pk_bf16_f32 v194, v204, v205\n"
    "v_mfma_f32_16x16x32_bf16 v[148:151], a[120:123], v[164:167], v[148:151]\n"
    "v_exp_f32 v207, v207\n"
    "v_mfma_f32_16x16x32_bf16 v[244:247], a[224:227], v[164:167], v[244:247]\n"
    "v_mfma_f32_16x16x32_bf16 v[244:247], a[224:227], v[172:175], v[244:247]\n"
    "buffer_load_dwordx4 v14, s[20:23], s45 offen offset:3072 nt lds\n"
    "v_cvt_pk_bf16_f32 v195, v206, v207\n"
    "v_mfma_f32_16x16x32_bf16 v[248:251], a[224:227], v[188:191], v[248:251]\n"
    "v_mfma_f32_16x16x32_bf16 v[248:251], a[224:227], v[196:199], v[248:251]\n"
    "s_waitcnt vmcnt(56) lgkmcnt(0)\n"
    "s_barrier\n"
    "v_mfma_f32_16x16x32_bf16 v[164:167], a[0:3], a[176:179], v[232:235]\n"
    "ds_read_b64_tr_b16 a[64:65], v25 offset:0\n"
    "s_mov_b32 m0, 12288\n"
    "s_mov_b32 s45, 2109440\n"
    "s_waitcnt vmcnt(12)\n"
    "buffer_load_dwordx4 v9, s[16:19], s45 offen nt lds\n"
    "v_mfma_f32_16x16x32_bf16 v[164:167], a[4:7], a[180:183], v[164:167]\n"
    "ds_read_b64_tr_b16 a[66:67], v25 offset:4096\n"
    "v_mfma_f32_16x16x32_bf16 v[164:167], a[8:11], a[184:187], v[164:167]\n"
    "ds_read_b64_tr_b16 a[68:69], v25 offset:8192\n"
    "buffer_load_dwordx4 v10, s[16:19], s45 offen offset:1024 nt lds\n"
    "v_mfma_f32_16x16x32_bf16 v[164:167], a[12:15], a[188:191], v[164:167]\n"
    "ds_read_b64_tr_b16 a[70:71], v25 offset:12288\n"
    "v_add_u32 v25, 0xffff0000, v25\n"
    "v_mfma_f32_16x16x32_bf16 v[188:191], a[12:15], a[204:207], v[236:239]\n"
    "ds_read_b64_tr_b16 a[72:73], v26 offset:0\n"
    "v_mfma_f32_16x16x32_bf16 v[188:191], a[8:11], a[200:203], v[188:191]\n"
    "ds_read_b64_tr_b16 a[74:75], v26 offset:4096\n"
    "s_waitcnt vmcnt(12)\n"
    "buffer_load_dwordx4 v11, s[16:19], s45 offen offset:2048 nt lds\n"
    "v_mfma_f32_16x16x32_bf16 v[188:191], a[4:7], a[196:199], v[188:191]\n"
    "ds_read_b64_tr_b16 a[76:77], v26 offset:8192\n"
    "v_mfma_f32_16x16x32_bf16 v[188:191], a[0:3], a[192:195], v[188:191]\n"
    "ds_read_b64_tr_b16 a[78:79], v26 offset:12288\n"
    "v_add_u32 v26, 0xffff0000, v26\n"
    "v_mfma_f32_16x16x32_bf16 v[200:203], a[16:19], a[192:195], v[236:239]\n"
    "ds_read_b64_tr_b16 a[80:81], v27 offset:0\n"
    "buffer_load_dwordx4 v12, s[16:19], s45 offen offset:3072 nt lds\n"
    "v_mfma_f32_16x16x32_bf16 v[200:203], a[20:23], a[196:199], v[200:203]\n"
    "ds_read_b64_tr_b16 a[82:83], v27 offset:4096\n"
    "v_mfma_f32_16x16x32_bf16 v[200:203], a[24:27], a[200:203], v[200:203]\n"
    "ds_read_b64_tr_b16 a[84:85], v27 offset:8192\n"
    "v_mfma_f32_16x16x32_bf16 v[200:203], a[28:31], a[204:207], v[200:203]\n"
    "ds_read_b64_tr_b16 a[86:87], v27 offset:12288\n"
    "v_add_u32 v27, 0xffff0000, v27\n"
    "s_mov_b32 m0, 94208\n"
    "s_mov_b32 s45, 110592\n"
    "s_waitcnt vmcnt(12)\n"
    "buffer_load_dwordx4 v13, s[20:23], s45 offen nt lds\n"
    "v_mfma_f32_16x16x32_bf16 v[176:179], a[28:31], a[188:191], v[232:235]\n"
    "ds_read_b64_tr_b16 a[88:89], v28 offset:0\n"
    "v_xor_b32 v33, 0x8000, v33\n"
    "v_mfma_f32_16x16x32_bf16 v[176:179], a[24:27], a[184:187], v[176:179]\n"
    "ds_read_b64_tr_b16 a[90:91], v28 offset:4096\n"
    "v_mfma_f32_16x16x32_bf16 v[176:179], a[20:23], a[180:183], v[176:179]\n"
    "ds_read_b64_tr_b16 a[92:93], v28 offset:8192\n"
    "v_xor_b32 v34, 0x8000, v34\n"
    "buffer_load_dwordx4 v14, s[20:23], s45 offen offset:1024 nt lds\n"
    "v_mfma_f32_16x16x32_bf16 v[176:179], a[16:19], a[176:179], v[176:179]\n"
    "ds_read_b64_tr_b16 a[94:95], v28 offset:12288\n"
    "v_add_u32 v28, 0xffff0000, v28\n"
    "v_mfma_f32_16x16x32_bf16 v[172:175], a[32:35], a[176:179], v[232:235]\n"
    "ds_read_b64_tr_b16 a[96:97], v29 offset:0\n"
    "v_xor_b32 v35, 0x8000, v35\n"
    "v_mfma_f32_16x16x32_bf16 v[172:175], a[36:39], a[180:183], v[172:175]\n"
    "ds_read_b64_tr_b16 a[98:99], v29 offset:4096\n"
    "s_waitcnt vmcnt(12)\n"
    "buffer_load_dwordx4 v13, s[20:23], s45 offen offset:2048 nt lds\n"
    "v_mfma_f32_16x16x32_bf16 v[172:175], a[40:43], a[184:187], v[172:175]\n"
    "ds_read_b64_tr_b16 a[100:101], v29 offset:8192\n"
    "v_xor_b32 v36, 0x8000, v36\n"
    "v_mfma_f32_16x16x32_bf16 v[172:175], a[44:47], a[188:191], v[172:175]\n"
    "ds_read_b64_tr_b16 a[102:103], v29 offset:12288\n"
    "v_add_u32 v29, 0xffff0000, v29\n"
    "buffer_load_dwordx4 v14, s[20:23], s45 offen offset:3072 nt lds\n"
    "v_mfma_f32_16x16x32_bf16 v[196:199], a[44:47], a[204:207], v[236:239]\n"
    "ds_read_b64_tr_b16 a[104:105], v30 offset:0\n"
    "v_mfma_f32_16x16x32_bf16 v[196:199], a[40:43], a[200:203], v[196:199]\n"
    "ds_read_b64_tr_b16 a[106:107], v30 offset:4096\n"
    "v_mfma_f32_16x16x32_bf16 v[196:199], a[36:39], a[196:199], v[196:199]\n"
    "ds_read_b64_tr_b16 a[108:109], v30 offset:8192\n"
    "s_mov_b32 m0, 16384\n"
    "s_mov_b32 s45, 2113536\n"
    "s_waitcnt vmcnt(12)\n"
    "buffer_load_dwordx4 v9, s[16:19], s45 offen nt lds\n"
    "v_mfma_f32_16x16x32_bf16 v[196:199], a[32:35], a[192:195], v[196:199]\n"
    "ds_read_b64_tr_b16 a[110:111], v30 offset:12288\n"
    "v_add_u32 v30, 0xffff0000, v30\n"
    "v_mfma_f32_16x16x32_bf16 v[204:207], a[48:51], a[192:195], v[236:239]\n"
    "ds_read_b64_tr_b16 a[112:113], v31 offset:0\n"
    "v_mfma_f32_16x16x32_bf16 v[204:207], a[52:55], a[196:199], v[204:207]\n"
    "ds_read_b64_tr_b16 a[114:115], v31 offset:4096\n"
    "buffer_load_dwordx4 v10, s[16:19], s45 offen offset:1024 nt lds\n"
    "v_mfma_f32_16x16x32_bf16 v[204:207], a[56:59], a[200:203], v[204:207]\n"
    "ds_read_b64_tr_b16 a[116:117], v31 offset:8192\n"
    "v_mfma_f32_16x16x32_bf16 v[204:207], a[60:63], a[204:207], v[204:207]\n"
    "ds_read_b64_tr_b16 a[118:119], v31 offset:12288\n"
    "v_add_u32 v31, 0xffff0000, v31\n"
    "v_mfma_f32_16x16x32_bf16 v[180:183], a[60:63], a[188:191], v[232:235]\n"
    "ds_read_b64_tr_b16 a[120:121], v32 offset:0\n"
    "s_waitcnt vmcnt(12)\n"
    "buffer_load_dwordx4 v11, s[16:19], s45 offen offset:2048 nt lds\n"
    "v_mfma_f32_16x16x32_bf16 v[180:183], a[56:59], a[184:187], v[180:183]\n"
    "ds_read_b64_tr_b16 a[122:123], v32 offset:4096\n"
    "v_mfma_f32_16x16x32_bf16 v[180:183], a[52:55], a[180:183], v[180:183]\n"
    "ds_read_b64_tr_b16 a[124:125], v32 offset:8192\n"
    "v_mfma_f32_16x16x32_bf16 v[180:183], a[48:51], a[176:179], v[180:183]\n"
    "ds_read_b64_tr_b16 a[126:127], v32 offset:12288\n"
    "v_add_u32 v32, 0xffff0000, v32\n"
    "buffer_load_dwordx4 v12, s[16:19], s45 offen offset:3072 nt lds\n"
    "s_waitcnt lgkmcnt(0)\n"
    "v_mfma_f32_16x16x32_bf16 v[64:67], a[64:67], v[160:163], v[64:67]\n"
    "ds_read_b128 a[0:3], v33 offset:0\n"
    "v_exp_f32 v164, v164\n"
    "v_mfma_f32_16x16x32_bf16 v[64:67], a[68:71], v[168:171], v[64:67]\n"
    "v_exp_f32 v165, v165\n"
    "v_mfma_f32_16x16x32_bf16 v[68:71], a[68:71], v[192:195], v[68:71]\n"
    "s_mov_b32 m0, 98304\n"
    "s_mov_b32 s45, 114688\n"
    "s_waitcnt vmcnt(12)\n"
    "buffer_load_dwordx4 v13, s[20:23], s45 offen nt lds\n"
    "ds_read_b128 a[4:7], v34 offset:0\n"
    "v_exp_f32 v166, v166\n"
    "v_cvt_pk_bf16_f32 v164, v164, v165\n"
    "v_mfma_f32_16x16x32_bf16 v[68:71], a[64:67], v[184:187], v[68:71]\n"
    "v_exp_f32 v167, v167\n"
    "v_mfma_f32_16x16x32_bf16 v[80:83], a[72:75], v[184:187], v[80:83]\n"
    "buffer_load_dwordx4 v14, s[20:23], s45 offen offset:1024 nt lds\n"
    "ds_read_b128 a[8:11], v35 offset:0\n"
    "v_exp_f32 v188, v188\n"
    "v_cvt_pk_bf16_f32 v165, v166, v167\n"
    "v_mfma_f32_16x16x32_bf16 v[80:83], a[76:79], v[192:195], v[80:83]\n"
    "v_exp_f32 v189, v189\n"
    "v_mfma_f32_16x16x32_bf16 v[76:79], a[76:79], v[168:171], v[76:79]\n"
    "ds_read_b128 a[12:15], v36 offset:0\n"
    "v_exp_f32 v190, v190\n"
    "v_cvt_pk_bf16_f32 v188, v188, v189\n"
    "v_mfma_f32_16x16x32_bf16 v[76:79], a[72:75], v[160:163], v[76:79]\n"
    "s_waitcnt vmcnt(12)\n"
    "buffer_load_dwordx4 v13, s[20:23], s45 offen offset:2048 nt lds\n"
    "v_exp_f32 v191, v191\n"
    "v_mfma_f32_16x16x32_bf16 v[88:91], a[80:83], v[160:163], v[88:91]\n"
    "ds_read_b128 a[16:19], v33 offset:4096\n"
    "v_exp_f32 v176, v176\n"
    "v_cvt_pk_bf16_f32 v189, v190, v191\n"
    "v_mfma_f32_16x16x32_bf16 v[88:91], a[84:87], v[168:171], v[88:91]\n"
    "v_exp_f32 v177, v177\n"
    "v_mfma_f32_16x16x32_bf16 v[92:95], a[84:87], v[192:195], v[92:95]\n"
    "buffer_load_dwordx4 v14, s[20:23], s45 offen offset:3072 nt lds\n"
    "ds_read_b128 a[20:23], v34 offset:4096\n"
    "v_exp_f32 v178, v178\n"
    "v_cvt_pk_bf16_f32 v166, v176, v177\n"
    "v_mfma_f32_16x16x32_bf16 v[92:95], a[80:83], v[184:187], v[92:95]\n"
    "v_exp_f32 v179, v179\n"
    "v_mfma_f32_16x16x32_bf16 v[104:107], a[88:91], v[184:187], v[104:107]\n"
    "ds_read_b128 a[24:27], v35 offset:4096\n"
    "v_exp_f32 v200, v200\n"
    "v_cvt_pk_bf16_f32 v167, v178, v179\n"
    "v_mfma_f32_16x16x32_bf16 v[104:107], a[92:95], v[192:195], v[104:107]\n"
    "s_mov_b32 m0, 20480\n"
    "s_mov_b32 s45, 2117632\n"
    "s_waitcnt vmcnt(12)\n"
    "buffer_load_dwordx4 v9, s[16:19], s45 offen nt lds\n"
    "v_exp_f32 v201, v201\n"
    "v_mfma_f32_16x16x32_bf16 v[100:103], a[92:95], v[168:171], v[100:103]\n"
    "ds_read_b128 a[28:31], v36 offset:4096\n"
    "v_exp_f32 v202, v202\n"
    "v_cvt_pk_bf16_f32 v190, v200, v201\n"
    "v_mfma_f32_16x16x32_bf16 v[100:103], a[88:91], v[160:163], v[100:103]\n"
    "v_exp_f32 v203, v203\n"
    "v_mfma_f32_16x16x32_bf16 v[112:115], a[96:99], v[160:163], v[112:115]\n"
    "buffer_load_dwordx4 v10, s[16:19], s45 offen offset:1024 nt lds\n"
    "ds_read_b128 a[32:35], v33 offset:8192\n"
    "v_exp_f32 v172, v172\n"
    "v_cvt_pk_bf16_f32 v191, v202, v203\n"
    "v_mfma_f32_16x16x32_bf16 v[112:115], a[100:103], v[168:171], v[112:115]\n"
    "v_exp_f32 v173, v173\n"
    "v_mfma_f32_16x16x32_bf16 v[116:119], a[100:103], v[192:195], v[116:119]\n"
    "ds_read_b128 a[36:39], v34 offset:8192\n"
    "v_exp_f32 v174, v174\n"
    "v_cvt_pk_bf16_f32 v172, v172, v173\n"
    "v_mfma_f32_16x16x32_bf16 v[116:119], a[96:99], v[184:187], v[116:119]\n"
    "s_waitcnt vmcnt(12)\n"
    "buffer_load_dwordx4 v11, s[16:19], s45 offen offset:2048 nt lds\n"
    "v_exp_f32 v175, v175\n"
    "v_mfma_f32_16x16x32_bf16 v[128:131], a[104:107], v[184:187], v[128:131]\n"
    "ds_read_b128 a[40:43], v35 offset:8192\n"
    "v_exp_f32 v196, v196\n"
    "v_cvt_pk_bf16_f32 v173, v174, v175\n"
    "v_mfma_f32_16x16x32_bf16 v[128:131], a[108:111], v[192:195], v[128:131]\n"
    "buffer_load_dwordx4 v12, s[16:19], s45 offen offset:3072 nt lds\n"
    "v_exp_f32 v197, v197\n"
    "v_mfma_f32_16x16x32_bf16 v[124:127], a[108:111], v[168:171], v[124:127]\n"
    "ds_read_b128 a[44:47], v36 offset:8192\n"
    "v_exp_f32 v198, v198\n"
    "v_cvt_pk_bf16_f32 v196, v196, v197\n"
    "v_mfma_f32_16x16x32_bf16 v[124:127], a[104:107], v[160:163], v[124:127]\n"
    "v_exp_f32 v199, v199\n"
    "v_mfma_f32_16x16x32_bf16 v[136:139], a[112:115], v[160:163], v[136:139]\n"
    "s_mov_b32 m0, 102400\n"
    "s_mov_b32 s45, 118784\n"
    "s_waitcnt vmcnt(12)\n"
    "buffer_load_dwordx4 v13, s[20:23], s45 offen nt lds\n"
    "ds_read_b128 a[48:51], v33 offset:12288\n"
    "v_exp_f32 v180, v180\n"
    "v_cvt_pk_bf16_f32 v197, v198, v199\n"
    "v_mfma_f32_16x16x32_bf16 v[136:139], a[116:119], v[168:171], v[136:139]\n"
    "v_exp_f32 v181, v181\n"
    "v_mfma_f32_16x16x32_bf16 v[140:143], a[116:119], v[192:195], v[140:143]\n"
    "ds_read_b128 a[52:55], v34 offset:12288\n"
    "v_exp_f32 v182, v182\n"
    "v_cvt_pk_bf16_f32 v174, v180, v181\n"
    "v_mfma_f32_16x16x32_bf16 v[140:143], a[112:115], v[184:187], v[140:143]\n"
    "buffer_load_dwordx4 v14, s[20:23], s45 offen offset:1024 nt lds\n"
    "v_exp_f32 v183, v183\n"
    "v_mfma_f32_16x16x32_bf16 v[152:155], a[120:123], v[184:187], v[152:155]\n"
    "ds_read_b128 a[56:59], v35 offset:12288\n"
    "v_exp_f32 v204, v204\n"
    "v_cvt_pk_bf16_f32 v175, v182, v183\n"
    "v_mfma_f32_16x16x32_bf16 v[152:155], a[124:127], v[192:195], v[152:155]\n"
    "v_exp_f32 v205, v205\n"
    "v_mfma_f32_16x16x32_bf16 v[148:151], a[124:127], v[168:171], v[148:151]\n"
    "s_waitcnt vmcnt(12)\n"
    "buffer_load_dwordx4 v13, s[20:23], s45 offen offset:2048 nt lds\n"
    "ds_read_b128 a[60:63], v36 offset:12288\n"
    "v_exp_f32 v206, v206\n"
    "v_cvt_pk_bf16_f32 v198, v204, v205\n"
    "v_mfma_f32_16x16x32_bf16 v[148:151], a[120:123], v[160:163], v[148:151]\n"
    "v_exp_f32 v207, v207\n"
    "v_mfma_f32_16x16x32_bf16 v[244:247], a[224:227], v[160:163], v[244:247]\n"
    "v_mfma_f32_16x16x32_bf16 v[244:247], a[224:227], v[168:171], v[244:247]\n"
    "buffer_load_dwordx4 v14, s[20:23], s45 offen offset:3072 nt lds\n"
    "v_cvt_pk_bf16_f32 v199, v206, v207\n"
    "v_mfma_f32_16x16x32_bf16 v[248:251], a[224:227], v[184:187], v[248:251]\n"
    "v_mfma_f32_16x16x32_bf16 v[248:251], a[224:227], v[192:195], v[248:251]\n"
    "s_waitcnt vmcnt(48) lgkmcnt(0)\n"
    "s_barrier\n"
    "v_mfma_f32_16x16x32_bf16 v[160:163], a[0:3], a[176:179], v[232:235]\n"
    "ds_read_b64_tr_b16 a[64:65], v25 offset:0\n"
    "s_mov_b32 m0, 24576\n"
    "s_mov_b32 s45, 2121728\n"
    "s_waitcnt vmcnt(12)\n"
    "buffer_load_dwordx4 v9, s[16:19], s45 offen nt lds\n"
    "v_mfma_f32_16x16x32_bf16 v[160:163], a[4:7], a[180:183], v[160:163]\n"
    "ds_read_b64_tr_b16 a[66:67], v25 offset:4096\n"
    "v_mfma_f32_16x16x32_bf16 v[160:163], a[8:11], a[184:187], v[160:163]\n"
    "ds_read_b64_tr_b16 a[68:69], v25 offset:8192\n"
    "buffer_load_dwordx4 v10, s[16:19], s45 offen offset:1024 nt lds\n"
    "v_mfma_f32_16x16x32_bf16 v[160:163], a[12:15], a[188:191], v[160:163]\n"
    "ds_read_b64_tr_b16 a[70:71], v25 offset:12288\n"
    "v_add_u32 v25, 0x4000, v25\n"
    "v_mfma_f32_16x16x32_bf16 v[184:187], a[12:15], a[204:207], v[236:239]\n"
    "ds_read_b64_tr_b16 a[72:73], v26 offset:0\n"
    "v_mfma_f32_16x16x32_bf16 v[184:187], a[8:11], a[200:203], v[184:187]\n"
    "ds_read_b64_tr_b16 a[74:75], v26 offset:4096\n"
    "s_waitcnt vmcnt(12)\n"
    "buffer_load_dwordx4 v11, s[16:19], s45 offen offset:2048 nt lds\n"
    "v_mfma_f32_16x16x32_bf16 v[184:187], a[4:7], a[196:199], v[184:187]\n"
    "ds_read_b64_tr_b16 a[76:77], v26 offset:8192\n"
    "v_mfma_f32_16x16x32_bf16 v[184:187], a[0:3], a[192:195], v[184:187]\n"
    "ds_read_b64_tr_b16 a[78:79], v26 offset:12288\n"
    "v_add_u32 v26, 0x4000, v26\n"
    "v_mfma_f32_16x16x32_bf16 v[200:203], a[16:19], a[192:195], v[236:239]\n"
    "ds_read_b64_tr_b16 a[80:81], v27 offset:0\n"
    "buffer_load_dwordx4 v12, s[16:19], s45 offen offset:3072 nt lds\n"
    "v_mfma_f32_16x16x32_bf16 v[200:203], a[20:23], a[196:199], v[200:203]\n"
    "ds_read_b64_tr_b16 a[82:83], v27 offset:4096\n"
    "v_mfma_f32_16x16x32_bf16 v[200:203], a[24:27], a[200:203], v[200:203]\n"
    "ds_read_b64_tr_b16 a[84:85], v27 offset:8192\n"
    "v_mfma_f32_16x16x32_bf16 v[200:203], a[28:31], a[204:207], v[200:203]\n"
    "ds_read_b64_tr_b16 a[86:87], v27 offset:12288\n"
    "v_add_u32 v27, 0x4000, v27\n"
    "s_mov_b32 m0, 106496\n"
    "s_mov_b32 s45, 122880\n"
    "s_waitcnt vmcnt(12)\n"
    "buffer_load_dwordx4 v13, s[20:23], s45 offen nt lds\n"
    "v_mfma_f32_16x16x32_bf16 v[176:179], a[28:31], a[188:191], v[232:235]\n"
    "ds_read_b64_tr_b16 a[88:89], v28 offset:0\n"
    "v_mfma_f32_16x16x32_bf16 v[176:179], a[24:27], a[184:187], v[176:179]\n"
    "ds_read_b64_tr_b16 a[90:91], v28 offset:4096\n"
    "v_mfma_f32_16x16x32_bf16 v[176:179], a[20:23], a[180:183], v[176:179]\n"
    "ds_read_b64_tr_b16 a[92:93], v28 offset:8192\n"
    "buffer_load_dwordx4 v14, s[20:23], s45 offen offset:1024 nt lds\n"
    "v_mfma_f32_16x16x32_bf16 v[176:179], a[16:19], a[176:179], v[176:179]\n"
    "ds_read_b64_tr_b16 a[94:95], v28 offset:12288\n"
    "v_add_u32 v28, 0x4000, v28\n"
    "v_mfma_f32_16x16x32_bf16 v[168:171], a[32:35], a[176:179], v[232:235]\n"
    "ds_read_b64_tr_b16 a[96:97], v29 offset:0\n"
    "v_mfma_f32_16x16x32_bf16 v[168:171], a[36:39], a[180:183], v[168:171]\n"
    "ds_read_b64_tr_b16 a[98:99], v29 offset:4096\n"
    "s_waitcnt vmcnt(12)\n"
    "buffer_load_dwordx4 v13, s[20:23], s45 offen offset:2048 nt lds\n"
    "v_mfma_f32_16x16x32_bf16 v[168:171], a[40:43], a[184:187], v[168:171]\n"
    "ds_read_b64_tr_b16 a[100:101], v29 offset:8192\n"
    "v_mfma_f32_16x16x32_bf16 v[168:171], a[44:47], a[188:191], v[168:171]\n"
    "ds_read_b64_tr_b16 a[102:103], v29 offset:12288\n"
    "v_add_u32 v29, 0x4000, v29\n"
    "buffer_load_dwordx4 v14, s[20:23], s45 offen offset:3072 nt lds\n"
    "v_mfma_f32_16x16x32_bf16 v[192:195], a[44:47], a[204:207], v[236:239]\n"
    "ds_read_b64_tr_b16 a[104:105], v30 offset:0\n"
    "v_mfma_f32_16x16x32_bf16 v[192:195], a[40:43], a[200:203], v[192:195]\n"
    "ds_read_b64_tr_b16 a[106:107], v30 offset:4096\n"
    "v_mfma_f32_16x16x32_bf16 v[192:195], a[36:39], a[196:199], v[192:195]\n"
    "ds_read_b64_tr_b16 a[108:109], v30 offset:8192\n"
    "s_mov_b32 m0, 28672\n"
    "s_mov_b32 s45, 2125824\n"
    "s_waitcnt vmcnt(12)\n"
    "buffer_load_dwordx4 v9, s[16:19], s45 offen nt lds\n"
    "v_mfma_f32_16x16x32_bf16 v[192:195], a[32:35], a[192:195], v[192:195]\n"
    "ds_read_b64_tr_b16 a[110:111], v30 offset:12288\n"
    "v_add_u32 v30, 0x4000, v30\n"
    "v_mfma_f32_16x16x32_bf16 v[204:207], a[48:51], a[192:195], v[236:239]\n"
    "ds_read_b64_tr_b16 a[112:113], v31 offset:0\n"
    "v_mfma_f32_16x16x32_bf16 v[204:207], a[52:55], a[196:199], v[204:207]\n"
    "ds_read_b64_tr_b16 a[114:115], v31 offset:4096\n"
    "buffer_load_dwordx4 v10, s[16:19], s45 offen offset:1024 nt lds\n"
    "v_mfma_f32_16x16x32_bf16 v[204:207], a[56:59], a[200:203], v[204:207]\n"
    "ds_read_b64_tr_b16 a[116:117], v31 offset:8192\n"
    "v_mfma_f32_16x16x32_bf16 v[204:207], a[60:63], a[204:207], v[204:207]\n"
    "ds_read_b64_tr_b16 a[118:119], v31 offset:12288\n"
    "v_add_u32 v31, 0x4000, v31\n"
    "v_mfma_f32_16x16x32_bf16 v[180:183], a[60:63], a[188:191], v[232:235]\n"
    "ds_read_b64_tr_b16 a[120:121], v32 offset:0\n"
    "s_waitcnt vmcnt(12)\n"
    "buffer_load_dwordx4 v11, s[16:19], s45 offen offset:2048 nt lds\n"
    "v_mfma_f32_16x16x32_bf16 v[180:183], a[56:59], a[184:187], v[180:183]\n"
    "ds_read_b64_tr_b16 a[122:123], v32 offset:4096\n"
    "v_mfma_f32_16x16x32_bf16 v[180:183], a[52:55], a[180:183], v[180:183]\n"
    "ds_read_b64_tr_b16 a[124:125], v32 offset:8192\n"
    "v_mfma_f32_16x16x32_bf16 v[180:183], a[48:51], a[176:179], v[180:183]\n"
    "ds_read_b64_tr_b16 a[126:127], v32 offset:12288\n"
    "v_add_u32 v32, 0x4000, v32\n"
    "buffer_load_dwordx4 v12, s[16:19], s45 offen offset:3072 nt lds\n"
    "s_waitcnt lgkmcnt(0)\n"
    "v_mfma_f32_16x16x32_bf16 v[64:67], a[64:67], v[164:167], v[64:67]\n"
    "ds_read_b128 a[0:3], v33 offset:16384\n"
    "v_exp_f32 v160, v160\n"
    "v_mfma_f32_16x16x32_bf16 v[64:67], a[68:71], v[172:175], v[64:67]\n"
    "v_exp_f32 v161, v161\n"
    "v_mfma_f32_16x16x32_bf16 v[68:71], a[68:71], v[196:199], v[68:71]\n"
    "s_mov_b32 m0, 110592\n"
    "s_mov_b32 s45, 126976\n"
    "s_waitcnt vmcnt(12)\n"
    "buffer_load_dwordx4 v13, s[20:23], s45 offen nt lds\n"
    "ds_read_b128 a[4:7], v34 offset:16384\n"
    "v_exp_f32 v162, v162\n"
    "v_cvt_pk_bf16_f32 v160, v160, v161\n"
    "v_mfma_f32_16x16x32_bf16 v[68:71], a[64:67], v[188:191], v[68:71]\n"
    "v_exp_f32 v163, v163\n"
    "v_mfma_f32_16x16x32_bf16 v[80:83], a[72:75], v[188:191], v[80:83]\n"
    "buffer_load_dwordx4 v14, s[20:23], s45 offen offset:1024 nt lds\n"
    "ds_read_b128 a[8:11], v35 offset:16384\n"
    "v_exp_f32 v184, v184\n"
    "v_cvt_pk_bf16_f32 v161, v162, v163\n"
    "v_mfma_f32_16x16x32_bf16 v[80:83], a[76:79], v[196:199], v[80:83]\n"
    "v_exp_f32 v185, v185\n"
    "v_mfma_f32_16x16x32_bf16 v[76:79], a[76:79], v[172:175], v[76:79]\n"
    "ds_read_b128 a[12:15], v36 offset:16384\n"
    "v_exp_f32 v186, v186\n"
    "v_cvt_pk_bf16_f32 v184, v184, v185\n"
    "v_mfma_f32_16x16x32_bf16 v[76:79], a[72:75], v[164:167], v[76:79]\n"
    "s_waitcnt vmcnt(12)\n"
    "buffer_load_dwordx4 v13, s[20:23], s45 offen offset:2048 nt lds\n"
    "v_exp_f32 v187, v187\n"
    "v_mfma_f32_16x16x32_bf16 v[88:91], a[80:83], v[164:167], v[88:91]\n"
    "ds_read_b128 a[16:19], v33 offset:20480\n"
    "v_exp_f32 v176, v176\n"
    "v_cvt_pk_bf16_f32 v185, v186, v187\n"
    "v_mfma_f32_16x16x32_bf16 v[88:91], a[84:87], v[172:175], v[88:91]\n"
    "v_exp_f32 v177, v177\n"
    "v_mfma_f32_16x16x32_bf16 v[92:95], a[84:87], v[196:199], v[92:95]\n"
    "buffer_load_dwordx4 v14, s[20:23], s45 offen offset:3072 nt lds\n"
    "ds_read_b128 a[20:23], v34 offset:20480\n"
    "v_exp_f32 v178, v178\n"
    "v_cvt_pk_bf16_f32 v162, v176, v177\n"
    "v_mfma_f32_16x16x32_bf16 v[92:95], a[80:83], v[188:191], v[92:95]\n"
    "v_exp_f32 v179, v179\n"
    "v_mfma_f32_16x16x32_bf16 v[104:107], a[88:91], v[188:191], v[104:107]\n"
    "ds_read_b128 a[24:27], v35 offset:20480\n"
    "v_exp_f32 v200, v200\n"
    "v_cvt_pk_bf16_f32 v163, v178, v179\n"
    "v_mfma_f32_16x16x32_bf16 v[104:107], a[92:95], v[196:199], v[104:107]\n"
    "s_mov_b32 m0, 32768\n"
    "s_mov_b32 s45, 2129920\n"
    "s_waitcnt vmcnt(12)\n"
    "buffer_load_dwordx4 v9, s[16:19], s45 offen nt lds\n"
    "v_exp_f32 v201, v201\n"
    "v_mfma_f32_16x16x32_bf16 v[100:103], a[92:95], v[172:175], v[100:103]\n"
    "ds_read_b128 a[28:31], v36 offset:20480\n"
    "v_exp_f32 v202, v202\n"
    "v_cvt_pk_bf16_f32 v186, v200, v201\n"
    "v_mfma_f32_16x16x32_bf16 v[100:103], a[88:91], v[164:167], v[100:103]\n"
    "v_exp_f32 v203, v203\n"
    "v_mfma_f32_16x16x32_bf16 v[112:115], a[96:99], v[164:167], v[112:115]\n"
    "buffer_load_dwordx4 v10, s[16:19], s45 offen offset:1024 nt lds\n"
    "ds_read_b128 a[32:35], v33 offset:24576\n"
    "v_exp_f32 v168, v168\n"
    "v_cvt_pk_bf16_f32 v187, v202, v203\n"
    "v_mfma_f32_16x16x32_bf16 v[112:115], a[100:103], v[172:175], v[112:115]\n"
    "v_exp_f32 v169, v169\n"
    "v_mfma_f32_16x16x32_bf16 v[116:119], a[100:103], v[196:199], v[116:119]\n"
    "ds_read_b128 a[36:39], v34 offset:24576\n"
    "v_exp_f32 v170, v170\n"
    "v_cvt_pk_bf16_f32 v168, v168, v169\n"
    "v_mfma_f32_16x16x32_bf16 v[116:119], a[96:99], v[188:191], v[116:119]\n"
    "s_waitcnt vmcnt(12)\n"
    "buffer_load_dwordx4 v11, s[16:19], s45 offen offset:2048 nt lds\n"
    "v_exp_f32 v171, v171\n"
    "v_mfma_f32_16x16x32_bf16 v[128:131], a[104:107], v[188:191], v[128:131]\n"
    "ds_read_b128 a[40:43], v35 offset:24576\n"
    "v_exp_f32 v192, v192\n"
    "v_cvt_pk_bf16_f32 v169, v170, v171\n"
    "v_mfma_f32_16x16x32_bf16 v[128:131], a[108:111], v[196:199], v[128:131]\n"
    "buffer_load_dwordx4 v12, s[16:19], s45 offen offset:3072 nt lds\n"
    "v_exp_f32 v193, v193\n"
    "v_mfma_f32_16x16x32_bf16 v[124:127], a[108:111], v[172:175], v[124:127]\n"
    "ds_read_b128 a[44:47], v36 offset:24576\n"
    "v_exp_f32 v194, v194\n"
    "v_cvt_pk_bf16_f32 v192, v192, v193\n"
    "v_mfma_f32_16x16x32_bf16 v[124:127], a[104:107], v[164:167], v[124:127]\n"
    "v_exp_f32 v195, v195\n"
    "v_mfma_f32_16x16x32_bf16 v[136:139], a[112:115], v[164:167], v[136:139]\n"
    "s_mov_b32 m0, 114688\n"
    "s_mov_b32 s45, 2097152\n"
    "s_waitcnt vmcnt(12)\n"
    "buffer_load_dwordx4 v13, s[20:23], s45 offen nt lds\n"
    "ds_read_b128 a[48:51], v33 offset:28672\n"
    "v_exp_f32 v180, v180\n"
    "v_cvt_pk_bf16_f32 v193, v194, v195\n"
    "v_mfma_f32_16x16x32_bf16 v[136:139], a[116:119], v[172:175], v[136:139]\n"
    "v_exp_f32 v181, v181\n"
    "v_mfma_f32_16x16x32_bf16 v[140:143], a[116:119], v[196:199], v[140:143]\n"
    "ds_read_b128 a[52:55], v34 offset:28672\n"
    "v_exp_f32 v182, v182\n"
    "v_cvt_pk_bf16_f32 v170, v180, v181\n"
    "v_mfma_f32_16x16x32_bf16 v[140:143], a[112:115], v[188:191], v[140:143]\n"
    "buffer_load_dwordx4 v14, s[20:23], s45 offen offset:1024 nt lds\n"
    "v_exp_f32 v183, v183\n"
    "v_mfma_f32_16x16x32_bf16 v[152:155], a[120:123], v[188:191], v[152:155]\n"
    "ds_read_b128 a[56:59], v35 offset:28672\n"
    "v_exp_f32 v204, v204\n"
    "v_cvt_pk_bf16_f32 v171, v182, v183\n"
    "v_mfma_f32_16x16x32_bf16 v[152:155], a[124:127], v[196:199], v[152:155]\n"
    "v_exp_f32 v205, v205\n"
    "v_mfma_f32_16x16x32_bf16 v[148:151], a[124:127], v[172:175], v[148:151]\n"
    "s_waitcnt vmcnt(12)\n"
    "buffer_load_dwordx4 v13, s[20:23], s45 offen offset:2048 nt lds\n"
    "ds_read_b128 a[60:63], v36 offset:28672\n"
    "v_exp_f32 v206, v206\n"
    "v_cvt_pk_bf16_f32 v194, v204, v205\n"
    "v_mfma_f32_16x16x32_bf16 v[148:151], a[120:123], v[164:167], v[148:151]\n"
    "v_exp_f32 v207, v207\n"
    "v_mfma_f32_16x16x32_bf16 v[244:247], a[224:227], v[164:167], v[244:247]\n"
    "v_mfma_f32_16x16x32_bf16 v[244:247], a[224:227], v[172:175], v[244:247]\n"
    "buffer_load_dwordx4 v14, s[20:23], s45 offen offset:3072 nt lds\n"
    "v_cvt_pk_bf16_f32 v195, v206, v207\n"
    "v_mfma_f32_16x16x32_bf16 v[248:251], a[224:227], v[188:191], v[248:251]\n"
    "v_mfma_f32_16x16x32_bf16 v[248:251], a[224:227], v[196:199], v[248:251]\n"
    "s_waitcnt vmcnt(40) lgkmcnt(0)\n"
    "s_barrier\n"
    "v_mfma_f32_16x16x32_bf16 v[164:167], a[0:3], a[176:179], v[232:235]\n"
    "ds_read_b64_tr_b16 a[64:65], v25 offset:0\n"
    "s_mov_b32 m0, 36864\n"
    "s_mov_b32 s45, 2134016\n"
    "s_waitcnt vmcnt(12)\n"
    "buffer_load_dwordx4 v9, s[16:19], s45 offen nt lds\n"
    "v_mfma_f32_16x16x32_bf16 v[164:167], a[4:7], a[180:183], v[164:167]\n"
    "ds_read_b64_tr_b16 a[66:67], v25 offset:4096\n"
    "v_mfma_f32_16x16x32_bf16 v[164:167], a[8:11], a[184:187], v[164:167]\n"
    "ds_read_b64_tr_b16 a[68:69], v25 offset:8192\n"
    "buffer_load_dwordx4 v10, s[16:19], s45 offen offset:1024 nt lds\n"
    "v_mfma_f32_16x16x32_bf16 v[164:167], a[12:15], a[188:191], v[164:167]\n"
    "ds_read_b64_tr_b16 a[70:71], v25 offset:12288\n"
    "v_add_u32 v25, 0x4000, v25\n"
    "v_mfma_f32_16x16x32_bf16 v[188:191], a[12:15], a[204:207], v[236:239]\n"
    "ds_read_b64_tr_b16 a[72:73], v26 offset:0\n"
    "v_mfma_f32_16x16x32_bf16 v[188:191], a[8:11], a[200:203], v[188:191]\n"
    "ds_read_b64_tr_b16 a[74:75], v26 offset:4096\n"
    "s_waitcnt vmcnt(12)\n"
    "buffer_load_dwordx4 v11, s[16:19], s45 offen offset:2048 nt lds\n"
    "v_mfma_f32_16x16x32_bf16 v[188:191], a[4:7], a[196:199], v[188:191]\n"
    "ds_read_b64_tr_b16 a[76:77], v26 offset:8192\n"
    "v_mfma_f32_16x16x32_bf16 v[188:191], a[0:3], a[192:195], v[188:191]\n"
    "ds_read_b64_tr_b16 a[78:79], v26 offset:12288\n"
    "v_add_u32 v26, 0x4000, v26\n"
    "v_mfma_f32_16x16x32_bf16 v[200:203], a[16:19], a[192:195], v[236:239]\n"
    "ds_read_b64_tr_b16 a[80:81], v27 offset:0\n"
    "buffer_load_dwordx4 v12, s[16:19], s45 offen offset:3072 nt lds\n"
    "v_mfma_f32_16x16x32_bf16 v[200:203], a[20:23], a[196:199], v[200:203]\n"
    "ds_read_b64_tr_b16 a[82:83], v27 offset:4096\n"
    "v_mfma_f32_16x16x32_bf16 v[200:203], a[24:27], a[200:203], v[200:203]\n"
    "ds_read_b64_tr_b16 a[84:85], v27 offset:8192\n"
    "v_mfma_f32_16x16x32_bf16 v[200:203], a[28:31], a[204:207], v[200:203]\n"
    "ds_read_b64_tr_b16 a[86:87], v27 offset:12288\n"
    "v_add_u32 v27, 0x4000, v27\n"
    "s_mov_b32 m0, 118784\n"
    "s_mov_b32 s45, 2101248\n"
    "s_waitcnt vmcnt(12)\n"
    "buffer_load_dwordx4 v13, s[20:23], s45 offen nt lds\n"
    "v_mfma_f32_16x16x32_bf16 v[176:179], a[28:31], a[188:191], v[232:235]\n"
    "ds_read_b64_tr_b16 a[88:89], v28 offset:0\n"
    "v_xor_b32 v33, 0x8000, v33\n"
    "v_mfma_f32_16x16x32_bf16 v[176:179], a[24:27], a[184:187], v[176:179]\n"
    "ds_read_b64_tr_b16 a[90:91], v28 offset:4096\n"
    "v_mfma_f32_16x16x32_bf16 v[176:179], a[20:23], a[180:183], v[176:179]\n"
    "ds_read_b64_tr_b16 a[92:93], v28 offset:8192\n"
    "v_xor_b32 v34, 0x8000, v34\n"
    "buffer_load_dwordx4 v14, s[20:23], s45 offen offset:1024 nt lds\n"
    "v_mfma_f32_16x16x32_bf16 v[176:179], a[16:19], a[176:179], v[176:179]\n"
    "ds_read_b64_tr_b16 a[94:95], v28 offset:12288\n"
    "v_add_u32 v28, 0x4000, v28\n"
    "v_mfma_f32_16x16x32_bf16 v[172:175], a[32:35], a[176:179], v[232:235]\n"
    "ds_read_b64_tr_b16 a[96:97], v29 offset:0\n"
    "v_xor_b32 v35, 0x8000, v35\n"
    "v_mfma_f32_16x16x32_bf16 v[172:175], a[36:39], a[180:183], v[172:175]\n"
    "ds_read_b64_tr_b16 a[98:99], v29 offset:4096\n"
    "s_waitcnt vmcnt(12)\n"
    "buffer_load_dwordx4 v13, s[20:23], s45 offen offset:2048 nt lds\n"
    "v_mfma_f32_16x16x32_bf16 v[172:175], a[40:43], a[184:187], v[172:175]\n"
    "ds_read_b64_tr_b16 a[100:101], v29 offset:8192\n"
    "v_xor_b32 v36, 0x8000, v36\n"
    "v_mfma_f32_16x16x32_bf16 v[172:175], a[44:47], a[188:191], v[172:175]\n"
    "ds_read_b64_tr_b16 a[102:103], v29 offset:12288\n"
    "v_add_u32 v29, 0x4000, v29\n"
    "buffer_load_dwordx4 v14, s[20:23], s45 offen offset:3072 nt lds\n"
    "v_mfma_f32_16x16x32_bf16 v[196:199], a[44:47], a[204:207], v[236:239]\n"
    "ds_read_b64_tr_b16 a[104:105], v30 offset:0\n"
    "v_mfma_f32_16x16x32_bf16 v[196:199], a[40:43], a[200:203], v[196:199]\n"
    "ds_read_b64_tr_b16 a[106:107], v30 offset:4096\n"
    "v_mfma_f32_16x16x32_bf16 v[196:199], a[36:39], a[196:199], v[196:199]\n"
    "ds_read_b64_tr_b16 a[108:109], v30 offset:8192\n"
    "s_mov_b32 m0, 40960\n"
    "s_mov_b32 s45, 2138112\n"
    "s_waitcnt vmcnt(12)\n"
    "buffer_load_dwordx4 v9, s[16:19], s45 offen nt lds\n"
    "v_mfma_f32_16x16x32_bf16 v[196:199], a[32:35], a[192:195], v[196:199]\n"
    "ds_read_b64_tr_b16 a[110:111], v30 offset:12288\n"
    "v_add_u32 v30, 0x4000, v30\n"
    "v_mfma_f32_16x16x32_bf16 v[204:207], a[48:51], a[192:195], v[236:239]\n"
    "ds_read_b64_tr_b16 a[112:113], v31 offset:0\n"
    "v_mfma_f32_16x16x32_bf16 v[204:207], a[52:55], a[196:199], v[204:207]\n"
    "ds_read_b64_tr_b16 a[114:115], v31 offset:4096\n"
    "buffer_load_dwordx4 v10, s[16:19], s45 offen offset:1024 nt lds\n"
    "v_mfma_f32_16x16x32_bf16 v[204:207], a[56:59], a[200:203], v[204:207]\n"
    "ds_read_b64_tr_b16 a[116:117], v31 offset:8192\n"
    "v_mfma_f32_16x16x32_bf16 v[204:207], a[60:63], a[204:207], v[204:207]\n"
    "ds_read_b64_tr_b16 a[118:119], v31 offset:12288\n"
    "v_add_u32 v31, 0x4000, v31\n"
    "v_mfma_f32_16x16x32_bf16 v[180:183], a[60:63], a[188:191], v[232:235]\n"
    "ds_read_b64_tr_b16 a[120:121], v32 offset:0\n"
    "s_waitcnt vmcnt(12)\n"
    "buffer_load_dwordx4 v11, s[16:19], s45 offen offset:2048 nt lds\n"
    "v_mfma_f32_16x16x32_bf16 v[180:183], a[56:59], a[184:187], v[180:183]\n"
    "ds_read_b64_tr_b16 a[122:123], v32 offset:4096\n"
    "v_mfma_f32_16x16x32_bf16 v[180:183], a[52:55], a[180:183], v[180:183]\n"
    "ds_read_b64_tr_b16 a[124:125], v32 offset:8192\n"
    "v_mfma_f32_16x16x32_bf16 v[180:183], a[48:51], a[176:179], v[180:183]\n"
    "ds_read_b64_tr_b16 a[126:127], v32 offset:12288\n"
    "v_add_u32 v32, 0x4000, v32\n"
    "buffer_load_dwordx4 v12, s[16:19], s45 offen offset:3072 nt lds\n"
    "s_waitcnt lgkmcnt(0)\n"
    "v_mfma_f32_16x16x32_bf16 v[64:67], a[64:67], v[160:163], v[64:67]\n"
    "ds_read_b128 a[0:3], v33 offset:0\n"
    "v_exp_f32 v164, v164\n"
    "v_mfma_f32_16x16x32_bf16 v[64:67], a[68:71], v[168:171], v[64:67]\n"
    "v_exp_f32 v165, v165\n"
    "v_mfma_f32_16x16x32_bf16 v[68:71], a[68:71], v[192:195], v[68:71]\n"
    "s_mov_b32 m0, 122880\n"
    "s_mov_b32 s45, 2105344\n"
    "s_waitcnt vmcnt(12)\n"
    "buffer_load_dwordx4 v13, s[20:23], s45 offen nt lds\n"
    "ds_read_b128 a[4:7], v34 offset:0\n"
    "v_exp_f32 v166, v166\n"
    "v_cvt_pk_bf16_f32 v164, v164, v165\n"
    "v_mfma_f32_16x16x32_bf16 v[68:71], a[64:67], v[184:187], v[68:71]\n"
    "v_exp_f32 v167, v167\n"
    "v_mfma_f32_16x16x32_bf16 v[80:83], a[72:75], v[184:187], v[80:83]\n"
    "buffer_load_dwordx4 v14, s[20:23], s45 offen offset:1024 nt lds\n"
    "ds_read_b128 a[8:11], v35 offset:0\n"
    "v_exp_f32 v188, v188\n"
    "v_cvt_pk_bf16_f32 v165, v166, v167\n"
    "v_mfma_f32_16x16x32_bf16 v[80:83], a[76:79], v[192:195], v[80:83]\n"
    "v_exp_f32 v189, v189\n"
    "v_mfma_f32_16x16x32_bf16 v[76:79], a[76:79], v[168:171], v[76:79]\n"
    "ds_read_b128 a[12:15], v36 offset:0\n"
    "v_exp_f32 v190, v190\n"
    "v_cvt_pk_bf16_f32 v188, v188, v189\n"
    "v_mfma_f32_16x16x32_bf16 v[76:79], a[72:75], v[160:163], v[76:79]\n"
    "s_waitcnt vmcnt(12)\n"
    "buffer_load_dwordx4 v13, s[20:23], s45 offen offset:2048 nt lds\n"
    "v_exp_f32 v191, v191\n"
    "v_mfma_f32_16x16x32_bf16 v[88:91], a[80:83], v[160:163], v[88:91]\n"
    "ds_read_b128 a[16:19], v33 offset:4096\n"
    "v_exp_f32 v176, v176\n"
    "v_cvt_pk_bf16_f32 v189, v190, v191\n"
    "v_mfma_f32_16x16x32_bf16 v[88:91], a[84:87], v[168:171], v[88:91]\n"
    "v_exp_f32 v177, v177\n"
    "v_mfma_f32_16x16x32_bf16 v[92:95], a[84:87], v[192:195], v[92:95]\n"
    "buffer_load_dwordx4 v14, s[20:23], s45 offen offset:3072 nt lds\n"
    "ds_read_b128 a[20:23], v34 offset:4096\n"
    "v_exp_f32 v178, v178\n"
    "v_cvt_pk_bf16_f32 v166, v176, v177\n"
    "v_mfma_f32_16x16x32_bf16 v[92:95], a[80:83], v[184:187], v[92:95]\n"
    "v_exp_f32 v179, v179\n"
    "v_mfma_f32_16x16x32_bf16 v[104:107], a[88:91], v[184:187], v[104:107]\n"
    "ds_read_b128 a[24:27], v35 offset:4096\n"
    "v_exp_f32 v200, v200\n"
    "v_cvt_pk_bf16_f32 v167, v178, v179\n"
    "v_mfma_f32_16x16x32_bf16 v[104:107], a[92:95], v[192:195], v[104:107]\n"
    "s_mov_b32 m0, 45056\n"
    "s_mov_b32 s45, 2142208\n"
    "s_waitcnt vmcnt(12)\n"
    "buffer_load_dwordx4 v9, s[16:19], s45 offen nt lds\n"
    "v_exp_f32 v201, v201\n"
    "v_mfma_f32_16x16x32_bf16 v[100:103], a[92:95], v[168:171], v[100:103]\n"
    "ds_read_b128 a[28:31], v36 offset:4096\n"
    "v_exp_f32 v202, v202\n"
    "v_cvt_pk_bf16_f32 v190, v200, v201\n"
    "v_mfma_f32_16x16x32_bf16 v[100:103], a[88:91], v[160:163], v[100:103]\n"
    "v_exp_f32 v203, v203\n"
    "v_mfma_f32_16x16x32_bf16 v[112:115], a[96:99], v[160:163], v[112:115]\n"
    "buffer_load_dwordx4 v10, s[16:19], s45 offen offset:1024 nt lds\n"
    "ds_read_b128 a[32:35], v33 offset:8192\n"
    "v_exp_f32 v172, v172\n"
    "v_cvt_pk_bf16_f32 v191, v202, v203\n"
    "v_mfma_f32_16x16x32_bf16 v[112:115], a[100:103], v[168:171], v[112:115]\n"
    "v_exp_f32 v173, v173\n"
    "v_mfma_f32_16x16x32_bf16 v[116:119], a[100:103], v[192:195], v[116:119]\n"
    "ds_read_b128 a[36:39], v34 offset:8192\n"
    "v_exp_f32 v174, v174\n"
    "v_cvt_pk_bf16_f32 v172, v172, v173\n"
    "v_mfma_f32_16x16x32_bf16 v[116:119], a[96:99], v[184:187], v[116:119]\n"
    "s_waitcnt vmcnt(12)\n"
    "buffer_load_dwordx4 v11, s[16:19], s45 offen offset:2048 nt lds\n"
    "v_exp_f32 v175, v175\n"
    "v_mfma_f32_16x16x32_bf16 v[128:131], a[104:107], v[184:187], v[128:131]\n"
    "ds_read_b128 a[40:43], v35 offset:8192\n"
    "v_exp_f32 v196, v196\n"
    "v_cvt_pk_bf16_f32 v173, v174, v175\n"
    "v_mfma_f32_16x16x32_bf16 v[128:131], a[108:111], v[192:195], v[128:131]\n"
    "buffer_load_dwordx4 v12, s[16:19], s45 offen offset:3072 nt lds\n"
    "v_exp_f32 v197, v197\n"
    "v_mfma_f32_16x16x32_bf16 v[124:127], a[108:111], v[168:171], v[124:127]\n"
    "ds_read_b128 a[44:47], v36 offset:8192\n"
    "v_exp_f32 v198, v198\n"
    "v_cvt_pk_bf16_f32 v196, v196, v197\n"
    "v_mfma_f32_16x16x32_bf16 v[124:127], a[104:107], v[160:163], v[124:127]\n"
    "v_exp_f32 v199, v199\n"
    "v_mfma_f32_16x16x32_bf16 v[136:139], a[112:115], v[160:163], v[136:139]\n"
    "s_mov_b32 m0, 126976\n"
    "s_mov_b32 s45, 2109440\n"
    "s_waitcnt vmcnt(12)\n"
    "buffer_load_dwordx4 v13, s[20:23], s45 offen nt lds\n"
    "ds_read_b128 a[48:51], v33 offset:12288\n"
    "v_exp_f32 v180, v180\n"
    "v_cvt_pk_bf16_f32 v197, v198, v199\n"
    "v_mfma_f32_16x16x32_bf16 v[136:139], a[116:119], v[168:171], v[136:139]\n"
    "v_exp_f32 v181, v181\n"
    "v_mfma_f32_16x16x32_bf16 v[140:143], a[116:119], v[192:195], v[140:143]\n"
    "ds_read_b128 a[52:55], v34 offset:12288\n"
    "v_exp_f32 v182, v182\n"
    "v_cvt_pk_bf16_f32 v174, v180, v181\n"
    "v_mfma_f32_16x16x32_bf16 v[140:143], a[112:115], v[184:187], v[140:143]\n"
    "buffer_load_dwordx4 v14, s[20:23], s45 offen offset:1024 nt lds\n"
    "v_exp_f32 v183, v183\n"
    "v_mfma_f32_16x16x32_bf16 v[152:155], a[120:123], v[184:187], v[152:155]\n"
    "ds_read_b128 a[56:59], v35 offset:12288\n"
    "v_exp_f32 v204, v204\n"
    "v_cvt_pk_bf16_f32 v175, v182, v183\n"
    "v_mfma_f32_16x16x32_bf16 v[152:155], a[124:127], v[192:195], v[152:155]\n"
    "v_exp_f32 v205, v205\n"
    "v_mfma_f32_16x16x32_bf16 v[148:151], a[124:127], v[168:171], v[148:151]\n"
    "s_waitcnt vmcnt(12)\n"
    "buffer_load_dwordx4 v13, s[20:23], s45 offen offset:2048 nt lds\n"
    "ds_read_b128 a[60:63], v36 offset:12288\n"
    "v_exp_f32 v206, v206\n"
    "v_cvt_pk_bf16_f32 v198, v204, v205\n"
    "v_mfma_f32_16x16x32_bf16 v[148:151], a[120:123], v[160:163], v[148:151]\n"
    "v_exp_f32 v207, v207\n"
    "v_mfma_f32_16x16x32_bf16 v[244:247], a[224:227], v[160:163], v[244:247]\n"
    "v_mfma_f32_16x16x32_bf16 v[244:247], a[224:227], v[168:171], v[244:247]\n"
    "buffer_load_dwordx4 v14, s[20:23], s45 offen offset:3072 nt lds\n"
    "v_cvt_pk_bf16_f32 v199, v206, v207\n"
    "v_mfma_f32_16x16x32_bf16 v[248:251], a[224:227], v[184:187], v[248:251]\n"
    "v_mfma_f32_16x16x32_bf16 v[248:251], a[224:227], v[192:195], v[248:251]\n"
    "s_waitcnt vmcnt(32) lgkmcnt(0)\n"
    "s_barrier\n"
    "ds_read_b64_tr_b16 a[64:65], v25 offset:0\n"
    "s_mov_b32 m0, 49152\n"
    "s_mov_b32 s45, 2146304\n"
    "s_waitcnt vmcnt(18)\n"
    "buffer_load_dwordx4 v9, s[16:19], s45 offen nt lds\n"
    "ds_read_b64_tr_b16 a[66:67], v25 offset:4096\n"
    "ds_read_b64_tr_b16 a[68:69], v25 offset:8192\n"
    "ds_read_b64_tr_b16 a[70:71], v25 offset:12288\n"
    "v_mfma_f32_16x16x32_bf16 v[244:247], a[224:227], v[164:167], v[244:247]\n"
    "ds_read_b64_tr_b16 a[72:73], v26 offset:0\n"
    "v_mfma_f32_16x16x32_bf16 v[244:247], a[224:227], v[172:175], v[244:247]\n"
    "ds_read_b64_tr_b16 a[74:75], v26 offset:4096\n"
    "buffer_load_dwordx4 v10, s[16:19], s45 offen offset:1024 nt lds\n"
    "v_mfma_f32_16x16x32_bf16 v[248:251], a[224:227], v[188:191], v[248:251]\n"
    "ds_read_b64_tr_b16 a[76:77], v26 offset:8192\n"
    "v_mfma_f32_16x16x32_bf16 v[248:251], a[224:227], v[196:199], v[248:251]\n"
    "s_waitcnt lgkmcnt(3)\n"
    "ds_read_b64_tr_b16 a[78:79], v26 offset:12288\n"
    "v_mfma_f32_16x16x32_bf16 v[64:67], a[64:67], v[164:167], v[64:67]\n"
    "ds_read_b64_tr_b16 a[80:81], v27 offset:0\n"
    "s_waitcnt vmcnt(18)\n"
    "buffer_load_dwordx4 v11, s[16:19], s45 offen offset:2048 nt lds\n"
    "v_mfma_f32_16x16x32_bf16 v[64:67], a[68:71], v[172:175], v[64:67]\n"
    "ds_read_b64_tr_b16 a[82:83], v27 offset:4096\n"
    "v_mfma_f32_16x16x32_bf16 v[68:71], a[68:71], v[196:199], v[68:71]\n"
    "ds_read_b64_tr_b16 a[84:85], v27 offset:8192\n"
    "v_mfma_f32_16x16x32_bf16 v[68:71], a[64:67], v[188:191], v[68:71]\n"
    "s_waitcnt lgkmcnt(3)\n"
    "ds_read_b64_tr_b16 a[86:87], v27 offset:12288\n"
    "buffer_load_dwordx4 v12, s[16:19], s45 offen offset:3072 nt lds\n"
    "v_mfma_f32_16x16x32_bf16 v[80:83], a[72:75], v[188:191], v[80:83]\n"
    "ds_read_b64_tr_b16 a[88:89], v28 offset:0\n"
    "v_mfma_f32_16x16x32_bf16 v[80:83], a[76:79], v[196:199], v[80:83]\n"
    "ds_read_b64_tr_b16 a[90:91], v28 offset:4096\n"
    "v_mfma_f32_16x16x32_bf16 v[76:79], a[76:79], v[172:175], v[76:79]\n"
    "ds_read_b64_tr_b16 a[92:93], v28 offset:8192\n"
    "v_mfma_f32_16x16x32_bf16 v[76:79], a[72:75], v[164:167], v[76:79]\n"
    "s_mov_b32 m0, 131072\n"
    "s_mov_b32 s45, 2113536\n"
    "s_waitcnt vmcnt(18)\n"
    "buffer_load_dwordx4 v13, s[20:23], s45 offen nt lds\n"
    "s_waitcnt lgkmcnt(3)\n"
    "ds_read_b64_tr_b16 a[94:95], v28 offset:12288\n"
    "v_mfma_f32_16x16x32_bf16 v[88:91], a[80:83], v[164:167], v[88:91]\n"
    "ds_read_b64_tr_b16 a[96:97], v29 offset:0\n"
    "v_mfma_f32_16x16x32_bf16 v[88:91], a[84:87], v[172:175], v[88:91]\n"
    "ds_read_b64_tr_b16 a[98:99], v29 offset:4096\n"
    "v_mfma_f32_16x16x32_bf16 v[92:95], a[84:87], v[196:199], v[92:95]\n"
    "buffer_load_dwordx4 v14, s[20:23], s45 offen offset:1024 nt lds\n"
    "ds_read_b64_tr_b16 a[100:101], v29 offset:8192\n"
    "v_mfma_f32_16x16x32_bf16 v[92:95], a[80:83], v[188:191], v[92:95]\n"
    "s_waitcnt lgkmcnt(3)\n"
    "ds_read_b64_tr_b16 a[102:103], v29 offset:12288\n"
    "v_mfma_f32_16x16x32_bf16 v[104:107], a[88:91], v[188:191], v[104:107]\n"
    "ds_read_b64_tr_b16 a[104:105], v30 offset:0\n"
    "v_mfma_f32_16x16x32_bf16 v[104:107], a[92:95], v[196:199], v[104:107]\n"
    "s_waitcnt vmcnt(18)\n"
    "buffer_load_dwordx4 v13, s[20:23], s45 offen offset:2048 nt lds\n"
    "ds_read_b64_tr_b16 a[106:107], v30 offset:4096\n"
    "v_mfma_f32_16x16x32_bf16 v[100:103], a[92:95], v[172:175], v[100:103]\n"
    "ds_read_b64_tr_b16 a[108:109], v30 offset:8192\n"
    "v_mfma_f32_16x16x32_bf16 v[100:103], a[88:91], v[164:167], v[100:103]\n"
    "s_waitcnt lgkmcnt(3)\n"
    "ds_read_b64_tr_b16 a[110:111], v30 offset:12288\n"
    "v_mfma_f32_16x16x32_bf16 v[112:115], a[96:99], v[164:167], v[112:115]\n"
    "buffer_load_dwordx4 v14, s[20:23], s45 offen offset:3072 nt lds\n"
    "ds_read_b64_tr_b16 a[112:113], v31 offset:0\n"
    "v_mfma_f32_16x16x32_bf16 v[112:115], a[100:103], v[172:175], v[112:115]\n"
    "ds_read_b64_tr_b16 a[114:115], v31 offset:4096\n"
    "v_mfma_f32_16x16x32_bf16 v[116:119], a[100:103], v[196:199], v[116:119]\n"
    "ds_read_b64_tr_b16 a[116:117], v31 offset:8192\n"
    "v_mfma_f32_16x16x32_bf16 v[116:119], a[96:99], v[188:191], v[116:119]\n"
    "s_waitcnt lgkmcnt(3)\n"
    "s_mov_b32 m0, 53248\n"
    "s_mov_b32 s45, 2150400\n"
    "s_waitcnt vmcnt(18)\n"
    "buffer_load_dwordx4 v9, s[16:19], s45 offen nt lds\n"
    "ds_read_b64_tr_b16 a[118:119], v31 offset:12288\n"
    "v_mfma_f32_16x16x32_bf16 v[128:131], a[104:107], v[188:191], v[128:131]\n"
    "ds_read_b64_tr_b16 a[120:121], v32 offset:0\n"
    "v_mfma_f32_16x16x32_bf16 v[128:131], a[108:111], v[196:199], v[128:131]\n"
    "ds_read_b64_tr_b16 a[122:123], v32 offset:4096\n"
    "v_mfma_f32_16x16x32_bf16 v[124:127], a[108:111], v[172:175], v[124:127]\n"
    "ds_read_b64_tr_b16 a[124:125], v32 offset:8192\n"
    "buffer_load_dwordx4 v10, s[16:19], s45 offen offset:1024 nt lds\n"
    "v_mfma_f32_16x16x32_bf16 v[124:127], a[104:107], v[164:167], v[124:127]\n"
    "s_waitcnt lgkmcnt(3)\n"
    "ds_read_b64_tr_b16 a[126:127], v32 offset:12288\n"
    "v_mfma_f32_16x16x32_bf16 v[136:139], a[112:115], v[164:167], v[136:139]\n"
    "v_add_u32 v25, 0x4000, v25\n"
    "v_add_u32 v26, 0x4000, v26\n"
    "v_mfma_f32_16x16x32_bf16 v[136:139], a[116:119], v[172:175], v[136:139]\n"
    "s_waitcnt vmcnt(18)\n"
    "buffer_load_dwordx4 v11, s[16:19], s45 offen offset:2048 nt lds\n"
    "v_add_u32 v27, 0x4000, v27\n"
    "v_add_u32 v28, 0x4000, v28\n"
    "v_mfma_f32_16x16x32_bf16 v[140:143], a[116:119], v[196:199], v[140:143]\n"
    "v_add_u32 v29, 0x4000, v29\n"
    "v_add_u32 v30, 0x4000, v30\n"
    "v_mfma_f32_16x16x32_bf16 v[140:143], a[112:115], v[188:191], v[140:143]\n"
    "s_waitcnt lgkmcnt(0)\n"
    "buffer_load_dwordx4 v12, s[16:19], s45 offen offset:3072 nt lds\n"
    "v_add_u32 v31, 0x4000, v31\n"
    "v_add_u32 v32, 0x4000, v32\n"
    "v_mfma_f32_16x16x32_bf16 v[152:155], a[120:123], v[188:191], v[152:155]\n"
    "v_mfma_f32_16x16x32_bf16 v[152:155], a[124:127], v[196:199], v[152:155]\n"
    "v_mfma_f32_16x16x32_bf16 v[148:151], a[124:127], v[172:175], v[148:151]\n"
    "v_mfma_f32_16x16x32_bf16 v[148:151], a[120:123], v[164:167], v[148:151]\n"
    "s_nop 7\n"
    "s_mov_b32 s57, 0x53800000\n"
    "v_cmp_nge_f32 vcc, s57, v244\n"
    "v_cmp_nge_f32 s[50:51], s57, v248\n"
    "s_or_b64 vcc, vcc, s[50:51]\n"
    "s_cbranch_vccnz .L_fbD_%=\n"
    ".L_fbretD0_%=:\n"
    "v_accvgpr_mov_b32 a176, a128\n"
    "s_mov_b32 m0, 135168\n"
    "s_mov_b32 s45, 2117632\n"
    "s_waitcnt vmcnt(18)\n"
    "buffer_load_dwordx4 v13, s[20:23], s45 offen nt lds\n"
    "v_accvgpr_mov_b32 a177, a129\n"
    "v_accvgpr_mov_b32 a178, a130\n"
    "v_accvgpr_mov_b32 a179, a131\n"
    "v_accvgpr_mov_b32 a180, a132\n"
    "v_accvgpr_mov_b32 a181, a133\n"
    "v_accvgpr_mov_b32 a182, a134\n"
    "v_accvgpr_mov_b32 a183, a135\n"
    "v_accvgpr_mov_b32 a184, a136\n"
    "v_accvgpr_mov_b32 a185, a137\n"
    "v_accvgpr_mov_b32 a186, a138\n"
    "v_accvgpr_mov_b32 a187, a139\n"
    "buffer_load_dwordx4 v14, s[20:23], s45 offen offset:1024 nt lds\n"
    "v_accvgpr_mov_b32 a188, a140\n"
    "v_accvgpr_mov_b32 a189, a141\n"
    "v_accvgpr_mov_b32 a190, a142\n"
    "v_accvgpr_mov_b32 a191, a143\n"
    "v_accvgpr_mov_b32 a192, a144\n"
    "v_accvgpr_mov_b32 a193, a145\n"
    "v_accvgpr_mov_b32 a194, a146\n"
    "v_accvgpr_mov_b32 a195, a147\n"
    "v_accvgpr_mov_b32 a196, a148\n"
    "v_accvgpr_mov_b32 a197, a149\n"
    "v_accvgpr_mov_b32 a198, a150\n"
    "s_waitcnt vmcnt(18)\n"
    "buffer_load_dwordx4 v13, s[20:23], s45 offen offset:2048 nt lds\n"
    "v_accvgpr_mov_b32 a199, a151\n"
    "v_accvgpr_mov_b32 a200, a152\n"
    "v_accvgpr_mov_b32 a201, a153\n"
    "v_accvgpr_mov_b32 a202, a154\n"
    "v_accvgpr_mov_b32 a203, a155\n"
    "v_accvgpr_mov_b32 a204, a156\n"
    "v_accvgpr_mov_b32 a205, a157\n"
    "v_accvgpr_mov_b32 a206, a158\n"
    "v_accvgpr_mov_b32 a207, a159\n"
    "v_rcp_f32 v44, v244\n"
    "v_rcp_f32 v45, v244\n"
    "buffer_load_dwordx4 v14, s[20:23], s45 offen offset:3072 nt lds\n"
    "v_rcp_f32 v46, v248\n"
    "v_rcp_f32 v47, v248\n"
    "v_pk_mul_f32 v[64:65], v[44:45], v[64:65]\n"
    "v_pk_mul_f32 v[66:67], v[44:45], v[66:67]\n"
    "v_pk_mul_f32 v[76:77], v[44:45], v[76:77]\n"
    "v_pk_mul_f32 v[78:79], v[44:45], v[78:79]\n"
    "v_cvt_pk_bf16_f32 v64, v64, v65\n"
    "v_cvt_pk_bf16_f32 v65, v66, v67\n"
    "v_cvt_pk_bf16_f32 v66, v76, v77\n"
    "v_cvt_pk_bf16_f32 v67, v78, v79\n"
    "v_accvgpr_write_b32 a128, v64\n"
    "s_mov_b32 m0, 57344\n"
    "s_mov_b32 s45, 2154496\n"
    "s_waitcnt vmcnt(18)\n"
    "buffer_load_dwordx4 v9, s[16:19], s45 offen nt lds\n"
    "v_accvgpr_write_b32 a129, v65\n"
    "v_accvgpr_write_b32 a130, v66\n"
    "v_accvgpr_write_b32 a131, v67\n"
    "v_pk_mul_f32 v[68:69], v[46:47], v[68:69]\n"
    "v_pk_mul_f32 v[70:71], v[46:47], v[70:71]\n"
    "v_pk_mul_f32 v[80:81], v[46:47], v[80:81]\n"
    "v_pk_mul_f32 v[82:83], v[46:47], v[82:83]\n"
    "v_cvt_pk_bf16_f32 v68, v68, v69\n"
    "v_cvt_pk_bf16_f32 v69, v70, v71\n"
    "v_cvt_pk_bf16_f32 v70, v80, v81\n"
    "v_cvt_pk_bf16_f32 v71, v82, v83\n"
    "buffer_load_dwordx4 v10, s[16:19], s45 offen offset:1024 nt lds\n"
    "v_accvgpr_write_b32 a144, v68\n"
    "v_accvgpr_write_b32 a145, v69\n"
    "v_accvgpr_write_b32 a146, v70\n"
    "v_accvgpr_write_b32 a147, v71\n"
    "v_pk_mul_f32 v[88:89], v[44:45], v[88:89]\n"
    "v_pk_mul_f32 v[90:91], v[44:45], v[90:91]\n"
    "v_pk_mul_f32 v[100:101], v[44:45], v[100:101]\n"
    "v_pk_mul_f32 v[102:103], v[44:45], v[102:103]\n"
    "v_cvt_pk_bf16_f32 v88, v88, v89\n"
    "v_cvt_pk_bf16_f32 v89, v90, v91\n"
    "v_cvt_pk_bf16_f32 v90, v100, v101\n"
    "s_waitcnt vmcnt(18)\n"
    "buffer_load_dwordx4 v11, s[16:19], s45 offen offset:2048 nt lds\n"
    "v_cvt_pk_bf16_f32 v91, v102, v103\n"
    "v_accvgpr_write_b32 a132, v88\n"
    "v_accvgpr_write_b32 a133, v89\n"
    "v_accvgpr_write_b32 a134, v90\n"
    "v_accvgpr_write_b32 a135, v91\n"
    "v_pk_mul_f32 v[92:93], v[46:47], v[92:93]\n"
    "v_pk_mul_f32 v[94:95], v[46:47], v[94:95]\n"
    "v_pk_mul_f32 v[104:105], v[46:47], v[104:105]\n"
    "v_pk_mul_f32 v[106:107], v[46:47], v[106:107]\n"
    "v_cvt_pk_bf16_f32 v92, v92, v93\n"
    "v_cvt_pk_bf16_f32 v93, v94, v95\n"
    "buffer_load_dwordx4 v12, s[16:19], s45 offen offset:3072 nt lds\n"
    "v_cvt_pk_bf16_f32 v94, v104, v105\n"
    "v_cvt_pk_bf16_f32 v95, v106, v107\n"
    "v_accvgpr_write_b32 a148, v92\n"
    "v_accvgpr_write_b32 a149, v93\n"
    "v_accvgpr_write_b32 a150, v94\n"
    "v_accvgpr_write_b32 a151, v95\n"
    "v_pk_mul_f32 v[112:113], v[44:45], v[112:113]\n"
    "v_pk_mul_f32 v[114:115], v[44:45], v[114:115]\n"
    "v_pk_mul_f32 v[124:125], v[44:45], v[124:125]\n"
    "v_pk_mul_f32 v[126:127], v[44:45], v[126:127]\n"
    "v_cvt_pk_bf16_f32 v112, v112, v113\n"
    "s_mov_b32 m0, 139264\n"
    "s_mov_b32 s45, 2121728\n"
    "s_waitcnt vmcnt(18)\n"
    "buffer_load_dwordx4 v13, s[20:23], s45 offen nt lds\n"
    "v_cvt_pk_bf16_f32 v113, v114, v115\n"
    "v_cvt_pk_bf16_f32 v114, v124, v125\n"
    "v_cvt_pk_bf16_f32 v115, v126, v127\n"
    "v_accvgpr_write_b32 a136, v112\n"
    "v_accvgpr_write_b32 a137, v113\n"
    "v_accvgpr_write_b32 a138, v114\n"
    "v_accvgpr_write_b32 a139, v115\n"
    "v_pk_mul_f32 v[116:117], v[46:47], v[116:117]\n"
    "v_pk_mul_f32 v[118:119], v[46:47], v[118:119]\n"
    "v_pk_mul_f32 v[128:129], v[46:47], v[128:129]\n"
    "v_pk_mul_f32 v[130:131], v[46:47], v[130:131]\n"
    "buffer_load_dwordx4 v14, s[20:23], s45 offen offset:1024 nt lds\n"
    "v_cvt_pk_bf16_f32 v116, v116, v117\n"
    "v_cvt_pk_bf16_f32 v117, v118, v119\n"
    "v_cvt_pk_bf16_f32 v118, v128, v129\n"
    "v_cvt_pk_bf16_f32 v119, v130, v131\n"
    "v_accvgpr_write_b32 a152, v116\n"
    "v_accvgpr_write_b32 a153, v117\n"
    "v_accvgpr_write_b32 a154, v118\n"
    "v_accvgpr_write_b32 a155, v119\n"
    "v_pk_mul_f32 v[136:137], v[44:45], v[136:137]\n"
    "v_pk_mul_f32 v[138:139], v[44:45], v[138:139]\n"
    "v_pk_mul_f32 v[148:149], v[44:45], v[148:149]\n"
    "s_waitcnt vmcnt(18)\n"
    "buffer_load_dwordx4 v13, s[20:23], s45 offen offset:2048 nt lds\n"
    "v_pk_mul_f32 v[150:151], v[44:45], v[150:151]\n"
    "v_cvt_pk_bf16_f32 v136, v136, v137\n"
    "v_cvt_pk_bf16_f32 v137, v138, v139\n"
    "v_cvt_pk_bf16_f32 v138, v148, v149\n"
    "v_cvt_pk_bf16_f32 v139, v150, v151\n"
    "v_accvgpr_write_b32 a140, v136\n"
    "v_accvgpr_write_b32 a141, v137\n"
    "v_accvgpr_write_b32 a142, v138\n"
    "v_accvgpr_write_b32 a143, v139\n"
    "v_pk_mul_f32 v[140:141], v[46:47], v[140:141]\n"
    "v_pk_mul_f32 v[142:143], v[46:47], v[142:143]\n"
    "buffer_load_dwordx4 v14, s[20:23], s45 offen offset:3072 nt lds\n"
    "v_pk_mul_f32 v[152:153], v[46:47], v[152:153]\n"
    "v_pk_mul_f32 v[154:155], v[46:47], v[154:155]\n"
    "v_cvt_pk_bf16_f32 v140, v140, v141\n"
    "v_cvt_pk_bf16_f32 v141, v142, v143\n"
    "v_cvt_pk_bf16_f32 v142, v152, v153\n"
    "v_cvt_pk_bf16_f32 v143, v154, v155\n"
    "v_accvgpr_write_b32 a156, v140\n"
    "v_accvgpr_write_b32 a157, v141\n"
    "v_accvgpr_write_b32 a158, v142\n"
    "v_accvgpr_write_b32 a159, v143\n"
    "s_mov_b32 s55, 1\n"
    "v_mfma_f32_16x16x32_bf16 v[160:163], a[0:3], a[176:179], 0\n"
    "s_mov_b32 m0, 61440\n"
    "s_mov_b32 s45, 2158592\n"
    "s_waitcnt vmcnt(18)\n"
    "buffer_load_dwordx4 v9, s[16:19], s45 offen nt lds\n"
    "v_and_b32 v44, 63, %7\n"
    "v_lshrrev_b32 v45, 4, v44\n"
    "v_mfma_f32_16x16x32_bf16 v[160:163], a[4:7], a[180:183], v[160:163]\n"
    "v_and_b32 v46, 15, v44\n"
    "v_bfe_u32 v47, v44, 2, 2\n"
    "v_mfma_f32_16x16x32_bf16 v[160:163], a[8:11], a[184:187], v[160:163]\n"
    "v_and_b32 v48, 3, v44\n"
    "v_and_b32 v49, 1, v45\n"
    "v_mfma_f32_16x16x32_bf16 v[160:163], a[12:15], a[188:191], v[160:163]\n"
    "v_lshl_or_b32 v49, v49, 2, v47\n"
    "buffer_load_dwordx4 v10, s[16:19], s45 offen offset:1024 nt lds\n"
    "v_lshrrev_b32 v50, 3, v44\n"
    "v_mfma_f32_16x16x32_bf16 v[184:187], a[12:15], a[204:207], 0\n"
    "v_and_b32 v51, 7, v44\n"
    "s_lshr_b32 s46, %5, 1\n"
    "s_lshl_b32 s46, s46, 8\n"
    "s_sub_u32 s46, 8388608, s46\n"
    "s_mov_b64 s[12:13], %9\n"
    "s_and_b32 s13, s13, 0xffff\n"
    "s_mov_b32 s14, s46\n"
    "s_mov_b32 s15, 0x20000\n"
    "s_waitcnt vmcnt(18)\n"
    "buffer_load_dwordx4 v11, s[16:19], s45 offen offset:2048 nt lds\n"
    "s_mov_b64 s[8:9], %10\n"
    "s_and_b32 s9, s9, 0xffff\n"
    "s_mov_b32 s10, 716928\n"
    "s_mov_b32 s11, 0x20000\n"
    "v_lshlrev_b32 v52, 1, v45\n"
    "v_mfma_f32_16x16x32_bf16 v[184:187], a[8:11], a[200:203], v[184:187]\n"
    "v_xor_b32 v52, v51, v52\n"
    "v_lshlrev_b32 v52, 4, v52\n"
    "v_mfma_f32_16x16x32_bf16 v[184:187], a[4:7], a[196:199], v[184:187]\n"
    "v_lshl_add_u32 v1, v50, 11, v52\n"
    "v_add_u32 v2, 15360, v1\n"
    "buffer_load_dwordx4 v12, s[16:19], s45 offen offset:3072 nt lds\n"
    "v_mfma_f32_16x16x32_bf16 v[184:187], a[0:3], a[192:195], v[184:187]\n"
    "v_add_u32 v3, 30720, v1\n"
    "v_add_u32 v4, 46080, v1\n"
    "v_mfma_f32_16x16x32_bf16 v[200:203], a[16:19], a[192:195], 0\n"
    "ds_read_b128 a[0:3], v33 offset:16384\n"
    "v_lshl_add_u32 v54, v45, 2, v47\n"
    "v_mfma_f32_16x16x32_bf16 v[200:203], a[20:23], a[196:199], v[200:203]\n"
    "ds_read_b128 a[4:7], v34 offset:16384\n"
    "v_bfe_u32 v52, v54, 1, 2\n"
    "v_mfma_f32_16x16x32_bf16 v[200:203], a[24:27], a[200:203], v[200:203]\n"
    "s_mov_b32 m0, 143360\n"
    "s_mov_b32 s45, 2125824\n"
    "s_waitcnt vmcnt(18)\n"
    "buffer_load_dwordx4 v13, s[20:23], s45 offen nt lds\n"
    "ds_read_b128 a[8:11], v35 offset:16384\n"
    "v_lshlrev_b32 v54, 7, v54\n"
    "v_mfma_f32_16x16x32_bf16 v[200:203], a[28:31], a[204:207], v[200:203]\n"
    "ds_read_b128 a[12:15], v36 offset:16384\n"
    "v_lshl_add_u32 v54, v48, 3, v54\n"
    "v_mfma_f32_16x16x32_bf16 v[176:179], a[28:31], a[188:191], 0\n"
    "v_lshl_add_u32 v5, v52, 5, v54\n"
    "v_xor_b32 v6, 32, v5\n"
    "v_mfma_f32_16x16x32_bf16 v[176:179], a[24:27], a[184:187], v[176:179]\n"
    "v_xor_b32 v7, 64, v5\n"
    "v_xor_b32 v8, 96, v5\n"
    "buffer_load_dwordx4 v14, s[20:23], s45 offen offset:1024 nt lds\n"
    "s_mul_i32 s46, s36, 3072\n"
    "v_mfma_f32_16x16x32_bf16 v[176:179], a[20:23], a[180:183], v[176:179]\n"
    "v_add_u32 v15, s46, v37\n"
    "s_mov_b32 s40, 0\n"
    "s_mov_b32 s43, 8\n"
    "s_mov_b32 s52, 2\n"
    "s_and_b32 s46, %5, 1\n"
    "s_lshr_b32 s57, %5, 1\n"
    "s_mov_b32 s58, 0x46b1a200\n"
    "s_mov_b32 s59, 0x36b634\n"
    "s_waitcnt vmcnt(18)\n"
    "buffer_load_dwordx4 v13, s[20:23], s45 offen offset:2048 nt lds\n"
    "v_lshlrev_b32 v38, 1, v51\n"
    "v_mfma_f32_16x16x32_bf16 v[176:179], a[16:19], a[176:179], v[176:179]\n"
    "v_add_u32 v38, s46, v38\n"
    "v_lshlrev_b32 v38, 3, v38\n"
    "v_mfma_f32_16x16x32_bf16 v[168:171], a[32:35], a[176:179], 0\n"
    "ds_read_b128 a[16:19], v33 offset:20480\n"
    "v_mul_u32_u24 v54, 3, v44\n"
    "v_mfma_f32_16x16x32_bf16 v[168:171], a[36:39], a[180:183], v[168:171]\n"
    "ds_read_b128 a[20:23], v34 offset:20480\n"
    "v_lshrrev_b64 v[52:53], v54, s[58:59]\n"
    "v_mfma_f32_16x16x32_bf16 v[168:171], a[40:43], a[184:187], v[168:171]\n"
    "buffer_load_dwordx4 v14, s[20:23], s45 offen offset:3072 nt lds\n"
    "ds_read_b128 a[24:27], v35 offset:20480\n"
    "v_and_b32 v52, 7, v52\n"
    "v_mfma_f32_16x16x32_bf16 v[168:171], a[44:47], a[188:191], v[168:171]\n"
    "ds_read_b128 a[28:31], v36 offset:20480\n"
    "v_cmp_le_u32 vcc, s57, v52\n"
    "s_nop 1\n"
    "v_mfma_f32_16x16x32_bf16 v[192:195], a[44:47], a[204:207], 0\n"
    "v_addc_co_u32 v52, vcc, 0, v52, vcc\n"
    "v_mov_b32 v54, s57\n"
    "v_mfma_f32_16x16x32_bf16 v[192:195], a[40:43], a[200:203], v[192:195]\n"
    "s_mov_b32 m0, 0\n"
    "s_mov_b32 s45, 2162688\n"
    "s_waitcnt vmcnt(18)\n"
    "buffer_load_dwordx4 v9, s[16:19], s45 offen nt lds\n"
    "v_cmp_gt_u32 vcc, 2, v44\n"
    "s_nop 1\n"
    "v_cndmask_b32 v52, v52, v54, vcc\n"
    "v_mfma_f32_16x16x32_bf16 v[192:195], a[36:39], a[196:199], v[192:195]\n"
    "v_mov_b32 v54, 0x3fe02\n"
    "v_bfe_u32 v54, v54, v44, 1\n"
    "v_mfma_f32_16x16x32_bf16 v[192:195], a[32:35], a[192:195], v[192:195]\n"
    "v_lshl_add_u32 v52, v54, 3, v52\n"
    "v_and_b32 v53, 1, v44\n"
    "v_mfma_f32_16x16x32_bf16 v[204:207], a[48:51], a[192:195], 0\n"
    "ds_read_b128 a[32:35], v33 offset:24576\n"
    "buffer_load_dwordx4 v10, s[16:19], s45 offen offset:1024 nt lds\n"
    "v_mov_b32 v54, s46\n"
    "v_mfma_f32_16x16x32_bf16 v[204:207], a[52:55], a[196:199], v[204:207]\n"
    "ds_read_b128 a[36:39], v34 offset:24576\n"
    "v_cmp_gt_u32 vcc, 14, v44\n"
    "s_nop 1\n"
    "v_mfma_f32_16x16x32_bf16 v[204:207], a[56:59], a[200:203], v[204:207]\n"
    "ds_read_b128 a[40:43], v35 offset:24576\n"
    "v_cndmask_b32 v54, v53, v54, vcc\n"
    "v_mfma_f32_16x16x32_bf16 v[204:207], a[60:63], a[204:207], v[204:207]\n"
    "ds_read_b128 a[44:47], v36 offset:24576\n"
    "s_waitcnt vmcnt(18)\n"
    "buffer_load_dwordx4 v11, s[16:19], s45 offen offset:2048 nt lds\n"
    "v_lshl_add_u32 v43, v52, 1, v54\n"
    "v_mfma_f32_16x16x32_bf16 v[180:183], a[60:63], a[188:191], 0\n"
    "v_mul_u32_u24 v43, 45056, v43\n"
    "v_cmp_gt_u32 vcc, 25, v44\n"
    "v_mfma_f32_16x16x32_bf16 v[180:183], a[56:59], a[184:187], v[180:183]\n"
    "v_subrev_u32 v56, 14, v44\n"
    "v_subrev_u32 v57, 11, v44\n"
    "v_mfma_f32_16x16x32_bf16 v[180:183], a[52:55], a[180:183], v[180:183]\n"
    "v_cndmask_b32 v56, v56, v57, vcc\n"
    "v_cndmask_b32 v57, 0, 1, vcc\n"
    "v_mfma_f32_16x16x32_bf16 v[180:183], a[48:51], a[176:179], v[180:183]\n"
    "buffer_load_dwordx4 v12, s[16:19], s45 offen offset:3072 nt lds\n"
    "v_cmp_gt_u32 vcc, 22, v44\n"
    "v_lshrrev_b32 v58, 1, v44\n"
    "ds_read_b128 a[48:51], v33 offset:28672\n"
    "ds_read_b128 a[52:55], v34 offset:28672\n"
    "ds_read_b128 a[56:59], v35 offset:28672\n"
    "ds_read_b128 a[60:63], v36 offset:28672\n"
    "v_and_b32 v59, 1, v44\n"
    "v_cndmask_b32 v56, v56, v58, vcc\n"
    "v_xor_b32 v59, 1, v59\n"
    "v_cndmask_b32 v57, v57, v59, vcc\n"
    "s_mov_b32 m0, 65536\n"
    "s_mov_b32 s45, 2129920\n"
    "s_waitcnt vmcnt(18)\n"
    "buffer_load_dwordx4 v13, s[20:23], s45 offen nt lds\n"
    "v_xor_b32 v57, s46, v57\n"
    "v_lshlrev_b32 v57, 7, v57\n"
    "v_mul_u32_u24 v54, 3, v56\n"
    "v_lshrrev_b64 v[52:53], v54, s[58:59]\n"
    "v_and_b32 v52, 7, v52\n"
    "v_cmp_le_u32 vcc, s57, v52\n"
    "s_nop 1\n"
    "v_addc_co_u32 v52, vcc, 0, v52, vcc\n"
    "v_mov_b32 v54, s57\n"
    "v_cmp_gt_u32 vcc, 2, v56\n"
    "s_nop 1\n"
    "buffer_load_dwordx4 v14, s[20:23], s45 offen offset:1024 nt lds\n"
    "v_cndmask_b32 v52, v52, v54, vcc\n"
    "v_mov_b32 v54, 0x3fe02\n"
    "v_bfe_u32 v54, v54, v56, 1\n"
    "v_lshl_add_u32 v52, v54, 3, v52\n"
    "v_and_b32 v53, 1, v56\n"
    "v_mov_b32 v54, s46\n"
    "v_cmp_gt_u32 vcc, 14, v56\n"
    "s_nop 1\n"
    "v_cndmask_b32 v54, v53, v54, vcc\n"
    "v_lshl_add_u32 v16, v52, 1, v54\n"
    "s_waitcnt vmcnt(18)\n"
    "buffer_load_dwordx4 v13, s[20:23], s45 offen offset:2048 nt lds\n"
    "v_lshl_add_u32 v16, v16, 18, v57\n"
    "v_and_b32 v52, 1, v48\n"
    "v_lshlrev_b32 v52, 4, v52\n"
    "v_mov_b32 v54, 0x3f80\n"
    "v_lshlrev_b32 v52, v52, v54\n"
    "v_cmp_eq_u32 vcc, v45, v47\n"
    "s_nop 1\n"
    "v_cndmask_b32 v52, 0, v52, vcc\n"
    "v_cmp_gt_u32 vcc, 2, v48\n"
    "s_nop 1\n"
    "buffer_load_dwordx4 v14, s[20:23], s45 offen offset:3072 nt lds\n"
    "v_cndmask_b32 v54, 0, v52, vcc\n"
    "v_sub_u32 v52, v52, v54\n"
    "v_accvgpr_write_b32 a232, v54\n"
    "v_accvgpr_write_b32 a233, v52\n"
    "v_accvgpr_write_b32 a234, 0\n"
    "v_accvgpr_write_b32 a235, 0\n"
    "v_accvgpr_write_b32 a236, 0\n"
    "v_accvgpr_write_b32 a237, 0\n"
    "v_accvgpr_write_b32 a238, v54\n"
    "v_accvgpr_write_b32 a239, v52\n"
    "v_mfma_f32_16x16x32_bf16 v[64:67], a[228:231], a[228:231], 0\n"
    "s_mov_b32 m0, 4096\n"
    "s_mov_b32 s45, 2166784\n"
    "s_waitcnt vmcnt(18)\n"
    "buffer_load_dwordx4 v9, s[16:19], s45 offen nt lds\n"
    "v_mfma_f32_16x16x32_bf16 v[68:71], a[228:231], a[228:231], 0\n"
    "v_mfma_f32_16x16x32_bf16 v[76:79], a[228:231], a[228:231], 0\n"
    "v_mfma_f32_16x16x32_bf16 v[80:83], a[228:231], a[228:231], 0\n"
    "v_mfma_f32_16x16x32_bf16 v[88:91], a[228:231], a[228:231], 0\n"
    "v_mfma_f32_16x16x32_bf16 v[92:95], a[228:231], a[228:231], 0\n"
    "v_mfma_f32_16x16x32_bf16 v[100:103], a[228:231], a[228:231], 0\n"
    "v_mfma_f32_16x16x32_bf16 v[104:107], a[228:231], a[228:231], 0\n"
    "v_mfma_f32_16x16x32_bf16 v[112:115], a[228:231], a[228:231], 0\n"
    "v_mfma_f32_16x16x32_bf16 v[116:119], a[228:231], a[228:231], 0\n"
    "v_mfma_f32_16x16x32_bf16 v[124:127], a[228:231], a[228:231], 0\n"
    "buffer_load_dwordx4 v10, s[16:19], s45 offen offset:1024 nt lds\n"
    "v_mfma_f32_16x16x32_bf16 v[128:131], a[228:231], a[228:231], 0\n"
    "v_mfma_f32_16x16x32_bf16 v[136:139], a[228:231], a[228:231], 0\n"
    "v_mfma_f32_16x16x32_bf16 v[140:143], a[228:231], a[228:231], 0\n"
    "v_mfma_f32_16x16x32_bf16 v[148:151], a[228:231], a[228:231], 0\n"
    "v_mfma_f32_16x16x32_bf16 v[152:155], a[228:231], a[228:231], 0\n"
    "v_mov_b32 v244, 0\n"
    "v_mov_b32 v245, 0\n"
    "v_mov_b32 v246, 0\n"
    "v_mov_b32 v247, 0\n"
    "v_mov_b32 v248, 0\n"
    "v_mov_b32 v249, 0\n"
    "s_waitcnt vmcnt(18)\n"
    "buffer_load_dwordx4 v11, s[16:19], s45 offen offset:2048 nt lds\n"
    "v_mov_b32 v250, 0\n"
    "v_mov_b32 v251, 0\n"
    "v_max3_f32 v44, v160, v161, v162\n"
    "v_max3_f32 v44, v44, v163, v176\n"
    "v_max3_f32 v44, v44, v177, v178\n"
    "v_max3_f32 v44, v44, v179, v168\n"
    "v_max3_f32 v44, v44, v169, v170\n"
    "v_max3_f32 v44, v44, v171, v180\n"
    "v_max3_f32 v44, v44, v181, v182\n"
    "v_max_f32 v44, v44, v183\n"
    "buffer_load_dwordx4 v12, s[16:19], s45 offen offset:3072 nt lds\n"
    "v_max3_f32 v46, v184, v185, v186\n"
    "v_max3_f32 v46, v46, v187, v200\n"
    "v_max3_f32 v46, v46, v201, v202\n"
    "v_max3_f32 v46, v46, v203, v192\n"
    "v_max3_f32 v46, v46, v193, v194\n"
    "v_max3_f32 v46, v46, v195, v204\n"
    "v_max3_f32 v46, v46, v205, v206\n"
    "v_max_f32 v46, v46, v207\n"
    "v_mov_b32 v45, v44\n"
    "v_mov_b32 v47, v46\n"
    "s_nop 1\n"
    "s_mov_b32 m0, 69632\n"
    "s_mov_b32 s45, 2134016\n"
    "s_waitcnt vmcnt(18)\n"
    "buffer_load_dwordx4 v13, s[20:23], s45 offen nt lds\n"
    "v_permlane32_swap_b32 v44, v45\n"
    "v_permlane32_swap_b32 v46, v47\n"
    "v_max_f32 v44, v44, v45\n"
    "v_max_f32 v46, v46, v47\n"
    "v_mov_b32 v45, v44\n"
    "v_mov_b32 v47, v46\n"
    "s_nop 1\n"
    "v_permlane16_swap_b32 v44, v45\n"
    "v_permlane16_swap_b32 v46, v47\n"
    "v_max_f32 v44, v44, v45\n"
    "buffer_load_dwordx4 v14, s[20:23], s45 offen offset:1024 nt lds\n"
    "v_max_f32 v46, v46, v47\n"
    "v_sub_f32 v232, 0, v44\n"
    "v_sub_f32 v233, 0, v44\n"
    "v_sub_f32 v234, 0, v44\n"
    "v_sub_f32 v235, 0, v44\n"
    "v_sub_f32 v236, 0, v46\n"
    "v_sub_f32 v237, 0, v46\n"
    "v_sub_f32 v238, 0, v46\n"
    "v_sub_f32 v239, 0, v46\n"
    "v_pk_add_f32 v[160:161], v[232:233], v[160:161]\n"
    "v_pk_add_f32 v[162:163], v[232:233], v[162:163]\n"
    "s_waitcnt vmcnt(18)\n"
    "buffer_load_dwordx4 v13, s[20:23], s45 offen offset:2048 nt lds\n"
    "v_pk_add_f32 v[184:185], v[236:237], v[184:185]\n"
    "v_pk_add_f32 v[186:187], v[236:237], v[186:187]\n"
    "v_pk_add_f32 v[176:177], v[232:233], v[176:177]\n"
    "v_pk_add_f32 v[178:179], v[232:233], v[178:179]\n"
    "v_pk_add_f32 v[200:201], v[236:237], v[200:201]\n"
    "v_pk_add_f32 v[202:203], v[236:237], v[202:203]\n"
    "v_pk_add_f32 v[168:169], v[232:233], v[168:169]\n"
    "v_pk_add_f32 v[170:171], v[232:233], v[170:171]\n"
    "v_pk_add_f32 v[192:193], v[236:237], v[192:193]\n"
    "v_pk_add_f32 v[194:195], v[236:237], v[194:195]\n"
    "buffer_load_dwordx4 v14, s[20:23], s45 offen offset:3072 nt lds\n"
    "v_pk_add_f32 v[180:181], v[232:233], v[180:181]\n"
    "v_pk_add_f32 v[182:183], v[232:233], v[182:183]\n"
    "v_pk_add_f32 v[204:205], v[236:237], v[204:205]\n"
    "v_pk_add_f32 v[206:207], v[236:237], v[206:207]\n"
    "v_exp_f32 v160, v160\n"
    "v_exp_f32 v161, v161\n"
    "v_exp_f32 v162, v162\n"
    "v_exp_f32 v163, v163\n"
    "v_exp_f32 v184, v184\n"
    "v_exp_f32 v185, v185\n"
    "v_exp_f32 v186, v186\n"
    "s_mov_b32 m0, 8192\n"
    "s_mov_b32 s45, 2170880\n"
    "s_waitcnt vmcnt(18)\n"
    "buffer_load_dwordx4 v9, s[16:19], s45 offen nt lds\n"
    "v_exp_f32 v187, v187\n"
    "v_exp_f32 v176, v176\n"
    "v_exp_f32 v177, v177\n"
    "v_exp_f32 v178, v178\n"
    "v_exp_f32 v179, v179\n"
    "v_exp_f32 v200, v200\n"
    "v_exp_f32 v201, v201\n"
    "v_exp_f32 v202, v202\n"
    "v_exp_f32 v203, v203\n"
    "v_exp_f32 v168, v168\n"
    "buffer_load_dwordx4 v10, s[16:19], s45 offen offset:1024 nt lds\n"
    "v_exp_f32 v169, v169\n"
    "v_exp_f32 v170, v170\n"
    "v_exp_f32 v171, v171\n"
    "v_exp_f32 v192, v192\n"
    "v_exp_f32 v193, v193\n"
    "v_exp_f32 v194, v194\n"
    "v_exp_f32 v195, v195\n"
    "v_exp_f32 v180, v180\n"
    "v_exp_f32 v181, v181\n"
    "v_exp_f32 v182, v182\n"
    "v_exp_f32 v183, v183\n"
    "s_waitcnt vmcnt(18)\n"
    "buffer_load_dwordx4 v11, s[16:19], s45 offen offset:2048 nt lds\n"
    "v_exp_f32 v204, v204\n"
    "v_exp_f32 v205, v205\n"
    "v_exp_f32 v206, v206\n"
    "v_exp_f32 v207, v207\n"
    "v_cvt_pk_bf16_f32 v160, v160, v161\n"
    "v_cvt_pk_bf16_f32 v161, v162, v163\n"
    "v_cvt_pk_bf16_f32 v184, v184, v185\n"
    "v_cvt_pk_bf16_f32 v185, v186, v187\n"
    "v_cvt_pk_bf16_f32 v162, v176, v177\n"
    "v_cvt_pk_bf16_f32 v163, v178, v179\n"
    "buffer_load_dwordx4 v12, s[16:19], s45 offen offset:3072 nt lds\n"
    "v_cvt_pk_bf16_f32 v186, v200, v201\n"
    "v_cvt_pk_bf16_f32 v187, v202, v203\n"
    "v_cvt_pk_bf16_f32 v168, v168, v169\n"
    "v_cvt_pk_bf16_f32 v169, v170, v171\n"
    "v_cvt_pk_bf16_f32 v192, v192, v193\n"
    "v_cvt_pk_bf16_f32 v193, v194, v195\n"
    "v_cvt_pk_bf16_f32 v170, v180, v181\n"
    "v_cvt_pk_bf16_f32 v171, v182, v183\n"
    "v_cvt_pk_bf16_f32 v194, v204, v205\n"
    "v_cvt_pk_bf16_f32 v195, v206, v207\n"
    "s_waitcnt vmcnt(52) lgkmcnt(0)\n"
    "s_barrier\n"
    "v_mfma_f32_16x16x32_bf16 v[164:167], a[0:3], a[176:179], v[232:235]\n"
    "ds_read_b64_tr_b16 a[64:65], v25 offset:0\n"
    "s_mov_b32 m0, 73728\n"
    "s_mov_b32 s45, 2138112\n"
    "s_waitcnt vmcnt(12)\n"
    "buffer_load_dwordx4 v13, s[20:23], s45 offen nt lds\n"
    "v_mfma_f32_16x16x32_bf16 v[164:167], a[4:7], a[180:183], v[164:167]\n"
    "ds_read_b64_tr_b16 a[66:67], v25 offset:4096\n"
    "v_mfma_f32_16x16x32_bf16 v[164:167], a[8:11], a[184:187], v[164:167]\n"
    "ds_read_b64_tr_b16 a[68:69], v25 offset:8192\n"
    "buffer_load_dwordx4 v14, s[20:23], s45 offen offset:1024 nt lds\n"
    "v_mfma_f32_16x16x32_bf16 v[164:167], a[12:15], a[188:191], v[164:167]\n"
    "ds_read_b64_tr_b16 a[70:71], v25 offset:12288\n"
    "v_add_u32 v25, 0x4000, v25\n"
    "v_mfma_f32_16x16x32_bf16 v[188:191], a[12:15], a[204:207], v[236:239]\n"
    "ds_read_b64_tr_b16 a[72:73], v26 offset:0\n"
    "v_mfma_f32_16x16x32_bf16 v[188:191], a[8:11], a[200:203], v[188:191]\n"
    "ds_read_b64_tr_b16 a[74:75], v26 offset:4096\n"
    "s_waitcnt vmcnt(12)\n"
    "buffer_load_dwordx4 v13, s[20:23], s45 offen offset:2048 nt lds\n"
    "v_mfma_f32_16x16x32_bf16 v[188:191], a[4:7], a[196:199], v[188:191]\n"
    "ds_read_b64_tr_b16 a[76:77], v26 offset:8192\n"
    "v_mfma_f32_16x16x32_bf16 v[188:191], a[0:3], a[192:195], v[188:191]\n"
    "ds_read_b64_tr_b16 a[78:79], v26 offset:12288\n"
    "v_add_u32 v26, 0x4000, v26\n"
    "v_mfma_f32_16x16x32_bf16 v[200:203], a[16:19], a[192:195], v[236:239]\n"
    "ds_read_b64_tr_b16 a[80:81], v27 offset:0\n"
    "buffer_load_dwordx4 v14, s[20:23], s45 offen offset:3072 nt lds\n"
    "v_mfma_f32_16x16x32_bf16 v[200:203], a[20:23], a[196:199], v[200:203]\n"
    "ds_read_b64_tr_b16 a[82:83], v27 offset:4096\n"
    "v_mfma_f32_16x16x32_bf16 v[200:203], a[24:27], a[200:203], v[200:203]\n"
    "ds_read_b64_tr_b16 a[84:85], v27 offset:8192\n"
    "v_mfma_f32_16x16x32_bf16 v[200:203], a[28:31], a[204:207], v[200:203]\n"
    "ds_read_b64_tr_b16 a[86:87], v27 offset:12288\n"
    "v_add_u32 v27, 0x4000, v27\n"
    "s_mov_b32 m0, 12288\n"
    "s_mov_b32 s45, 2174976\n"
    "s_waitcnt vmcnt(12)\n"
    "buffer_load_dwordx4 v9, s[16:19], s45 offen nt lds\n"
    "v_mfma_f32_16x16x32_bf16 v[176:179], a[28:31], a[188:191], v[232:235]\n"
    "ds_read_b64_tr_b16 a[88:89], v28 offset:0\n"
    "v_xor_b32 v33, 0x8000, v33\n"
    "v_mfma_f32_16x16x32_bf16 v[176:179], a[24:27], a[184:187], v[176:179]\n"
    "ds_read_b64_tr_b16 a[90:91], v28 offset:4096\n"
    "v_mfma_f32_16x16x32_bf16 v[176:179], a[20:23], a[180:183], v[176:179]\n"
    "ds_read_b64_tr_b16 a[92:93], v28 offset:8192\n"
    "v_xor_b32 v34, 0x8000, v34\n"
    "buffer_load_dwordx4 v10, s[16:19], s45 offen offset:1024 nt lds\n"
    "v_mfma_f32_16x16x32_bf16 v[176:179], a[16:19], a[176:179], v[176:179]\n"
    "ds_read_b64_tr_b16 a[94:95], v28 offset:12288\n"
    "v_add_u32 v28, 0x4000, v28\n"
    "v_mfma_f32_16x16x32_bf16 v[172:175], a[32:35], a[176:179], v[232:235]\n"
    "ds_read_b64_tr_b16 a[96:97], v29 offset:0\n"
    "v_xor_b32 v35, 0x8000, v35\n"
    "v_mfma_f32_16x16x32_bf16 v[172:175], a[36:39], a[180:183], v[172:175]\n"
    "ds_read_b64_tr_b16 a[98:99], v29 offset:4096\n"
    "s_waitcnt vmcnt(12)\n"
    "buffer_load_dwordx4 v11, s[16:19], s45 offen offset:2048 nt lds\n"
    "v_mfma_f32_16x16x32_bf16 v[172:175], a[40:43], a[184:187], v[172:175]\n"
    "ds_read_b64_tr_b16 a[100:101], v29 offset:8192\n"
    "v_xor_b32 v36, 0x8000, v36\n"
    "v_mfma_f32_16x16x32_bf16 v[172:175], a[44:47], a[188:191], v[172:175]\n"
    "ds_read_b64_tr_b16 a[102:103], v29 offset:12288\n"
    "v_add_u32 v29, 0x4000, v29\n"
    "buffer_load_dwordx4 v12, s[16:19], s45 offen offset:3072 nt lds\n"
    "v_mfma_f32_16x16x32_bf16 v[196:199], a[44:47], a[204:207], v[236:239]\n"
    "ds_read_b64_tr_b16 a[104:105], v30 offset:0\n"
    "v_mfma_f32_16x16x32_bf16 v[196:199], a[40:43], a[200:203], v[196:199]\n"
    "ds_read_b64_tr_b16 a[106:107], v30 offset:4096\n"
    "v_mfma_f32_16x16x32_bf16 v[196:199], a[36:39], a[196:199], v[196:199]\n"
    "ds_read_b64_tr_b16 a[108:109], v30 offset:8192\n"
    "s_mov_b32 m0, 77824\n"
    "s_mov_b32 s45, 2142208\n"
    "s_waitcnt vmcnt(12)\n"
    "buffer_load_dwordx4 v13, s[20:23], s45 offen nt lds\n"
    "v_mfma_f32_16x16x32_bf16 v[196:199], a[32:35], a[192:195], v[196:199]\n"
    "ds_read_b64_tr_b16 a[110:111], v30 offset:12288\n"
    "v_add_u32 v30, 0x4000, v30\n"
    "v_mfma_f32_16x16x32_bf16 v[204:207], a[48:51], a[192:195], v[236:239]\n"
    "ds_read_b64_tr_b16 a[112:113], v31 offset:0\n"
    "v_mfma_f32_16x16x32_bf16 v[204:207], a[52:55], a[196:199], v[204:207]\n"
    "ds_read_b64_tr_b16 a[114:115], v31 offset:4096\n"
    "buffer_load_dwordx4 v14, s[20:23], s45 offen offset:1024 nt lds\n"
    "v_mfma_f32_16x16x32_bf16 v[204:207], a[56:59], a[200:203], v[204:207]\n"
    "ds_read_b64_tr_b16 a[116:117], v31 offset:8192\n"
    "v_mfma_f32_16x16x32_bf16 v[204:207], a[60:63], a[204:207], v[204:207]\n"
    "ds_read_b64_tr_b16 a[118:119], v31 offset:12288\n"
    "v_add_u32 v31, 0x4000, v31\n"
    "v_mfma_f32_16x16x32_bf16 v[180:183], a[60:63], a[188:191], v[232:235]\n"
    "ds_read_b64_tr_b16 a[120:121], v32 offset:0\n"
    "s_waitcnt vmcnt(12)\n"
    "buffer_load_dwordx4 v13, s[20:23], s45 offen offset:2048 nt lds\n"
    "v_mfma_f32_16x16x32_bf16 v[180:183], a[56:59], a[184:187], v[180:183]\n"
    "ds_read_b64_tr_b16 a[122:123], v32 offset:4096\n"
    "v_mfma_f32_16x16x32_bf16 v[180:183], a[52:55], a[180:183], v[180:183]\n"
    "ds_read_b64_tr_b16 a[124:125], v32 offset:8192\n"
    "v_mfma_f32_16x16x32_bf16 v[180:183], a[48:51], a[176:179], v[180:183]\n"
    "ds_read_b64_tr_b16 a[126:127], v32 offset:12288\n"
    "v_add_u32 v32, 0x4000, v32\n"
    "buffer_load_dwordx4 v14, s[20:23], s45 offen offset:3072 nt lds\n"
    "s_waitcnt lgkmcnt(0)\n"
    "v_mfma_f32_16x16x32_bf16 v[64:67], a[64:67], v[160:163], v[64:67]\n"
    "ds_read_b128 a[0:3], v33 offset:0\n"
    "v_exp_f32 v164, v164\n"
    "v_mfma_f32_16x16x32_bf16 v[64:67], a[68:71], v[168:171], v[64:67]\n"
    "v_exp_f32 v165, v165\n"
    "v_mfma_f32_16x16x32_bf16 v[68:71], a[68:71], v[192:195], v[68:71]\n"
    "s_mov_b32 m0, 16384\n"
    "s_mov_b32 s45, 2179072\n"
    "s_waitcnt vmcnt(12)\n"
    "buffer_load_dwordx4 v9, s[16:19], s45 offen nt lds\n"
    "ds_read_b128 a[4:7], v34 offset:0\n"
    "v_exp_f32 v166, v166\n"
    "v_cvt_pk_bf16_f32 v164, v164, v165\n"
    "v_mfma_f32_16x16x32_bf16 v[68:71], a[64:67], v[184:187], v[68:71]\n"
    "v_exp_f32 v167, v167\n"
    "v_mfma_f32_16x16x32_bf16 v[80:83], a[72:75], v[184:187], v[80:83]\n"
    "buffer_load_dwordx4 v10, s[16:19], s45 offen offset:1024 nt lds\n"
    "ds_read_b128 a[8:11], v35 offset:0\n"
    "v_exp_f32 v188, v188\n"
    "v_cvt_pk_bf16_f32 v165, v166, v167\n"
    "v_mfma_f32_16x16x32_bf16 v[80:83], a[76:79], v[192:195], v[80:83]\n"
    "v_exp_f32 v189, v189\n"
    "v_mfma_f32_16x16x32_bf16 v[76:79], a[76:79], v[168:171], v[76:79]\n"
    "ds_read_b128 a[12:15], v36 offset:0\n"
    "v_exp_f32 v190, v190\n"
    "v_cvt_pk_bf16_f32 v188, v188, v189\n"
    "v_mfma_f32_16x16x32_bf16 v[76:79], a[72:75], v[160:163], v[76:79]\n"
    "s_waitcnt vmcnt(12)\n"
    "buffer_load_dwordx4 v11, s[16:19], s45 offen offset:2048 nt lds\n"
    "v_exp_f32 v191, v191\n"
    "v_mfma_f32_16x16x32_bf16 v[88:91], a[80:83], v[160:163], v[88:91]\n"
    "ds_read_b128 a[16:19], v33 offset:4096\n"
    "v_exp_f32 v176, v176\n"
    "v_cvt_pk_bf16_f32 v189, v190, v191\n"
    "v_mfma_f32_16x16x32_bf16 v[88:91], a[84:87], v[168:171], v[88:91]\n"
    "v_exp_f32 v177, v177\n"
    "v_mfma_f32_16x16x32_bf16 v[92:95], a[84:87], v[192:195], v[92:95]\n"
    "buffer_load_dwordx4 v12, s[16:19], s45 offen offset:3072 nt lds\n"
    "ds_read_b128 a[20:23], v34 offset:4096\n"
    "v_exp_f32 v178, v178\n"
    "v_cvt_pk_bf16_f32 v166, v176, v177\n"
    "v_mfma_f32_16x16x32_bf16 v[92:95], a[80:83], v[184:187], v[92:95]\n"
    "v_exp_f32 v179, v179\n"
    "v_mfma_f32_16x16x32_bf16 v[104:107], a[88:91], v[184:187], v[104:107]\n"
    "ds_read_b128 a[24:27], v35 offset:4096\n"
    "v_exp_f32 v200, v200\n"
    "v_cvt_pk_bf16_f32 v167, v178, v179\n"
    "v_mfma_f32_16x16x32_bf16 v[104:107], a[92:95], v[192:195], v[104:107]\n"
    "s_mov_b32 m0, 81920\n"
    "s_mov_b32 s45, 2146304\n"
    "s_waitcnt vmcnt(12)\n"
    "buffer_load_dwordx4 v13, s[20:23], s45 offen nt lds\n"
    "v_exp_f32 v201, v201\n"
    "v_mfma_f32_16x16x32_bf16 v[100:103], a[92:95], v[168:171], v[100:103]\n"
    "ds_read_b128 a[28:31], v36 offset:4096\n"
    "v_exp_f32 v202, v202\n"
    "v_cvt_pk_bf16_f32 v190, v200, v201\n"
    "v_mfma_f32_16x16x32_bf16 v[100:103], a[88:91], v[160:163], v[100:103]\n"
    "v_exp_f32 v203, v203\n"
    "v_mfma_f32_16x16x32_bf16 v[112:115], a[96:99], v[160:163], v[112:115]\n"
    "buffer_load_dwordx4 v14, s[20:23], s45 offen offset:1024 nt lds\n"
    "ds_read_b128 a[32:35], v33 offset:8192\n"
    "v_exp_f32 v172, v172\n"
    "v_cvt_pk_bf16_f32 v191, v202, v203\n"
    "v_mfma_f32_16x16x32_bf16 v[112:115], a[100:103], v[168:171], v[112:115]\n"
    "v_exp_f32 v173, v173\n"
    "v_mfma_f32_16x16x32_bf16 v[116:119], a[100:103], v[192:195], v[116:119]\n"
    "ds_read_b128 a[36:39], v34 offset:8192\n"
    "v_exp_f32 v174, v174\n"
    "v_cvt_pk_bf16_f32 v172, v172, v173\n"
    "v_mfma_f32_16x16x32_bf16 v[116:119], a[96:99], v[184:187], v[116:119]\n"
    "s_waitcnt vmcnt(12)\n"
    "buffer_load_dwordx4 v13, s[20:23], s45 offen offset:2048 nt lds\n"
    "v_exp_f32 v175, v175\n"
    "v_mfma_f32_16x16x32_bf16 v[128:131], a[104:107], v[184:187], v[128:131]\n"
    "ds_read_b128 a[40:43], v35 offset:8192\n"
    "v_exp_f32 v196, v196\n"
    "v_cvt_pk_bf16_f32 v173, v174, v175\n"
    "v_mfma_f32_16x16x32_bf16 v[128:131], a[108:111], v[192:195], v[128:131]\n"
    "buffer_load_dwordx4 v14, s[20:23], s45 offen offset:3072 nt lds\n"
    "v_exp_f32 v197, v197\n"
    "v_mfma_f32_16x16x32_bf16 v[124:127], a[108:111], v[168:171], v[124:127]\n"
    "ds_read_b128 a[44:47], v36 offset:8192\n"
    "v_exp_f32 v198, v198\n"
    "v_cvt_pk_bf16_f32 v196, v196, v197\n"
    "v_mfma_f32_16x16x32_bf16 v[124:127], a[104:107], v[160:163], v[124:127]\n"
    "v_exp_f32 v199, v199\n"
    "v_mfma_f32_16x16x32_bf16 v[136:139], a[112:115], v[160:163], v[136:139]\n"
    "s_mov_b32 m0, 20480\n"
    "s_mov_b32 s45, 2183168\n"
    "s_waitcnt vmcnt(12)\n"
    "buffer_load_dwordx4 v9, s[16:19], s45 offen nt lds\n"
    "ds_read_b128 a[48:51], v33 offset:12288\n"
    "v_exp_f32 v180, v180\n"
    "v_cvt_pk_bf16_f32 v197, v198, v199\n"
    "v_mfma_f32_16x16x32_bf16 v[136:139], a[116:119], v[168:171], v[136:139]\n"
    "v_exp_f32 v181, v181\n"
    "v_mfma_f32_16x16x32_bf16 v[140:143], a[116:119], v[192:195], v[140:143]\n"
    "ds_read_b128 a[52:55], v34 offset:12288\n"
    "v_exp_f32 v182, v182\n"
    "v_cvt_pk_bf16_f32 v174, v180, v181\n"
    "v_mfma_f32_16x16x32_bf16 v[140:143], a[112:115], v[184:187], v[140:143]\n"
    "buffer_load_dwordx4 v10, s[16:19], s45 offen offset:1024 nt lds\n"
    "v_exp_f32 v183, v183\n"
    "v_mfma_f32_16x16x32_bf16 v[152:155], a[120:123], v[184:187], v[152:155]\n"
    "ds_read_b128 a[56:59], v35 offset:12288\n"
    "v_exp_f32 v204, v204\n"
    "v_cvt_pk_bf16_f32 v175, v182, v183\n"
    "v_mfma_f32_16x16x32_bf16 v[152:155], a[124:127], v[192:195], v[152:155]\n"
    "v_exp_f32 v205, v205\n"
    "v_mfma_f32_16x16x32_bf16 v[148:151], a[124:127], v[168:171], v[148:151]\n"
    "s_waitcnt vmcnt(12)\n"
    "buffer_load_dwordx4 v11, s[16:19], s45 offen offset:2048 nt lds\n"
    "ds_read_b128 a[60:63], v36 offset:12288\n"
    "v_exp_f32 v206, v206\n"
    "v_cvt_pk_bf16_f32 v198, v204, v205\n"
    "v_mfma_f32_16x16x32_bf16 v[148:151], a[120:123], v[160:163], v[148:151]\n"
    "v_exp_f32 v207, v207\n"
    "v_mfma_f32_16x16x32_bf16 v[244:247], a[224:227], v[160:163], v[244:247]\n"
    "v_mfma_f32_16x16x32_bf16 v[244:247], a[224:227], v[168:171], v[244:247]\n"
    "buffer_load_dwordx4 v12, s[16:19], s45 offen offset:3072 nt lds\n"
    "v_cvt_pk_bf16_f32 v199, v206, v207\n"
    "v_mfma_f32_16x16x32_bf16 v[248:251], a[224:227], v[184:187], v[248:251]\n"
    "v_mfma_f32_16x16x32_bf16 v[248:251], a[224:227], v[192:195], v[248:251]\n"
    "s_waitcnt vmcnt(44) lgkmcnt(0)\n"
    "s_barrier\n"
    "v_mfma_f32_16x16x32_bf16 v[160:163], a[0:3], a[176:179], v[232:235]\n"
    "ds_read_b64_tr_b16 a[64:65], v25 offset:0\n"
    "s_mov_b32 m0, 86016\n"
    "s_mov_b32 s45, 2150400\n"
    "s_waitcnt vmcnt(12)\n"
    "buffer_load_dwordx4 v13, s[20:23], s45 offen nt lds\n"
    "v_mfma_f32_16x16x32_bf16 v[160:163], a[4:7], a[180:183], v[160:163]\n"
    "ds_read_b64_tr_b16 a[66:67], v25 offset:4096\n"
    "v_mfma_f32_16x16x32_bf16 v[160:163], a[8:11], a[184:187], v[160:163]\n"
    "ds_read_b64_tr_b16 a[68:69], v25 offset:8192\n"
    "buffer_load_dwordx4 v14, s[20:23], s45 offen offset:1024 nt lds\n"
    "v_mfma_f32_16x16x32_bf16 v[160:163], a[12:15], a[188:191], v[160:163]\n"
    "ds_read_b64_tr_b16 a[70:71], v25 offset:12288\n"
    "v_add_u32 v25, 0xffff0000, v25\n"
    "v_mfma_f32_16x16x32_bf16 v[184:187], a[12:15], a[204:207], v[236:239]\n"
    "ds_read_b64_tr_b16 a[72:73], v26 offset:0\n"
    "v_mfma_f32_16x16x32_bf16 v[184:187], a[8:11], a[200:203], v[184:187]\n"
    "ds_read_b64_tr_b16 a[74:75], v26 offset:4096\n"
    "s_waitcnt vmcnt(12)\n"
    "buffer_load_dwordx4 v13, s[20:23], s45 offen offset:2048 nt lds\n"
    "v_mfma_f32_16x16x32_bf16 v[184:187], a[4:7], a[196:199], v[184:187]\n"
    "ds_read_b64_tr_b16 a[76:77], v26 offset:8192\n"
    "v_mfma_f32_16x16x32_bf16 v[184:187], a[0:3], a[192:195], v[184:187]\n"
    "ds_read_b64_tr_b16 a[78:79], v26 offset:12288\n"
    "v_add_u32 v26, 0xffff0000, v26\n"
    "v_mfma_f32_16x16x32_bf16 v[200:203], a[16:19], a[192:195], v[236:239]\n"
    "ds_read_b64_tr_b16 a[80:81], v27 offset:0\n"
    "buffer_load_dwordx4 v14, s[20:23], s45 offen offset:3072 nt lds\n"
    "v_mfma_f32_16x16x32_bf16 v[200:203], a[20:23], a[196:199], v[200:203]\n"
    "ds_read_b64_tr_b16 a[82:83], v27 offset:4096\n"
    "v_mfma_f32_16x16x32_bf16 v[200:203], a[24:27], a[200:203], v[200:203]\n"
    "ds_read_b64_tr_b16 a[84:85], v27 offset:8192\n"
    "v_mfma_f32_16x16x32_bf16 v[200:203], a[28:31], a[204:207], v[200:203]\n"
    "ds_read_b64_tr_b16 a[86:87], v27 offset:12288\n"
    "v_add_u32 v27, 0xffff0000, v27\n"
    "s_mov_b32 m0, 24576\n"
    "s_mov_b32 s45, 2187264\n"
    "s_waitcnt vmcnt(12)\n"
    "buffer_load_dwordx4 v9, s[16:19], s45 offen nt lds\n"
    "v_mfma_f32_16x16x32_bf16 v[176:179], a[28:31], a[188:191], v[232:235]\n"
    "ds_read_b64_tr_b16 a[88:89], v28 offset:0\n"
    "v_mfma_f32_16x16x32_bf16 v[176:179], a[24:27], a[184:187], v[176:179]\n"
    "ds_read_b64_tr_b16 a[90:91], v28 offset:4096\n"
    "v_mfma_f32_16x16x32_bf16 v[176:179], a[20:23], a[180:183], v[176:179]\n"
    "ds_read_b64_tr_b16 a[92:93], v28 offset:8192\n"
    "buffer_load_dwordx4 v10, s[16:19], s45 offen offset:1024 nt lds\n"
    "v_mfma_f32_16x16x32_bf16 v[176:179], a[16:19], a[176:179], v[176:179]\n"
    "ds_read_b64_tr_b16 a[94:95], v28 offset:12288\n"
    "v_add_u32 v28, 0xffff0000, v28\n"
    "v_mfma_f32_16x16x32_bf16 v[168:171], a[32:35], a[176:179], v[232:235]\n"
    "ds_read_b64_tr_b16 a[96:97], v29 offset:0\n"
    "v_mfma_f32_16x16x32_bf16 v[168:171], a[36:39], a[180:183], v[168:171]\n"
    "ds_read_b64_tr_b16 a[98:99], v29 offset:4096\n"
    "s_waitcnt vmcnt(12)\n"
    "buffer_load_dwordx4 v11, s[16:19], s45 offen offset:2048 nt lds\n"
    "v_mfma_f32_16x16x32_bf16 v[168:171], a[40:43], a[184:187], v[168:171]\n"
    "ds_read_b64_tr_b16 a[100:101], v29 offset:8192\n"
    "v_mfma_f32_16x16x32_bf16 v[168:171], a[44:47], a[188:191], v[168:171]\n"
    "ds_read_b64_tr_b16 a[102:103], v29 offset:12288\n"
    "v_add_u32 v29, 0xffff0000, v29\n"
    "buffer_load_dwordx4 v12, s[16:19], s45 offen offset:3072 nt lds\n"
    "v_mfma_f32_16x16x32_bf16 v[192:195], a[44:47], a[204:207], v[236:239]\n"
    "ds_read_b64_tr_b16 a[104:105], v30 offset:0\n"
    "v_mfma_f32_16x16x32_bf16 v[192:195], a[40:43], a[200:203], v[192:195]\n"
    "ds_read_b64_tr_b16 a[106:107], v30 offset:4096\n"
    "v_mfma_f32_16x16x32_bf16 v[192:195], a[36:39], a[196:199], v[192:195]\n"
    "ds_read_b64_tr_b16 a[108:109], v30 offset:8192\n"
    "s_mov_b32 m0, 90112\n"
    "s_mov_b32 s45, 2154496\n"
    "s_waitcnt vmcnt(12)\n"
    "buffer_load_dwordx4 v13, s[20:23], s45 offen nt lds\n"
    "v_mfma_f32_16x16x32_bf16 v[192:195], a[32:35], a[192:195], v[192:195]\n"
    "ds_read_b64_tr_b16 a[110:111], v30 offset:12288\n"
    "v_add_u32 v30, 0xffff0000, v30\n"
    "v_mfma_f32_16x16x32_bf16 v[204:207], a[48:51], a[192:195], v[236:239]\n"
    "ds_read_b64_tr_b16 a[112:113], v31 offset:0\n"
    "v_mfma_f32_16x16x32_bf16 v[204:207], a[52:55], a[196:199], v[204:207]\n"
    "ds_read_b64_tr_b16 a[114:115], v31 offset:4096\n"
    "buffer_load_dwordx4 v14, s[20:23], s45 offen offset:1024 nt lds\n"
    "v_mfma_f32_16x16x32_bf16 v[204:207], a[56:59], a[200:203], v[204:207]\n"
    "ds_read_b64_tr_b16 a[116:117], v31 offset:8192\n"
    "v_mfma_f32_16x16x32_bf16 v[204:207], a[60:63], a[204:207], v[204:207]\n"
    "ds_read_b64_tr_b16 a[118:119], v31 offset:12288\n"
    "v_add_u32 v31, 0xffff0000, v31\n"
    "v_mfma_f32_16x16x32_bf16 v[180:183], a[60:63], a[188:191], v[232:235]\n"
    "ds_read_b64_tr_b16 a[120:121], v32 offset:0\n"
    "s_waitcnt vmcnt(12)\n"
    "buffer_load_dwordx4 v13, s[20:23], s45 offen offset:2048 nt lds\n"
    "v_mfma_f32_16x16x32_bf16 v[180:183], a[56:59], a[184:187], v[180:183]\n"
    "ds_read_b64_tr_b16 a[122:123], v32 offset:4096\n"
    "v_mfma_f32_16x16x32_bf16 v[180:183], a[52:55], a[180:183], v[180:183]\n"
    "ds_read_b64_tr_b16 a[124:125], v32 offset:8192\n"
    "v_mfma_f32_16x16x32_bf16 v[180:183], a[48:51], a[176:179], v[180:183]\n"
    "ds_read_b64_tr_b16 a[126:127], v32 offset:12288\n"
    "v_add_u32 v32, 0xffff0000, v32\n"
    "buffer_load_dwordx4 v14, s[20:23], s45 offen offset:3072 nt lds\n"
    "s_waitcnt lgkmcnt(0)\n"
    "v_mfma_f32_16x16x32_bf16 v[64:67], a[64:67], v[164:167], v[64:67]\n"
    "ds_read_b128 a[0:3], v33 offset:16384\n"
    "v_exp_f32 v160, v160\n"
    "v_mfma_f32_16x16x32_bf16 v[64:67], a[68:71], v[172:175], v[64:67]\n"
    "v_exp_f32 v161, v161\n"
    "v_mfma_f32_16x16x32_bf16 v[68:71], a[68:71], v[196:199], v[68:71]\n"
    "s_mov_b32 m0, 28672\n"
    "s_mov_b32 s45, 2191360\n"
    "s_waitcnt vmcnt(12)\n"
    "buffer_load_dwordx4 v9, s[16:19], s45 offen nt lds\n"
    "ds_read_b128 a[4:7], v34 offset:16384\n"
    "v_exp_f32 v162, v162\n"
    "v_cvt_pk_bf16_f32 v160, v160, v161\n"
    "v_mfma_f32_16x16x32_bf16 v[68:71], a[64:67], v[188:191], v[68:71]\n"
    "v_exp_f32 v163, v163\n"
    "v_mfma_f32_16x16x32_bf16 v[80:83], a[72:75], v[188:191], v[80:83]\n"
    "buffer_load_dwordx4 v10, s[16:19], s45 offen offset:1024 nt lds\n"
    "ds_read_b128 a[8:11], v35 offset:16384\n"
    "v_exp_f32 v184, v184\n"
    "v_cvt_pk_bf16_f32 v161, v162, v163\n"
    "v_mfma_f32_16x16x32_bf16 v[80:83], a[76:79], v[196:199], v[80:83]\n"
    "v_exp_f32 v185, v185\n"
    "v_mfma_f32_16x16x32_bf16 v[76:79], a[76:79], v[172:175], v[76:79]\n"
    "ds_read_b128 a[12:15], v36 offset:16384\n"
    "v_exp_f32 v186, v186\n"
    "v_cvt_pk_bf16_f32 v184, v184, v185\n"
    "v_mfma_f32_16x16x32_bf16 v[76:79], a[72:75], v[164:167], v[76:79]\n"
    "s_waitcnt vmcnt(12)\n"
    "buffer_load_dwordx4 v11, s[16:19], s45 offen offset:2048 nt lds\n"
    "v_exp_f32 v187, v187\n"
    "v_mfma_f32_16x16x32_bf16 v[88:91], a[80:83], v[164:167], v[88:91]\n"
    "ds_read_b128 a[16:19], v33 offset:20480\n"
    "v_exp_f32 v176, v176\n"
    "v_cvt_pk_bf16_f32 v185, v186, v187\n"
    "v_mfma_f32_16x16x32_bf16 v[88:91], a[84:87], v[172:175], v[88:91]\n"
    "v_exp_f32 v177, v177\n"
    "v_mfma_f32_16x16x32_bf16 v[92:95], a[84:87], v[196:199], v[92:95]\n"
    "buffer_load_dwordx4 v12, s[16:19], s45 offen offset:3072 nt lds\n"
    "ds_read_b128 a[20:23], v34 offset:20480\n"
    "v_exp_f32 v178, v178\n"
    "v_cvt_pk_bf16_f32 v162, v176, v177\n"
    "v_mfma_f32_16x16x32_bf16 v[92:95], a[80:83], v[188:191], v[92:95]\n"
    "v_exp_f32 v179, v179\n"
    "v_mfma_f32_16x16x32_bf16 v[104:107], a[88:91], v[188:191], v[104:107]\n"
    "ds_read_b128 a[24:27], v35 offset:20480\n"
    "v_exp_f32 v200, v200\n"
    "v_cvt_pk_bf16_f32 v163, v178, v179\n"
    "v_mfma_f32_16x16x32_bf16 v[104:107], a[92:95], v[196:199], v[104:107]\n"
    "s_mov_b32 m0, 94208\n"
    "s_mov_b32 s45, 2158592\n"
    "s_waitcnt vmcnt(12)\n"
    "buffer_load_dwordx4 v13, s[20:23], s45 offen nt lds\n"
    "v_exp_f32 v201, v201\n"
    "v_mfma_f32_16x16x32_bf16 v[100:103], a[92:95], v[172:175], v[100:103]\n"
    "ds_read_b128 a[28:31], v36 offset:20480\n"
    "v_exp_f32 v202, v202\n"
    "v_cvt_pk_bf16_f32 v186, v200, v201\n"
    "v_mfma_f32_16x16x32_bf16 v[100:103], a[88:91], v[164:167], v[100:103]\n"
    "v_exp_f32 v203, v203\n"
    "v_mfma_f32_16x16x32_bf16 v[112:115], a[96:99], v[164:167], v[112:115]\n"
    "buffer_load_dwordx4 v14, s[20:23], s45 offen offset:1024 nt lds\n"
    "ds_read_b128 a[32:35], v33 offset:24576\n"
    "v_exp_f32 v168, v168\n"
    "v_cvt_pk_bf16_f32 v187, v202, v203\n"
    "v_mfma_f32_16x16x32_bf16 v[112:115], a[100:103], v[172:175], v[112:115]\n"
    "v_exp_f32 v169, v169\n"
    "v_mfma_f32_16x16x32_bf16 v[116:119], a[100:103], v[196:199], v[116:119]\n"
    "ds_read_b128 a[36:39], v34 offset:24576\n"
    "v_exp_f32 v170, v170\n"
    "v_cvt_pk_bf16_f32 v168, v168, v169\n"
    "v_mfma_f32_16x16x32_bf16 v[116:119], a[96:99], v[188:191], v[116:119]\n"
    "s_waitcnt vmcnt(12)\n"
    "buffer_load_dwordx4 v13, s[20:23], s45 offen offset:2048 nt lds\n"
    "v_exp_f32 v171, v171\n"
    "v_mfma_f32_16x16x32_bf16 v[128:131], a[104:107], v[188:191], v[128:131]\n"
    "ds_read_b128 a[40:43], v35 offset:24576\n"
    "v_exp_f32 v192, v192\n"
    "v_cvt_pk_bf16_f32 v169, v170, v171\n"
    "v_mfma_f32_16x16x32_bf16 v[128:131], a[108:111], v[196:199], v[128:131]\n"
    "buffer_load_dwordx4 v14, s[20:23], s45 offen offset:3072 nt lds\n"
    "v_exp_f32 v193, v193\n"
    "v_mfma_f32_16x16x32_bf16 v[124:127], a[108:111], v[172:175], v[124:127]\n"
    "ds_read_b128 a[44:47], v36 offset:24576\n"
    "v_exp_f32 v194, v194\n"
    "v_cvt_pk_bf16_f32 v192, v192, v193\n"
    "v_mfma_f32_16x16x32_bf16 v[124:127], a[104:107], v[164:167], v[124:127]\n"
    "v_exp_f32 v195, v195\n"
    "v_mfma_f32_16x16x32_bf16 v[136:139], a[112:115], v[164:167], v[136:139]\n"
    "s_mov_b32 m0, 32768\n"
    "s_mov_b32 s45, 2195456\n"
    "s_waitcnt vmcnt(12)\n"
    "buffer_load_dwordx4 v9, s[16:19], s45 offen nt lds\n"
    "ds_read_b128 a[48:51], v33 offset:28672\n"
    "v_exp_f32 v180, v180\n"
    "v_cvt_pk_bf16_f32 v193, v194, v195\n"
    "v_mfma_f32_16x16x32_bf16 v[136:139], a[116:119], v[172:175], v[136:139]\n"
    "v_exp_f32 v181, v181\n"
    "v_mfma_f32_16x16x32_bf16 v[140:143], a[116:119], v[196:199], v[140:143]\n"
    "ds_read_b128 a[52:55], v34 offset:28672\n"
    "v_exp_f32 v182, v182\n"
    "v_cvt_pk_bf16_f32 v170, v180, v181\n"
    "v_mfma_f32_16x16x32_bf16 v[140:143], a[112:115], v[188:191], v[140:143]\n"
    "buffer_load_dwordx4 v10, s[16:19], s45 offen offset:1024 nt lds\n"
    "v_exp_f32 v183, v183\n"
    "v_mfma_f32_16x16x32_bf16 v[152:155], a[120:123], v[188:191], v[152:155]\n"
    "ds_read_b128 a[56:59], v35 offset:28672\n"
    "v_exp_f32 v204, v204\n"
    "v_cvt_pk_bf16_f32 v171, v182, v183\n"
    "v_mfma_f32_16x16x32_bf16 v[152:155], a[124:127], v[196:199], v[152:155]\n"
    "v_exp_f32 v205, v205\n"
    "v_mfma_f32_16x16x32_bf16 v[148:151], a[124:127], v[172:175], v[148:151]\n"
    "s_waitcnt vmcnt(12)\n"
    "buffer_load_dwordx4 v11, s[16:19], s45 offen offset:2048 nt lds\n"
    "ds_read_b128 a[60:63], v36 offset:28672\n"
    "v_exp_f32 v206, v206\n"
    "v_cvt_pk_bf16_f32 v194, v204, v205\n"
    "v_mfma_f32_16x16x32_bf16 v[148:151], a[120:123], v[164:167], v[148:151]\n"
    "v_exp_f32 v207, v207\n"
    "v_mfma_f32_16x16x32_bf16 v[244:247], a[224:227], v[164:167], v[244:247]\n"
    "v_mfma_f32_16x16x32_bf16 v[244:247], a[224:227], v[172:175], v[244:247]\n"
    "buffer_load_dwordx4 v12, s[16:19], s45 offen offset:3072 nt lds\n"
    "v_cvt_pk_bf16_f32 v195, v206, v207\n"
    "v_mfma_f32_16x16x32_bf16 v[248:251], a[224:227], v[188:191], v[248:251]\n"
    "v_mfma_f32_16x16x32_bf16 v[248:251], a[224:227], v[196:199], v[248:251]\n"
    "s_waitcnt vmcnt(36) lgkmcnt(0)\n"
    "s_barrier\n"
    "v_mfma_f32_16x16x32_bf16 v[164:167], a[0:3], a[176:179], v[232:235]\n"
    "ds_read_b64_tr_b16 a[64:65], v25 offset:0\n"
    "s_mov_b32 m0, 98304\n"
    "s_mov_b32 s45, 2162688\n"
    "s_waitcnt vmcnt(12)\n"
    "buffer_load_dwordx4 v13, s[20:23], s45 offen nt lds\n"
    "v_mfma_f32_16x16x32_bf16 v[164:167], a[4:7], a[180:183], v[164:167]\n"
    "ds_read_b64_tr_b16 a[66:67], v25 offset:4096\n"
    "v_mfma_f32_16x16x32_bf16 v[164:167], a[8:11], a[184:187], v[164:167]\n"
    "ds_read_b64_tr_b16 a[68:69], v25 offset:8192\n"
    "buffer_load_dwordx4 v14, s[20:23], s45 offen offset:1024 nt lds\n"
    "v_mfma_f32_16x16x32_bf16 v[164:167], a[12:15], a[188:191], v[164:167]\n"
    "ds_read_b64_tr_b16 a[70:71], v25 offset:12288\n"
    "v_add_u32 v25, 0x4000, v25\n"
    "v_mfma_f32_16x16x32_bf16 v[188:191], a[12:15], a[204:207], v[236:239]\n"
    "ds_read_b64_tr_b16 a[72:73], v26 offset:0\n"
    "v_mfma_f32_16x16x32_bf16 v[188:191], a[8:11], a[200:203], v[188:191]\n"
    "ds_read_b64_tr_b16 a[74:75], v26 offset:4096\n"
    "s_waitcnt vmcnt(12)\n"
    "buffer_load_dwordx4 v13, s[20:23], s45 offen offset:2048 nt lds\n"
    "v_mfma_f32_16x16x32_bf16 v[188:191], a[4:7], a[196:199], v[188:191]\n"
    "ds_read_b64_tr_b16 a[76:77], v26 offset:8192\n"
    "v_mfma_f32_16x16x32_bf16 v[188:191], a[0:3], a[192:195], v[188:191]\n"
    "ds_read_b64_tr_b16 a[78:79], v26 offset:12288\n"
    "v_add_u32 v26, 0x4000, v26\n"
    "v_mfma_f32_16x16x32_bf16 v[200:203], a[16:19], a[192:195], v[236:239]\n"
    "ds_read_b64_tr_b16 a[80:81], v27 offset:0\n"
    "buffer_load_dwordx4 v14, s[20:23], s45 offen offset:3072 nt lds\n"
    "v_mfma_f32_16x16x32_bf16 v[200:203], a[20:23], a[196:199], v[200:203]\n"
    "ds_read_b64_tr_b16 a[82:83], v27 offset:4096\n"
    "v_mfma_f32_16x16x32_bf16 v[200:203], a[24:27], a[200:203], v[200:203]\n"
    "ds_read_b64_tr_b16 a[84:85], v27 offset:8192\n"
    "v_mfma_f32_16x16x32_bf16 v[200:203], a[28:31], a[204:207], v[200:203]\n"
    "ds_read_b64_tr_b16 a[86:87], v27 offset:12288\n"
    "v_add_u32 v27, 0x4000, v27\n"
    "s_mov_b32 m0, 36864\n"
    "s_mov_b32 s45, 2199552\n"
    "s_waitcnt vmcnt(12)\n"
    "buffer_load_dwordx4 v9, s[16:19], s45 offen nt lds\n"
    "v_mfma_f32_16x16x32_bf16 v[176:179], a[28:31], a[188:191], v[232:235]\n"
    "ds_read_b64_tr_b16 a[88:89], v28 offset:0\n"
    "v_xor_b32 v33, 0x8000, v33\n"
    "v_mfma_f32_16x16x32_bf16 v[176:179], a[24:27], a[184:187], v[176:179]\n"
    "ds_read_b64_tr_b16 a[90:91], v28 offset:4096\n"
    "v_mfma_f32_16x16x32_bf16 v[176:179], a[20:23], a[180:183], v[176:179]\n"
    "ds_read_b64_tr_b16 a[92:93], v28 offset:8192\n"
    "v_xor_b32 v34, 0x8000, v34\n"
    "buffer_load_dwordx4 v10, s[16:19], s45 offen offset:1024 nt lds\n"
    "v_mfma_f32_16x16x32_bf16 v[176:179], a[16:19], a[176:179], v[176:179]\n"
    "ds_read_b64_tr_b16 a[94:95], v28 offset:12288\n"
    "v_add_u32 v28, 0x4000, v28\n"
    "v_mfma_f32_16x16x32_bf16 v[172:175], a[32:35], a[176:179], v[232:235]\n"
    "ds_read_b64_tr_b16 a[96:97], v29 offset:0\n"
    "v_xor_b32 v35, 0x8000, v35\n"
    "v_mfma_f32_16x16x32_bf16 v[172:175], a[36:39], a[180:183], v[172:175]\n"
    "ds_read_b64_tr_b16 a[98:99], v29 offset:4096\n"
    "s_waitcnt vmcnt(12)\n"
    "buffer_load_dwordx4 v11, s[16:19], s45 offen offset:2048 nt lds\n"
    "v_mfma_f32_16x16x32_bf16 v[172:175], a[40:43], a[184:187], v[172:175]\n"
    "ds_read_b64_tr_b16 a[100:101], v29 offset:8192\n"
    "v_xor_b32 v36, 0x8000, v36\n"
    "v_mfma_f32_16x16x32_bf16 v[172:175], a[44:47], a[188:191], v[172:175]\n"
    "ds_read_b64_tr_b16 a[102:103], v29 offset:12288\n"
    "v_add_u32 v29, 0x4000, v29\n"
    "buffer_load_dwordx4 v12, s[16:19], s45 offen offset:3072 nt lds\n"
    "v_mfma_f32_16x16x32_bf16 v[196:199], a[44:47], a[204:207], v[236:239]\n"
    "ds_read_b64_tr_b16 a[104:105], v30 offset:0\n"
    "v_mfma_f32_16x16x32_bf16 v[196:199], a[40:43], a[200:203], v[196:199]\n"
    "ds_read_b64_tr_b16 a[106:107], v30 offset:4096\n"
    "v_mfma_f32_16x16x32_bf16 v[196:199], a[36:39], a[196:199], v[196:199]\n"
    "ds_read_b64_tr_b16 a[108:109], v30 offset:8192\n"
    "s_mov_b32 m0, 102400\n"
    "s_mov_b32 s45, 2166784\n"
    "s_waitcnt vmcnt(12)\n"
    "buffer_load_dwordx4 v13, s[20:23], s45 offen nt lds\n"
    "v_mfma_f32_16x16x32_bf16 v[196:199], a[32:35], a[192:195], v[196:199]\n"
    "ds_read_b64_tr_b16 a[110:111], v30 offset:12288\n"
    "v_add_u32 v30, 0x4000, v30\n"
    "v_mfma_f32_16x16x32_bf16 v[204:207], a[48:51], a[192:195], v[236:239]\n"
    "ds_read_b64_tr_b16 a[112:113], v31 offset:0\n"
    "v_mfma_f32_16x16x32_bf16 v[204:207], a[52:55], a[196:199], v[204:207]\n"
    "ds_read_b64_tr_b16 a[114:115], v31 offset:4096\n"
    "buffer_load_dwordx4 v14, s[20:23], s45 offen offset:1024 nt lds\n"
    "v_mfma_f32_16x16x32_bf16 v[204:207], a[56:59], a[200:203], v[204:207]\n"
    "ds_read_b64_tr_b16 a[116:117], v31 offset:8192\n"
    "v_mfma_f32_16x16x32_bf16 v[204:207], a[60:63], a[204:207], v[204:207]\n"
    "ds_read_b64_tr_b16 a[118:119], v31 offset:12288\n"
    "v_add_u32 v31, 0x4000, v31\n"
    "v_mfma_f32_16x16x32_bf16 v[180:183], a[60:63], a[188:191], v[232:235]\n"
    "ds_read_b64_tr_b16 a[120:121], v32 offset:0\n"
    "s_waitcnt vmcnt(12)\n"
    "buffer_load_dwordx4 v13, s[20:23], s45 offen offset:2048 nt lds\n"
    "v_mfma_f32_16x16x32_bf16 v[180:183], a[56:59], a[184:187], v[180:183]\n"
    "ds_read_b64_tr_b16 a[122:123], v32 offset:4096\n"
    "v_mfma_f32_16x16x32_bf16 v[180:183], a[52:55], a[180:183], v[180:183]\n"
    "ds_read_b64_tr_b16 a[124:125], v32 offset:8192\n"
    "v_mfma_f32_16x16x32_bf16 v[180:183], a[48:51], a[176:179], v[180:183]\n"
    "ds_read_b64_tr_b16 a[126:127], v32 offset:12288\n"
    "v_add_u32 v32, 0x4000, v32\n"
    "buffer_load_dwordx4 v14, s[20:23], s45 offen offset:3072 nt lds\n"
    "s_waitcnt lgkmcnt(0)\n"
    "v_mfma_f32_16x16x32_bf16 v[64:67], a[64:67], v[160:163], v[64:67]\n"
    "ds_read_b128 a[0:3], v33 offset:0\n"
    "v_exp_f32 v164, v164\n"
    "v_mfma_f32_16x16x32_bf16 v[64:67], a[68:71], v[168:171], v[64:67]\n"
    "v_exp_f32 v165, v165\n"
    "v_mfma_f32_16x16x32_bf16 v[68:71], a[68:71], v[192:195], v[68:71]\n"
    "s_mov_b32 m0, 40960\n"
    "s_mov_b32 s45, 2203648\n"
    "s_waitcnt vmcnt(12)\n"
    "buffer_load_dwordx4 v9, s[16:19], s45 offen nt lds\n"
    "ds_read_b128 a[4:7], v34 offset:0\n"
    "v_exp_f32 v166, v166\n"
    "v_cvt_pk_bf16_f32 v164, v164, v165\n"
    "v_mfma_f32_16x16x32_bf16 v[68:71], a[64:67], v[184:187], v[68:71]\n"
    "v_exp_f32 v167, v167\n"
    "v_mfma_f32_16x16x32_bf16 v[80:83], a[72:75], v[184:187], v[80:83]\n"
    "buffer_load_dwordx4 v10, s[16:19], s45 offen offset:1024 nt lds\n"
    "ds_read_b128 a[8:11], v35 offset:0\n"
    "v_exp_f32 v188, v188\n"
    "v_cvt_pk_bf16_f32 v165, v166, v167\n"
    "v_mfma_f32_16x16x32_bf16 v[80:83], a[76:79], v[192:195], v[80:83]\n"
    "v_exp_f32 v189, v189\n"
    "v_mfma_f32_16x16x32_bf16 v[76:79], a[76:79], v[168:171], v[76:79]\n"
    "ds_read_b128 a[12:15], v36 offset:0\n"
    "v_exp_f32 v190, v190\n"
    "v_cvt_pk_bf16_f32 v188, v188, v189\n"
    "v_mfma_f32_16x16x32_bf16 v[76:79], a[72:75], v[160:163], v[76:79]\n"
    "s_waitcnt vmcnt(12)\n"
    "buffer_load_dwordx4 v11, s[16:19], s45 offen offset:2048 nt lds\n"
    "v_exp_f32 v191, v191\n"
    "v_mfma_f32_16x16x32_bf16 v[88:91], a[80:83], v[160:163], v[88:91]\n"
    "ds_read_b128 a[16:19], v33 offset:4096\n"
    "v_exp_f32 v176, v176\n"
    "v_cvt_pk_bf16_f32 v189, v190, v191\n"
    "v_mfma_f32_16x16x32_bf16 v[88:91], a[84:87], v[168:171], v[88:91]\n"
    "v_exp_f32 v177, v177\n"
    "v_mfma_f32_16x16x32_bf16 v[92:95], a[84:87], v[192:195], v[92:95]\n"
    "buffer_load_dwordx4 v12, s[16:19], s45 offen offset:3072 nt lds\n"
    "ds_read_b128 a[20:23], v34 offset:4096\n"
    "v_exp_f32 v178, v178\n"
    "v_cvt_pk_bf16_f32 v166, v176, v177\n"
    "v_mfma_f32_16x16x32_bf16 v[92:95], a[80:83], v[184:187], v[92:95]\n"
    "v_exp_f32 v179, v179\n"
    "v_mfma_f32_16x16x32_bf16 v[104:107], a[88:91], v[184:187], v[104:107]\n"
    "ds_read_b128 a[24:27], v35 offset:4096\n"
    "v_exp_f32 v200, v200\n"
    "v_cvt_pk_bf16_f32 v167, v178, v179\n"
    "v_mfma_f32_16x16x32_bf16 v[104:107], a[92:95], v[192:195], v[104:107]\n"
    "s_mov_b32 m0, 106496\n"
    "s_mov_b32 s45, 2170880\n"
    "s_waitcnt vmcnt(12)\n"
    "buffer_load_dwordx4 v13, s[20:23], s45 offen nt lds\n"
    "v_exp_f32 v201, v201\n"
    "v_mfma_f32_16x16x32_bf16 v[100:103], a[92:95], v[168:171], v[100:103]\n"
    "ds_read_b128 a[28:31], v36 offset:4096\n"
    "v_exp_f32 v202, v202\n"
    "v_cvt_pk_bf16_f32 v190, v200, v201\n"
    "v_mfma_f32_16x16x32_bf16 v[100:103], a[88:91], v[160:163], v[100:103]\n"
    "v_exp_f32 v203, v203\n"
    "v_mfma_f32_16x16x32_bf16 v[112:115], a[96:99], v[160:163], v[112:115]\n"
    "buffer_load_dwordx4 v14, s[20:23], s45 offen offset:1024 nt lds\n"
    "ds_read_b128 a[32:35], v33 offset:8192\n"
    "v_exp_f32 v172, v172\n"
    "v_cvt_pk_bf16_f32 v191, v202, v203\n"
    "v_mfma_f32_16x16x32_bf16 v[112:115], a[100:103], v[168:171], v[112:115]\n"
    "v_exp_f32 v173, v173\n"
    "v_mfma_f32_16x16x32_bf16 v[116:119], a[100:103], v[192:195], v[116:119]\n"
    "ds_read_b128 a[36:39], v34 offset:8192\n"
    "v_exp_f32 v174, v174\n"
    "v_cvt_pk_bf16_f32 v172, v172, v173\n"
    "v_mfma_f32_16x16x32_bf16 v[116:119], a[96:99], v[184:187], v[116:119]\n"
    "s_waitcnt vmcnt(12)\n"
    "buffer_load_dwordx4 v13, s[20:23], s45 offen offset:2048 nt lds\n"
    "v_exp_f32 v175, v175\n"
    "v_mfma_f32_16x16x32_bf16 v[128:131], a[104:107], v[184:187], v[128:131]\n"
    "ds_read_b128 a[40:43], v35 offset:8192\n"
    "v_exp_f32 v196, v196\n"
    "v_cvt_pk_bf16_f32 v173, v174, v175\n"
    "v_mfma_f32_16x16x32_bf16 v[128:131], a[108:111], v[192:195], v[128:131]\n"
    "buffer_load_dwordx4 v14, s[20:23], s45 offen offset:3072 nt lds\n"
    "v_exp_f32 v197, v197\n"
    "v_mfma_f32_16x16x32_bf16 v[124:127], a[108:111], v[168:171], v[124:127]\n"
    "ds_read_b128 a[44:47], v36 offset:8192\n"
    "v_exp_f32 v198, v198\n"
    "v_cvt_pk_bf16_f32 v196, v196, v197\n"
    "v_mfma_f32_16x16x32_bf16 v[124:127], a[104:107], v[160:163], v[124:127]\n"
    "v_exp_f32 v199, v199\n"
    "v_mfma_f32_16x16x32_bf16 v[136:139], a[112:115], v[160:163], v[136:139]\n"
    "s_mov_b32 m0, 45056\n"
    "s_mov_b32 s45, 2207744\n"
    "s_waitcnt vmcnt(12)\n"
    "buffer_load_dwordx4 v9, s[16:19], s45 offen nt lds\n"
    "ds_read_b128 a[48:51], v33 offset:12288\n"
    "v_exp_f32 v180, v180\n"
    "v_cvt_pk_bf16_f32 v197, v198, v199\n"
    "v_mfma_f32_16x16x32_bf16 v[136:139], a[116:119], v[168:171], v[136:139]\n"
    "v_exp_f32 v181, v181\n"
    "v_mfma_f32_16x16x32_bf16 v[140:143], a[116:119], v[192:195], v[140:143]\n"
    "ds_read_b128 a[52:55], v34 offset:12288\n"
    "v_exp_f32 v182, v182\n"
    "v_cvt_pk_bf16_f32 v174, v180, v181\n"
    "v_mfma_f32_16x16x32_bf16 v[140:143], a[112:115], v[184:187], v[140:143]\n"
    "buffer_load_dwordx4 v10, s[16:19], s45 offen offset:1024 nt lds\n"
    "v_exp_f32 v183, v183\n"
    "v_mfma_f32_16x16x32_bf16 v[152:155], a[120:123], v[184:187], v[152:155]\n"
    "ds_read_b128 a[56:59], v35 offset:12288\n"
    "v_exp_f32 v204, v204\n"
    "v_cvt_pk_bf16_f32 v175, v182, v183\n"
    "v_mfma_f32_16x16x32_bf16 v[152:155], a[124:127], v[192:195], v[152:155]\n"
    "v_exp_f32 v205, v205\n"
    "v_mfma_f32_16x16x32_bf16 v[148:151], a[124:127], v[168:171], v[148:151]\n"
    "s_waitcnt vmcnt(12)\n"
    "buffer_load_dwordx4 v11, s[16:19], s45 offen offset:2048 nt lds\n"
    "ds_read_b128 a[60:63], v36 offset:12288\n"
    "v_exp_f32 v206, v206\n"
    "v_cvt_pk_bf16_f32 v198, v204, v205\n"
    "v_mfma_f32_16x16x32_bf16 v[148:151], a[120:123], v[160:163], v[148:151]\n"
    "v_exp_f32 v207, v207\n"
    "v_mfma_f32_16x16x32_bf16 v[244:247], a[224:227], v[160:163], v[244:247]\n"
    "v_mfma_f32_16x16x32_bf16 v[244:247], a[224:227], v[168:171], v[244:247]\n"
    "buffer_load_dwordx4 v12, s[16:19], s45 offen offset:3072 nt lds\n"
    "v_cvt_pk_bf16_f32 v199, v206, v207\n"
    "v_mfma_f32_16x16x32_bf16 v[248:251], a[224:227], v[184:187], v[248:251]\n"
    "v_mfma_f32_16x16x32_bf16 v[248:251], a[224:227], v[192:195], v[248:251]\n"
    "s_waitcnt vmcnt(28) lgkmcnt(0)\n"
    "s_barrier\n"
    "v_mfma_f32_16x16x32_bf16 v[160:163], a[0:3], a[176:179], v[232:235]\n"
    "ds_read_b64_tr_b16 a[64:65], v25 offset:0\n"
    "s_mov_b32 m0, 110592\n"
    "s_mov_b32 s45, 2174976\n"
    "s_waitcnt vmcnt(12)\n"
    "buffer_load_dwordx4 v13, s[20:23], s45 offen nt lds\n"
    "v_mfma_f32_16x16x32_bf16 v[160:163], a[4:7], a[180:183], v[160:163]\n"
    "ds_read_b64_tr_b16 a[66:67], v25 offset:4096\n"
    "buffer_load_dwordx4 v14, s[20:23], s45 offen offset:1024 nt lds\n"
    "v_mfma_f32_16x16x32_bf16 v[160:163], a[8:11], a[184:187], v[160:163]\n"
    "ds_read_b64_tr_b16 a[68:69], v25 offset:8192\n"
    "v_mfma_f32_16x16x32_bf16 v[160:163], a[12:15], a[188:191], v[160:163]\n"
    "ds_read_b64_tr_b16 a[70:71], v25 offset:12288\n"
    "v_add_u32 v25, 0x4000, v25\n"
    "s_waitcnt vmcnt(12)\n"
    "buffer_load_dwordx4 v13, s[20:23], s45 offen offset:2048 nt lds\n"
    "v_mfma_f32_16x16x32_bf16 v[184:187], a[12:15], a[204:207], v[236:239]\n"
    "ds_read_b64_tr_b16 a[72:73], v26 offset:0\n"
    "v_mfma_f32_16x16x32_bf16 v[184:187], a[8:11], a[200:203], v[184:187]\n"
    "ds_read_b64_tr_b16 a[74:75], v26 offset:4096\n"
    "buffer_load_dwordx4 v14, s[20:23], s45 offen offset:3072 nt lds\n"
    "v_mfma_f32_16x16x32_bf16 v[184:187], a[4:7], a[196:199], v[184:187]\n"
    "ds_read_b64_tr_b16 a[76:77], v26 offset:8192\n"
    "v_readlane_b32 s39, v16, 0\n"
    "s_mov_b32 m0, 0\n"
    "s_add_u32 s45, s39, 0\n"
    "s_waitcnt vmcnt(40)\n"
    "buffer_load_dwordx4 v1, s[12:15], s45 offen lds\n"
    "v_mfma_f32_16x16x32_bf16 v[184:187], a[0:3], a[192:195], v[184:187]\n"
    "ds_read_b64_tr_b16 a[78:79], v26 offset:12288\n"
    "v_add_u32 v26, 0x4000, v26\n"
    "v_mfma_f32_16x16x32_bf16 v[200:203], a[16:19], a[192:195], v[236:239]\n"
    "ds_read_b64_tr_b16 a[80:81], v27 offset:0\n"
    "buffer_load_dwordx4 v2, s[12:15], s45 offen offset:1024 lds\n"
    "v_mfma_f32_16x16x32_bf16 v[200:203], a[20:23], a[196:199], v[200:203]\n"
    "ds_read_b64_tr_b16 a[82:83], v27 offset:4096\n"
    "v_mfma_f32_16x16x32_bf16 v[200:203], a[24:27], a[200:203], v[200:203]\n"
    "ds_read_b64_tr_b16 a[84:85], v27 offset:8192\n"
    "buffer_load_dwordx4 v3, s[12:15], s45 offen offset:2048 lds\n"
    "v_mfma_f32_16x16x32_bf16 v[200:203], a[28:31], a[204:207], v[200:203]\n"
    "ds_read_b64_tr_b16 a[86:87], v27 offset:12288\n"
    "v_add_u32 v27, 0x4000, v27\n"
    "buffer_load_dwordx4 v4, s[12:15], s45 offen offset:3072 lds\n"
    "v_mfma_f32_16x16x32_bf16 v[176:179], a[28:31], a[188:191], v[232:235]\n"
    "ds_read_b64_tr_b16 a[88:89], v28 offset:0\n"
    "v_mfma_f32_16x16x32_bf16 v[176:179], a[24:27], a[184:187], v[176:179]\n"
    "ds_read_b64_tr_b16 a[90:91], v28 offset:4096\n"
    "s_mov_b32 m0, 49152\n"
    "s_mov_b32 s45, 2211840\n"
    "s_waitcnt vmcnt(16)\n"
    "buffer_load_dwordx4 v9, s[16:19], s45 offen nt lds\n"
    "v_mfma_f32_16x16x32_bf16 v[176:179], a[20:23], a[180:183], v[176:179]\n"
    "ds_read_b64_tr_b16 a[92:93], v28 offset:8192\n"
    "v_mfma_f32_16x16x32_bf16 v[176:179], a[16:19], a[176:179], v[176:179]\n"
    "ds_read_b64_tr_b16 a[94:95], v28 offset:12288\n"
    "v_add_u32 v28, 0x4000, v28\n"
    "buffer_load_dwordx4 v10, s[16:19], s45 offen offset:1024 nt lds\n"
    "v_mfma_f32_16x16x32_bf16 v[168:171], a[32:35], a[176:179], v[232:235]\n"
    "ds_read_b64_tr_b16 a[96:97], v29 offset:0\n"
    "v_mfma_f32_16x16x32_bf16 v[168:171], a[36:39], a[180:183], v[168:171]\n"
    "ds_read_b64_tr_b16 a[98:99], v29 offset:4096\n"
    "s_waitcnt vmcnt(16)\n"
    "buffer_load_dwordx4 v11, s[16:19], s45 offen offset:2048 nt lds\n"
    "v_mfma_f32_16x16x32_bf16 v[168:171], a[40:43], a[184:187], v[168:171]\n"
    "ds_read_b64_tr_b16 a[100:101], v29 offset:8192\n"
    "buffer_load_dwordx4 v12, s[16:19], s45 offen offset:3072 nt lds\n"
    "v_mfma_f32_16x16x32_bf16 v[168:171], a[44:47], a[188:191], v[168:171]\n"
    "ds_read_b64_tr_b16 a[102:103], v29 offset:12288\n"
    "v_add_u32 v29, 0x4000, v29\n"
    "v_mfma_f32_16x16x32_bf16 v[192:195], a[44:47], a[204:207], v[236:239]\n"
    "ds_read_b64_tr_b16 a[104:105], v30 offset:0\n"
    "s_mov_b32 m0, 4096\n"
    "s_add_u32 s45, s39, 65536\n"
    "s_waitcnt vmcnt(40)\n"
    "buffer_load_dwordx4 v1, s[12:15], s45 offen lds\n"
    "v_mfma_f32_16x16x32_bf16 v[192:195], a[40:43], a[200:203], v[192:195]\n"
    "ds_read_b64_tr_b16 a[106:107], v30 offset:4096\n"
    "v_mfma_f32_16x16x32_bf16 v[192:195], a[36:39], a[196:199], v[192:195]\n"
    "ds_read_b64_tr_b16 a[108:109], v30 offset:8192\n"
    "buffer_load_dwordx4 v2, s[12:15], s45 offen offset:1024 lds\n"
    "v_mfma_f32_16x16x32_bf16 v[192:195], a[32:35], a[192:195], v[192:195]\n"
    "ds_read_b64_tr_b16 a[110:111], v30 offset:12288\n"
    "v_add_u32 v30, 0x4000, v30\n"
    "buffer_load_dwordx4 v3, s[12:15], s45 offen offset:2048 lds\n"
    "v_mfma_f32_16x16x32_bf16 v[204:207], a[48:51], a[192:195], v[236:239]\n"
    "ds_read_b64_tr_b16 a[112:113], v31 offset:0\n"
    "v_mfma_f32_16x16x32_bf16 v[204:207], a[52:55], a[196:199], v[204:207]\n"
    "ds_read_b64_tr_b16 a[114:115], v31 offset:4096\n"
    "buffer_load_dwordx4 v4, s[12:15], s45 offen offset:3072 lds\n"
    "v_mfma_f32_16x16x32_bf16 v[204:207], a[56:59], a[200:203], v[204:207]\n"
    "ds_read_b64_tr_b16 a[116:117], v31 offset:8192\n"
    "v_mfma_f32_16x16x32_bf16 v[204:207], a[60:63], a[204:207], v[204:207]\n"
    "ds_read_b64_tr_b16 a[118:119], v31 offset:12288\n"
    "v_add_u32 v31, 0x4000, v31\n"
    "s_mov_b32 m0, 114688\n"
    "s_mov_b32 s45, 2179072\n"
    "s_waitcnt vmcnt(20)\n"
    "buffer_load_dwordx4 v13, s[20:23], s45 offen nt lds\n"
    "v_mfma_f32_16x16x32_bf16 v[180:183], a[60:63], a[188:191], v[232:235]\n"
    "ds_read_b64_tr_b16 a[120:121], v32 offset:0\n"
    "buffer_load_dwordx4 v14, s[20:23], s45 offen offset:1024 nt lds\n"
    "v_mfma_f32_16x16x32_bf16 v[180:183], a[56:59], a[184:187], v[180:183]\n"
    "ds_read_b64_tr_b16 a[122:123], v32 offset:4096\n"
    "v_mfma_f32_16x16x32_bf16 v[180:183], a[52:55], a[180:183], v[180:183]\n"
    "ds_read_b64_tr_b16 a[124:125], v32 offset:8192\n"
    "s_waitcnt vmcnt(20)\n"
    "buffer_load_dwordx4 v13, s[20:23], s45 offen offset:2048 nt lds\n"
    "v_mfma_f32_16x16x32_bf16 v[180:183], a[48:51], a[176:179], v[180:183]\n"
    "ds_read_b64_tr_b16 a[126:127], v32 offset:12288\n"
    "v_add_u32 v32, 0x4000, v32\n"
    "s_waitcnt lgkmcnt(0)\n"
    "v_mfma_f32_16x16x32_bf16 v[64:67], a[64:67], v[164:167], v[64:67]\n"
    "buffer_load_dwordx4 v14, s[20:23], s45 offen offset:3072 nt lds\n"
    "ds_read_b128 a[0:3], v33 offset:16384\n"
    "v_exp_f32 v160, v160\n"
    "v_mfma_f32_16x16x32_bf16 v[64:67], a[68:71], v[172:175], v[64:67]\n"
    "v_exp_f32 v161, v161\n"
    "v_mfma_f32_16x16x32_bf16 v[68:71], a[68:71], v[196:199], v[68:71]\n"
    "s_mov_b32 m0, 53248\n"
    "s_mov_b32 s45, 2215936\n"
    "s_waitcnt vmcnt(20)\n"
    "buffer_load_dwordx4 v9, s[16:19], s45 offen nt lds\n"
    "ds_read_b128 a[4:7], v34 offset:16384\n"
    "v_exp_f32 v162, v162\n"
    "v_cvt_pk_bf16_f32 v160, v160, v161\n"
    "v_mfma_f32_16x16x32_bf16 v[68:71], a[64:67], v[188:191], v[68:71]\n"
    "buffer_load_dwordx4 v10, s[16:19], s45 offen offset:1024 nt lds\n"
    "v_exp_f32 v163, v163\n"
    "v_mfma_f32_16x16x32_bf16 v[80:83], a[72:75], v[188:191], v[80:83]\n"
    "ds_read_b128 a[8:11], v35 offset:16384\n"
    "v_exp_f32 v184, v184\n"
    "v_cvt_pk_bf16_f32 v161, v162, v163\n"
    "v_mfma_f32_16x16x32_bf16 v[80:83], a[76:79], v[196:199], v[80:83]\n"
    "s_waitcnt vmcnt(20)\n"
    "buffer_load_dwordx4 v11, s[16:19], s45 offen offset:2048 nt lds\n"
    "v_exp_f32 v185, v185\n"
    "v_mfma_f32_16x16x32_bf16 v[76:79], a[76:79], v[172:175], v[76:79]\n"
    "ds_read_b128 a[12:15], v36 offset:16384\n"
    "v_exp_f32 v186, v186\n"
    "v_cvt_pk_bf16_f32 v184, v184, v185\n"
    "v_mfma_f32_16x16x32_bf16 v[76:79], a[72:75], v[164:167], v[76:79]\n"
    "buffer_load_dwordx4 v12, s[16:19], s45 offen offset:3072 nt lds\n"
    "v_exp_f32 v187, v187\n"
    "v_mfma_f32_16x16x32_bf16 v[88:91], a[80:83], v[164:167], v[88:91]\n"
    "s_mov_b32 m0, 8192\n"
    "s_add_u32 s45, s39, 131072\n"
    "s_waitcnt vmcnt(40)\n"
    "buffer_load_dwordx4 v1, s[12:15], s45 offen lds\n"
    "ds_read_b128 a[16:19], v33 offset:20480\n"
    "v_exp_f32 v176, v176\n"
    "v_cvt_pk_bf16_f32 v185, v186, v187\n"
    "v_mfma_f32_16x16x32_bf16 v[88:91], a[84:87], v[172:175], v[88:91]\n"
    "v_exp_f32 v177, v177\n"
    "v_mfma_f32_16x16x32_bf16 v[92:95], a[84:87], v[196:199], v[92:95]\n"
    "buffer_load_dwordx4 v2, s[12:15], s45 offen offset:1024 lds\n"
    "ds_read_b128 a[20:23], v34 offset:20480\n"
    "v_exp_f32 v178, v178\n"
    "v_cvt_pk_bf16_f32 v162, v176, v177\n"
    "v_mfma_f32_16x16x32_bf16 v[92:95], a[80:83], v[188:191], v[92:95]\n"
    "v_exp_f32 v179, v179\n"
    "v_mfma_f32_16x16x32_bf16 v[104:107], a[88:91], v[188:191], v[104:107]\n"
    "buffer_load_dwordx4 v3, s[12:15], s45 offen offset:2048 lds\n"
    "ds_read_b128 a[24:27], v35 offset:20480\n"
    "v_exp_f32 v200, v200\n"
    "v_cvt_pk_bf16_f32 v163, v178, v179\n"
    "v_mfma_f32_16x16x32_bf16 v[104:107], a[92:95], v[196:199], v[104:107]\n"
    "buffer_load_dwordx4 v4, s[12:15], s45 offen offset:3072 lds\n"
    "v_exp_f32 v201, v201\n"
    "v_mfma_f32_16x16x32_bf16 v[100:103], a[92:95], v[172:175], v[100:103]\n"
    "ds_read_b128 a[28:31], v36 offset:20480\n"
    "v_exp_f32 v202, v202\n"
    "v_cvt_pk_bf16_f32 v186, v200, v201\n"
    "v_mfma_f32_16x16x32_bf16 v[100:103], a[88:91], v[164:167], v[100:103]\n"
    "s_mov_b32 m0, 118784\n"
    "s_mov_b32 s45, 2183168\n"
    "s_waitcnt vmcnt(24)\n"
    "buffer_load_dwordx4 v13, s[20:23], s45 offen nt lds\n"
    "v_exp_f32 v203, v203\n"
    "v_mfma_f32_16x16x32_bf16 v[112:115], a[96:99], v[164:167], v[112:115]\n"
    "ds_read_b128 a[32:35], v33 offset:24576\n"
    "v_exp_f32 v168, v168\n"
    "v_cvt_pk_bf16_f32 v187, v202, v203\n"
    "v_mfma_f32_16x16x32_bf16 v[112:115], a[100:103], v[172:175], v[112:115]\n"
    "buffer_load_dwordx4 v14, s[20:23], s45 offen offset:1024 nt lds\n"
    "v_exp_f32 v169, v169\n"
    "v_mfma_f32_16x16x32_bf16 v[116:119], a[100:103], v[196:199], v[116:119]\n"
    "ds_read_b128 a[36:39], v34 offset:24576\n"
    "v_exp_f32 v170, v170\n"
    "v_cvt_pk_bf16_f32 v168, v168, v169\n"
    "v_mfma_f32_16x16x32_bf16 v[116:119], a[96:99], v[188:191], v[116:119]\n"
    "s_waitcnt vmcnt(20)\n"
    "buffer_load_dwordx4 v13, s[20:23], s45 offen offset:2048 nt lds\n"
    "v_exp_f32 v171, v171\n"
    "v_mfma_f32_16x16x32_bf16 v[128:131], a[104:107], v[188:191], v[128:131]\n"
    "buffer_load_dwordx4 v14, s[20:23], s45 offen offset:3072 nt lds\n"
    "ds_read_b128 a[40:43], v35 offset:24576\n"
    "v_exp_f32 v192, v192\n"
    "v_cvt_pk_bf16_f32 v169, v170, v171\n"
    "v_mfma_f32_16x16x32_bf16 v[128:131], a[108:111], v[196:199], v[128:131]\n"
    "v_exp_f32 v193, v193\n"
    "v_mfma_f32_16x16x32_bf16 v[124:127], a[108:111], v[172:175], v[124:127]\n"
    "s_mov_b32 m0, 12288\n"
    "s_add_u32 s45, s39, 196608\n"
    "s_waitcnt vmcnt(40)\n"
    "buffer_load_dwordx4 v1, s[12:15], s45 offen lds\n"
    "ds_read_b128 a[44:47], v36 offset:24576\n"
    "v_exp_f32 v194, v194\n"
    "v_cvt_pk_bf16_f32 v192, v192, v193\n"
    "v_mfma_f32_16x16x32_bf16 v[124:127], a[104:107], v[164:167], v[124:127]\n"
    "v_exp_f32 v195, v195\n"
    "v_mfma_f32_16x16x32_bf16 v[136:139], a[112:115], v[164:167], v[136:139]\n"
    "buffer_load_dwordx4 v2, s[12:15], s45 offen offset:1024 lds\n"
    "ds_read_b128 a[48:51], v33 offset:28672\n"
    "v_exp_f32 v180, v180\n"
    "v_cvt_pk_bf16_f32 v193, v194, v195\n"
    "v_mfma_f32_16x16x32_bf16 v[136:139], a[116:119], v[172:175], v[136:139]\n"
    "buffer_load_dwordx4 v3, s[12:15], s45 offen offset:2048 lds\n"
    "v_exp_f32 v181, v181\n"
    "v_mfma_f32_16x16x32_bf16 v[140:143], a[116:119], v[196:199], v[140:143]\n"
    "ds_read_b128 a[52:55], v34 offset:28672\n"
    "v_exp_f32 v182, v182\n"
    "v_cvt_pk_bf16_f32 v170, v180, v181\n"
    "v_mfma_f32_16x16x32_bf16 v[140:143], a[112:115], v[188:191], v[140:143]\n"
    "buffer_load_dwordx4 v4, s[12:15], s45 offen offset:3072 lds\n"
    "v_exp_f32 v183, v183\n"
    "v_mfma_f32_16x16x32_bf16 v[152:155], a[120:123], v[188:191], v[152:155]\n"
    "ds_read_b128 a[56:59], v35 offset:28672\n"
    "v_exp_f32 v204, v204\n"
    "v_cvt_pk_bf16_f32 v171, v182, v183\n"
    "v_mfma_f32_16x16x32_bf16 v[152:155], a[124:127], v[196:199], v[152:155]\n"
    "s_mov_b32 m0, 57344\n"
    "s_mov_b32 s45, 2220032\n"
    "s_waitcnt vmcnt(24)\n"
    "buffer_load_dwordx4 v9, s[16:19], s45 offen nt lds\n"
    "v_exp_f32 v205, v205\n"
    "v_mfma_f32_16x16x32_bf16 v[148:151], a[124:127], v[172:175], v[148:151]\n"
    "buffer_load_dwordx4 v10, s[16:19], s45 offen offset:1024 nt lds\n"
    "ds_read_b128 a[60:63], v36 offset:28672\n"
    "v_exp_f32 v206, v206\n"
    "v_cvt_pk_bf16_f32 v194, v204, v205\n"
    "v_mfma_f32_16x16x32_bf16 v[148:151], a[120:123], v[164:167], v[148:151]\n"
    "v_exp_f32 v207, v207\n"
    "v_mfma_f32_16x16x32_bf16 v[244:247], a[224:227], v[164:167], v[244:247]\n"
    "s_waitcnt vmcnt(20)\n"
    "buffer_load_dwordx4 v11, s[16:19], s45 offen offset:2048 nt lds\n"
    "v_mfma_f32_16x16x32_bf16 v[244:247], a[224:227], v[172:175], v[244:247]\n"
    "v_cvt_pk_bf16_f32 v195, v206, v207\n"
    "v_mfma_f32_16x16x32_bf16 v[248:251], a[224:227], v[188:191], v[248:251]\n"
    "buffer_load_dwordx4 v12, s[16:19], s45 offen offset:3072 nt lds\n"
    "v_mfma_f32_16x16x32_bf16 v[248:251], a[224:227], v[196:199], v[248:251]\n"
    "s_waitcnt vmcnt(36) lgkmcnt(0)\n"
    "s_barrier\n"
    "v_mfma_f32_16x16x32_bf16 v[164:167], a[0:3], a[176:179], v[232:235]\n"
    "ds_read_b64_tr_b16 a[64:65], v25 offset:0\n"
    "s_mov_b32 m0, 122880\n"
    "s_mov_b32 s45, 2187264\n"
    "s_waitcnt vmcnt(20)\n"
    "buffer_load_dwordx4 v13, s[20:23], s45 offen nt lds\n"
    "v_mfma_f32_16x16x32_bf16 v[164:167], a[4:7], a[180:183], v[164:167]\n"
    "ds_read_b64_tr_b16 a[66:67], v25 offset:4096\n"
    "buffer_load_dwordx4 v14, s[20:23], s45 offen offset:1024 nt lds\n"
    "v_mfma_f32_16x16x32_bf16 v[164:167], a[8:11], a[184:187], v[164:167]\n"
    "ds_read_b64_tr_b16 a[68:69], v25 offset:8192\n"
    "v_mfma_f32_16x16x32_bf16 v[164:167], a[12:15], a[188:191], v[164:167]\n"
    "ds_read_b64_tr_b16 a[70:71], v25 offset:12288\n"
    "v_add_u32 v25, 0x4000, v25\n"
    "s_waitcnt vmcnt(20)\n"
    "buffer_load_dwordx4 v13, s[20:23], s45 offen offset:2048 nt lds\n"
    "v_mfma_f32_16x16x32_bf16 v[188:191], a[12:15], a[204:207], v[236:239]\n"
    "ds_read_b64_tr_b16 a[72:73], v26 offset:0\n"
    "v_mfma_f32_16x16x32_bf16 v[188:191], a[8:11], a[200:203], v[188:191]\n"
    "ds_read_b64_tr_b16 a[74:75], v26 offset:4096\n"
    "buffer_load_dwordx4 v14, s[20:23], s45 offen offset:3072 nt lds\n"
    "v_mfma_f32_16x16x32_bf16 v[188:191], a[4:7], a[196:199], v[188:191]\n"
    "ds_read_b64_tr_b16 a[76:77], v26 offset:8192\n"
    "v_readlane_b32 s39, v16, 1\n"
    "s_mov_b32 m0, 16384\n"
    "s_add_u32 s45, s39, 0\n"
    "s_waitcnt vmcnt(40)\n"
    "buffer_load_dwordx4 v1, s[12:15], s45 offen lds\n"
    "v_mfma_f32_16x16x32_bf16 v[188:191], a[0:3], a[192:195], v[188:191]\n"
    "ds_read_b64_tr_b16 a[78:79], v26 offset:12288\n"
    "v_add_u32 v26, 0x4000, v26\n"
    "v_mfma_f32_16x16x32_bf16 v[200:203], a[16:19], a[192:195], v[236:239]\n"
    "ds_read_b64_tr_b16 a[80:81], v27 offset:0\n"
    "buffer_load_dwordx4 v2, s[12:15], s45 offen offset:1024 lds\n"
    "v_mfma_f32_16x16x32_bf16 v[200:203], a[20:23], a[196:199], v[200:203]\n"
    "ds_read_b64_tr_b16 a[82:83], v27 offset:4096\n"
    "v_mfma_f32_16x16x32_bf16 v[200:203], a[24:27], a[200:203], v[200:203]\n"
    "ds_read_b64_tr_b16 a[84:85], v27 offset:8192\n"
    "buffer_load_dwordx4 v3, s[12:15], s45 offen offset:2048 lds\n"
    "v_mfma_f32_16x16x32_bf16 v[200:203], a[28:31], a[204:207], v[200:203]\n"
    "ds_read_b64_tr_b16 a[86:87], v27 offset:12288\n"
    "v_add_u32 v27, 0x4000, v27\n"
    "buffer_load_dwordx4 v4, s[12:15], s45 offen offset:3072 lds\n"
    "v_mfma_f32_16x16x32_bf16 v[176:179], a[28:31], a[188:191], v[232:235]\n"
    "ds_read_b64_tr_b16 a[88:89], v28 offset:0\n"
    "v_xor_b32 v33, 0x8000, v33\n"
    "v_mfma_f32_16x16x32_bf16 v[176:179], a[24:27], a[184:187], v[176:179]\n"
    "ds_read_b64_tr_b16 a[90:91], v28 offset:4096\n"
    "s_mov_b32 m0, 61440\n"
    "s_mov_b32 s45, 2224128\n"
    "s_waitcnt vmcnt(24)\n"
    "buffer_load_dwordx4 v9, s[16:19], s45 offen nt lds\n"
    "v_mfma_f32_16x16x32_bf16 v[176:179], a[20:23], a[180:183], v[176:179]\n"
    "ds_read_b64_tr_b16 a[92:93], v28 offset:8192\n"
    "v_xor_b32 v34, 0x8000, v34\n"
    "v_mfma_f32_16x16x32_bf16 v[176:179], a[16:19], a[176:179], v[176:179]\n"
    "ds_read_b64_tr_b16 a[94:95], v28 offset:12288\n"
    "v_add_u32 v28, 0x4000, v28\n"
    "buffer_load_dwordx4 v10, s[16:19], s45 offen offset:1024 nt lds\n"
    "v_mfma_f32_16x16x32_bf16 v[172:175], a[32:35], a[176:179], v[232:235]\n"
    "ds_read_b64_tr_b16 a[96:97], v29 offset:0\n"
    "v_xor_b32 v35, 0x8000, v35\n"
    "v_mfma_f32_16x16x32_bf16 v[172:175], a[36:39], a[180:183], v[172:175]\n"
    "ds_read_b64_tr_b16 a[98:99], v29 offset:4096\n"
    "s_waitcnt vmcnt(20)\n"
    "buffer_load_dwordx4 v11, s[16:19], s45 offen offset:2048 nt lds\n"
    "v_mfma_f32_16x16x32_bf16 v[172:175], a[40:43], a[184:187], v[172:175]\n"
    "ds_read_b64_tr_b16 a[100:101], v29 offset:8192\n"
    "v_xor_b32 v36, 0x8000, v36\n"
    "buffer_load_dwordx4 v12, s[16:19], s45 offen offset:3072 nt lds\n"
    "v_mfma_f32_16x16x32_bf16 v[172:175], a[44:47], a[188:191], v[172:175]\n"
    "ds_read_b64_tr_b16 a[102:103], v29 offset:12288\n"
    "v_add_u32 v29, 0x4000, v29\n"
    "v_mfma_f32_16x16x32_bf16 v[196:199], a[44:47], a[204:207], v[236:239]\n"
    "ds_read_b64_tr_b16 a[104:105], v30 offset:0\n"
    "s_mov_b32 m0, 20480\n"
    "s_add_u32 s45, s39, 65536\n"
    "s_waitcnt vmcnt(40)\n"
    "buffer_load_dwordx4 v1, s[12:15], s45 offen lds\n"
    "v_mfma_f32_16x16x32_bf16 v[196:199], a[40:43], a[200:203], v[196:199]\n"
    "ds_read_b64_tr_b16 a[106:107], v30 offset:4096\n"
    "v_mfma_f32_16x16x32_bf16 v[196:199], a[36:39], a[196:199], v[196:199]\n"
    "ds_read_b64_tr_b16 a[108:109], v30 offset:8192\n"
    "buffer_load_dwordx4 v2, s[12:15], s45 offen offset:1024 lds\n"
    "v_mfma_f32_16x16x32_bf16 v[196:199], a[32:35], a[192:195], v[196:199]\n"
    "ds_read_b64_tr_b16 a[110:111], v30 offset:12288\n"
    "v_add_u32 v30, 0x4000, v30\n"
    "buffer_load_dwordx4 v3, s[12:15], s45 offen offset:2048 lds\n"
    "v_mfma_f32_16x16x32_bf16 v[204:207], a[48:51], a[192:195], v[236:239]\n"
    "ds_read_b64_tr_b16 a[112:113], v31 offset:0\n"
    "v_mfma_f32_16x16x32_bf16 v[204:207], a[52:55], a[196:199], v[204:207]\n"
    "ds_read_b64_tr_b16 a[114:115], v31 offset:4096\n"
    "buffer_load_dwordx4 v4, s[12:15], s45 offen offset:3072 lds\n"
    "v_mfma_f32_16x16x32_bf16 v[204:207], a[56:59], a[200:203], v[204:207]\n"
    "ds_read_b64_tr_b16 a[116:117], v31 offset:8192\n"
    "v_mfma_f32_16x16x32_bf16 v[204:207], a[60:63], a[204:207], v[204:207]\n"
    "ds_read_b64_tr_b16 a[118:119], v31 offset:12288\n"
    "v_add_u32 v31, 0x4000, v31\n"
    "s_mov_b32 m0, 126976\n"
    "s_mov_b32 s45, 2191360\n"
    "s_waitcnt vmcnt(24)\n"
    "buffer_load_dwordx4 v13, s[20:23], s45 offen nt lds\n"
    "v_mfma_f32_16x16x32_bf16 v[180:183], a[60:63], a[188:191], v[232:235]\n"
    "ds_read_b64_tr_b16 a[120:121], v32 offset:0\n"
    "buffer_load_dwordx4 v14, s[20:23], s45 offen offset:1024 nt lds\n"
    "v_mfma_f32_16x16x32_bf16 v[180:183], a[56:59], a[184:187], v[180:183]\n"
    "ds_read_b64_tr_b16 a[122:123], v32 offset:4096\n"
    "v_mfma_f32_16x16x32_bf16 v[180:183], a[52:55], a[180:183], v[180:183]\n"
    "ds_read_b64_tr_b16 a[124:125], v32 offset:8192\n"
    "s_waitcnt vmcnt(20)\n"
    "buffer_load_dwordx4 v13, s[20:23], s45 offen offset:2048 nt lds\n"
    "v_mfma_f32_16x16x32_bf16 v[180:183], a[48:51], a[176:179], v[180:183]\n"
    "ds_read_b64_tr_b16 a[126:127], v32 offset:12288\n"
    "v_add_u32 v32, 0x4000, v32\n"
    "s_waitcnt lgkmcnt(0)\n"
    "v_mfma_f32_16x16x32_bf16 v[64:67], a[64:67], v[160:163], v[64:67]\n"
    "buffer_load_dwordx4 v14, s[20:23], s45 offen offset:3072 nt lds\n"
    "ds_read_b128 a[0:3], v33 offset:0\n"
    "v_exp_f32 v164, v164\n"
    "v_mfma_f32_16x16x32_bf16 v[64:67], a[68:71], v[168:171], v[64:67]\n"
    "v_exp_f32 v165, v165\n"
    "v_mfma_f32_16x16x32_bf16 v[68:71], a[68:71], v[192:195], v[68:71]\n"
    "s_mov_b32 m0, 131072\n"
    "s_mov_b32 s45, 2195456\n"
    "s_waitcnt vmcnt(20)\n"
    "buffer_load_dwordx4 v13, s[20:23], s45 offen nt lds\n"
    "ds_read_b128 a[4:7], v34 offset:0\n"
    "v_exp_f32 v166, v166\n"
    "v_cvt_pk_bf16_f32 v164, v164, v165\n"
    "v_mfma_f32_16x16x32_bf16 v[68:71], a[64:67], v[184:187], v[68:71]\n"
    "buffer_load_dwordx4 v14, s[20:23], s45 offen offset:1024 nt lds\n"
    "v_exp_f32 v167, v167\n"
    "v_mfma_f32_16x16x32_bf16 v[80:83], a[72:75], v[184:187], v[80:83]\n"
    "ds_read_b128 a[8:11], v35 offset:0\n"
    "v_exp_f32 v188, v188\n"
    "v_cvt_pk_bf16_f32 v165, v166, v167\n"
    "v_mfma_f32_16x16x32_bf16 v[80:83], a[76:79], v[192:195], v[80:83]\n"
    "s_waitcnt vmcnt(20)\n"
    "buffer_load_dwordx4 v13, s[20:23], s45 offen offset:2048 nt lds\n"
    "v_exp_f32 v189, v189\n"
    "v_mfma_f32_16x16x32_bf16 v[76:79], a[76:79], v[168:171], v[76:79]\n"
    "ds_read_b128 a[12:15], v36 offset:0\n"
    "v_exp_f32 v190, v190\n"
    "v_cvt_pk_bf16_f32 v188, v188, v189\n"
    "v_mfma_f32_16x16x32_bf16 v[76:79], a[72:75], v[160:163], v[76:79]\n"
    "buffer_load_dwordx4 v14, s[20:23], s45 offen offset:3072 nt lds\n"
    "v_exp_f32 v191, v191\n"
    "v_mfma_f32_16x16x32_bf16 v[88:91], a[80:83], v[160:163], v[88:91]\n"
    "s_mov_b32 m0, 24576\n"
    "s_add_u32 s45, s39, 131072\n"
    "s_waitcnt vmcnt(40)\n"
    "buffer_load_dwordx4 v1, s[12:15], s45 offen lds\n"
    "ds_read_b128 a[16:19], v33 offset:4096\n"
    "v_exp_f32 v176, v176\n"
    "v_cvt_pk_bf16_f32 v189, v190, v191\n"
    "v_mfma_f32_16x16x32_bf16 v[88:91], a[84:87], v[168:171], v[88:91]\n"
    "v_exp_f32 v177, v177\n"
    "v_mfma_f32_16x16x32_bf16 v[92:95], a[84:87], v[192:195], v[92:95]\n"
    "buffer_load_dwordx4 v2, s[12:15], s45 offen offset:1024 lds\n"
    "ds_read_b128 a[20:23], v34 offset:4096\n"
    "v_exp_f32 v178, v178\n"
    "v_cvt_pk_bf16_f32 v166, v176, v177\n"
    "v_mfma_f32_16x16x32_bf16 v[92:95], a[80:83], v[184:187], v[92:95]\n"
    "v_exp_f32 v179, v179\n"
    "v_mfma_f32_16x16x32_bf16 v[104:107], a[88:91], v[184:187], v[104:107]\n"
    "buffer_load_dwordx4 v3, s[12:15], s45 offen offset:2048 lds\n"
    "ds_read_b128 a[24:27], v35 offset:4096\n"
    "v_exp_f32 v200, v200\n"
    "v_cvt_pk_bf16_f32 v167, v178, v179\n"
    "v_mfma_f32_16x16x32_bf16 v[104:107], a[92:95], v[192:195], v[104:107]\n"
    "buffer_load_dwordx4 v4, s[12:15], s45 offen offset:3072 lds\n"
    "v_exp_f32 v201, v201\n"
    "v_mfma_f32_16x16x32_bf16 v[100:103], a[92:95], v[168:171], v[100:103]\n"
    "ds_read_b128 a[28:31], v36 offset:4096\n"
    "v_exp_f32 v202, v202\n"
    "v_cvt_pk_bf16_f32 v190, v200, v201\n"
    "v_mfma_f32_16x16x32_bf16 v[100:103], a[88:91], v[160:163], v[100:103]\n"
    "s_mov_b32 m0, 135168\n"
    "s_mov_b32 s45, 2199552\n"
    "s_waitcnt vmcnt(24)\n"
    "buffer_load_dwordx4 v13, s[20:23], s45 offen nt lds\n"
    "v_exp_f32 v203, v203\n"
    "v_mfma_f32_16x16x32_bf16 v[112:115], a[96:99], v[160:163], v[112:115]\n"
    "ds_read_b128 a[32:35], v33 offset:8192\n"
    "v_exp_f32 v172, v172\n"
    "v_cvt_pk_bf16_f32 v191, v202, v203\n"
    "v_mfma_f32_16x16x32_bf16 v[112:115], a[100:103], v[168:171], v[112:115]\n"
    "buffer_load_dwordx4 v14, s[20:23], s45 offen offset:1024 nt lds\n"
    "v_exp_f32 v173, v173\n"
    "v_mfma_f32_16x16x32_bf16 v[116:119], a[100:103], v[192:195], v[116:119]\n"
    "ds_read_b128 a[36:39], v34 offset:8192\n"
    "v_exp_f32 v174, v174\n"
    "v_cvt_pk_bf16_f32 v172, v172, v173\n"
    "v_mfma_f32_16x16x32_bf16 v[116:119], a[96:99], v[184:187], v[116:119]\n"
    "s_waitcnt vmcnt(20)\n"
    "buffer_load_dwordx4 v13, s[20:23], s45 offen offset:2048 nt lds\n"
    "v_exp_f32 v175, v175\n"
    "v_mfma_f32_16x16x32_bf16 v[128:131], a[104:107], v[184:187], v[128:131]\n"
    "buffer_load_dwordx4 v14, s[20:23], s45 offen offset:3072 nt lds\n"
    "ds_read_b128 a[40:43], v35 offset:8192\n"
    "v_exp_f32 v196, v196\n"
    "v_cvt_pk_bf16_f32 v173, v174, v175\n"
    "v_mfma_f32_16x16x32_bf16 v[128:131], a[108:111], v[192:195], v[128:131]\n"
    "v_exp_f32 v197, v197\n"
    "v_mfma_f32_16x16x32_bf16 v[124:127], a[108:111], v[168:171], v[124:127]\n"
    "s_mov_b32 m0, 28672\n"
    "s_add_u32 s45, s39, 196608\n"
    "s_waitcnt vmcnt(40)\n"
    "buffer_load_dwordx4 v1, s[12:15], s45 offen lds\n"
    "ds_read_b128 a[44:47], v36 offset:8192\n"
    "v_exp_f32 v198, v198\n"
    "v_cvt_pk_bf16_f32 v196, v196, v197\n"
    "v_mfma_f32_16x16x32_bf16 v[124:127], a[104:107], v[160:163], v[124:127]\n"
    "v_exp_f32 v199, v199\n"
    "v_mfma_f32_16x16x32_bf16 v[136:139], a[112:115], v[160:163], v[136:139]\n"
    "buffer_load_dwordx4 v2, s[12:15], s45 offen offset:1024 lds\n"
    "ds_read_b128 a[48:51], v33 offset:12288\n"
    "v_exp_f32 v180, v180\n"
    "v_cvt_pk_bf16_f32 v197, v198, v199\n"
    "v_mfma_f32_16x16x32_bf16 v[136:139], a[116:119], v[168:171], v[136:139]\n"
    "buffer_load_dwordx4 v3, s[12:15], s45 offen offset:2048 lds\n"
    "v_exp_f32 v181, v181\n"
    "v_mfma_f32_16x16x32_bf16 v[140:143], a[116:119], v[192:195], v[140:143]\n"
    "ds_read_b128 a[52:55], v34 offset:12288\n"
    "v_exp_f32 v182, v182\n"
    "v_cvt_pk_bf16_f32 v174, v180, v181\n"
    "v_mfma_f32_16x16x32_bf16 v[140:143], a[112:115], v[184:187], v[140:143]\n"
    "buffer_load_dwordx4 v4, s[12:15], s45 offen offset:3072 lds\n"
    "v_exp_f32 v183, v183\n"
    "v_mfma_f32_16x16x32_bf16 v[152:155], a[120:123], v[184:187], v[152:155]\n"
    "ds_read_b128 a[56:59], v35 offset:12288\n"
    "v_exp_f32 v204, v204\n"
    "v_cvt_pk_bf16_f32 v175, v182, v183\n"
    "v_mfma_f32_16x16x32_bf16 v[152:155], a[124:127], v[192:195], v[152:155]\n"
    "s_mov_b32 m0, 139264\n"
    "s_mov_b32 s45, 2203648\n"
    "s_waitcnt vmcnt(24)\n"
    "buffer_load_dwordx4 v13, s[20:23], s45 offen nt lds\n"
    "v_exp_f32 v205, v205\n"
    "v_mfma_f32_16x16x32_bf16 v[148:151], a[124:127], v[168:171], v[148:151]\n"
    "buffer_load_dwordx4 v14, s[20:23], s45 offen offset:1024 nt lds\n"
    "ds_read_b128 a[60:63], v36 offset:12288\n"
    "v_exp_f32 v206, v206\n"
    "v_cvt_pk_bf16_f32 v198, v204, v205\n"
    "v_mfma_f32_16x16x32_bf16 v[148:151], a[120:123], v[160:163], v[148:151]\n"
    "v_exp_f32 v207, v207\n"
    "v_mfma_f32_16x16x32_bf16 v[244:247], a[224:227], v[160:163], v[244:247]\n"
    "s_waitcnt vmcnt(20)\n"
    "buffer_load_dwordx4 v13, s[20:23], s45 offen offset:2048 nt lds\n"
    "v_mfma_f32_16x16x32_bf16 v[244:247], a[224:227], v[168:171], v[244:247]\n"
    "v_cvt_pk_bf16_f32 v199, v206, v207\n"
    "v_mfma_f32_16x16x32_bf16 v[248:251], a[224:227], v[184:187], v[248:251]\n"
    "buffer_load_dwordx4 v14, s[20:23], s45 offen offset:3072 nt lds\n"
    "v_mfma_f32_16x16x32_bf16 v[248:251], a[224:227], v[192:195], v[248:251]\n"
    "s_waitcnt vmcnt(20) lgkmcnt(0)\n"
    "s_barrier\n"
    "v_mfma_f32_16x16x32_bf16 v[160:163], a[0:3], a[176:179], v[232:235]\n"
    "ds_read_b64_tr_b16 a[64:65], v25 offset:0\n"
    "s_mov_b32 m0, 143360\n"
    "s_mov_b32 s45, 2207744\n"
    "s_waitcnt vmcnt(20)\n"
    "buffer_load_dwordx4 v13, s[20:23], s45 offen nt lds\n"
    "v_mfma_f32_16x16x32_bf16 v[160:163], a[4:7], a[180:183], v[160:163]\n"
    "ds_read_b64_tr_b16 a[66:67], v25 offset:4096\n"
    "buffer_load_dwordx4 v14, s[20:23], s45 offen offset:1024 nt lds\n"
    "v_mfma_f32_16x16x32_bf16 v[160:163], a[8:11], a[184:187], v[160:163]\n"
    "ds_read_b64_tr_b16 a[68:69], v25 offset:8192\n"
    "s_add_u32 s46, s56, 0\n"
    "buffer_store_dwordx4 a[128:131], v37, s[24:27], s46 offen offset:0 sc1\n"
    "s_add_u32 s46, s56, 0\n"
    "buffer_store_dwordx4 a[144:147], v37, s[24:27], s46 offen offset:1024 sc1\n"
    "v_mfma_f32_16x16x32_bf16 v[160:163], a[12:15], a[188:191], v[160:163]\n"
    "ds_read_b64_tr_b16 a[70:71], v25 offset:12288\n"
    "v_add_u32 v25, 0x4000, v25\n"
    "s_waitcnt vmcnt(22)\n"
    "buffer_load_dwordx4 v13, s[20:23], s45 offen offset:2048 nt lds\n"
    "v_mfma_f32_16x16x32_bf16 v[184:187], a[12:15], a[204:207], v[236:239]\n"
    "ds_read_b64_tr_b16 a[72:73], v26 offset:0\n"
    "v_mfma_f32_16x16x32_bf16 v[184:187], a[8:11], a[200:203], v[184:187]\n"
    "ds_read_b64_tr_b16 a[74:75], v26 offset:4096\n"
    "buffer_load_dwordx4 v14, s[20:23], s45 offen offset:3072 nt lds\n"
    "s_add_u32 s46, s56, 11264\n"
    "buffer_store_dwordx4 a[132:135], v37, s[24:27], s46 offen offset:0 sc1\n"
    "v_mfma_f32_16x16x32_bf16 v[184:187], a[4:7], a[196:199], v[184:187]\n"
    "ds_read_b64_tr_b16 a[76:77], v26 offset:8192\n"
    "v_mfma_f32_16x16x32_bf16 v[184:187], a[0:3], a[192:195], v[184:187]\n"
    "ds_read_b64_tr_b16 a[78:79], v26 offset:12288\n"
    "v_add_u32 v26, 0x4000, v26\n"
    "v_readlane_b32 s39, v16, 2\n"
    "s_mov_b32 m0, 32768\n"
    "s_add_u32 s45, s39, 0\n"
    "s_waitcnt vmcnt(40)\n"
    "buffer_load_dwordx4 v1, s[12:15], s45 offen lds\n"
    "s_add_u32 s46, s56, 11264\n"
    "buffer_store_dwordx4 a[148:151], v37, s[24:27], s46 offen offset:1024 sc1\n"
    "v_mfma_f32_16x16x32_bf16 v[200:203], a[16:19], a[192:195], v[236:239]\n"
    "ds_read_b64_tr_b16 a[80:81], v27 offset:0\n"
    "v_mfma_f32_16x16x32_bf16 v[200:203], a[20:23], a[196:199], v[200:203]\n"
    "ds_read_b64_tr_b16 a[82:83], v27 offset:4096\n"
    "buffer_load_dwordx4 v2, s[12:15], s45 offen offset:1024 lds\n"
    "v_mfma_f32_16x16x32_bf16 v[200:203], a[24:27], a[200:203], v[200:203]\n"
    "ds_read_b64_tr_b16 a[84:85], v27 offset:8192\n"
    "s_add_u32 s46, s56, 22528\n"
    "buffer_store_dwordx4 a[136:139], v37, s[24:27], s46 offen offset:0 sc1\n"
    "v_mfma_f32_16x16x32_bf16 v[200:203], a[28:31], a[204:207], v[200:203]\n"
    "ds_read_b64_tr_b16 a[86:87], v27 offset:12288\n"
    "v_add_u32 v27, 0x4000, v27\n"
    "buffer_load_dwordx4 v3, s[12:15], s45 offen offset:2048 lds\n"
    "v_mfma_f32_16x16x32_bf16 v[176:179], a[28:31], a[188:191], v[232:235]\n"
    "ds_read_b64_tr_b16 a[88:89], v28 offset:0\n"
    "s_add_u32 s46, s56, 22528\n"
    "buffer_store_dwordx4 a[152:155], v37, s[24:27], s46 offen offset:1024 sc1\n"
    "v_mfma_f32_16x16x32_bf16 v[176:179], a[24:27], a[184:187], v[176:179]\n"
    "ds_read_b64_tr_b16 a[90:91], v28 offset:4096\n"
    "buffer_load_dwordx4 v4, s[12:15], s45 offen offset:3072 lds\n"
    "v_mfma_f32_16x16x32_bf16 v[176:179], a[20:23], a[180:183], v[176:179]\n"
    "ds_read_b64_tr_b16 a[92:93], v28 offset:8192\n"
    "v_mfma_f32_16x16x32_bf16 v[176:179], a[16:19], a[176:179], v[176:179]\n"
    "ds_read_b64_tr_b16 a[94:95], v28 offset:12288\n"
    "v_add_u32 v28, 0x4000, v28\n"
    "s_mov_b32 m0, 65536\n"
    "s_mov_b32 s45, 2211840\n"
    "s_waitcnt vmcnt(30)\n"
    "buffer_load_dwordx4 v13, s[20:23], s45 offen nt lds\n"
    "s_add_u32 s46, s56, 33792\n"
    "buffer_store_dwordx4 a[140:143], v37, s[24:27], s46 offen offset:0 sc1\n"
    "v_mfma_f32_16x16x32_bf16 v[168:171], a[32:35], a[176:179], v[232:235]\n"
    "ds_read_b64_tr_b16 a[96:97], v29 offset:0\n"
    "v_mfma_f32_16x16x32_bf16 v[168:171], a[36:39], a[180:183], v[168:171]\n"
    "ds_read_b64_tr_b16 a[98:99], v29 offset:4096\n"
    "buffer_load_dwordx4 v14, s[20:23], s45 offen offset:1024 nt lds\n"
    "s_add_u32 s46, s56, 33792\n"
    "buffer_store_dwordx4 a[156:159], v37, s[24:27], s46 offen offset:1024 sc1\n"
    "v_mfma_f32_16x16x32_bf16 v[168:171], a[40:43], a[184:187], v[168:171]\n"
    "ds_read_b64_tr_b16 a[100:101], v29 offset:8192\n"
    "s_waitcnt vmcnt(28)\n"
    "buffer_load_dwordx4 v13, s[20:23], s45 offen offset:2048 nt lds\n"
    "v_mfma_f32_16x16x32_bf16 v[168:171], a[44:47], a[188:191], v[168:171]\n"
    "ds_read_b64_tr_b16 a[102:103], v29 offset:12288\n"
    "v_add_u32 v29, 0x4000, v29\n"
    "v_mfma_f32_16x16x32_bf16 v[192:195], a[44:47], a[204:207], v[236:239]\n"
    "ds_read_b64_tr_b16 a[104:105], v30 offset:0\n"
    "buffer_load_dwordx4 v14, s[20:23], s45 offen offset:3072 nt lds\n"
    "v_mfma_f32_16x16x32_bf16 v[192:195], a[40:43], a[200:203], v[192:195]\n"
    "ds_read_b64_tr_b16 a[106:107], v30 offset:4096\n"
    "v_mfma_f32_16x16x32_bf16 v[192:195], a[36:39], a[196:199], v[192:195]\n"
    "ds_read_b64_tr_b16 a[108:109], v30 offset:8192\n"
    "s_mov_b32 m0, 36864\n"
    "s_add_u32 s45, s39, 65536\n"
    "s_waitcnt vmcnt(40)\n"
    "buffer_load_dwordx4 v1, s[12:15], s45 offen lds\n"
    "v_mfma_f32_16x16x32_bf16 v[192:195], a[32:35], a[192:195], v[192:195]\n"
    "ds_read_b64_tr_b16 a[110:111], v30 offset:12288\n"
    "v_add_u32 v30, 0x4000, v30\n"
    "v_mfma_f32_16x16x32_bf16 v[204:207], a[48:51], a[192:195], v[236:239]\n"
    "ds_read_b64_tr_b16 a[112:113], v31 offset:0\n"
    "buffer_load_dwordx4 v2, s[12:15], s45 offen offset:1024 lds\n"
    "v_mfma_f32_16x16x32_bf16 v[204:207], a[52:55], a[196:199], v[204:207]\n"
    "ds_read_b64_tr_b16 a[114:115], v31 offset:4096\n"
    "v_mfma_f32_16x16x32_bf16 v[204:207], a[56:59], a[200:203], v[204:207]\n"
    "ds_read_b64_tr_b16 a[116:117], v31 offset:8192\n"
    "buffer_load_dwordx4 v3, s[12:15], s45 offen offset:2048 lds\n"
    "v_mfma_f32_16x16x32_bf16 v[204:207], a[60:63], a[204:207], v[204:207]\n"
    "ds_read_b64_tr_b16 a[118:119], v31 offset:12288\n"
    "v_add_u32 v31, 0x4000, v31\n"
    "v_mfma_f32_16x16x32_bf16 v[180:183], a[60:63], a[188:191], v[232:235]\n"
    "ds_read_b64_tr_b16 a[120:121], v32 offset:0\n"
    "buffer_load_dwordx4 v4, s[12:15], s45 offen offset:3072 lds\n"
    "v_mfma_f32_16x16x32_bf16 v[180:183], a[56:59], a[184:187], v[180:183]\n"
    "ds_read_b64_tr_b16 a[122:123], v32 offset:4096\n"
    "v_mfma_f32_16x16x32_bf16 v[180:183], a[52:55], a[180:183], v[180:183]\n"
    "ds_read_b64_tr_b16 a[124:125], v32 offset:8192\n"
    "s_mov_b32 m0, 69632\n"
    "s_mov_b32 s45, 2215936\n"
    "s_waitcnt vmcnt(32)\n"
    "buffer_load_dwordx4 v13, s[20:23], s45 offen nt lds\n"
    "v_mfma_f32_16x16x32_bf16 v[180:183], a[48:51], a[176:179], v[180:183]\n"
    "ds_read_b64_tr_b16 a[126:127], v32 offset:12288\n"
    "v_add_u32 v32, 0x4000, v32\n"
    "s_waitcnt lgkmcnt(0)\n"
    "v_mfma_f32_16x16x32_bf16 v[64:67], a[64:67], v[164:167], v[64:67]\n"
    "buffer_load_dwordx4 v14, s[20:23], s45 offen offset:1024 nt lds\n"
    "ds_read_b128 a[0:3], v33 offset:16384\n"
    "v_exp_f32 v160, v160\n"
    "v_mfma_f32_16x16x32_bf16 v[64:67], a[68:71], v[172:175], v[64:67]\n"
    "v_exp_f32 v161, v161\n"
    "v_mfma_f32_16x16x32_bf16 v[68:71], a[68:71], v[196:199], v[68:71]\n"
    "s_waitcnt vmcnt(28)\n"
    "buffer_load_dwordx4 v13, s[20:23], s45 offen offset:2048 nt lds\n"
    "ds_read_b128 a[4:7], v34 offset:16384\n"
    "v_exp_f32 v162, v162\n"
    "v_cvt_pk_bf16_f32 v160, v160, v161\n"
    "v_mfma_f32_16x16x32_bf16 v[68:71], a[64:67], v[188:191], v[68:71]\n"
    "buffer_load_dwordx4 v14, s[20:23], s45 offen offset:3072 nt lds\n"
    "v_exp_f32 v163, v163\n"
    "v_mfma_f32_16x16x32_bf16 v[80:83], a[72:75], v[188:191], v[80:83]\n"
    "ds_read_b128 a[8:11], v35 offset:16384\n"
    "v_exp_f32 v184, v184\n"
    "v_cvt_pk_bf16_f32 v161, v162, v163\n"
    "v_mfma_f32_16x16x32_bf16 v[80:83], a[76:79], v[196:199], v[80:83]\n"
    "s_mov_b32 m0, 40960\n"
    "s_add_u32 s45, s39, 131072\n"
    "s_waitcnt vmcnt(40)\n"
    "buffer_load_dwordx4 v1, s[12:15], s45 offen lds\n"
    "v_exp_f32 v185, v185\n"
    "v_mfma_f32_16x16x32_bf16 v[76:79], a[76:79], v[172:175], v[76:79]\n"
    "ds_read_b128 a[12:15], v36 offset:16384\n"
    "v_exp_f32 v186, v186\n"
    "v_cvt_pk_bf16_f32 v184, v184, v185\n"
    "v_mfma_f32_16x16x32_bf16 v[76:79], a[72:75], v[164:167], v[76:79]\n"
    "buffer_load_dwordx4 v2, s[12:15], s45 offen offset:1024 lds\n"
    "v_exp_f32 v187, v187\n"
    "v_mfma_f32_16x16x32_bf16 v[88:91], a[80:83], v[164:167], v[88:91]\n"
    "ds_read_b128 a[16:19], v33 offset:20480\n"
    "v_exp_f32 v176, v176\n"
    "v_cvt_pk_bf16_f32 v185, v186, v187\n"
    "v_mfma_f32_16x16x32_bf16 v[88:91], a[84:87], v[172:175], v[88:91]\n"
    "buffer_load_dwordx4 v3, s[12:15], s45 offen offset:2048 lds\n"
    "v_exp_f32 v177, v177\n"
    "v_mfma_f32_16x16x32_bf16 v[92:95], a[84:87], v[196:199], v[92:95]\n"
    "ds_read_b128 a[20:23], v34 offset:20480\n"
    "v_exp_f32 v178, v178\n"
    "v_cvt_pk_bf16_f32 v162, v176, v177\n"
    "v_mfma_f32_16x16x32_bf16 v[92:95], a[80:83], v[188:191], v[92:95]\n"
    "buffer_load_dwordx4 v4, s[12:15], s45 offen offset:3072 lds\n"
    "v_exp_f32 v179, v179\n"
    "v_mfma_f32_16x16x32_bf16 v[104:107], a[88:91], v[188:191], v[104:107]\n"
    "ds_read_b128 a[24:27], v35 offset:20480\n"
    "v_exp_f32 v200, v200\n"
    "v_cvt_pk_bf16_f32 v163, v178, v179\n"
    "v_mfma_f32_16x16x32_bf16 v[104:107], a[92:95], v[196:199], v[104:107]\n"
    "s_mov_b32 m0, 73728\n"
    "s_mov_b32 s45, 2220032\n"
    "s_waitcnt vmcnt(32)\n"
    "buffer_load_dwordx4 v13, s[20:23], s45 offen nt lds\n"
    "v_exp_f32 v201, v201\n"
    "v_mfma_f32_16x16x32_bf16 v[100:103], a[92:95], v[172:175], v[100:103]\n"
    "ds_read_b128 a[28:31], v36 offset:20480\n"
    "v_exp_f32 v202, v202\n"
    "v_cvt_pk_bf16_f32 v186, v200, v201\n"
    "v_mfma_f32_16x16x32_bf16 v[100:103], a[88:91], v[164:167], v[100:103]\n"
    "buffer_load_dwordx4 v14, s[20:23], s45 offen offset:1024 nt lds\n"
    "v_exp_f32 v203, v203\n"
    "v_mfma_f32_16x16x32_bf16 v[112:115], a[96:99], v[164:167], v[112:115]\n"
    "ds_read_b128 a[32:35], v33 offset:24576\n"
    "v_exp_f32 v168, v168\n"
    "v_cvt_pk_bf16_f32 v187, v202, v203\n"
    "v_mfma_f32_16x16x32_bf16 v[112:115], a[100:103], v[172:175], v[112:115]\n"
    "s_waitcnt vmcnt(32)\n"
    "buffer_load_dwordx4 v13, s[20:23], s45 offen offset:2048 nt lds\n"
    "v_exp_f32 v169, v169\n"
    "v_mfma_f32_16x16x32_bf16 v[116:119], a[100:103], v[196:199], v[116:119]\n"
    "ds_read_b128 a[36:39], v34 offset:24576\n"
    "v_exp_f32 v170, v170\n"
    "v_cvt_pk_bf16_f32 v168, v168, v169\n"
    "v_mfma_f32_16x16x32_bf16 v[116:119], a[96:99], v[188:191], v[116:119]\n"
    "buffer_load_dwordx4 v14, s[20:23], s45 offen offset:3072 nt lds\n"
    "v_exp_f32 v171, v171\n"
    "v_mfma_f32_16x16x32_bf16 v[128:131], a[104:107], v[188:191], v[128:131]\n"
    "s_mov_b32 m0, 45056\n"
    "s_add_u32 s45, s39, 196608\n"
    "s_waitcnt vmcnt(40)\n"
    "buffer_load_dwordx4 v1, s[12:15], s45 offen lds\n"
    "ds_read_b128 a[40:43], v35 offset:24576\n"
    "v_exp_f32 v192, v192\n"
    "v_cvt_pk_bf16_f32 v169, v170, v171\n"
    "v_mfma_f32_16x16x32_bf16 v[128:131], a[108:111], v[196:199], v[128:131]\n"
    "v_exp_f32 v193, v193\n"
    "v_mfma_f32_16x16x32_bf16 v[124:127], a[108:111], v[172:175], v[124:127]\n"
    "buffer_load_dwordx4 v2, s[12:15], s45 offen offset:1024 lds\n"
    "ds_read_b128 a[44:47], v36 offset:24576\n"
    "v_exp_f32 v194, v194\n"
    "v_cvt_pk_bf16_f32 v192, v192, v193\n"
    "v_mfma_f32_16x16x32_bf16 v[124:127], a[104:107], v[164:167], v[124:127]\n"
    "v_exp_f32 v195, v195\n"
    "v_mfma_f32_16x16x32_bf16 v[136:139], a[112:115], v[164:167], v[136:139]\n"
    "buffer_load_dwordx4 v3, s[12:15], s45 offen offset:2048 lds\n"
    "ds_read_b128 a[48:51], v33 offset:28672\n"
    "v_exp_f32 v180, v180\n"
    "v_cvt_pk_bf16_f32 v193, v194, v195\n"
    "v_mfma_f32_16x16x32_bf16 v[136:139], a[116:119], v[172:175], v[136:139]\n"
    "v_exp_f32 v181, v181\n"
    "v_mfma_f32_16x16x32_bf16 v[140:143], a[116:119], v[196:199], v[140:143]\n"
    "buffer_load_dwordx4 v4, s[12:15], s45 offen offset:3072 lds\n"
    "ds_read_b128 a[52:55], v34 offset:28672\n"
    "v_exp_f32 v182, v182\n"
    "v_cvt_pk_bf16_f32 v170, v180, v181\n"
    "v_mfma_f32_16x16x32_bf16 v[140:143], a[112:115], v[188:191], v[140:143]\n"
    "v_exp_f32 v183, v183\n"
    "v_mfma_f32_16x16x32_bf16 v[152:155], a[120:123], v[188:191], v[152:155]\n"
    "s_mov_b32 m0, 77824\n"
    "s_mov_b32 s45, 2224128\n"
    "s_waitcnt vmcnt(34)\n"
    "buffer_load_dwordx4 v13, s[20:23], s45 offen nt lds\n"
    "ds_read_b128 a[56:59], v35 offset:28672\n"
    "v_exp_f32 v204, v204\n"
    "v_cvt_pk_bf16_f32 v171, v182, v183\n"
    "v_mfma_f32_16x16x32_bf16 v[152:155], a[124:127], v[196:199], v[152:155]\n"
    "v_exp_f32 v205, v205\n"
    "v_mfma_f32_16x16x32_bf16 v[148:151], a[124:127], v[172:175], v[148:151]\n"
    "buffer_load_dwordx4 v14, s[20:23], s45 offen offset:1024 nt lds\n"
    "ds_read_b128 a[60:63], v36 offset:28672\n"
    "v_exp_f32 v206, v206\n"
    "v_cvt_pk_bf16_f32 v194, v204, v205\n"
    "v_mfma_f32_16x16x32_bf16 v[148:151], a[120:123], v[164:167], v[148:151]\n"
    "v_exp_f32 v207, v207\n"
    "v_mfma_f32_16x16x32_bf16 v[244:247], a[224:227], v[164:167], v[244:247]\n"
    "s_waitcnt vmcnt(25)\n"
    "buffer_load_dwordx4 v13, s[20:23], s45 offen offset:2048 nt lds\n"
    "v_mfma_f32_16x16x32_bf16 v[244:247], a[224:227], v[172:175], v[244:247]\n"
    "v_cvt_pk_bf16_f32 v195, v206, v207\n"
    "v_mfma_f32_16x16x32_bf16 v[248:251], a[224:227], v[188:191], v[248:251]\n"
    "buffer_load_dwordx4 v14, s[20:23], s45 offen offset:3072 nt lds\n"
    "v_mfma_f32_16x16x32_bf16 v[248:251], a[224:227], v[196:199], v[248:251]\n"
    "s_waitcnt vmcnt(26) lgkmcnt(0)\n"
    "s_barrier\n"
    "s_lshl_b32 s57, %5, 3\n"
    "v_mov_b64 v[62:63], s[62:63]\n"
    "s_mov_b64 exec, 1\n"
    "buffer_store_dwordx2 v[62:63], off, s[32:35], s57 sc1\n"
    "s_mov_b64 exec, -1\n"
    "v_mfma_f32_16x16x32_bf16 v[164:167], a[0:3], a[176:179], v[232:235]\n"
    "ds_read_b64_tr_b16 a[64:65], v25 offset:0\n"
    "v_readlane_b32 s39, v16, 3\n"
    "s_mov_b32 m0, 49152\n"
    "s_add_u32 s45, s39, 0\n"
    "s_waitcnt vmcnt(40)\n"
    "buffer_load_dwordx4 v1, s[12:15], s45 offen lds\n"
    "v_mfma_f32_16x16x32_bf16 v[164:167], a[4:7], a[180:183], v[164:167]\n"
    "ds_read_b64_tr_b16 a[66:67], v25 offset:4096\n"
    "v_mfma_f32_16x16x32_bf16 v[164:167], a[8:11], a[184:187], v[164:167]\n"
    "ds_read_b64_tr_b16 a[68:69], v25 offset:8192\n"
    "buffer_load_dwordx4 v2, s[12:15], s45 offen offset:1024 lds\n"
    "v_mfma_f32_16x16x32_bf16 v[164:167], a[12:15], a[188:191], v[164:167]\n"
    "ds_read_b64_tr_b16 a[70:71], v25 offset:12288\n"
    "v_add_u32 v25, 0xffff0000, v25\n"
    "v_mfma_f32_16x16x32_bf16 v[188:191], a[12:15], a[204:207], v[236:239]\n"
    "ds_read_b64_tr_b16 a[72:73], v26 offset:0\n"
    "buffer_load_dwordx4 v3, s[12:15], s45 offen offset:2048 lds\n"
    "v_mfma_f32_16x16x32_bf16 v[188:191], a[8:11], a[200:203], v[188:191]\n"
    "ds_read_b64_tr_b16 a[74:75], v26 offset:4096\n"
    "v_mfma_f32_16x16x32_bf16 v[188:191], a[4:7], a[196:199], v[188:191]\n"
    "ds_read_b64_tr_b16 a[76:77], v26 offset:8192\n"
    "buffer_load_dwordx4 v4, s[12:15], s45 offen offset:3072 lds\n"
    "v_mfma_f32_16x16x32_bf16 v[188:191], a[0:3], a[192:195], v[188:191]\n"
    "ds_read_b64_tr_b16 a[78:79], v26 offset:12288\n"
    "v_add_u32 v26, 0xffff0000, v26\n"
    "v_mfma_f32_16x16x32_bf16 v[200:203], a[16:19], a[192:195], v[236:239]\n"
    "ds_read_b64_tr_b16 a[80:81], v27 offset:0\n"
    "s_mov_b32 m0, 53248\n"
    "s_add_u32 s45, s39, 65536\n"
    "s_waitcnt vmcnt(40)\n"
    "buffer_load_dwordx4 v1, s[12:15], s45 offen lds\n"
    "v_mfma_f32_16x16x32_bf16 v[200:203], a[20:23], a[196:199], v[200:203]\n"
    "ds_read_b64_tr_b16 a[82:83], v27 offset:4096\n"
    "v_mfma_f32_16x16x32_bf16 v[200:203], a[24:27], a[200:203], v[200:203]\n"
    "ds_read_b64_tr_b16 a[84:85], v27 offset:8192\n"
    "buffer_load_dwordx4 v2, s[12:15], s45 offen offset:1024 lds\n"
    "v_mfma_f32_16x16x32_bf16 v[200:203], a[28:31], a[204:207], v[200:203]\n"
    "ds_read_b64_tr_b16 a[86:87], v27 offset:12288\n"
    "v_add_u32 v27, 0xffff0000, v27\n"
    "v_mfma_f32_16x16x32_bf16 v[176:179], a[28:31], a[188:191], v[232:235]\n"
    "ds_read_b64_tr_b16 a[88:89], v28 offset:0\n"
    "v_xor_b32 v33, 0x8000, v33\n"
    "buffer_load_dwordx4 v3, s[12:15], s45 offen offset:2048 lds\n"
    "v_mfma_f32_16x16x32_bf16 v[176:179], a[24:27], a[184:187], v[176:179]\n"
    "ds_read_b64_tr_b16 a[90:91], v28 offset:4096\n"
    "v_mfma_f32_16x16x32_bf16 v[176:179], a[20:23], a[180:183], v[176:179]\n"
    "ds_read_b64_tr_b16 a[92:93], v28 offset:8192\n"
    "v_xor_b32 v34, 0x8000, v34\n"
    "buffer_load_dwordx4 v4, s[12:15], s45 offen offset:3072 lds\n"
    "v_mfma_f32_16x16x32_bf16 v[176:179], a[16:19], a[176:179], v[176:179]\n"
    "ds_read_b64_tr_b16 a[94:95], v28 offset:12288\n"
    "v_add_u32 v28, 0xffff0000, v28\n"
    "v_mfma_f32_16x16x32_bf16 v[172:175], a[32:35], a[176:179], v[232:235]\n"
    "ds_read_b64_tr_b16 a[96:97], v29 offset:0\n"
    "v_xor_b32 v35, 0x8000, v35\n"
    "v_mfma_f32_16x16x32_bf16 v[172:175], a[36:39], a[180:183], v[172:175]\n"
    "ds_read_b64_tr_b16 a[98:99], v29 offset:4096\n"
    "s_mov_b32 m0, 57344\n"
    "s_add_u32 s45, s39, 131072\n"
    "s_waitcnt vmcnt(40)\n"
    "buffer_load_dwordx4 v1, s[12:15], s45 offen lds\n"
    "v_mfma_f32_16x16x32_bf16 v[172:175], a[40:43], a[184:187], v[172:175]\n"
    "ds_read_b64_tr_b16 a[100:101], v29 offset:8192\n"
    "v_xor_b32 v36, 0x8000, v36\n"
    "v_mfma_f32_16x16x32_bf16 v[172:175], a[44:47], a[188:191], v[172:175]\n"
    "ds_read_b64_tr_b16 a[102:103], v29 offset:12288\n"
    "v_add_u32 v29, 0xffff0000, v29\n"
    "buffer_load_dwordx4 v2, s[12:15], s45 offen offset:1024 lds\n"
    "v_mfma_f32_16x16x32_bf16 v[196:199], a[44:47], a[204:207], v[236:239]\n"
    "ds_read_b64_tr_b16 a[104:105], v30 offset:0\n"
    "v_mfma_f32_16x16x32_bf16 v[196:199], a[40:43], a[200:203], v[196:199]\n"
    "ds_read_b64_tr_b16 a[106:107], v30 offset:4096\n"
    "buffer_load_dwordx4 v3, s[12:15], s45 offen offset:2048 lds\n"
    "v_mfma_f32_16x16x32_bf16 v[196:199], a[36:39], a[196:199], v[196:199]\n"
    "ds_read_b64_tr_b16 a[108:109], v30 offset:8192\n"
    "v_mfma_f32_16x16x32_bf16 v[196:199], a[32:35], a[192:195], v[196:199]\n"
    "ds_read_b64_tr_b16 a[110:111], v30 offset:12288\n"
    "v_add_u32 v30, 0xffff0000, v30\n"
    "buffer_load_dwordx4 v4, s[12:15], s45 offen offset:3072 lds\n"
    "v_mfma_f32_16x16x32_bf16 v[204:207], a[48:51], a[192:195], v[236:239]\n"
    "ds_read_b64_tr_b16 a[112:113], v31 offset:0\n"
    "v_mfma_f32_16x16x32_bf16 v[204:207], a[52:55], a[196:199], v[204:207]\n"
    "ds_read_b64_tr_b16 a[114:115], v31 offset:4096\n"
    "s_mov_b32 m0, 61440\n"
    "s_add_u32 s45, s39, 196608\n"
    "s_waitcnt vmcnt(40)\n"
    "buffer_load_dwordx4 v1, s[12:15], s45 offen lds\n"
    "v_mfma_f32_16x16x32_bf16 v[204:207], a[56:59], a[200:203], v[204:207]\n"
    "ds_read_b64_tr_b16 a[116:117], v31 offset:8192\n"
    "v_mfma_f32_16x16x32_bf16 v[204:207], a[60:63], a[204:207], v[204:207]\n"
    "ds_read_b64_tr_b16 a[118:119], v31 offset:12288\n"
    "v_add_u32 v31, 0xffff0000, v31\n"
    "buffer_load_dwordx4 v2, s[12:15], s45 offen offset:1024 lds\n"
    "v_mfma_f32_16x16x32_bf16 v[180:183], a[60:63], a[188:191], v[232:235]\n"
    "ds_read_b64_tr_b16 a[120:121], v32 offset:0\n"
    "v_mfma_f32_16x16x32_bf16 v[180:183], a[56:59], a[184:187], v[180:183]\n"
    "ds_read_b64_tr_b16 a[122:123], v32 offset:4096\n"
    "buffer_load_dwordx4 v3, s[12:15], s45 offen offset:2048 lds\n"
    "v_mfma_f32_16x16x32_bf16 v[180:183], a[52:55], a[180:183], v[180:183]\n"
    "ds_read_b64_tr_b16 a[124:125], v32 offset:8192\n"
    "v_mfma_f32_16x16x32_bf16 v[180:183], a[48:51], a[176:179], v[180:183]\n"
    "ds_read_b64_tr_b16 a[126:127], v32 offset:12288\n"
    "v_add_u32 v32, 0xffff0000, v32\n"
    "buffer_load_dwordx4 v4, s[12:15], s45 offen offset:3072 lds\n"
    "s_waitcnt lgkmcnt(0)\n"
    "v_mfma_f32_16x16x32_bf16 v[64:67], a[64:67], v[160:163], v[64:67]\n"
    "v_exp_f32 v164, v164\n"
    "v_mfma_f32_16x16x32_bf16 v[64:67], a[68:71], v[168:171], v[64:67]\n"
    "v_exp_f32 v165, v165\n"
    "v_mfma_f32_16x16x32_bf16 v[68:71], a[68:71], v[192:195], v[68:71]\n"
    "v_readlane_b32 s39, v16, 5\n"
    "s_mov_b32 m0, 81920\n"
    "s_add_u32 s45, s39, 0\n"
    "s_waitcnt vmcnt(40)\n"
    "buffer_load_dwordx4 v1, s[12:15], s45 offen lds\n"
    "v_exp_f32 v166, v166\n"
    "v_cvt_pk_bf16_f32 v164, v164, v165\n"
    "v_mfma_f32_16x16x32_bf16 v[68:71], a[64:67], v[184:187], v[68:71]\n"
    "v_exp_f32 v167, v167\n"
    "v_mfma_f32_16x16x32_bf16 v[80:83], a[72:75], v[184:187], v[80:83]\n"
    "buffer_load_dwordx4 v2, s[12:15], s45 offen offset:1024 lds\n"
    "v_exp_f32 v188, v188\n"
    "v_cvt_pk_bf16_f32 v165, v166, v167\n"
    "v_mfma_f32_16x16x32_bf16 v[80:83], a[76:79], v[192:195], v[80:83]\n"
    "v_exp_f32 v189, v189\n"
    "v_mfma_f32_16x16x32_bf16 v[76:79], a[76:79], v[168:171], v[76:79]\n"
    "buffer_load_dwordx4 v3, s[12:15], s45 offen offset:2048 lds\n"
    "v_exp_f32 v190, v190\n"
    "v_cvt_pk_bf16_f32 v188, v188, v189\n"
    "v_mfma_f32_16x16x32_bf16 v[76:79], a[72:75], v[160:163], v[76:79]\n"
    "v_exp_f32 v191, v191\n"
    "v_mfma_f32_16x16x32_bf16 v[88:91], a[80:83], v[160:163], v[88:91]\n"
    "buffer_load_dwordx4 v4, s[12:15], s45 offen offset:3072 lds\n"
    "v_exp_f32 v176, v176\n"
    "v_cvt_pk_bf16_f32 v189, v190, v191\n"
    "v_mfma_f32_16x16x32_bf16 v[88:91], a[84:87], v[168:171], v[88:91]\n"
    "v_exp_f32 v177, v177\n"
    "v_mfma_f32_16x16x32_bf16 v[92:95], a[84:87], v[192:195], v[92:95]\n"
    "s_mov_b32 m0, 86016\n"
    "s_add_u32 s45, s39, 65536\n"
    "s_waitcnt vmcnt(40)\n"
    "buffer_load_dwordx4 v1, s[12:15], s45 offen lds\n"
    "v_exp_f32 v178, v178\n"
    "v_cvt_pk_bf16_f32 v166, v176, v177\n"
    "v_mfma_f32_16x16x32_bf16 v[92:95], a[80:83], v[184:187], v[92:95]\n"
    "v_exp_f32 v179, v179\n"
    "v_mfma_f32_16x16x32_bf16 v[104:107], a[88:91], v[184:187], v[104:107]\n"
    "buffer_load_dwordx4 v2, s[12:15], s45 offen offset:1024 lds\n"
    "v_exp_f32 v200, v200\n"
    "v_cvt_pk_bf16_f32 v167, v178, v179\n"
    "v_mfma_f32_16x16x32_bf16 v[104:107], a[92:95], v[192:195], v[104:107]\n"
    "v_exp_f32 v201, v201\n"
    "v_mfma_f32_16x16x32_bf16 v[100:103], a[92:95], v[168:171], v[100:103]\n"
    "buffer_load_dwordx4 v3, s[12:15], s45 offen offset:2048 lds\n"
    "v_exp_f32 v202, v202\n"
    "v_cvt_pk_bf16_f32 v190, v200, v201\n"
    "v_mfma_f32_16x16x32_bf16 v[100:103], a[88:91], v[160:163], v[100:103]\n"
    "v_exp_f32 v203, v203\n"
    "v_mfma_f32_16x16x32_bf16 v[112:115], a[96:99], v[160:163], v[112:115]\n"
    "buffer_load_dwordx4 v4, s[12:15], s45 offen offset:3072 lds\n"
    "v_exp_f32 v172, v172\n"
    "v_cvt_pk_bf16_f32 v191, v202, v203\n"
    "v_mfma_f32_16x16x32_bf16 v[112:115], a[100:103], v[168:171], v[112:115]\n"
    "v_exp_f32 v173, v173\n"
    "v_mfma_f32_16x16x32_bf16 v[116:119], a[100:103], v[192:195], v[116:119]\n"
    "v_exp_f32 v174, v174\n"
    "v_cvt_pk_bf16_f32 v172, v172, v173\n"
    "v_mfma_f32_16x16x32_bf16 v[116:119], a[96:99], v[184:187], v[116:119]\n"
    "s_mov_b32 m0, 90112\n"
    "s_add_u32 s45, s39, 131072\n"
    "s_waitcnt vmcnt(40)\n"
    "buffer_load_dwordx4 v1, s[12:15], s45 offen lds\n"
    "v_exp_f32 v175, v175\n"
    "v_mfma_f32_16x16x32_bf16 v[128:131], a[104:107], v[184:187], v[128:131]\n"
    "v_exp_f32 v196, v196\n"
    "v_cvt_pk_bf16_f32 v173, v174, v175\n"
    "v_mfma_f32_16x16x32_bf16 v[128:131], a[108:111], v[192:195], v[128:131]\n"
    "buffer_load_dwordx4 v2, s[12:15], s45 offen offset:1024 lds\n"
    "v_exp_f32 v197, v197\n"
    "v_mfma_f32_16x16x32_bf16 v[124:127], a[108:111], v[168:171], v[124:127]\n"
    "v_exp_f32 v198, v198\n"
    "v_cvt_pk_bf16_f32 v196, v196, v197\n"
    "v_mfma_f32_16x16x32_bf16 v[124:127], a[104:107], v[160:163], v[124:127]\n"
    "buffer_load_dwordx4 v3, s[12:15], s45 offen offset:2048 lds\n"
    "v_exp_f32 v199, v199\n"
    "v_mfma_f32_16x16x32_bf16 v[136:139], a[112:115], v[160:163], v[136:139]\n"
    "v_exp_f32 v180, v180\n"
    "v_cvt_pk_bf16_f32 v197, v198, v199\n"
    "v_mfma_f32_16x16x32_bf16 v[136:139], a[116:119], v[168:171], v[136:139]\n"
    "buffer_load_dwordx4 v4, s[12:15], s45 offen offset:3072 lds\n"
    "v_exp_f32 v181, v181\n"
    "v_mfma_f32_16x16x32_bf16 v[140:143], a[116:119], v[192:195], v[140:143]\n"
    "v_exp_f32 v182, v182\n"
    "v_cvt_pk_bf16_f32 v174, v180, v181\n"
    "v_mfma_f32_16x16x32_bf16 v[140:143], a[112:115], v[184:187], v[140:143]\n"
    "s_mov_b32 m0, 94208\n"
    "s_add_u32 s45, s39, 196608\n"
    "s_waitcnt vmcnt(40)\n"
    "buffer_load_dwordx4 v1, s[12:15], s45 offen lds\n"
    "v_exp_f32 v183, v183\n"
    "v_mfma_f32_16x16x32_bf16 v[152:155], a[120:123], v[184:187], v[152:155]\n"
    "v_exp_f32 v204, v204\n"
    "v_cvt_pk_bf16_f32 v175, v182, v183\n"
    "v_mfma_f32_16x16x32_bf16 v[152:155], a[124:127], v[192:195], v[152:155]\n"
    "buffer_load_dwordx4 v2, s[12:15], s45 offen offset:1024 lds\n"
    "v_exp_f32 v205, v205\n"
    "v_mfma_f32_16x16x32_bf16 v[148:151], a[124:127], v[168:171], v[148:151]\n"
    "v_exp_f32 v206, v206\n"
    "v_cvt_pk_bf16_f32 v198, v204, v205\n"
    "v_mfma_f32_16x16x32_bf16 v[148:151], a[120:123], v[160:163], v[148:151]\n"
    "buffer_load_dwordx4 v3, s[12:15], s45 offen offset:2048 lds\n"
    "v_exp_f32 v207, v207\n"
    "v_mfma_f32_16x16x32_bf16 v[244:247], a[224:227], v[160:163], v[244:247]\n"
    "v_mfma_f32_16x16x32_bf16 v[244:247], a[224:227], v[168:171], v[244:247]\n"
    "buffer_load_dwordx4 v4, s[12:15], s45 offen offset:3072 lds\n"
    "v_cvt_pk_bf16_f32 v199, v206, v207\n"
    "v_mfma_f32_16x16x32_bf16 v[248:251], a[224:227], v[184:187], v[248:251]\n"
    "v_mfma_f32_16x16x32_bf16 v[248:251], a[224:227], v[192:195], v[248:251]\n"
    "s_add_u32 s56, s56, 720896\n"
    "s_waitcnt vmcnt(62)\n"
    "s_waitcnt vmcnt(32) lgkmcnt(0)\n"
    "s_barrier\n"
    "ds_read_b64_tr_b16 a[64:65], v25 offset:0\n"
    "ds_read_b64_tr_b16 a[66:67], v25 offset:4096\n"
    "ds_read_b64_tr_b16 a[68:69], v25 offset:8192\n"
    "ds_read_b64_tr_b16 a[70:71], v25 offset:12288\n"
    "v_mfma_f32_16x16x32_bf16 v[244:247], a[224:227], v[164:167], v[244:247]\n"
    "ds_read_b64_tr_b16 a[72:73], v26 offset:0\n"
    "v_readlane_b32 s39, v16, 6\n"
    "s_mov_b32 m0, 98304\n"
    "s_add_u32 s45, s39, 0\n"
    "s_waitcnt vmcnt(40)\n"
    "v_mfma_f32_16x16x32_bf16 v[244:247], a[224:227], v[172:175], v[244:247]\n"
    "ds_read_b64_tr_b16 a[74:75], v26 offset:4096\n"
    "buffer_load_dwordx4 v1, s[12:15], s45 offen lds\n"
    "v_mfma_f32_16x16x32_bf16 v[248:251], a[224:227], v[188:191], v[248:251]\n"
    "ds_read_b64_tr_b16 a[76:77], v26 offset:8192\n"
    "buffer_load_dwordx4 v2, s[12:15], s45 offen offset:1024 lds\n"
    "v_mfma_f32_16x16x32_bf16 v[248:251], a[224:227], v[196:199], v[248:251]\n"
    "s_waitcnt lgkmcnt(3)\n"
    "ds_read_b64_tr_b16 a[78:79], v26 offset:12288\n"
    "buffer_load_dwordx4 v3, s[12:15], s45 offen offset:2048 lds\n"
    "v_mfma_f32_16x16x32_bf16 v[64:67], a[64:67], v[164:167], v[64:67]\n"
    "ds_read_b64_tr_b16 a[80:81], v27 offset:0\n"
    "buffer_load_dwordx4 v4, s[12:15], s45 offen offset:3072 lds\n"
    "s_mov_b32 m0, 102400\n"
    "s_add_u32 s45, s39, 65536\n"
    "s_waitcnt vmcnt(40)\n"
    "v_mfma_f32_16x16x32_bf16 v[64:67], a[68:71], v[172:175], v[64:67]\n"
    "ds_read_b64_tr_b16 a[82:83], v27 offset:4096\n"
    "buffer_load_dwordx4 v1, s[12:15], s45 offen lds\n"
    "v_mfma_f32_16x16x32_bf16 v[68:71], a[68:71], v[196:199], v[68:71]\n"
    "ds_read_b64_tr_b16 a[84:85], v27 offset:8192\n"
    "buffer_load_dwordx4 v2, s[12:15], s45 offen offset:1024 lds\n"
    "v_mfma_f32_16x16x32_bf16 v[68:71], a[64:67], v[188:191], v[68:71]\n"
    "s_waitcnt lgkmcnt(3)\n"
    "ds_read_b64_tr_b16 a[86:87], v27 offset:12288\n"
    "buffer_load_dwordx4 v3, s[12:15], s45 offen offset:2048 lds\n"
    "v_mfma_f32_16x16x32_bf16 v[80:83], a[72:75], v[188:191], v[80:83]\n"
    "ds_read_b64_tr_b16 a[88:89], v28 offset:0\n"
    "buffer_load_dwordx4 v4, s[12:15], s45 offen offset:3072 lds\n"
    "s_mov_b32 m0, 106496\n"
    "s_add_u32 s45, s39, 131072\n"
    "s_waitcnt vmcnt(40)\n"
    "v_mfma_f32_16x16x32_bf16 v[80:83], a[76:79], v[196:199], v[80:83]\n"
    "ds_read_b64_tr_b16 a[90:91], v28 offset:4096\n"
    "buffer_load_dwordx4 v1, s[12:15], s45 offen lds\n"
    "v_mfma_f32_16x16x32_bf16 v[76:79], a[76:79], v[172:175], v[76:79]\n"
    "ds_read_b64_tr_b16 a[92:93], v28 offset:8192\n"
    "buffer_load_dwordx4 v2, s[12:15], s45 offen offset:1024 lds\n"
    "v_mfma_f32_16x16x32_bf16 v[76:79], a[72:75], v[164:167], v[76:79]\n"
    "s_waitcnt lgkmcnt(3)\n"
    "ds_read_b64_tr_b16 a[94:95], v28 offset:12288\n"
    "buffer_load_dwordx4 v3, s[12:15], s45 offen offset:2048 lds\n"
    "v_mfma_f32_16x16x32_bf16 v[88:91], a[80:83], v[164:167], v[88:91]\n"
    "ds_read_b64_tr_b16 a[96:97], v29 offset:0\n"
    "buffer_load_dwordx4 v4, s[12:15], s45 offen offset:3072 lds\n"
    "s_mov_b32 m0, 110592\n"
    "s_add_u32 s45, s39, 196608\n"
    "s_waitcnt vmcnt(40)\n"
    "v_mfma_f32_16x16x32_bf16 v[88:91], a[84:87], v[172:175], v[88:91]\n"
    "ds_read_b64_tr_b16 a[98:99], v29 offset:4096\n"
    "buffer_load_dwordx4 v1, s[12:15], s45 offen lds\n"
    "v_mfma_f32_16x16x32_bf16 v[92:95], a[84:87], v[196:199], v[92:95]\n"
    "ds_read_b64_tr_b16 a[100:101], v29 offset:8192\n"
    "buffer_load_dwordx4 v2, s[12:15], s45 offen offset:1024 lds\n"
    "v_mfma_f32_16x16x32_bf16 v[92:95], a[80:83], v[188:191], v[92:95]\n"
    "s_waitcnt lgkmcnt(3)\n"
    "ds_read_b64_tr_b16 a[102:103], v29 offset:12288\n"
    "buffer_load_dwordx4 v3, s[12:15], s45 offen offset:2048 lds\n"
    "v_mfma_f32_16x16x32_bf16 v[104:107], a[88:91], v[188:191], v[104:107]\n"
    "ds_read_b64_tr_b16 a[104:105], v30 offset:0\n"
    "buffer_load_dwordx4 v4, s[12:15], s45 offen offset:3072 lds\n"
    "v_mfma_f32_16x16x32_bf16 v[104:107], a[92:95], v[196:199], v[104:107]\n"
    "ds_read_b64_tr_b16 a[106:107], v30 offset:4096\n"
    "buffer_load_dwordx2 v[40:41], v38, s[32:35], 0 offen offset:0 sc1\n"
    "v_mfma_f32_16x16x32_bf16 v[100:103], a[92:95], v[172:175], v[100:103]\n"
    "ds_read_b64_tr_b16 a[108:109], v30 offset:8192\n"
    "v_mfma_f32_16x16x32_bf16 v[100:103], a[88:91], v[164:167], v[100:103]\n"
    "s_waitcnt lgkmcnt(3)\n"
    "ds_read_b64_tr_b16 a[110:111], v30 offset:12288\n"
    "v_mfma_f32_16x16x32_bf16 v[112:115], a[96:99], v[164:167], v[112:115]\n"
    "ds_read_b64_tr_b16 a[112:113], v31 offset:0\n"
    "v_mfma_f32_16x16x32_bf16 v[112:115], a[100:103], v[172:175], v[112:115]\n"
    "ds_read_b64_tr_b16 a[114:115], v31 offset:4096\n"
    "v_mfma_f32_16x16x32_bf16 v[116:119], a[100:103], v[196:199], v[116:119]\n"
    "ds_read_b64_tr_b16 a[116:117], v31 offset:8192\n"
    "v_mfma_f32_16x16x32_bf16 v[116:119], a[96:99], v[188:191], v[116:119]\n"
    "s_waitcnt lgkmcnt(3)\n"
    "ds_read_b64_tr_b16 a[118:119], v31 offset:12288\n"
    "v_mfma_f32_16x16x32_bf16 v[128:131], a[104:107], v[188:191], v[128:131]\n"
    "ds_read_b64_tr_b16 a[120:121], v32 offset:0\n"
    "v_mfma_f32_16x16x32_bf16 v[128:131], a[108:111], v[196:199], v[128:131]\n"
    "ds_read_b64_tr_b16 a[122:123], v32 offset:4096\n"
    "v_mfma_f32_16x16x32_bf16 v[124:127], a[108:111], v[172:175], v[124:127]\n"
    "ds_read_b64_tr_b16 a[124:125], v32 offset:8192\n"
    "v_mfma_f32_16x16x32_bf16 v[124:127], a[104:107], v[164:167], v[124:127]\n"
    "s_waitcnt lgkmcnt(3)\n"
    "ds_read_b64_tr_b16 a[126:127], v32 offset:12288\n"
    "v_mfma_f32_16x16x32_bf16 v[136:139], a[112:115], v[164:167], v[136:139]\n"
    "ds_read_b64_tr_b16 a[0:1], v5 offset:0\n"
    "v_mfma_f32_16x16x32_bf16 v[136:139], a[116:119], v[172:175], v[136:139]\n"
    "ds_read_b64_tr_b16 a[2:3], v5 offset:2048\n"
    "v_mfma_f32_16x16x32_bf16 v[140:143], a[116:119], v[196:199], v[140:143]\n"
    "ds_read_b64_tr_b16 a[4:5], v5 offset:4096\n"
    "v_mfma_f32_16x16x32_bf16 v[140:143], a[112:115], v[188:191], v[140:143]\n"
    "s_waitcnt lgkmcnt(3)\n"
    "ds_read_b64_tr_b16 a[6:7], v5 offset:6144\n"
    "v_mfma_f32_16x16x32_bf16 v[152:155], a[120:123], v[188:191], v[152:155]\n"
    "ds_read_b64_tr_b16 a[8:9], v5 offset:8192\n"
    "v_mfma_f32_16x16x32_bf16 v[152:155], a[124:127], v[196:199], v[152:155]\n"
    "ds_read_b64_tr_b16 a[10:11], v5 offset:10240\n"
    "v_mfma_f32_16x16x32_bf16 v[148:151], a[124:127], v[172:175], v[148:151]\n"
    "ds_read_b64_tr_b16 a[12:13], v5 offset:12288\n"
    "v_mfma_f32_16x16x32_bf16 v[148:151], a[120:123], v[164:167], v[148:151]\n"
    "ds_read_b64_tr_b16 a[14:15], v5 offset:14336\n"
    "ds_read_b64_tr_b16 a[16:17], v6 offset:0\n"
    "ds_read_b64_tr_b16 a[18:19], v6 offset:2048\n"
    "ds_read_b64_tr_b16 a[20:21], v6 offset:4096\n"
    "ds_read_b64_tr_b16 a[22:23], v6 offset:6144\n"
    "ds_read_b64_tr_b16 a[24:25], v6 offset:8192\n"
    "ds_read_b64_tr_b16 a[26:27], v6 offset:10240\n"
    "ds_read_b64_tr_b16 a[28:29], v6 offset:12288\n"
    "ds_read_b64_tr_b16 a[30:31], v6 offset:14336\n"
    "ds_read_b64_tr_b16 a[32:33], v7 offset:0\n"
    "ds_read_b64_tr_b16 a[34:35], v7 offset:2048\n"
    "ds_read_b64_tr_b16 a[36:37], v7 offset:4096\n"
    "ds_read_b64_tr_b16 a[38:39], v7 offset:6144\n"
    "ds_read_b64_tr_b16 a[40:41], v7 offset:8192\n"
    "ds_read_b64_tr_b16 a[42:43], v7 offset:10240\n"
    "ds_read_b64_tr_b16 a[44:45], v7 offset:12288\n"
    "ds_read_b64_tr_b16 a[46:47], v7 offset:14336\n"
    "ds_read_b64_tr_b16 a[48:49], v8 offset:0\n"
    "ds_read_b64_tr_b16 a[50:51], v8 offset:2048\n"
    "ds_read_b64_tr_b16 a[52:53], v8 offset:4096\n"
    "ds_read_b64_tr_b16 a[54:55], v8 offset:6144\n"
    "ds_read_b64_tr_b16 a[56:57], v8 offset:8192\n"
    "ds_read_b64_tr_b16 a[58:59], v8 offset:10240\n"
    "ds_read_b64_tr_b16 a[60:61], v8 offset:12288\n"
    "ds_read_b64_tr_b16 a[62:63], v8 offset:14336\n"
    "s_nop 1\n"
    "s_mov_b32 s57, 0x53800000\n"
    "v_cmp_nge_f32 vcc, s57, v244\n"
    "v_cmp_nge_f32 s[50:51], s57, v248\n"
    "s_or_b64 vcc, vcc, s[50:51]\n"
    "s_cbranch_vccnz .L_fbD_%=\n"
    ".L_fbretD1_%=:\n"
    "v_rcp_f32 v44, v244\n"
    "v_rcp_f32 v46, v248\n"
    "s_add_u32 s16, s56, 0\n"
    "s_add_u32 s17, s56, 11264\n"
    "s_add_u32 s18, s56, 22528\n"
    "s_add_u32 s19, s56, 33792\n"
    "s_waitcnt vmcnt(0)\n"
    "s_mov_b32 s57, 0x40000\n"
    ".L_fspDa_%=:\n"
    "v_cmp_ne_u64 vcc, s[62:63], v[40:41]\n"
    "s_cbranch_vccz .L_fokDa_%=\n"
    "s_sleep 4\n"
    "buffer_load_dwordx2 v[40:41], v38, s[32:35], 0 offen offset:0 sc1\n"
    "s_sub_u32 s57, s57, 1\n"
    "s_waitcnt vmcnt(0)\n"
    "s_cmp_lg_u32 s57, 0\n"
    "s_cbranch_scc1 .L_fspDa_%=\n"
    ".L_fokDa_%=:\n"
    "s_waitcnt vmcnt(40) lgkmcnt(0)\n"
    "s_barrier\n"
    "v_mfma_f32_16x16x32_bf16 v[160:163], a[0:3], a[128:131], 0\n"
    "ds_read_b64_tr_b16 a[64:65], v5 offset:16384\n"
    "ds_read_b64_tr_b16 a[66:67], v5 offset:18432\n"
    "v_readlane_b32 s38, v43, s52\n"
    "s_add_u32 s52, s52, 1\n"
    "s_add_u32 s48, s38, 0\n"
    "s_add_u32 s49, s38, 11264\n"
    "s_add_u32 s50, s38, 22528\n"
    "s_add_u32 s51, s38, 33792\n"
    "buffer_load_dwordx4 a[176:179], v15, s[24:27], s48 offen offset:0 sc1\n"
    "v_mfma_f32_16x16x32_bf16 v[160:163], a[4:7], a[132:135], v[160:163]\n"
    "ds_read_b64_tr_b16 a[68:69], v5 offset:20480\n"
    "buffer_load_dwordx4 a[180:183], v15, s[24:27], s49 offen offset:0 sc1\n"
    "v_mfma_f32_16x16x32_bf16 v[160:163], a[8:11], a[136:139], v[160:163]\n"
    "ds_read_b64_tr_b16 a[70:71], v5 offset:22528\n"
    "buffer_load_dwordx4 a[184:187], v15, s[24:27], s50 offen offset:0 sc1\n"
    "v_mfma_f32_16x16x32_bf16 v[160:163], a[12:15], a[140:143], v[160:163]\n"
    "ds_read_b64_tr_b16 a[72:73], v5 offset:24576\n"
    "v_mul_f32 v64, v44, v64\n"
    "v_mfma_f32_16x16x32_bf16 v[172:175], a[28:31], a[140:143], 0\n"
    "ds_read_b64_tr_b16 a[74:75], v5 offset:26624\n"
    "buffer_load_dwordx4 a[188:191], v15, s[24:27], s51 offen offset:0 sc1\n"
    "v_mfma_f32_16x16x32_bf16 v[172:175], a[24:27], a[136:139], v[172:175]\n"
    "ds_read_b64_tr_b16 a[76:77], v5 offset:28672\n"
    "buffer_load_dwordx4 a[192:195], v15, s[24:27], s48 offen offset:1024 sc1\n"
    "v_mfma_f32_16x16x32_bf16 v[172:175], a[20:23], a[132:135], v[172:175]\n"
    "ds_read_b64_tr_b16 a[78:79], v5 offset:30720\n"
    "buffer_load_dwordx4 a[196:199], v15, s[24:27], s49 offen offset:1024 sc1\n"
    "v_mfma_f32_16x16x32_bf16 v[172:175], a[16:19], a[128:131], v[172:175]\n"
    "ds_read_b64_tr_b16 a[80:81], v6 offset:16384\n"
    "v_mul_f32 v65, v44, v65\n"
    "v_mfma_f32_16x16x32_bf16 v[184:187], a[32:35], a[128:131], 0\n"
    "ds_read_b64_tr_b16 a[82:83], v6 offset:18432\n"
    "buffer_load_dwordx4 a[200:203], v15, s[24:27], s50 offen offset:1024 sc1\n"
    "v_mfma_f32_16x16x32_bf16 v[184:187], a[36:39], a[132:135], v[184:187]\n"
    "ds_read_b64_tr_b16 a[84:85], v6 offset:20480\n"
    "ds_read_b64_tr_b16 a[86:87], v6 offset:22528\n"
    "buffer_load_dwordx4 a[204:207], v15, s[24:27], s51 offen offset:1024 sc1\n"
    "v_mfma_f32_16x16x32_bf16 v[184:187], a[40:43], a[136:139], v[184:187]\n"
    "ds_read_b64_tr_b16 a[88:89], v6 offset:24576\n"
    "v_readlane_b32 s39, v16, 4\n"
    "s_mov_b32 m0, 65536\n"
    "s_add_u32 s45, s39, 0\n"
    "s_waitcnt vmcnt(40)\n"
    "buffer_load_dwordx4 v1, s[12:15], s45 offen lds\n"
    "v_mfma_f32_16x16x32_bf16 v[184:187], a[44:47], a[140:143], v[184:187]\n"
    "ds_read_b64_tr_b16 a[90:91], v6 offset:26624\n"
    "v_mul_f32 v66, v44, v66\n"
    "v_mfma_f32_16x16x32_bf16 v[196:199], a[60:63], a[140:143], 0\n"
    "ds_read_b64_tr_b16 a[92:93], v6 offset:28672\n"
    "buffer_load_dwordx4 v2, s[12:15], s45 offen offset:1024 lds\n"
    "v_mfma_f32_16x16x32_bf16 v[196:199], a[56:59], a[136:139], v[196:199]\n"
    "ds_read_b64_tr_b16 a[94:95], v6 offset:30720\n"
    "buffer_load_dwordx4 v3, s[12:15], s45 offen offset:2048 lds\n"
    "v_mfma_f32_16x16x32_bf16 v[196:199], a[52:55], a[132:135], v[196:199]\n"
    "ds_read_b64_tr_b16 a[96:97], v7 offset:16384\n"
    "buffer_load_dwordx4 v4, s[12:15], s45 offen offset:3072 lds\n"
    "v_mfma_f32_16x16x32_bf16 v[196:199], a[48:51], a[128:131], v[196:199]\n"
    "ds_read_b64_tr_b16 a[98:99], v7 offset:18432\n"
    "v_mul_f32 v67, v44, v67\n"
    "v_mfma_f32_16x16x32_bf16 v[200:203], a[48:51], a[144:147], 0\n"
    "ds_read_b64_tr_b16 a[100:101], v7 offset:20480\n"
    "s_mov_b32 m0, 69632\n"
    "s_add_u32 s45, s39, 65536\n"
    "s_waitcnt vmcnt(40)\n"
    "buffer_load_dwordx4 v1, s[12:15], s45 offen lds\n"
    "v_mfma_f32_16x16x32_bf16 v[200:203], a[52:55], a[148:151], v[200:203]\n"
    "ds_read_b64_tr_b16 a[102:103], v7 offset:22528\n"
    "buffer_load_dwordx4 v2, s[12:15], s45 offen offset:1024 lds\n"
    "v_mfma_f32_16x16x32_bf16 v[200:203], a[56:59], a[152:155], v[200:203]\n"
    "ds_read_b64_tr_b16 a[104:105], v7 offset:24576\n"
    "buffer_load_dwordx4 v3, s[12:15], s45 offen offset:2048 lds\n"
    "v_mfma_f32_16x16x32_bf16 v[200:203], a[60:63], a[156:159], v[200:203]\n"
    "ds_read_b64_tr_b16 a[106:107], v7 offset:26624\n"
    "ds_read_b64_tr_b16 a[108:109], v7 offset:28672\n"
    "v_mfma_f32_16x16x32_bf16 v[188:191], a[44:47], a[156:159], 0\n"
    "ds_read_b64_tr_b16 a[110:111], v7 offset:30720\n"
    "buffer_load_dwordx4 v4, s[12:15], s45 offen offset:3072 lds\n"
    "v_mfma_f32_16x16x32_bf16 v[188:191], a[40:43], a[152:155], v[188:191]\n"
    "ds_read_b64_tr_b16 a[112:113], v8 offset:16384\n"
    "s_mov_b32 m0, 73728\n"
    "s_add_u32 s45, s39, 131072\n"
    "s_waitcnt vmcnt(40)\n"
    "buffer_load_dwordx4 v1, s[12:15], s45 offen lds\n"
    "v_mfma_f32_16x16x32_bf16 v[188:191], a[36:39], a[148:151], v[188:191]\n"
    "ds_read_b64_tr_b16 a[114:115], v8 offset:18432\n"
    "buffer_load_dwordx4 v2, s[12:15], s45 offen offset:1024 lds\n"
    "v_mfma_f32_16x16x32_bf16 v[188:191], a[32:35], a[144:147], v[188:191]\n"
    "ds_read_b64_tr_b16 a[116:117], v8 offset:20480\n"
    "v_mul_f32 v76, v44, v76\n"
    "v_mfma_f32_16x16x32_bf16 v[176:179], a[16:19], a[144:147], 0\n"
    "ds_read_b64_tr_b16 a[118:119], v8 offset:22528\n"
    "buffer_load_dwordx4 v3, s[12:15], s45 offen offset:2048 lds\n"
    "v_mfma_f32_16x16x32_bf16 v[176:179], a[20:23], a[148:151], v[176:179]\n"
    "ds_read_b64_tr_b16 a[120:121], v8 offset:24576\n"
    "buffer_load_dwordx4 v4, s[12:15], s45 offen offset:3072 lds\n"
    "v_mfma_f32_16x16x32_bf16 v[176:179], a[24:27], a[152:155], v[176:179]\n"
    "ds_read_b64_tr_b16 a[122:123], v8 offset:26624\n"
    "s_mov_b32 m0, 77824\n"
    "s_add_u32 s45, s39, 196608\n"
    "s_waitcnt vmcnt(40)\n"
    "buffer_load_dwordx4 v1, s[12:15], s45 offen lds\n"
    "v_mfma_f32_16x16x32_bf16 v[176:179], a[28:31], a[156:159], v[176:179]\n"
    "ds_read_b64_tr_b16 a[124:125], v8 offset:28672\n"
    "v_mul_f32 v77, v44, v77\n"
    "v_mfma_f32_16x16x32_bf16 v[164:167], a[12:15], a[156:159], 0\n"
    "ds_read_b64_tr_b16 a[126:127], v8 offset:30720\n"
    "buffer_load_dwordx4 v2, s[12:15], s45 offen offset:1024 lds\n"
    "v_mfma_f32_16x16x32_bf16 v[164:167], a[8:11], a[152:155], v[164:167]\n"
    "buffer_load_dwordx4 v3, s[12:15], s45 offen offset:2048 lds\n"
    "v_mul_f32 v78, v44, v78\n"
    "v_mfma_f32_16x16x32_bf16 v[164:167], a[4:7], a[148:151], v[164:167]\n"
    "buffer_load_dwordx4 v4, s[12:15], s45 offen offset:3072 lds\n"
    "v_mul_f32 v79, v44, v79\n"
    "v_mfma_f32_16x16x32_bf16 v[164:167], a[0:3], a[144:147], v[164:167]\n"
    "v_cvt_pk_bf16_f32 v64, v64, v65\n"
    "v_cvt_pk_bf16_f32 v65, v66, v67\n"
    "s_waitcnt lgkmcnt(0)\n"
    "v_mfma_f32_16x16x32_bf16 v[208:211], a[64:67], a[128:131], 0\n"
    "ds_read_b64_tr_b16 a[0:1], v5 offset:32768\n"
    "ds_read_b64_tr_b16 a[2:3], v5 offset:34816\n"
    "v_readlane_b32 s39, v16, 7\n"
    "s_mov_b32 m0, 114688\n"
    "s_add_u32 s45, s39, 0\n"
    "s_waitcnt vmcnt(40)\n"
    "buffer_load_dwordx4 v1, s[12:15], s45 offen lds\n"
    "v_mfma_f32_16x16x32_bf16 v[208:211], a[68:71], a[132:135], v[208:211]\n"
    "ds_read_b64_tr_b16 a[4:5], v5 offset:36864\n"
    "v_cvt_pk_bf16_f32 v66, v76, v77\n"
    "v_mfma_f32_16x16x32_bf16 v[208:211], a[72:75], a[136:139], v[208:211]\n"
    "ds_read_b64_tr_b16 a[6:7], v5 offset:38912\n"
    "buffer_load_dwordx4 v2, s[12:15], s45 offen offset:1024 lds\n"
    "v_mfma_f32_16x16x32_bf16 v[208:211], a[76:79], a[140:143], v[208:211]\n"
    "ds_read_b64_tr_b16 a[8:9], v5 offset:40960\n"
    "v_cvt_pk_bf16_f32 v67, v78, v79\n"
    "v_mfma_f32_16x16x32_bf16 v[220:223], a[92:95], a[140:143], 0\n"
    "ds_read_b64_tr_b16 a[10:11], v5 offset:43008\n"
    "buffer_load_dwordx4 v3, s[12:15], s45 offen offset:2048 lds\n"
    "v_mfma_f32_16x16x32_bf16 v[220:223], a[88:91], a[136:139], v[220:223]\n"
    "ds_read_b64_tr_b16 a[12:13], v5 offset:45056\n"
    "buffer_store_dwordx4 v[64:67], v37, s[24:27], s16 offen offset:0 sc1\n"
    "v_mfma_f32_16x16x32_bf16 v[220:223], a[84:87], a[132:135], v[220:223]\n"
    "ds_read_b64_tr_b16 a[14:15], v5 offset:47104\n"
    "buffer_load_dwordx4 v4, s[12:15], s45 offen offset:3072 lds\n"
    "v_mfma_f32_16x16x32_bf16 v[220:223], a[80:83], a[128:131], v[220:223]\n"
    "ds_read_b64_tr_b16 a[16:17], v6 offset:32768\n"
    "v_mul_f32 v88, v44, v88\n"
    "v_mfma_f32_16x16x32_bf16 v[232:235], a[96:99], a[128:131], 0\n"
    "ds_read_b64_tr_b16 a[18:19], v6 offset:34816\n"
    "s_mov_b32 m0, 118784\n"
    "s_add_u32 s45, s39, 65536\n"
    "s_waitcnt vmcnt(40)\n"
    "buffer_load_dwordx4 v1, s[12:15], s45 offen lds\n"
    "v_mfma_f32_16x16x32_bf16 v[232:235], a[100:103], a[132:135], v[232:235]\n"
    "ds_read_b64_tr_b16 a[20:21], v6 offset:36864\n"
    "ds_read_b64_tr_b16 a[22:23], v6 offset:38912\n"
    "v_mfma_f32_16x16x32_bf16 v[232:235], a[104:107], a[136:139], v[232:235]\n"
    "ds_read_b64_tr_b16 a[24:25], v6 offset:40960\n"
    "buffer_load_dwordx4 v2, s[12:15], s45 offen offset:1024 lds\n"
    "v_mfma_f32_16x16x32_bf16 v[232:235], a[108:111], a[140:143], v[232:235]\n"
    "ds_read_b64_tr_b16 a[26:27], v6 offset:43008\n"
    "v_mul_f32 v89, v44, v89\n"
    "v_mfma_f32_16x16x32_bf16 v[244:247], a[124:127], a[140:143], 0\n"
    "ds_read_b64_tr_b16 a[28:29], v6 offset:45056\n"
    "buffer_load_dwordx4 v3, s[12:15], s45 offen offset:2048 lds\n"
    "v_mfma_f32_16x16x32_bf16 v[244:247], a[120:123], a[136:139], v[244:247]\n"
    "ds_read_b64_tr_b16 a[30:31], v6 offset:47104\n"
    "v_mul_f32 v90, v44, v90\n"
    "v_mfma_f32_16x16x32_bf16 v[244:247], a[116:119], a[132:135], v[244:247]\n"
    "ds_read_b64_tr_b16 a[32:33], v7 offset:32768\n"
    "buffer_load_dwordx4 v4, s[12:15], s45 offen offset:3072 lds\n"
    "v_mfma_f32_16x16x32_bf16 v[244:247], a[112:115], a[128:131], v[244:247]\n"
    "ds_read_b64_tr_b16 a[34:35], v7 offset:34816\n"
    "v_mul_f32 v91, v44, v91\n"
    "v_mfma_f32_16x16x32_bf16 v[248:251], a[112:115], a[144:147], 0\n"
    "ds_read_b64_tr_b16 a[36:37], v7 offset:36864\n"
    "s_mov_b32 m0, 122880\n"
    "s_add_u32 s45, s39, 131072\n"
    "s_waitcnt vmcnt(40)\n"
    "buffer_load_dwordx4 v1, s[12:15], s45 offen lds\n"
    "v_mfma_f32_16x16x32_bf16 v[248:251], a[116:119], a[148:151], v[248:251]\n"
    "ds_read_b64_tr_b16 a[38:39], v7 offset:38912\n"
    "v_mul_f32 v100, v44, v100\n"
    "v_mfma_f32_16x16x32_bf16 v[248:251], a[120:123], a[152:155], v[248:251]\n"
    "ds_read_b64_tr_b16 a[40:41], v7 offset:40960\n"
    "buffer_load_dwordx4 v2, s[12:15], s45 offen offset:1024 lds\n"
    "v_mfma_f32_16x16x32_bf16 v[248:251], a[124:127], a[156:159], v[248:251]\n"
    "ds_read_b64_tr_b16 a[42:43], v7 offset:43008\n"
    "ds_read_b64_tr_b16 a[44:45], v7 offset:45056\n"
    "v_mfma_f32_16x16x32_bf16 v[236:239], a[108:111], a[156:159], 0\n"
    "ds_read_b64_tr_b16 a[46:47], v7 offset:47104\n"
    "buffer_load_dwordx4 v3, s[12:15], s45 offen offset:2048 lds\n"
    "v_mfma_f32_16x16x32_bf16 v[236:239], a[104:107], a[152:155], v[236:239]\n"
    "ds_read_b64_tr_b16 a[48:49], v8 offset:32768\n"
    "v_mul_f32 v101, v44, v101\n"
    "v_mfma_f32_16x16x32_bf16 v[236:239], a[100:103], a[148:151], v[236:239]\n"
    "ds_read_b64_tr_b16 a[50:51], v8 offset:34816\n"
    "buffer_load_dwordx4 v4, s[12:15], s45 offen offset:3072 lds\n"
    "v_mfma_f32_16x16x32_bf16 v[236:239], a[96:99], a[144:147], v[236:239]\n"
    "ds_read_b64_tr_b16 a[52:53], v8 offset:36864\n"
    "v_mul_f32 v102, v44, v102\n"
    "v_mfma_f32_16x16x32_bf16 v[224:227], a[80:83], a[144:147], 0\n"
    "ds_read_b64_tr_b16 a[54:55], v8 offset:38912\n"
    "s_mov_b32 m0, 126976\n"
    "s_add_u32 s45, s39, 196608\n"
    "s_waitcnt vmcnt(40)\n"
    "buffer_load_dwordx4 v1, s[12:15], s45 offen lds\n"
    "v_mfma_f32_16x16x32_bf16 v[224:227], a[84:87], a[148:151], v[224:227]\n"
    "ds_read_b64_tr_b16 a[56:57], v8 offset:40960\n"
    "v_mul_f32 v103, v44, v103\n"
    "v_mfma_f32_16x16x32_bf16 v[224:227], a[88:91], a[152:155], v[224:227]\n"
    "ds_read_b64_tr_b16 a[58:59], v8 offset:43008\n"
    "buffer_load_dwordx4 v2, s[12:15], s45 offen offset:1024 lds\n"
    "v_mfma_f32_16x16x32_bf16 v[224:227], a[92:95], a[156:159], v[224:227]\n"
    "ds_read_b64_tr_b16 a[60:61], v8 offset:45056\n"
    "v_cvt_pk_bf16_f32 v88, v88, v89\n"
    "v_mfma_f32_16x16x32_bf16 v[212:215], a[76:79], a[156:159], 0\n"
    "ds_read_b64_tr_b16 a[62:63], v8 offset:47104\n"
    "buffer_load_dwordx4 v3, s[12:15], s45 offen offset:2048 lds\n"
    "v_mfma_f32_16x16x32_bf16 v[212:215], a[72:75], a[152:155], v[212:215]\n"
    "v_cvt_pk_bf16_f32 v89, v90, v91\n"
    "v_cvt_pk_bf16_f32 v90, v100, v101\n"
    "v_mfma_f32_16x16x32_bf16 v[212:215], a[68:71], a[148:151], v[212:215]\n"
    "buffer_load_dwordx4 v4, s[12:15], s45 offen offset:3072 lds\n"
    "v_cvt_pk_bf16_f32 v91, v102, v103\n"
    "v_mfma_f32_16x16x32_bf16 v[212:215], a[64:67], a[144:147], v[212:215]\n"
    "buffer_store_dwordx4 v[88:91], v37, s[24:27], s17 offen offset:0 sc1\n"
    "v_mul_f32 v112, v44, v112\n"
    "v_mul_f32 v113, v44, v113\n"
    "v_mul_f32 v114, v44, v114\n"
    "v_mul_f32 v115, v44, v115\n"
    "v_mul_f32 v124, v44, v124\n"
    "v_mul_f32 v125, v44, v125\n"
    "v_mul_f32 v126, v44, v126\n"
    "v_mul_f32 v127, v44, v127\n"
    "v_cvt_pk_bf16_f32 v112, v112, v113\n"
    "v_cvt_pk_bf16_f32 v113, v114, v115\n"
    "v_cvt_pk_bf16_f32 v114, v124, v125\n"
    "v_cvt_pk_bf16_f32 v115, v126, v127\n"
    "buffer_store_dwordx4 v[112:115], v37, s[24:27], s18 offen offset:0 sc1\n"
    "v_mul_f32 v136, v44, v136\n"
    "v_mul_f32 v137, v44, v137\n"
    "v_mul_f32 v138, v44, v138\n"
    "v_mul_f32 v139, v44, v139\n"
    "v_mul_f32 v148, v44, v148\n"
    "v_mul_f32 v149, v44, v149\n"
    "v_mul_f32 v150, v44, v150\n"
    "v_mul_f32 v151, v44, v151\n"
    "v_cvt_pk_bf16_f32 v136, v136, v137\n"
    "v_cvt_pk_bf16_f32 v137, v138, v139\n"
    "v_cvt_pk_bf16_f32 v138, v148, v149\n"
    "v_cvt_pk_bf16_f32 v139, v150, v151\n"
    "buffer_store_dwordx4 v[136:139], v37, s[24:27], s19 offen offset:0 sc1\n"
    "v_mul_f32 v68, v46, v68\n"
    "v_mul_f32 v69, v46, v69\n"
    "v_mul_f32 v70, v46, v70\n"
    "v_mul_f32 v71, v46, v71\n"
    "v_mul_f32 v80, v46, v80\n"
    "v_mul_f32 v81, v46, v81\n"
    "v_mul_f32 v82, v46, v82\n"
    "v_mul_f32 v83, v46, v83\n"
    "v_cvt_pk_bf16_f32 v68, v68, v69\n"
    "v_cvt_pk_bf16_f32 v69, v70, v71\n"
    "v_cvt_pk_bf16_f32 v70, v80, v81\n"
    "v_cvt_pk_bf16_f32 v71, v82, v83\n"
    "buffer_store_dwordx4 v[68:71], v37, s[24:27], s16 offen offset:1024 sc1\n"
    "v_mul_f32 v92, v46, v92\n"
    "v_mul_f32 v93, v46, v93\n"
    "v_mul_f32 v94, v46, v94\n"
    "v_mul_f32 v95, v46, v95\n"
    "v_mul_f32 v104, v46, v104\n"
    "v_mul_f32 v105, v46, v105\n"
    "v_mul_f32 v106, v46, v106\n"
    "v_mul_f32 v107, v46, v107\n"
    "v_cvt_pk_bf16_f32 v92, v92, v93\n"
    "v_cvt_pk_bf16_f32 v93, v94, v95\n"
    "v_cvt_pk_bf16_f32 v94, v104, v105\n"
    "v_cvt_pk_bf16_f32 v95, v106, v107\n"
    "buffer_store_dwordx4 v[92:95], v37, s[24:27], s17 offen offset:1024 sc1\n"
    "v_mul_f32 v116, v46, v116\n"
    "v_mul_f32 v117, v46, v117\n"
    "v_mul_f32 v118, v46, v118\n"
    "v_mul_f32 v119, v46, v119\n"
    "v_mul_f32 v128, v46, v128\n"
    "v_mul_f32 v129, v46, v129\n"
    "v_mul_f32 v130, v46, v130\n"
    "v_mul_f32 v131, v46, v131\n"
    "v_cvt_pk_bf16_f32 v116, v116, v117\n"
    "v_cvt_pk_bf16_f32 v117, v118, v119\n"
    "v_cvt_pk_bf16_f32 v118, v128, v129\n"
    "v_cvt_pk_bf16_f32 v119, v130, v131\n"
    "buffer_store_dwordx4 v[116:119], v37, s[24:27], s18 offen offset:1024 sc1\n"
    "v_mul_f32 v140, v46, v140\n"
    "v_mul_f32 v141, v46, v141\n"
    "v_mul_f32 v142, v46, v142\n"
    "v_mul_f32 v143, v46, v143\n"
    "v_mul_f32 v152, v46, v152\n"
    "v_mul_f32 v153, v46, v153\n"
    "v_mul_f32 v154, v46, v154\n"
    "v_mul_f32 v155, v46, v155\n"
    "v_cvt_pk_bf16_f32 v140, v140, v141\n"
    "v_cvt_pk_bf16_f32 v141, v142, v143\n"
    "v_cvt_pk_bf16_f32 v142, v152, v153\n"
    "v_cvt_pk_bf16_f32 v143, v154, v155\n"
    "buffer_store_dwordx4 v[140:143], v37, s[24:27], s19 offen offset:1024 sc1\n"
    "s_waitcnt vmcnt(24) lgkmcnt(0)\n"
    "s_barrier\n"
    "v_mfma_f32_16x16x32_bf16 v[160:163], a[0:3], v[64:67], v[160:163]\n"
    "ds_read_b64_tr_b16 a[64:65], v5 offset:49152\n"
    "ds_read_b64_tr_b16 a[66:67], v5 offset:51200\n"
    "v_mfma_f32_16x16x32_bf16 v[160:163], a[4:7], v[88:91], v[160:163]\n"
    "ds_read_b64_tr_b16 a[68:69], v5 offset:53248\n"
    "v_mfma_f32_16x16x32_bf16 v[160:163], a[8:11], v[112:115], v[160:163]\n"
    "ds_read_b64_tr_b16 a[70:71], v5 offset:55296\n"
    "v_mfma_f32_16x16x32_bf16 v[160:163], a[12:15], v[136:139], v[160:163]\n"
    "ds_read_b64_tr_b16 a[72:73], v5 offset:57344\n"
    "v_mfma_f32_16x16x32_bf16 v[172:175], a[28:31], v[136:139], v[172:175]\n"
    "ds_read_b64_tr_b16 a[74:75], v5 offset:59392\n"
    "v_mfma_f32_16x16x32_bf16 v[172:175], a[24:27], v[112:115], v[172:175]\n"
    "ds_read_b64_tr_b16 a[76:77], v5 offset:61440\n"
    "v_mfma_f32_16x16x32_bf16 v[172:175], a[20:23], v[88:91], v[172:175]\n"
    "ds_read_b64_tr_b16 a[78:79], v5 offset:63488\n"
    "v_xor_b32 v5, 0x10000, v5\n"
    "v_mfma_f32_16x16x32_bf16 v[172:175], a[16:19], v[64:67], v[172:175]\n"
    "ds_read_b64_tr_b16 a[80:81], v6 offset:49152\n"
    "v_mfma_f32_16x16x32_bf16 v[184:187], a[32:35], v[64:67], v[184:187]\n"
    "ds_read_b64_tr_b16 a[82:83], v6 offset:51200\n"
    "v_mfma_f32_16x16x32_bf16 v[184:187], a[36:39], v[88:91], v[184:187]\n"
    "ds_read_b64_tr_b16 a[84:85], v6 offset:53248\n"
    "ds_read_b64_tr_b16 a[86:87], v6 offset:55296\n"
    "v_mfma_f32_16x16x32_bf16 v[184:187], a[40:43], v[112:115], v[184:187]\n"
    "ds_read_b64_tr_b16 a[88:89], v6 offset:57344\n"
    "v_mfma_f32_16x16x32_bf16 v[184:187], a[44:47], v[136:139], v[184:187]\n"
    "ds_read_b64_tr_b16 a[90:91], v6 offset:59392\n"
    "v_mfma_f32_16x16x32_bf16 v[196:199], a[60:63], v[136:139], v[196:199]\n"
    "ds_read_b64_tr_b16 a[92:93], v6 offset:61440\n"
    "v_mfma_f32_16x16x32_bf16 v[196:199], a[56:59], v[112:115], v[196:199]\n"
    "ds_read_b64_tr_b16 a[94:95], v6 offset:63488\n"
    "v_xor_b32 v6, 0x10000, v6\n"
    "v_mfma_f32_16x16x32_bf16 v[196:199], a[52:55], v[88:91], v[196:199]\n"
    "ds_read_b64_tr_b16 a[96:97], v7 offset:49152\n"
    "v_mfma_f32_16x16x32_bf16 v[196:199], a[48:51], v[64:67], v[196:199]\n"
    "ds_read_b64_tr_b16 a[98:99], v7 offset:51200\n"
    "v_mfma_f32_16x16x32_bf16 v[200:203], a[48:51], v[68:71], v[200:203]\n"
    "ds_read_b64_tr_b16 a[100:101], v7 offset:53248\n"
    "v_mfma_f32_16x16x32_bf16 v[200:203], a[52:55], v[92:95], v[200:203]\n"
    "ds_read_b64_tr_b16 a[102:103], v7 offset:55296\n"
    "v_mfma_f32_16x16x32_bf16 v[200:203], a[56:59], v[116:119], v[200:203]\n"
    "ds_read_b64_tr_b16 a[104:105], v7 offset:57344\n"
    "v_mfma_f32_16x16x32_bf16 v[200:203], a[60:63], v[140:143], v[200:203]\n"
    "ds_read_b64_tr_b16 a[106:107], v7 offset:59392\n"
    "ds_read_b64_tr_b16 a[108:109], v7 offset:61440\n"
    "v_mfma_f32_16x16x32_bf16 v[188:191], a[44:47], v[140:143], v[188:191]\n"
    "ds_read_b64_tr_b16 a[110:111], v7 offset:63488\n"
    "v_xor_b32 v7, 0x10000, v7\n"
    "v_mfma_f32_16x16x32_bf16 v[188:191], a[40:43], v[116:119], v[188:191]\n"
    "ds_read_b64_tr_b16 a[112:113], v8 offset:49152\n"
    "v_mfma_f32_16x16x32_bf16 v[188:191], a[36:39], v[92:95], v[188:191]\n"
    "ds_read_b64_tr_b16 a[114:115], v8 offset:51200\n"
    "v_mfma_f32_16x16x32_bf16 v[188:191], a[32:35], v[68:71], v[188:191]\n"
    "ds_read_b64_tr_b16 a[116:117], v8 offset:53248\n"
    "v_mfma_f32_16x16x32_bf16 v[176:179], a[16:19], v[68:71], v[176:179]\n"
    "ds_read_b64_tr_b16 a[118:119], v8 offset:55296\n"
    "v_mfma_f32_16x16x32_bf16 v[176:179], a[20:23], v[92:95], v[176:179]\n"
    "ds_read_b64_tr_b16 a[120:121], v8 offset:57344\n"
    "v_mfma_f32_16x16x32_bf16 v[176:179], a[24:27], v[116:119], v[176:179]\n"
    "ds_read_b64_tr_b16 a[122:123], v8 offset:59392\n"
    "v_mfma_f32_16x16x32_bf16 v[176:179], a[28:31], v[140:143], v[176:179]\n"
    "ds_read_b64_tr_b16 a[124:125], v8 offset:61440\n"
    "v_mfma_f32_16x16x32_bf16 v[164:167], a[12:15], v[140:143], v[164:167]\n"
    "ds_read_b64_tr_b16 a[126:127], v8 offset:63488\n"
    "v_xor_b32 v8, 0x10000, v8\n"
    "v_mfma_f32_16x16x32_bf16 v[164:167], a[8:11], v[116:119], v[164:167]\n"
    "v_mfma_f32_16x16x32_bf16 v[164:167], a[4:7], v[92:95], v[164:167]\n"
    "v_mfma_f32_16x16x32_bf16 v[164:167], a[0:3], v[68:71], v[164:167]\n"
    "s_waitcnt lgkmcnt(0)\n"
    "v_mfma_f32_16x16x32_bf16 v[208:211], a[64:67], v[64:67], v[208:211]\n"
    "ds_read_b64_tr_b16 a[0:1], v5 offset:0\n"
    "ds_read_b64_tr_b16 a[2:3], v5 offset:2048\n"
    "v_readlane_b32 s38, v43, s52\n"
    "s_add_u32 s52, s52, 1\n"
    "s_add_u32 s48, s38, 0\n"
    "s_add_u32 s49, s38, 11264\n"
    "s_add_u32 s50, s38, 22528\n"
    "s_add_u32 s51, s38, 33792\n"
    "buffer_load_dwordx4 a[128:131], v15, s[24:27], s48 offen offset:0 sc1\n"
    "v_mfma_f32_16x16x32_bf16 v[208:211], a[68:71], v[88:91], v[208:211]\n"
    "ds_read_b64_tr_b16 a[4:5], v5 offset:4096\n"
    "v_mfma_f32_16x16x32_bf16 v[208:211], a[72:75], v[112:115], v[208:211]\n"
    "ds_read_b64_tr_b16 a[6:7], v5 offset:6144\n"
    "v_mfma_f32_16x16x32_bf16 v[208:211], a[76:79], v[136:139], v[208:211]\n"
    "ds_read_b64_tr_b16 a[8:9], v5 offset:8192\n"
    "v_mfma_f32_16x16x32_bf16 v[220:223], a[92:95], v[136:139], v[220:223]\n"
    "ds_read_b64_tr_b16 a[10:11], v5 offset:10240\n"
    "v_mfma_f32_16x16x32_bf16 v[220:223], a[88:91], v[112:115], v[220:223]\n"
    "ds_read_b64_tr_b16 a[12:13], v5 offset:12288\n"
    "v_mfma_f32_16x16x32_bf16 v[220:223], a[84:87], v[88:91], v[220:223]\n"
    "ds_read_b64_tr_b16 a[14:15], v5 offset:14336\n"
    "v_mfma_f32_16x16x32_bf16 v[220:223], a[80:83], v[64:67], v[220:223]\n"
    "ds_read_b64_tr_b16 a[16:17], v6 offset:0\n"
    "v_mfma_f32_16x16x32_bf16 v[232:235], a[96:99], v[64:67], v[232:235]\n"
    "ds_read_b64_tr_b16 a[18:19], v6 offset:2048\n"
    "buffer_load_dwordx4 a[132:135], v15, s[24:27], s49 offen offset:0 sc1\n"
    "v_mfma_f32_16x16x32_bf16 v[232:235], a[100:103], v[88:91], v[232:235]\n"
    "ds_read_b64_tr_b16 a[20:21], v6 offset:4096\n"
    "ds_read_b64_tr_b16 a[22:23], v6 offset:6144\n"
    "v_mfma_f32_16x16x32_bf16 v[232:235], a[104:107], v[112:115], v[232:235]\n"
    "ds_read_b64_tr_b16 a[24:25], v6 offset:8192\n"
    "v_mfma_f32_16x16x32_bf16 v[232:235], a[108:111], v[136:139], v[232:235]\n"
    "ds_read_b64_tr_b16 a[26:27], v6 offset:10240\n"
    "v_mfma_f32_16x16x32_bf16 v[244:247], a[124:127], v[136:139], v[244:247]\n"
    "ds_read_b64_tr_b16 a[28:29], v6 offset:12288\n"
    "v_mfma_f32_16x16x32_bf16 v[244:247], a[120:123], v[112:115], v[244:247]\n"
    "ds_read_b64_tr_b16 a[30:31], v6 offset:14336\n"
    "v_mfma_f32_16x16x32_bf16 v[244:247], a[116:119], v[88:91], v[244:247]\n"
    "ds_read_b64_tr_b16 a[32:33], v7 offset:0\n"
    "v_mfma_f32_16x16x32_bf16 v[244:247], a[112:115], v[64:67], v[244:247]\n"
    "ds_read_b64_tr_b16 a[34:35], v7 offset:2048\n"
    "v_mfma_f32_16x16x32_bf16 v[248:251], a[112:115], v[68:71], v[248:251]\n"
    "ds_read_b64_tr_b16 a[36:37], v7 offset:4096\n"
    "buffer_load_dwordx4 a[136:139], v15, s[24:27], s50 offen offset:0 sc1\n"
    "v_mfma_f32_16x16x32_bf16 v[248:251], a[116:119], v[92:95], v[248:251]\n"
    "ds_read_b64_tr_b16 a[38:39], v7 offset:6144\n"
    "v_mfma_f32_16x16x32_bf16 v[248:251], a[120:123], v[116:119], v[248:251]\n"
    "ds_read_b64_tr_b16 a[40:41], v7 offset:8192\n"
    "v_mfma_f32_16x16x32_bf16 v[248:251], a[124:127], v[140:143], v[248:251]\n"
    "ds_read_b64_tr_b16 a[42:43], v7 offset:10240\n"
    "ds_read_b64_tr_b16 a[44:45], v7 offset:12288\n"
    "v_mfma_f32_16x16x32_bf16 v[236:239], a[108:111], v[140:143], v[236:239]\n"
    "ds_read_b64_tr_b16 a[46:47], v7 offset:14336\n"
    "v_mfma_f32_16x16x32_bf16 v[236:239], a[104:107], v[116:119], v[236:239]\n"
    "ds_read_b64_tr_b16 a[48:49], v8 offset:0\n"
    "v_mfma_f32_16x16x32_bf16 v[236:239], a[100:103], v[92:95], v[236:239]\n"
    "ds_read_b64_tr_b16 a[50:51], v8 offset:2048\n"
    "v_mfma_f32_16x16x32_bf16 v[236:239], a[96:99], v[68:71], v[236:239]\n"
    "ds_read_b64_tr_b16 a[52:53], v8 offset:4096\n"
    "v_mfma_f32_16x16x32_bf16 v[224:227], a[80:83], v[68:71], v[224:227]\n"
    "ds_read_b64_tr_b16 a[54:55], v8 offset:6144\n"
    "buffer_load_dwordx4 a[140:143], v15, s[24:27], s51 offen offset:0 sc1\n"
    "v_mfma_f32_16x16x32_bf16 v[224:227], a[84:87], v[92:95], v[224:227]\n"
    "ds_read_b64_tr_b16 a[56:57], v8 offset:8192\n"
    "v_mfma_f32_16x16x32_bf16 v[224:227], a[88:91], v[116:119], v[224:227]\n"
    "ds_read_b64_tr_b16 a[58:59], v8 offset:10240\n"
    "v_mfma_f32_16x16x32_bf16 v[224:227], a[92:95], v[140:143], v[224:227]\n"
    "ds_read_b64_tr_b16 a[60:61], v8 offset:12288\n"
    "v_mfma_f32_16x16x32_bf16 v[212:215], a[76:79], v[140:143], v[212:215]\n"
    "ds_read_b64_tr_b16 a[62:63], v8 offset:14336\n"
    "v_mfma_f32_16x16x32_bf16 v[212:215], a[72:75], v[116:119], v[212:215]\n"
    "v_mfma_f32_16x16x32_bf16 v[212:215], a[68:71], v[92:95], v[212:215]\n"
    "v_mfma_f32_16x16x32_bf16 v[212:215], a[64:67], v[68:71], v[212:215]\n"
    "s_add_u32 s55, %5, 32\n"
    "s_mul_i32 s55, s55, 45056\n"
    "s_mul_i32 s46, s36, 6144\n"
    "s_add_u32 s55, s55, s46\n"
    "s_xor_b32 s56, %5, 1\n"
    "s_lshl_b32 s56, s56, 2\n"
    "s_add_u32 s56, s56, s36\n"
    "s_lshl_b32 s56, s56, 3\n"
    "s_add_u32 s56, s56, 512\n"
    "s_mov_b32 s37, 4\n"
    ".L_oD_%=:\n"
    "s_cmp_eq_u32 s37, 2\n"
    "s_cbranch_scc0 .L_ocD_%=\n"
    "s_waitcnt vmcnt(20)\n"
    "s_mov_b32 s57, 0x40000\n"
    ".L_fspDo_%=:\n"
    "v_cmp_ne_u64 vcc, s[62:63], v[40:41]\n"
    "s_cbranch_vccz .L_fokDo_%=\n"
    "s_sleep 4\n"
    "buffer_load_dwordx2 v[40:41], v42, s[32:35], 0 offen offset:128 sc1\n"
    "s_sub_u32 s57, s57, 1\n"
    "s_waitcnt vmcnt(0)\n"
    "s_cmp_lg_u32 s57, 0\n"
    "s_cbranch_scc1 .L_fspDo_%=\n"
    ".L_fokDo_%=:\n"
    ".L_ocD_%=:\n"
    "s_waitcnt vmcnt(16) lgkmcnt(0)\n"
    "s_barrier\n"
    "v_mfma_f32_16x16x32_bf16 v[160:163], a[0:3], a[176:179], v[160:163]\n"
    "ds_read_b64_tr_b16 a[64:65], v5 offset:16384\n"
    "ds_read_b64_tr_b16 a[66:67], v5 offset:18432\n"
    "v_readlane_b32 s39, v16, s43\n"
    "s_add_u32 s43, s43, 1\n"
    "s_add_u32 m0, s40, 0\n"
    "s_add_u32 s45, s39, 0\n"
    "buffer_load_dwordx4 v1, s[12:15], s45 offen lds\n"
    "v_mfma_f32_16x16x32_bf16 v[160:163], a[4:7], a[180:183], v[160:163]\n"
    "ds_read_b64_tr_b16 a[68:69], v5 offset:20480\n"
    "buffer_load_dwordx4 v2, s[12:15], s45 offen offset:1024 lds\n"
    "v_mfma_f32_16x16x32_bf16 v[160:163], a[8:11], a[184:187], v[160:163]\n"
    "ds_read_b64_tr_b16 a[70:71], v5 offset:22528\n"
    "v_mfma_f32_16x16x32_bf16 v[160:163], a[12:15], a[188:191], v[160:163]\n"
    "ds_read_b64_tr_b16 a[72:73], v5 offset:24576\n"
    "buffer_load_dwordx4 v3, s[12:15], s45 offen offset:2048 lds\n"
    "v_mfma_f32_16x16x32_bf16 v[172:175], a[28:31], a[188:191], v[172:175]\n"
    "ds_read_b64_tr_b16 a[74:75], v5 offset:26624\n"
    "buffer_load_dwordx4 v4, s[12:15], s45 offen offset:3072 lds\n"
    "v_mfma_f32_16x16x32_bf16 v[172:175], a[24:27], a[184:187], v[172:175]\n"
    "ds_read_b64_tr_b16 a[76:77], v5 offset:28672\n"
    "v_mfma_f32_16x16x32_bf16 v[172:175], a[20:23], a[180:183], v[172:175]\n"
    "ds_read_b64_tr_b16 a[78:79], v5 offset:30720\n"
    "s_add_u32 m0, s40, 4096\n"
    "s_add_u32 s45, s39, 65536\n"
    "buffer_load_dwordx4 v1, s[12:15], s45 offen lds\n"
    "v_mfma_f32_16x16x32_bf16 v[172:175], a[16:19], a[176:179], v[172:175]\n"
    "ds_read_b64_tr_b16 a[80:81], v6 offset:16384\n"
    "v_mfma_f32_16x16x32_bf16 v[184:187], a[32:35], a[176:179], v[184:187]\n"
    "ds_read_b64_tr_b16 a[82:83], v6 offset:18432\n"
    "buffer_load_dwordx4 v2, s[12:15], s45 offen offset:1024 lds\n"
    "v_mfma_f32_16x16x32_bf16 v[184:187], a[36:39], a[180:183], v[184:187]\n"
    "ds_read_b64_tr_b16 a[84:85], v6 offset:20480\n"
    "ds_read_b64_tr_b16 a[86:87], v6 offset:22528\n"
    "buffer_load_dwordx4 v3, s[12:15], s45 offen offset:2048 lds\n"
    "v_mfma_f32_16x16x32_bf16 v[184:187], a[40:43], a[184:187], v[184:187]\n"
    "ds_read_b64_tr_b16 a[88:89], v6 offset:24576\n"
    "v_mfma_f32_16x16x32_bf16 v[184:187], a[44:47], a[188:191], v[184:187]\n"
    "ds_read_b64_tr_b16 a[90:91], v6 offset:26624\n"
    "buffer_load_dwordx4 v4, s[12:15], s45 offen offset:3072 lds\n"
    "v_mfma_f32_16x16x32_bf16 v[196:199], a[60:63], a[188:191], v[196:199]\n"
    "ds_read_b64_tr_b16 a[92:93], v6 offset:28672\n"
    "s_add_u32 m0, s40, 8192\n"
    "s_add_u32 s45, s39, 131072\n"
    "buffer_load_dwordx4 v1, s[12:15], s45 offen lds\n"
    "v_mfma_f32_16x16x32_bf16 v[196:199], a[56:59], a[184:187], v[196:199]\n"
    "ds_read_b64_tr_b16 a[94:95], v6 offset:30720\n"
    "v_mfma_f32_16x16x32_bf16 v[196:199], a[52:55], a[180:183], v[196:199]\n"
    "ds_read_b64_tr_b16 a[96:97], v7 offset:16384\n"
    "buffer_load_dwordx4 v2, s[12:15], s45 offen offset:1024 lds\n"
    "v_mfma_f32_16x16x32_bf16 v[196:199], a[48:51], a[176:179], v[196:199]\n"
    "s_waitcnt vmcnt(31)\n"
    "ds_read_b64_tr_b16 a[98:99], v7 offset:18432\n"
    "v_mfma_f32_16x16x32_bf16 v[200:203], a[48:51], a[192:195], v[200:203]\n"
    "ds_read_b64_tr_b16 a[100:101], v7 offset:20480\n"
    "buffer_load_dwordx4 v3, s[12:15], s45 offen offset:2048 lds\n"
    "v_mfma_f32_16x16x32_bf16 v[200:203], a[52:55], a[196:199], v[200:203]\n"
    "ds_read_b64_tr_b16 a[102:103], v7 offset:22528\n"
    "buffer_load_dwordx4 v4, s[12:15], s45 offen offset:3072 lds\n"
    "v_mfma_f32_16x16x32_bf16 v[200:203], a[56:59], a[200:203], v[200:203]\n"
    "ds_read_b64_tr_b16 a[104:105], v7 offset:24576\n"
    "v_mfma_f32_16x16x32_bf16 v[200:203], a[60:63], a[204:207], v[200:203]\n"
    "ds_read_b64_tr_b16 a[106:107], v7 offset:26624\n"
    "ds_read_b64_tr_b16 a[108:109], v7 offset:28672\n"
    "s_add_u32 m0, s40, 12288\n"
    "s_add_u32 s45, s39, 196608\n"
    "buffer_load_dwordx4 v1, s[12:15], s45 offen lds\n"
    "v_mfma_f32_16x16x32_bf16 v[188:191], a[44:47], a[204:207], v[188:191]\n"
    "ds_read_b64_tr_b16 a[110:111], v7 offset:30720\n"
    "buffer_load_dwordx4 v2, s[12:15], s45 offen offset:1024 lds\n"
    "v_mfma_f32_16x16x32_bf16 v[188:191], a[40:43], a[200:203], v[188:191]\n"
    "ds_read_b64_tr_b16 a[112:113], v8 offset:16384\n"
    "v_mfma_f32_16x16x32_bf16 v[188:191], a[36:39], a[196:199], v[188:191]\n"
    "ds_read_b64_tr_b16 a[114:115], v8 offset:18432\n"
    "buffer_load_dwordx4 v3, s[12:15], s45 offen offset:2048 lds\n"
    "v_mfma_f32_16x16x32_bf16 v[188:191], a[32:35], a[192:195], v[188:191]\n"
    "ds_read_b64_tr_b16 a[116:117], v8 offset:20480\n"
    "v_mfma_f32_16x16x32_bf16 v[176:179], a[16:19], a[192:195], v[176:179]\n"
    "ds_read_b64_tr_b16 a[118:119], v8 offset:22528\n"
    "buffer_load_dwordx4 v4, s[12:15], s45 offen offset:3072 lds\n"
    "v_mfma_f32_16x16x32_bf16 v[176:179], a[20:23], a[196:199], v[176:179]\n"
    "ds_read_b64_tr_b16 a[120:121], v8 offset:24576\n"
    "s_add_u32 s40, s40, 0x4000\n"
    "s_and_b32 s40, s40, 0x1ffff\n"
    "buffer_load_dwordx4 a[144:147], v15, s[24:27], s48 offen offset:1024 sc1\n"
    "v_mfma_f32_16x16x32_bf16 v[176:179], a[24:27], a[200:203], v[176:179]\n"
    "ds_read_b64_tr_b16 a[122:123], v8 offset:26624\n"
    "v_mfma_f32_16x16x32_bf16 v[176:179], a[28:31], a[204:207], v[176:179]\n"
    "ds_read_b64_tr_b16 a[124:125], v8 offset:28672\n"
    "buffer_load_dwordx4 a[148:151], v15, s[24:27], s49 offen offset:1024 sc1\n"
    "v_mfma_f32_16x16x32_bf16 v[164:167], a[12:15], a[204:207], v[164:167]\n"
    "ds_read_b64_tr_b16 a[126:127], v8 offset:30720\n"
    "buffer_load_dwordx4 a[152:155], v15, s[24:27], s50 offen offset:1024 sc1\n"
    "v_mfma_f32_16x16x32_bf16 v[164:167], a[8:11], a[200:203], v[164:167]\n"
    "v_mfma_f32_16x16x32_bf16 v[164:167], a[4:7], a[196:199], v[164:167]\n"
    "buffer_load_dwordx4 a[156:159], v15, s[24:27], s51 offen offset:1024 sc1\n"
    "v_mfma_f32_16x16x32_bf16 v[164:167], a[0:3], a[192:195], v[164:167]\n"
    "s_waitcnt lgkmcnt(0)\n"
    "v_mfma_f32_16x16x32_bf16 v[208:211], a[64:67], a[176:179], v[208:211]\n"
    "ds_read_b64_tr_b16 a[0:1], v5 offset:32768\n"
    "ds_read_b64_tr_b16 a[2:3], v5 offset:34816\n"
    "v_readlane_b32 s39, v16, s43\n"
    "s_add_u32 s43, s43, 1\n"
    "s_add_u32 m0, s40, 0\n"
    "s_add_u32 s45, s39, 0\n"
    "buffer_load_dwordx4 v1, s[12:15], s45 offen lds\n"
    "v_mfma_f32_16x16x32_bf16 v[208:211], a[68:71], a[180:183], v[208:211]\n"
    "ds_read_b64_tr_b16 a[4:5], v5 offset:36864\n"
    "buffer_load_dwordx4 v2, s[12:15], s45 offen offset:1024 lds\n"
    "v_mfma_f32_16x16x32_bf16 v[208:211], a[72:75], a[184:187], v[208:211]\n"
    "ds_read_b64_tr_b16 a[6:7], v5 offset:38912\n"
    "v_mfma_f32_16x16x32_bf16 v[208:211], a[76:79], a[188:191], v[208:211]\n"
    "ds_read_b64_tr_b16 a[8:9], v5 offset:40960\n"
    "buffer_load_dwordx4 v3, s[12:15], s45 offen offset:2048 lds\n"
    "v_mfma_f32_16x16x32_bf16 v[220:223], a[92:95], a[188:191], v[220:223]\n"
    "ds_read_b64_tr_b16 a[10:11], v5 offset:43008\n"
    "buffer_load_dwordx4 v4, s[12:15], s45 offen offset:3072 lds\n"
    "v_mfma_f32_16x16x32_bf16 v[220:223], a[88:91], a[184:187], v[220:223]\n"
    "ds_read_b64_tr_b16 a[12:13], v5 offset:45056\n"
    "v_mfma_f32_16x16x32_bf16 v[220:223], a[84:87], a[180:183], v[220:223]\n"
    "ds_read_b64_tr_b16 a[14:15], v5 offset:47104\n"
    "s_add_u32 m0, s40, 4096\n"
    "s_add_u32 s45, s39, 65536\n"
    "buffer_load_dwordx4 v1, s[12:15], s45 offen lds\n"
    "v_mfma_f32_16x16x32_bf16 v[220:223], a[80:83], a[176:179], v[220:223]\n"
    "ds_read_b64_tr_b16 a[16:17], v6 offset:32768\n"
    "v_mfma_f32_16x16x32_bf16 v[232:235], a[96:99], a[176:179], v[232:235]\n"
    "ds_read_b64_tr_b16 a[18:19], v6 offset:34816\n"
    "buffer_load_dwordx4 v2, s[12:15], s45 offen offset:1024 lds\n"
    "v_mfma_f32_16x16x32_bf16 v[232:235], a[100:103], a[180:183], v[232:235]\n"
    "ds_read_b64_tr_b16 a[20:21], v6 offset:36864\n"
    "ds_read_b64_tr_b16 a[22:23], v6 offset:38912\n"
    "buffer_load_dwordx4 v3, s[12:15], s45 offen offset:2048 lds\n"
    "v_mfma_f32_16x16x32_bf16 v[232:235], a[104:107], a[184:187], v[232:235]\n"
    "ds_read_b64_tr_b16 a[24:25], v6 offset:40960\n"
    "v_mfma_f32_16x16x32_bf16 v[232:235], a[108:111], a[188:191], v[232:235]\n"
    "ds_read_b64_tr_b16 a[26:27], v6 offset:43008\n"
    "buffer_load_dwordx4 v4, s[12:15], s45 offen offset:3072 lds\n"
    "v_mfma_f32_16x16x32_bf16 v[244:247], a[124:127], a[188:191], v[244:247]\n"
    "ds_read_b64_tr_b16 a[28:29], v6 offset:45056\n"
    "s_add_u32 m0, s40, 8192\n"
    "s_add_u32 s45, s39, 131072\n"
    "buffer_load_dwordx4 v1, s[12:15], s45 offen lds\n"
    "v_mfma_f32_16x16x32_bf16 v[244:247], a[120:123], a[184:187], v[244:247]\n"
    "ds_read_b64_tr_b16 a[30:31], v6 offset:47104\n"
    "v_mfma_f32_16x16x32_bf16 v[244:247], a[116:119], a[180:183], v[244:247]\n"
    "ds_read_b64_tr_b16 a[32:33], v7 offset:32768\n"
    "buffer_load_dwordx4 v2, s[12:15], s45 offen offset:1024 lds\n"
    "v_mfma_f32_16x16x32_bf16 v[244:247], a[112:115], a[176:179], v[244:247]\n"
    "ds_read_b64_tr_b16 a[34:35], v7 offset:34816\n"
    "v_mfma_f32_16x16x32_bf16 v[248:251], a[112:115], a[192:195], v[248:251]\n"
    "ds_read_b64_tr_b16 a[36:37], v7 offset:36864\n"
    "buffer_load_dwordx4 v3, s[12:15], s45 offen offset:2048 lds\n"
    "v_mfma_f32_16x16x32_bf16 v[248:251], a[116:119], a[196:199], v[248:251]\n"
    "ds_read_b64_tr_b16 a[38:39], v7 offset:38912\n"
    "buffer_load_dwordx4 v4, s[12:15], s45 offen offset:3072 lds\n"
    "v_mfma_f32_16x16x32_bf16 v[248:251], a[120:123], a[200:203], v[248:251]\n"
    "ds_read_b64_tr_b16 a[40:41], v7 offset:40960\n"
    "v_mfma_f32_16x16x32_bf16 v[248:251], a[124:127], a[204:207], v[248:251]\n"
    "ds_read_b64_tr_b16 a[42:43], v7 offset:43008\n"
    "ds_read_b64_tr_b16 a[44:45], v7 offset:45056\n"
    "s_add_u32 m0, s40, 12288\n"
    "s_add_u32 s45, s39, 196608\n"
    "buffer_load_dwordx4 v1, s[12:15], s45 offen lds\n"
    "v_mfma_f32_16x16x32_bf16 v[236:239], a[108:111], a[204:207], v[236:239]\n"
    "ds_read_b64_tr_b16 a[46:47], v7 offset:47104\n"
    "buffer_load_dwordx4 v2, s[12:15], s45 offen offset:1024 lds\n"
    "v_mfma_f32_16x16x32_bf16 v[236:239], a[104:107], a[200:203], v[236:239]\n"
    "ds_read_b64_tr_b16 a[48:49], v8 offset:32768\n"
    "v_mfma_f32_16x16x32_bf16 v[236:239], a[100:103], a[196:199], v[236:239]\n"
    "ds_read_b64_tr_b16 a[50:51], v8 offset:34816\n"
    "buffer_load_dwordx4 v3, s[12:15], s45 offen offset:2048 lds\n"
    "v_mfma_f32_16x16x32_bf16 v[236:239], a[96:99], a[192:195], v[236:239]\n"
    "ds_read_b64_tr_b16 a[52:53], v8 offset:36864\n"
    "v_mfma_f32_16x16x32_bf16 v[224:227], a[80:83], a[192:195], v[224:227]\n"
    "ds_read_b64_tr_b16 a[54:55], v8 offset:38912\n"
    "buffer_load_dwordx4 v4, s[12:15], s45 offen offset:3072 lds\n"
    "v_mfma_f32_16x16x32_bf16 v[224:227], a[84:87], a[196:199], v[224:227]\n"
    "ds_read_b64_tr_b16 a[56:57], v8 offset:40960\n"
    "s_add_u32 s40, s40, 0x4000\n"
    "s_and_b32 s40, s40, 0x1ffff\n"
    "v_readlane_b32 s38, v43, s52\n"
    "s_add_u32 s52, s52, 1\n"
    "s_add_u32 s48, s38, 0\n"
    "s_add_u32 s49, s38, 11264\n"
    "s_add_u32 s50, s38, 22528\n"
    "s_add_u32 s51, s38, 33792\n"
    "buffer_load_dwordx4 a[176:179], v15, s[24:27], s48 offen offset:0 sc1\n"
    "v_mfma_f32_16x16x32_bf16 v[224:227], a[88:91], a[200:203], v[224:227]\n"
    "ds_read_b64_tr_b16 a[58:59], v8 offset:43008\n"
    "v_mfma_f32_16x16x32_bf16 v[224:227], a[92:95], a[204:207], v[224:227]\n"
    "ds_read_b64_tr_b16 a[60:61], v8 offset:45056\n"
    "buffer_load_dwordx4 a[180:183], v15, s[24:27], s49 offen offset:0 sc1\n"
    "v_mfma_f32_16x16x32_bf16 v[212:215], a[76:79], a[204:207], v[212:215]\n"
    "ds_read_b64_tr_b16 a[62:63], v8 offset:47104\n"
    "buffer_load_dwordx4 a[184:187], v15, s[24:27], s50 offen offset:0 sc1\n"
    "v_mfma_f32_16x16x32_bf16 v[212:215], a[72:75], a[200:203], v[212:215]\n"
    "v_mfma_f32_16x16x32_bf16 v[212:215], a[68:71], a[196:199], v[212:215]\n"
    "buffer_load_dwordx4 a[188:191], v15, s[24:27], s51 offen offset:0 sc1\n"
    "v_mfma_f32_16x16x32_bf16 v[212:215], a[64:67], a[192:195], v[212:215]\n"
    "s_waitcnt vmcnt(16) lgkmcnt(0)\n"
    "s_barrier\n"
    "s_cmp_eq_u32 s37, 4\n"
    "s_cbranch_scc0 .L_fsto_%=\n"
    "s_lshl_b32 s57, %5, 3\n"
    "s_add_u32 s57, s57, 128\n"
    "v_mov_b64 v[62:63], s[62:63]\n"
    "s_mov_b64 exec, 1\n"
    "buffer_store_dwordx2 v[62:63], off, s[32:35], s57 sc1\n"
    "s_mov_b64 exec, -1\n"
    ".L_fsto_%=:\n"
    "v_mfma_f32_16x16x32_bf16 v[160:163], a[0:3], a[128:131], v[160:163]\n"
    "ds_read_b64_tr_b16 a[64:65], v5 offset:49152\n"
    "ds_read_b64_tr_b16 a[66:67], v5 offset:51200\n"
    "v_readlane_b32 s39, v16, s43\n"
    "s_add_u32 s43, s43, 1\n"
    "s_add_u32 m0, s40, 0\n"
    "s_add_u32 s45, s39, 0\n"
    "buffer_load_dwordx4 v1, s[12:15], s45 offen lds\n"
    "v_mfma_f32_16x16x32_bf16 v[160:163], a[4:7], a[132:135], v[160:163]\n"
    "ds_read_b64_tr_b16 a[68:69], v5 offset:53248\n"
    "buffer_load_dwordx4 v2, s[12:15], s45 offen offset:1024 lds\n"
    "v_mfma_f32_16x16x32_bf16 v[160:163], a[8:11], a[136:139], v[160:163]\n"
    "ds_read_b64_tr_b16 a[70:71], v5 offset:55296\n"
    "v_mfma_f32_16x16x32_bf16 v[160:163], a[12:15], a[140:143], v[160:163]\n"
    "ds_read_b64_tr_b16 a[72:73], v5 offset:57344\n"
    "buffer_load_dwordx4 v3, s[12:15], s45 offen offset:2048 lds\n"
    "v_mfma_f32_16x16x32_bf16 v[172:175], a[28:31], a[140:143], v[172:175]\n"
    "ds_read_b64_tr_b16 a[74:75], v5 offset:59392\n"
    "buffer_load_dwordx4 v4, s[12:15], s45 offen offset:3072 lds\n"
    "v_mfma_f32_16x16x32_bf16 v[172:175], a[24:27], a[136:139], v[172:175]\n"
    "ds_read_b64_tr_b16 a[76:77], v5 offset:61440\n"
    "v_mfma_f32_16x16x32_bf16 v[172:175], a[20:23], a[132:135], v[172:175]\n"
    "ds_read_b64_tr_b16 a[78:79], v5 offset:63488\n"
    "v_xor_b32 v5, 0x10000, v5\n"
    "s_add_u32 m0, s40, 4096\n"
    "s_add_u32 s45, s39, 65536\n"
    "buffer_load_dwordx4 v1, s[12:15], s45 offen lds\n"
    "v_mfma_f32_16x16x32_bf16 v[172:175], a[16:19], a[128:131], v[172:175]\n"
    "ds_read_b64_tr_b16 a[80:81], v6 offset:49152\n"
    "v_mfma_f32_16x16x32_bf16 v[184:187], a[32:35], a[128:131], v[184:187]\n"
    "ds_read_b64_tr_b16 a[82:83], v6 offset:51200\n"
    "buffer_load_dwordx4 v2, s[12:15], s45 offen offset:1024 lds\n"
    "v_mfma_f32_16x16x32_bf16 v[184:187], a[36:39], a[132:135], v[184:187]\n"
    "ds_read_b64_tr_b16 a[84:85], v6 offset:53248\n"
    "ds_read_b64_tr_b16 a[86:87], v6 offset:55296\n"
    "buffer_load_dwordx4 v3, s[12:15], s45 offen offset:2048 lds\n"
    "v_mfma_f32_16x16x32_bf16 v[184:187], a[40:43], a[136:139], v[184:187]\n"
    "ds_read_b64_tr_b16 a[88:89], v6 offset:57344\n"
    "v_mfma_f32_16x16x32_bf16 v[184:187], a[44:47], a[140:143], v[184:187]\n"
    "ds_read_b64_tr_b16 a[90:91], v6 offset:59392\n"
    "buffer_load_dwordx4 v4, s[12:15], s45 offen offset:3072 lds\n"
    "v_mfma_f32_16x16x32_bf16 v[196:199], a[60:63], a[140:143], v[196:199]\n"
    "ds_read_b64_tr_b16 a[92:93], v6 offset:61440\n"
    "s_add_u32 m0, s40, 8192\n"
    "s_add_u32 s45, s39, 131072\n"
    "buffer_load_dwordx4 v1, s[12:15], s45 offen lds\n"
    "v_mfma_f32_16x16x32_bf16 v[196:199], a[56:59], a[136:139], v[196:199]\n"
    "ds_read_b64_tr_b16 a[94:95], v6 offset:63488\n"
    "v_xor_b32 v6, 0x10000, v6\n"
    "v_mfma_f32_16x16x32_bf16 v[196:199], a[52:55], a[132:135], v[196:199]\n"
    "ds_read_b64_tr_b16 a[96:97], v7 offset:49152\n"
    "buffer_load_dwordx4 v2, s[12:15], s45 offen offset:1024 lds\n"
    "v_mfma_f32_16x16x32_bf16 v[196:199], a[48:51], a[128:131], v[196:199]\n"
    "s_waitcnt vmcnt(30)\n"
    "ds_read_b64_tr_b16 a[98:99], v7 offset:51200\n"
    "v_mfma_f32_16x16x32_bf16 v[200:203], a[48:51], a[144:147], v[200:203]\n"
    "ds_read_b64_tr_b16 a[100:101], v7 offset:53248\n"
    "buffer_load_dwordx4 v3, s[12:15], s45 offen offset:2048 lds\n"
    "v_mfma_f32_16x16x32_bf16 v[200:203], a[52:55], a[148:151], v[200:203]\n"
    "ds_read_b64_tr_b16 a[102:103], v7 offset:55296\n"
    "buffer_load_dwordx4 v4, s[12:15], s45 offen offset:3072 lds\n"
    "v_mfma_f32_16x16x32_bf16 v[200:203], a[56:59], a[152:155], v[200:203]\n"
    "ds_read_b64_tr_b16 a[104:105], v7 offset:57344\n"
    "v_mfma_f32_16x16x32_bf16 v[200:203], a[60:63], a[156:159], v[200:203]\n"
    "ds_read_b64_tr_b16 a[106:107], v7 offset:59392\n"
    "ds_read_b64_tr_b16 a[108:109], v7 offset:61440\n"
    "s_add_u32 m0, s40, 12288\n"
    "s_add_u32 s45, s39, 196608\n"
    "buffer_load_dwordx4 v1, s[12:15], s45 offen lds\n"
    "v_mfma_f32_16x16x32_bf16 v[188:191], a[44:47], a[156:159], v[188:191]\n"
    "ds_read_b64_tr_b16 a[110:111], v7 offset:63488\n"
    "v_xor_b32 v7, 0x10000, v7\n"
    "buffer_load_dwordx4 v2, s[12:15], s45 offen offset:1024 lds\n"
    "v_mfma_f32_16x16x32_bf16 v[188:191], a[40:43], a[152:155], v[188:191]\n"
    "ds_read_b64_tr_b16 a[112:113], v8 offset:49152\n"
    "v_mfma_f32_16x16x32_bf16 v[188:191], a[36:39], a[148:151], v[188:191]\n"
    "ds_read_b64_tr_b16 a[114:115], v8 offset:51200\n"
    "buffer_load_dwordx4 v3, s[12:15], s45 offen offset:2048 lds\n"
    "v_mfma_f32_16x16x32_bf16 v[188:191], a[32:35], a[144:147], v[188:191]\n"
    "ds_read_b64_tr_b16 a[116:117], v8 offset:53248\n"
    "v_mfma_f32_16x16x32_bf16 v[176:179], a[16:19], a[144:147], v[176:179]\n"
    "ds_read_b64_tr_b16 a[118:119], v8 offset:55296\n"
    "buffer_load_dwordx4 v4, s[12:15], s45 offen offset:3072 lds\n"
    "v_mfma_f32_16x16x32_bf16 v[176:179], a[20:23], a[148:151], v[176:179]\n"
    "ds_read_b64_tr_b16 a[120:121], v8 offset:57344\n"
    "s_add_u32 s40, s40, 0x4000\n"
    "s_and_b32 s40, s40, 0x1ffff\n"
    "buffer_load_dwordx4 a[192:195], v15, s[24:27], s48 offen offset:1024 sc1\n"
    "v_mfma_f32_16x16x32_bf16 v[176:179], a[24:27], a[152:155], v[176:179]\n"
    "ds_read_b64_tr_b16 a[122:123], v8 offset:59392\n"
    "v_mfma_f32_16x16x32_bf16 v[176:179], a[28:31], a[156:159], v[176:179]\n"
    "ds_read_b64_tr_b16 a[124:125], v8 offset:61440\n"
    "buffer_load_dwordx4 a[196:199], v15, s[24:27], s49 offen offset:1024 sc1\n"
    "v_mfma_f32_16x16x32_bf16 v[164:167], a[12:15], a[156:159], v[164:167]\n"
    "ds_read_b64_tr_b16 a[126:127], v8 offset:63488\n"
    "v_xor_b32 v8, 0x10000, v8\n"
    "buffer_load_dwordx4 a[200:203], v15, s[24:27], s50 offen offset:1024 sc1\n"
    "v_mfma_f32_16x16x32_bf16 v[164:167], a[8:11], a[152:155], v[164:167]\n"
    "v_mfma_f32_16x16x32_bf16 v[164:167], a[4:7], a[148:151], v[164:167]\n"
    "buffer_load_dwordx4 a[204:207], v15, s[24:27], s51 offen offset:1024 sc1\n"
    "v_mfma_f32_16x16x32_bf16 v[164:167], a[0:3], a[144:147], v[164:167]\n"
    "s_waitcnt lgkmcnt(0)\n"
    "v_mfma_f32_16x16x32_bf16 v[208:211], a[64:67], a[128:131], v[208:211]\n"
    "ds_read_b64_tr_b16 a[0:1], v5 offset:0\n"
    "ds_read_b64_tr_b16 a[2:3], v5 offset:2048\n"
    "buffer_load_dwordx2 v[40:41], v42, s[32:35], 0 offen offset:128 sc1\n"
    "v_mfma_f32_16x16x32_bf16 v[208:211], a[68:71], a[132:135], v[208:211]\n"
    "ds_read_b64_tr_b16 a[4:5], v5 offset:4096\n"
    "v_readlane_b32 s39, v16, s43\n"
    "s_add_u32 s43, s43, 1\n"
    "s_add_u32 m0, s40, 0\n"
    "s_add_u32 s45, s39, 0\n"
    "buffer_load_dwordx4 v1, s[12:15], s45 offen lds\n"
    "v_mfma_f32_16x16x32_bf16 v[208:211], a[72:75], a[136:139], v[208:211]\n"
    "ds_read_b64_tr_b16 a[6:7], v5 offset:6144\n"
    "v_mfma_f32_16x16x32_bf16 v[208:211], a[76:79], a[140:143], v[208:211]\n"
    "ds_read_b64_tr_b16 a[8:9], v5 offset:8192\n"
    "buffer_load_dwordx4 v2, s[12:15], s45 offen offset:1024 lds\n"
    "v_mfma_f32_16x16x32_bf16 v[220:223], a[92:95], a[140:143], v[220:223]\n"
    "ds_read_b64_tr_b16 a[10:11], v5 offset:10240\n"
    "buffer_load_dwordx4 v3, s[12:15], s45 offen offset:2048 lds\n"
    "v_mfma_f32_16x16x32_bf16 v[220:223], a[88:91], a[136:139], v[220:223]\n"
    "ds_read_b64_tr_b16 a[12:13], v5 offset:12288\n"
    "v_mfma_f32_16x16x32_bf16 v[220:223], a[84:87], a[132:135], v[220:223]\n"
    "ds_read_b64_tr_b16 a[14:15], v5 offset:14336\n"
    "buffer_load_dwordx4 v4, s[12:15], s45 offen offset:3072 lds\n"
    "v_mfma_f32_16x16x32_bf16 v[220:223], a[80:83], a[128:131], v[220:223]\n"
    "ds_read_b64_tr_b16 a[16:17], v6 offset:0\n"
    "s_add_u32 m0, s40, 4096\n"
    "s_add_u32 s45, s39, 65536\n"
    "buffer_load_dwordx4 v1, s[12:15], s45 offen lds\n"
    "v_mfma_f32_16x16x32_bf16 v[232:235], a[96:99], a[128:131], v[232:235]\n"
    "ds_read_b64_tr_b16 a[18:19], v6 offset:2048\n"
    "v_mfma_f32_16x16x32_bf16 v[232:235], a[100:103], a[132:135], v[232:235]\n"
    "ds_read_b64_tr_b16 a[20:21], v6 offset:4096\n"
    "ds_read_b64_tr_b16 a[22:23], v6 offset:6144\n"
    "buffer_load_dwordx4 v2, s[12:15], s45 offen offset:1024 lds\n"
    "v_mfma_f32_16x16x32_bf16 v[232:235], a[104:107], a[136:139], v[232:235]\n"
    "ds_read_b64_tr_b16 a[24:25], v6 offset:8192\n"
    "buffer_load_dwordx4 v3, s[12:15], s45 offen offset:2048 lds\n"
    "v_mfma_f32_16x16x32_bf16 v[232:235], a[108:111], a[140:143], v[232:235]\n"
    "ds_read_b64_tr_b16 a[26:27], v6 offset:10240\n"
    "v_mfma_f32_16x16x32_bf16 v[244:247], a[124:127], a[140:143], v[244:247]\n"
    "ds_read_b64_tr_b16 a[28:29], v6 offset:12288\n"
    "buffer_load_dwordx4 v4, s[12:15], s45 offen offset:3072 lds\n"
    "v_mfma_f32_16x16x32_bf16 v[244:247], a[120:123], a[136:139], v[244:247]\n"
    "ds_read_b64_tr_b16 a[30:31], v6 offset:14336\n"
    "s_add_u32 m0, s40, 8192\n"
    "s_add_u32 s45, s39, 131072\n"
    "buffer_load_dwordx4 v1, s[12:15], s45 offen lds\n"
    "v_mfma_f32_16x16x32_bf16 v[244:247], a[116:119], a[132:135], v[244:247]\n"
    "ds_read_b64_tr_b16 a[32:33], v7 offset:0\n"
    "v_mfma_f32_16x16x32_bf16 v[244:247], a[112:115], a[128:131], v[244:247]\n"
    "ds_read_b64_tr_b16 a[34:35], v7 offset:2048\n"
    "buffer_load_dwordx4 v2, s[12:15], s45 offen offset:1024 lds\n"
    "v_mfma_f32_16x16x32_bf16 v[248:251], a[112:115], a[144:147], v[248:251]\n"
    "ds_read_b64_tr_b16 a[36:37], v7 offset:4096\n"
    "buffer_load_dwordx4 v3, s[12:15], s45 offen offset:2048 lds\n"
    "v_mfma_f32_16x16x32_bf16 v[248:251], a[116:119], a[148:151], v[248:251]\n"
    "ds_read_b64_tr_b16 a[38:39], v7 offset:6144\n"
    "v_mfma_f32_16x16x32_bf16 v[248:251], a[120:123], a[152:155], v[248:251]\n"
    "ds_read_b64_tr_b16 a[40:41], v7 offset:8192\n"
    "buffer_load_dwordx4 v4, s[12:15], s45 offen offset:3072 lds\n"
    "v_mfma_f32_16x16x32_bf16 v[248:251], a[124:127], a[156:159], v[248:251]\n"
    "ds_read_b64_tr_b16 a[42:43], v7 offset:10240\n"
    "ds_read_b64_tr_b16 a[44:45], v7 offset:12288\n"
    "s_add_u32 m0, s40, 12288\n"
    "s_add_u32 s45, s39, 196608\n"
    "buffer_load_dwordx4 v1, s[12:15], s45 offen lds\n"
    "v_mfma_f32_16x16x32_bf16 v[236:239], a[108:111], a[156:159], v[236:239]\n"
    "ds_read_b64_tr_b16 a[46:47], v7 offset:14336\n"
    "v_mfma_f32_16x16x32_bf16 v[236:239], a[104:107], a[152:155], v[236:239]\n"
    "ds_read_b64_tr_b16 a[48:49], v8 offset:0\n"
    "buffer_load_dwordx4 v2, s[12:15], s45 offen offset:1024 lds\n"
    "v_mfma_f32_16x16x32_bf16 v[236:239], a[100:103], a[148:151], v[236:239]\n"
    "ds_read_b64_tr_b16 a[50:51], v8 offset:2048\n"
    "buffer_load_dwordx4 v3, s[12:15], s45 offen offset:2048 lds\n"
    "v_mfma_f32_16x16x32_bf16 v[236:239], a[96:99], a[144:147], v[236:239]\n"
    "ds_read_b64_tr_b16 a[52:53], v8 offset:4096\n"
    "v_mfma_f32_16x16x32_bf16 v[224:227], a[80:83], a[144:147], v[224:227]\n"
    "ds_read_b64_tr_b16 a[54:55], v8 offset:6144\n"
    "buffer_load_dwordx4 v4, s[12:15], s45 offen offset:3072 lds\n"
    "v_mfma_f32_16x16x32_bf16 v[224:227], a[84:87], a[148:151], v[224:227]\n"
    "ds_read_b64_tr_b16 a[56:57], v8 offset:8192\n"
    "s_add_u32 s40, s40, 0x4000\n"
    "s_and_b32 s40, s40, 0x1ffff\n"
    "v_readlane_b32 s38, v43, s52\n"
    "s_add_u32 s52, s52, 1\n"
    "s_add_u32 s48, s38, 0\n"
    "s_add_u32 s49, s38, 11264\n"
    "s_add_u32 s50, s38, 22528\n"
    "s_add_u32 s51, s38, 33792\n"
    "buffer_load_dwordx4 a[128:131], v15, s[24:27], s48 offen offset:0 sc1\n"
    "v_mfma_f32_16x16x32_bf16 v[224:227], a[88:91], a[152:155], v[224:227]\n"
    "ds_read_b64_tr_b16 a[58:59], v8 offset:10240\n"
    "v_mfma_f32_16x16x32_bf16 v[224:227], a[92:95], a[156:159], v[224:227]\n"
    "ds_read_b64_tr_b16 a[60:61], v8 offset:12288\n"
    "buffer_load_dwordx4 a[132:135], v15, s[24:27], s49 offen offset:0 sc1\n"
    "v_mfma_f32_16x16x32_bf16 v[212:215], a[76:79], a[156:159], v[212:215]\n"
    "ds_read_b64_tr_b16 a[62:63], v8 offset:14336\n"
    "buffer_load_dwordx4 a[136:139], v15, s[24:27], s50 offen offset:0 sc1\n"
    "v_mfma_f32_16x16x32_bf16 v[212:215], a[72:75], a[152:155], v[212:215]\n"
    "v_mfma_f32_16x16x32_bf16 v[212:215], a[68:71], a[148:151], v[212:215]\n"
    "buffer_load_dwordx4 a[140:143], v15, s[24:27], s51 offen offset:0 sc1\n"
    "v_mfma_f32_16x16x32_bf16 v[212:215], a[64:67], a[144:147], v[212:215]\n"
    "s_sub_u32 s37, s37, 1\n"
    "s_cmp_lg_u32 s37, 0\n"
    "s_cbranch_scc1 .L_oD_%=\n"
    "s_waitcnt vmcnt(16) lgkmcnt(0)\n"
    "s_barrier\n"
    "v_mfma_f32_16x16x32_bf16 v[160:163], a[0:3], a[176:179], v[160:163]\n"
    "ds_read_b64_tr_b16 a[64:65], v5 offset:16384\n"
    "ds_read_b64_tr_b16 a[66:67], v5 offset:18432\n"
    "buffer_load_dwordx4 a[144:147], v15, s[24:27], s48 offen offset:1024 sc1\n"
    "v_mfma_f32_16x16x32_bf16 v[160:163], a[4:7], a[180:183], v[160:163]\n"
    "ds_read_b64_tr_b16 a[68:69], v5 offset:20480\n"
    "buffer_load_dwordx4 a[148:151], v15, s[24:27], s49 offen offset:1024 sc1\n"
    "v_mfma_f32_16x16x32_bf16 v[160:163], a[8:11], a[184:187], v[160:163]\n"
    "ds_read_b64_tr_b16 a[70:71], v5 offset:22528\n"
    "buffer_load_dwordx4 a[152:155], v15, s[24:27], s50 offen offset:1024 sc1\n"
    "v_mfma_f32_16x16x32_bf16 v[160:163], a[12:15], a[188:191], v[160:163]\n"
    "ds_read_b64_tr_b16 a[72:73], v5 offset:24576\n"
    "v_mfma_f32_16x16x32_bf16 v[172:175], a[28:31], a[188:191], v[172:175]\n"
    "ds_read_b64_tr_b16 a[74:75], v5 offset:26624\n"
    "buffer_load_dwordx4 a[156:159], v15, s[24:27], s51 offen offset:1024 sc1\n"
    "v_mfma_f32_16x16x32_bf16 v[172:175], a[24:27], a[184:187], v[172:175]\n"
    "ds_read_b64_tr_b16 a[76:77], v5 offset:28672\n"
    "v_readlane_b32 s38, v43, s52\n"
    "s_add_u32 s52, s52, 1\n"
    "s_add_u32 s48, s38, 0\n"
    "s_add_u32 s49, s38, 11264\n"
    "s_add_u32 s50, s38, 22528\n"
    "s_add_u32 s51, s38, 33792\n"
    "buffer_load_dwordx4 v[64:67], v15, s[24:27], s48 offen offset:0 sc1\n"
    "v_mfma_f32_16x16x32_bf16 v[172:175], a[20:23], a[180:183], v[172:175]\n"
    "ds_read_b64_tr_b16 a[78:79], v5 offset:30720\n"
    "v_mfma_f32_16x16x32_bf16 v[172:175], a[16:19], a[176:179], v[172:175]\n"
    "ds_read_b64_tr_b16 a[80:81], v6 offset:16384\n"
    "buffer_load_dwordx4 v[68:71], v15, s[24:27], s49 offen offset:0 sc1\n"
    "v_mfma_f32_16x16x32_bf16 v[184:187], a[32:35], a[176:179], v[184:187]\n"
    "ds_read_b64_tr_b16 a[82:83], v6 offset:18432\n"
    "v_readlane_b32 s39, v16, s43\n"
    "s_add_u32 s43, s43, 1\n"
    "s_add_u32 m0, s40, 0\n"
    "s_add_u32 s45, s39, 0\n"
    "buffer_load_dwordx4 v1, s[12:15], s45 offen lds\n"
    "v_mfma_f32_16x16x32_bf16 v[184:187], a[36:39], a[180:183], v[184:187]\n"
    "ds_read_b64_tr_b16 a[84:85], v6 offset:20480\n"
    "ds_read_b64_tr_b16 a[86:87], v6 offset:22528\n"
    "v_mfma_f32_16x16x32_bf16 v[184:187], a[40:43], a[184:187], v[184:187]\n"
    "ds_read_b64_tr_b16 a[88:89], v6 offset:24576\n"
    "buffer_load_dwordx4 v2, s[12:15], s45 offen offset:1024 lds\n"
    "v_mfma_f32_16x16x32_bf16 v[184:187], a[44:47], a[188:191], v[184:187]\n"
    "ds_read_b64_tr_b16 a[90:91], v6 offset:26624\n"
    "buffer_load_dwordx4 v3, s[12:15], s45 offen offset:2048 lds\n"
    "v_mfma_f32_16x16x32_bf16 v[196:199], a[60:63], a[188:191], v[196:199]\n"
    "ds_read_b64_tr_b16 a[92:93], v6 offset:28672\n"
    "v_mfma_f32_16x16x32_bf16 v[196:199], a[56:59], a[184:187], v[196:199]\n"
    "ds_read_b64_tr_b16 a[94:95], v6 offset:30720\n"
    "buffer_load_dwordx4 v4, s[12:15], s45 offen offset:3072 lds\n"
    "v_mfma_f32_16x16x32_bf16 v[196:199], a[52:55], a[180:183], v[196:199]\n"
    "ds_read_b64_tr_b16 a[96:97], v7 offset:16384\n"
    "s_add_u32 m0, s40, 4096\n"
    "s_add_u32 s45, s39, 65536\n"
    "buffer_load_dwordx4 v1, s[12:15], s45 offen lds\n"
    "v_mfma_f32_16x16x32_bf16 v[196:199], a[48:51], a[176:179], v[196:199]\n"
    "s_waitcnt vmcnt(32)\n"
    "ds_read_b64_tr_b16 a[98:99], v7 offset:18432\n"
    "v_mfma_f32_16x16x32_bf16 v[200:203], a[48:51], a[192:195], v[200:203]\n"
    "ds_read_b64_tr_b16 a[100:101], v7 offset:20480\n"
    "buffer_load_dwordx4 v2, s[12:15], s45 offen offset:1024 lds\n"
    "v_mfma_f32_16x16x32_bf16 v[200:203], a[52:55], a[196:199], v[200:203]\n"
    "ds_read_b64_tr_b16 a[102:103], v7 offset:22528\n"
    "buffer_load_dwordx4 v3, s[12:15], s45 offen offset:2048 lds\n"
    "v_mfma_f32_16x16x32_bf16 v[200:203], a[56:59], a[200:203], v[200:203]\n"
    "ds_read_b64_tr_b16 a[104:105], v7 offset:24576\n"
    "buffer_load_dwordx4 v4, s[12:15], s45 offen offset:3072 lds\n"
    "v_mfma_f32_16x16x32_bf16 v[200:203], a[60:63], a[204:207], v[200:203]\n"
    "ds_read_b64_tr_b16 a[106:107], v7 offset:26624\n"
    "ds_read_b64_tr_b16 a[108:109], v7 offset:28672\n"
    "v_mfma_f32_16x16x32_bf16 v[188:191], a[44:47], a[204:207], v[188:191]\n"
    "ds_read_b64_tr_b16 a[110:111], v7 offset:30720\n"
    "s_add_u32 m0, s40, 8192\n"
    "s_add_u32 s45, s39, 131072\n"
    "buffer_load_dwordx4 v1, s[12:15], s45 offen lds\n"
    "v_mfma_f32_16x16x32_bf16 v[188:191], a[40:43], a[200:203], v[188:191]\n"
    "ds_read_b64_tr_b16 a[112:113], v8 offset:16384\n"
    "buffer_load_dwordx4 v2, s[12:15], s45 offen offset:1024 lds\n"
    "v_mfma_f32_16x16x32_bf16 v[188:191], a[36:39], a[196:199], v[188:191]\n"
    "ds_read_b64_tr_b16 a[114:115], v8 offset:18432\n"
    "v_mfma_f32_16x16x32_bf16 v[188:191], a[32:35], a[192:195], v[188:191]\n"
    "ds_read_b64_tr_b16 a[116:117], v8 offset:20480\n"
    "buffer_load_dwordx4 v3, s[12:15], s45 offen offset:2048 lds\n"
    "v_mfma_f32_16x16x32_bf16 v[176:179], a[16:19], a[192:195], v[176:179]\n"
    "ds_read_b64_tr_b16 a[118:119], v8 offset:22528\n"
    "buffer_load_dwordx4 v4, s[12:15], s45 offen offset:3072 lds\n"
    "v_mfma_f32_16x16x32_bf16 v[176:179], a[20:23], a[196:199], v[176:179]\n"
    "ds_read_b64_tr_b16 a[120:121], v8 offset:24576\n"
    "v_mfma_f32_16x16x32_bf16 v[176:179], a[24:27], a[200:203], v[176:179]\n"
    "ds_read_b64_tr_b16 a[122:123], v8 offset:26624\n"
    "s_add_u32 m0, s40, 12288\n"
    "s_add_u32 s45, s39, 196608\n"
    "buffer_load_dwordx4 v1, s[12:15], s45 offen lds\n"
    "v_mfma_f32_16x16x32_bf16 v[176:179], a[28:31], a[204:207], v[176:179]\n"
    "ds_read_b64_tr_b16 a[124:125], v8 offset:28672\n"
    "buffer_load_dwordx4 v2, s[12:15], s45 offen offset:1024 lds\n"
    "v_mfma_f32_16x16x32_bf16 v[164:167], a[12:15], a[204:207], v[164:167]\n"
    "ds_read_b64_tr_b16 a[126:127], v8 offset:30720\n"
    "v_mfma_f32_16x16x32_bf16 v[164:167], a[8:11], a[200:203], v[164:167]\n"
    "buffer_load_dwordx4 v3, s[12:15], s45 offen offset:2048 lds\n"
    "v_mfma_f32_16x16x32_bf16 v[164:167], a[4:7], a[196:199], v[164:167]\n"
    "buffer_load_dwordx4 v4, s[12:15], s45 offen offset:3072 lds\n"
    "v_mfma_f32_16x16x32_bf16 v[164:167], a[0:3], a[192:195], v[164:167]\n"
    "s_add_u32 s40, s40, 0x4000\n"
    "s_and_b32 s40, s40, 0x1ffff\n"
    "s_waitcnt lgkmcnt(0)\n"
    "v_mfma_f32_16x16x32_bf16 v[208:211], a[64:67], a[176:179], v[208:211]\n"
    "ds_read_b64_tr_b16 a[0:1], v5 offset:32768\n"
    "ds_read_b64_tr_b16 a[2:3], v5 offset:34816\n"
    "buffer_load_dwordx4 v[72:75], v15, s[24:27], s50 offen offset:0 sc1\n"
    "v_mfma_f32_16x16x32_bf16 v[208:211], a[68:71], a[180:183], v[208:211]\n"
    "ds_read_b64_tr_b16 a[4:5], v5 offset:36864\n"
    "buffer_load_dwordx4 v[76:79], v15, s[24:27], s51 offen offset:0 sc1\n"
    "v_mfma_f32_16x16x32_bf16 v[208:211], a[72:75], a[184:187], v[208:211]\n"
    "ds_read_b64_tr_b16 a[6:7], v5 offset:38912\n"
    "buffer_load_dwordx4 v[80:83], v15, s[24:27], s48 offen offset:1024 sc1\n"
    "v_mfma_f32_16x16x32_bf16 v[208:211], a[76:79], a[188:191], v[208:211]\n"
    "ds_read_b64_tr_b16 a[8:9], v5 offset:40960\n"
    "v_mfma_f32_16x16x32_bf16 v[220:223], a[92:95], a[188:191], v[220:223]\n"
    "ds_read_b64_tr_b16 a[10:11], v5 offset:43008\n"
    "buffer_load_dwordx4 v[84:87], v15, s[24:27], s49 offen offset:1024 sc1\n"
    "v_mfma_f32_16x16x32_bf16 v[220:223], a[88:91], a[184:187], v[220:223]\n"
    "ds_read_b64_tr_b16 a[12:13], v5 offset:45056\n"
    "buffer_load_dwordx4 v[88:91], v15, s[24:27], s50 offen offset:1024 sc1\n"
    "v_mfma_f32_16x16x32_bf16 v[220:223], a[84:87], a[180:183], v[220:223]\n"
    "ds_read_b64_tr_b16 a[14:15], v5 offset:47104\n"
    "v_mfma_f32_16x16x32_bf16 v[220:223], a[80:83], a[176:179], v[220:223]\n"
    "ds_read_b64_tr_b16 a[16:17], v6 offset:32768\n"
    "buffer_load_dwordx4 v[92:95], v15, s[24:27], s51 offen offset:1024 sc1\n"
    "v_mfma_f32_16x16x32_bf16 v[232:235], a[96:99], a[176:179], v[232:235]\n"
    "ds_read_b64_tr_b16 a[18:19], v6 offset:34816\n"
    "v_readlane_b32 s39, v16, s43\n"
    "s_add_u32 s43, s43, 1\n"
    "s_add_u32 m0, s40, 0\n"
    "s_add_u32 s45, s39, 0\n"
    "buffer_load_dwordx4 v1, s[12:15], s45 offen lds\n"
    "v_mfma_f32_16x16x32_bf16 v[232:235], a[100:103], a[180:183], v[232:235]\n"
    "ds_read_b64_tr_b16 a[20:21], v6 offset:36864\n"
    "ds_read_b64_tr_b16 a[22:23], v6 offset:38912\n"
    "v_mfma_f32_16x16x32_bf16 v[232:235], a[104:107], a[184:187], v[232:235]\n"
    "ds_read_b64_tr_b16 a[24:25], v6 offset:40960\n"
    "buffer_load_dwordx4 v2, s[12:15], s45 offen offset:1024 lds\n"
    "v_mfma_f32_16x16x32_bf16 v[232:235], a[108:111], a[188:191], v[232:235]\n"
    "ds_read_b64_tr_b16 a[26:27], v6 offset:43008\n"
    "buffer_load_dwordx4 v3, s[12:15], s45 offen offset:2048 lds\n"
    "v_mfma_f32_16x16x32_bf16 v[244:247], a[124:127], a[188:191], v[244:247]\n"
    "ds_read_b64_tr_b16 a[28:29], v6 offset:45056\n"
    "v_mfma_f32_16x16x32_bf16 v[244:247], a[120:123], a[184:187], v[244:247]\n"
    "ds_read_b64_tr_b16 a[30:31], v6 offset:47104\n"
    "buffer_load_dwordx4 v4, s[12:15], s45 offen offset:3072 lds\n"
    "v_mfma_f32_16x16x32_bf16 v[244:247], a[116:119], a[180:183], v[244:247]\n"
    "ds_read_b64_tr_b16 a[32:33], v7 offset:32768\n"
    "s_add_u32 m0, s40, 4096\n"
    "s_add_u32 s45, s39, 65536\n"
    "buffer_load_dwordx4 v1, s[12:15], s45 offen lds\n"
    "v_mfma_f32_16x16x32_bf16 v[244:247], a[112:115], a[176:179], v[244:247]\n"
    "ds_read_b64_tr_b16 a[34:35], v7 offset:34816\n"
    "v_mfma_f32_16x16x32_bf16 v[248:251], a[112:115], a[192:195], v[248:251]\n"
    "ds_read_b64_tr_b16 a[36:37], v7 offset:36864\n"
    "buffer_load_dwordx4 v2, s[12:15], s45 offen offset:1024 lds\n"
    "v_mfma_f32_16x16x32_bf16 v[248:251], a[116:119], a[196:199], v[248:251]\n"
    "ds_read_b64_tr_b16 a[38:39], v7 offset:38912\n"
    "buffer_load_dwordx4 v3, s[12:15], s45 offen offset:2048 lds\n"
    "v_mfma_f32_16x16x32_bf16 v[248:251], a[120:123], a[200:203], v[248:251]\n"
    "ds_read_b64_tr_b16 a[40:41], v7 offset:40960\n"
    "buffer_load_dwordx4 v4, s[12:15], s45 offen offset:3072 lds\n"
    "v_mfma_f32_16x16x32_bf16 v[248:251], a[124:127], a[204:207], v[248:251]\n"
    "ds_read_b64_tr_b16 a[42:43], v7 offset:43008\n"
    "ds_read_b64_tr_b16 a[44:45], v7 offset:45056\n"
    "v_mfma_f32_16x16x32_bf16 v[236:239], a[108:111], a[204:207], v[236:239]\n"
    "ds_read_b64_tr_b16 a[46:47], v7 offset:47104\n"
    "s_add_u32 m0, s40, 8192\n"
    "s_add_u32 s45, s39, 131072\n"
    "buffer_load_dwordx4 v1, s[12:15], s45 offen lds\n"
    "v_mfma_f32_16x16x32_bf16 v[236:239], a[104:107], a[200:203], v[236:239]\n"
    "ds_read_b64_tr_b16 a[48:49], v8 offset:32768\n"
    "buffer_load_dwordx4 v2, s[12:15], s45 offen offset:1024 lds\n"
    "v_mfma_f32_16x16x32_bf16 v[236:239], a[100:103], a[196:199], v[236:239]\n"
    "ds_read_b64_tr_b16 a[50:51], v8 offset:34816\n"
    "v_mfma_f32_16x16x32_bf16 v[236:239], a[96:99], a[192:195], v[236:239]\n"
    "ds_read_b64_tr_b16 a[52:53], v8 offset:36864\n"
    "buffer_load_dwordx4 v3, s[12:15], s45 offen offset:2048 lds\n"
    "v_mfma_f32_16x16x32_bf16 v[224:227], a[80:83], a[192:195], v[224:227]\n"
    "ds_read_b64_tr_b16 a[54:55], v8 offset:38912\n"
    "buffer_load_dwordx4 v4, s[12:15], s45 offen offset:3072 lds\n"
    "v_mfma_f32_16x16x32_bf16 v[224:227], a[84:87], a[196:199], v[224:227]\n"
    "ds_read_b64_tr_b16 a[56:57], v8 offset:40960\n"
    "v_mfma_f32_16x16x32_bf16 v[224:227], a[88:91], a[200:203], v[224:227]\n"
    "ds_read_b64_tr_b16 a[58:59], v8 offset:43008\n"
    "s_add_u32 m0, s40, 12288\n"
    "s_add_u32 s45, s39, 196608\n"
    "buffer_load_dwordx4 v1, s[12:15], s45 offen lds\n"
    "v_mfma_f32_16x16x32_bf16 v[224:227], a[92:95], a[204:207], v[224:227]\n"
    "ds_read_b64_tr_b16 a[60:61], v8 offset:45056\n"
    "buffer_load_dwordx4 v2, s[12:15], s45 offen offset:1024 lds\n"
    "v_mfma_f32_16x16x32_bf16 v[212:215], a[76:79], a[204:207], v[212:215]\n"
    "ds_read_b64_tr_b16 a[62:63], v8 offset:47104\n"
    "v_mfma_f32_16x16x32_bf16 v[212:215], a[72:75], a[200:203], v[212:215]\n"
    "buffer_load_dwordx4 v3, s[12:15], s45 offen offset:2048 lds\n"
    "v_mfma_f32_16x16x32_bf16 v[212:215], a[68:71], a[196:199], v[212:215]\n"
    "buffer_load_dwordx4 v4, s[12:15], s45 offen offset:3072 lds\n"
    "v_mfma_f32_16x16x32_bf16 v[212:215], a[64:67], a[192:195], v[212:215]\n"
    "s_add_u32 s40, s40, 0x4000\n"
    "s_and_b32 s40, s40, 0x1ffff\n"
    "s_waitcnt vmcnt(16) lgkmcnt(0)\n"
    "s_barrier\n"
    "v_mfma_f32_16x16x32_bf16 v[160:163], a[0:3], a[128:131], v[160:163]\n"
    "ds_read_b64_tr_b16 a[64:65], v5 offset:49152\n"
    "ds_read_b64_tr_b16 a[66:67], v5 offset:51200\n"
    "v_readlane_b32 s38, v43, s52\n"
    "s_add_u32 s52, s52, 1\n"
    "s_add_u32 s48, s38, 0\n"
    "s_add_u32 s49, s38, 11264\n"
    "s_add_u32 s50, s38, 22528\n"
    "s_add_u32 s51, s38, 33792\n"
    "buffer_load_dwordx4 a[176:179], v15, s[24:27], s48 offen offset:0 sc1\n"
    "v_mfma_f32_16x16x32_bf16 v[160:163], a[4:7], a[132:135], v[160:163]\n"
    "ds_read_b64_tr_b16 a[68:69], v5 offset:53248\n"
    "buffer_load_dwordx4 a[180:183], v15, s[24:27], s49 offen offset:0 sc1\n"
    "v_mfma_f32_16x16x32_bf16 v[160:163], a[8:11], a[136:139], v[160:163]\n"
    "ds_read_b64_tr_b16 a[70:71], v5 offset:55296\n"
    "buffer_load_dwordx4 a[184:187], v15, s[24:27], s50 offen offset:0 sc1\n"
    "v_mfma_f32_16x16x32_bf16 v[160:163], a[12:15], a[140:143], v[160:163]\n"
    "ds_read_b64_tr_b16 a[72:73], v5 offset:57344\n"
    "v_mfma_f32_16x16x32_bf16 v[172:175], a[28:31], a[140:143], v[172:175]\n"
    "ds_read_b64_tr_b16 a[74:75], v5 offset:59392\n"
    "buffer_load_dwordx4 a[188:191], v15, s[24:27], s51 offen offset:0 sc1\n"
    "v_mfma_f32_16x16x32_bf16 v[172:175], a[24:27], a[136:139], v[172:175]\n"
    "ds_read_b64_tr_b16 a[76:77], v5 offset:61440\n"
    "buffer_load_dwordx4 a[192:195], v15, s[24:27], s48 offen offset:1024 sc1\n"
    "v_mfma_f32_16x16x32_bf16 v[172:175], a[20:23], a[132:135], v[172:175]\n"
    "ds_read_b64_tr_b16 a[78:79], v5 offset:63488\n"
    "v_xor_b32 v5, 0x10000, v5\n"
    "buffer_load_dwordx4 a[196:199], v15, s[24:27], s49 offen offset:1024 sc1\n"
    "v_mfma_f32_16x16x32_bf16 v[172:175], a[16:19], a[128:131], v[172:175]\n"
    "ds_read_b64_tr_b16 a[80:81], v6 offset:49152\n"
    "v_mfma_f32_16x16x32_bf16 v[184:187], a[32:35], a[128:131], v[184:187]\n"
    "ds_read_b64_tr_b16 a[82:83], v6 offset:51200\n"
    "buffer_load_dwordx4 a[200:203], v15, s[24:27], s50 offen offset:1024 sc1\n"
    "v_mfma_f32_16x16x32_bf16 v[184:187], a[36:39], a[132:135], v[184:187]\n"
    "ds_read_b64_tr_b16 a[84:85], v6 offset:53248\n"
    "ds_read_b64_tr_b16 a[86:87], v6 offset:55296\n"
    "buffer_load_dwordx4 a[204:207], v15, s[24:27], s51 offen offset:1024 sc1\n"
    "v_mfma_f32_16x16x32_bf16 v[184:187], a[40:43], a[136:139], v[184:187]\n"
    "ds_read_b64_tr_b16 a[88:89], v6 offset:57344\n"
    "v_readlane_b32 s39, v16, s43\n"
    "s_add_u32 s43, s43, 1\n"
    "s_add_u32 m0, s40, 0\n"
    "s_add_u32 s45, s39, 0\n"
    "buffer_load_dwordx4 v1, s[12:15], s45 offen lds\n"
    "v_mfma_f32_16x16x32_bf16 v[184:187], a[44:47], a[140:143], v[184:187]\n"
    "ds_read_b64_tr_b16 a[90:91], v6 offset:59392\n"
    "v_mfma_f32_16x16x32_bf16 v[196:199], a[60:63], a[140:143], v[196:199]\n"
    "ds_read_b64_tr_b16 a[92:93], v6 offset:61440\n"
    "buffer_load_dwordx4 v2, s[12:15], s45 offen offset:1024 lds\n"
    "v_mfma_f32_16x16x32_bf16 v[196:199], a[56:59], a[136:139], v[196:199]\n"
    "ds_read_b64_tr_b16 a[94:95], v6 offset:63488\n"
    "v_xor_b32 v6, 0x10000, v6\n"
    "buffer_load_dwordx4 v3, s[12:15], s45 offen offset:2048 lds\n"
    "v_mfma_f32_16x16x32_bf16 v[196:199], a[52:55], a[132:135], v[196:199]\n"
    "ds_read_b64_tr_b16 a[96:97], v7 offset:49152\n"
    "buffer_load_dwordx4 v4, s[12:15], s45 offen offset:3072 lds\n"
    "v_mfma_f32_16x16x32_bf16 v[196:199], a[48:51], a[128:131], v[196:199]\n"
    "s_waitcnt vmcnt(52)\n"
    "ds_read_b64_tr_b16 a[98:99], v7 offset:51200\n"
    "v_mfma_f32_16x16x32_bf16 v[200:203], a[48:51], a[144:147], v[200:203]\n"
    "ds_read_b64_tr_b16 a[100:101], v7 offset:53248\n"
    "s_add_u32 m0, s40, 4096\n"
    "s_add_u32 s45, s39, 65536\n"
    "buffer_load_dwordx4 v1, s[12:15], s45 offen lds\n"
    "v_mfma_f32_16x16x32_bf16 v[200:203], a[52:55], a[148:151], v[200:203]\n"
    "ds_read_b64_tr_b16 a[102:103], v7 offset:55296\n"
    "buffer_load_dwordx4 v2, s[12:15], s45 offen offset:1024 lds\n"
    "v_mfma_f32_16x16x32_bf16 v[200:203], a[56:59], a[152:155], v[200:203]\n"
    "ds_read_b64_tr_b16 a[104:105], v7 offset:57344\n"
    "buffer_load_dwordx4 v3, s[12:15], s45 offen offset:2048 lds\n"
    "v_mfma_f32_16x16x32_bf16 v[200:203], a[60:63], a[156:159], v[200:203]\n"
    "ds_read_b64_tr_b16 a[106:107], v7 offset:59392\n"
    "ds_read_b64_tr_b16 a[108:109], v7 offset:61440\n"
    "v_mfma_f32_16x16x32_bf16 v[188:191], a[44:47], a[156:159], v[188:191]\n"
    "ds_read_b64_tr_b16 a[110:111], v7 offset:63488\n"
    "v_xor_b32 v7, 0x10000, v7\n"
    "buffer_load_dwordx4 v4, s[12:15], s45 offen offset:3072 lds\n"
    "v_mfma_f32_16x16x32_bf16 v[188:191], a[40:43], a[152:155], v[188:191]\n"
    "ds_read_b64_tr_b16 a[112:113], v8 offset:49152\n"
    "s_add_u32 m0, s40, 8192\n"
    "s_add_u32 s45, s39, 131072\n"
    "buffer_load_dwordx4 v1, s[12:15], s45 offen lds\n"
    "v_mfma_f32_16x16x32_bf16 v[188:191], a[36:39], a[148:151], v[188:191]\n"
    "ds_read_b64_tr_b16 a[114:115], v8 offset:51200\n"
    "buffer_load_dwordx4 v2, s[12:15], s45 offen offset:1024 lds\n"
    "v_mfma_f32_16x16x32_bf16 v[188:191], a[32:35], a[144:147], v[188:191]\n"
    "ds_read_b64_tr_b16 a[116:117], v8 offset:53248\n"
    "v_mfma_f32_16x16x32_bf16 v[176:179], a[16:19], a[144:147], v[176:179]\n"
    "ds_read_b64_tr_b16 a[118:119], v8 offset:55296\n"
    "buffer_load_dwordx4 v3, s[12:15], s45 offen offset:2048 lds\n"
    "v_mfma_f32_16x16x32_bf16 v[176:179], a[20:23], a[148:151], v[176:179]\n"
    "ds_read_b64_tr_b16 a[120:121], v8 offset:57344\n"
    "buffer_load_dwordx4 v4, s[12:15], s45 offen offset:3072 lds\n"
    "v_mfma_f32_16x16x32_bf16 v[176:179], a[24:27], a[152:155], v[176:179]\n"
    "ds_read_b64_tr_b16 a[122:123], v8 offset:59392\n"
    "s_add_u32 m0, s40, 12288\n"
    "s_add_u32 s45, s39, 196608\n"
    "buffer_load_dwordx4 v1, s[12:15], s45 offen lds\n"
    "v_mfma_f32_16x16x32_bf16 v[176:179], a[28:31], a[156:159], v[176:179]\n"
    "ds_read_b64_tr_b16 a[124:125], v8 offset:61440\n"
    "v_mfma_f32_16x16x32_bf16 v[164:167], a[12:15], a[156:159], v[164:167]\n"
    "ds_read_b64_tr_b16 a[126:127], v8 offset:63488\n"
    "v_xor_b32 v8, 0x10000, v8\n"
    "buffer_load_dwordx4 v2, s[12:15], s45 offen offset:1024 lds\n"
    "v_mfma_f32_16x16x32_bf16 v[164:167], a[8:11], a[152:155], v[164:167]\n"
    "buffer_load_dwordx4 v3, s[12:15], s45 offen offset:2048 lds\n"
    "v_mfma_f32_16x16x32_bf16 v[164:167], a[4:7], a[148:151], v[164:167]\n"
    "buffer_load_dwordx4 v4, s[12:15], s45 offen offset:3072 lds\n"
    "v_mfma_f32_16x16x32_bf16 v[164:167], a[0:3], a[144:147], v[164:167]\n"
    "s_add_u32 s40, s40, 0x4000\n"
    "s_and_b32 s40, s40, 0x1ffff\n"
    "s_waitcnt vmcnt(44) lgkmcnt(0)\n"
    "v_mfma_f32_16x16x32_bf16 v[160:163], a[64:67], v[64:67], v[160:163]\n"
    "ds_read_b64_tr_b16 a[0:1], v5 offset:0\n"
    "ds_read_b64_tr_b16 a[2:3], v5 offset:2048\n"
    "s_waitcnt vmcnt(20)\n"
    "v_readlane_b32 s39, v16, s43\n"
    "s_add_u32 s43, s43, 1\n"
    "s_add_u32 m0, s40, 0\n"
    "s_add_u32 s45, s39, 0\n"
    "buffer_load_dwordx4 v1, s[12:15], s45 offen lds\n"
    "v_mfma_f32_16x16x32_bf16 v[160:163], a[68:71], v[68:71], v[160:163]\n"
    "ds_read_b64_tr_b16 a[4:5], v5 offset:4096\n"
    "buffer_load_dwordx4 v2, s[12:15], s45 offen offset:1024 lds\n"
    "v_mfma_f32_16x16x32_bf16 v[160:163], a[72:75], v[72:75], v[160:163]\n"
    "ds_read_b64_tr_b16 a[6:7], v5 offset:6144\n"
    "v_mfma_f32_16x16x32_bf16 v[160:163], a[76:79], v[76:79], v[160:163]\n"
    "ds_read_b64_tr_b16 a[8:9], v5 offset:8192\n"
    "buffer_load_dwordx4 v3, s[12:15], s45 offen offset:2048 lds\n"
    "v_mfma_f32_16x16x32_bf16 v[172:175], a[92:95], v[76:79], v[172:175]\n"
    "ds_read_b64_tr_b16 a[10:11], v5 offset:10240\n"
    "buffer_load_dwordx4 v4, s[12:15], s45 offen offset:3072 lds\n"
    "v_mfma_f32_16x16x32_bf16 v[172:175], a[88:91], v[72:75], v[172:175]\n"
    "ds_read_b64_tr_b16 a[12:13], v5 offset:12288\n"
    "v_mfma_f32_16x16x32_bf16 v[172:175], a[84:87], v[68:71], v[172:175]\n"
    "ds_read_b64_tr_b16 a[14:15], v5 offset:14336\n"
    "s_add_u32 m0, s40, 4096\n"
    "s_add_u32 s45, s39, 65536\n"
    "buffer_load_dwordx4 v1, s[12:15], s45 offen lds\n"
    "v_mfma_f32_16x16x32_bf16 v[172:175], a[80:83], v[64:67], v[172:175]\n"
    "ds_read_b64_tr_b16 a[16:17], v6 offset:0\n"
    "v_mfma_f32_16x16x32_bf16 v[184:187], a[96:99], v[64:67], v[184:187]\n"
    "ds_read_b64_tr_b16 a[18:19], v6 offset:2048\n"
    "buffer_load_dwordx4 v2, s[12:15], s45 offen offset:1024 lds\n"
    "v_mfma_f32_16x16x32_bf16 v[184:187], a[100:103], v[68:71], v[184:187]\n"
    "ds_read_b64_tr_b16 a[20:21], v6 offset:4096\n"
    "ds_read_b64_tr_b16 a[22:23], v6 offset:6144\n"
    "buffer_load_dwordx4 v3, s[12:15], s45 offen offset:2048 lds\n"
    "v_mfma_f32_16x16x32_bf16 v[184:187], a[104:107], v[72:75], v[184:187]\n"
    "ds_read_b64_tr_b16 a[24:25], v6 offset:8192\n"
    "v_mfma_f32_16x16x32_bf16 v[184:187], a[108:111], v[76:79], v[184:187]\n"
    "ds_read_b64_tr_b16 a[26:27], v6 offset:10240\n"
    "buffer_load_dwordx4 v4, s[12:15], s45 offen offset:3072 lds\n"
    "v_mfma_f32_16x16x32_bf16 v[196:199], a[124:127], v[76:79], v[196:199]\n"
    "ds_read_b64_tr_b16 a[28:29], v6 offset:12288\n"
    "s_add_u32 m0, s40, 8192\n"
    "s_add_u32 s45, s39, 131072\n"
    "buffer_load_dwordx4 v1, s[12:15], s45 offen lds\n"
    "v_mfma_f32_16x16x32_bf16 v[196:199], a[120:123], v[72:75], v[196:199]\n"
    "ds_read_b64_tr_b16 a[30:31], v6 offset:14336\n"
    "v_mfma_f32_16x16x32_bf16 v[196:199], a[116:119], v[68:71], v[196:199]\n"
    "ds_read_b64_tr_b16 a[32:33], v7 offset:0\n"
    "buffer_load_dwordx4 v2, s[12:15], s45 offen offset:1024 lds\n"
    "v_mfma_f32_16x16x32_bf16 v[196:199], a[112:115], v[64:67], v[196:199]\n"
    "s_waitcnt vmcnt(50)\n"
    "ds_read_b64_tr_b16 a[34:35], v7 offset:2048\n"
    "v_mfma_f32_16x16x32_bf16 v[200:203], a[112:115], v[80:83], v[200:203]\n"
    "ds_read_b64_tr_b16 a[36:37], v7 offset:4096\n"
    "buffer_load_dwordx4 v3, s[12:15], s45 offen offset:2048 lds\n"
    "v_mfma_f32_16x16x32_bf16 v[200:203], a[116:119], v[84:87], v[200:203]\n"
    "ds_read_b64_tr_b16 a[38:39], v7 offset:6144\n"
    "buffer_load_dwordx4 v4, s[12:15], s45 offen offset:3072 lds\n"
    "v_mfma_f32_16x16x32_bf16 v[200:203], a[120:123], v[88:91], v[200:203]\n"
    "ds_read_b64_tr_b16 a[40:41], v7 offset:8192\n"
    "v_mfma_f32_16x16x32_bf16 v[200:203], a[124:127], v[92:95], v[200:203]\n"
    "ds_read_b64_tr_b16 a[42:43], v7 offset:10240\n"
    "ds_read_b64_tr_b16 a[44:45], v7 offset:12288\n"
    "s_add_u32 m0, s40, 12288\n"
    "s_add_u32 s45, s39, 196608\n"
    "buffer_load_dwordx4 v1, s[12:15], s45 offen lds\n"
    "v_mfma_f32_16x16x32_bf16 v[188:191], a[108:111], v[92:95], v[188:191]\n"
    "ds_read_b64_tr_b16 a[46:47], v7 offset:14336\n"
    "buffer_load_dwordx4 v2, s[12:15], s45 offen offset:1024 lds\n"
    "v_mfma_f32_16x16x32_bf16 v[188:191], a[104:107], v[88:91], v[188:191]\n"
    "ds_read_b64_tr_b16 a[48:49], v8 offset:0\n"
    "v_mfma_f32_16x16x32_bf16 v[188:191], a[100:103], v[84:87], v[188:191]\n"
    "ds_read_b64_tr_b16 a[50:51], v8 offset:2048\n"
    "buffer_load_dwordx4 v3, s[12:15], s45 offen offset:2048 lds\n"
    "v_mfma_f32_16x16x32_bf16 v[188:191], a[96:99], v[80:83], v[188:191]\n"
    "ds_read_b64_tr_b16 a[52:53], v8 offset:4096\n"
    "v_mfma_f32_16x16x32_bf16 v[176:179], a[80:83], v[80:83], v[176:179]\n"
    "ds_read_b64_tr_b16 a[54:55], v8 offset:6144\n"
    "buffer_load_dwordx4 v4, s[12:15], s45 offen offset:3072 lds\n"
    "v_mfma_f32_16x16x32_bf16 v[176:179], a[84:87], v[84:87], v[176:179]\n"
    "ds_read_b64_tr_b16 a[56:57], v8 offset:8192\n"
    "s_add_u32 s40, s40, 0x4000\n"
    "s_and_b32 s40, s40, 0x1ffff\n"
    "v_readlane_b32 s38, v43, s52\n"
    "s_add_u32 s52, s52, 1\n"
    "s_add_u32 s48, s38, 0\n"
    "s_add_u32 s49, s38, 11264\n"
    "s_add_u32 s50, s38, 22528\n"
    "s_add_u32 s51, s38, 33792\n"
    "buffer_load_dwordx4 v[112:115], v15, s[24:27], s48 offen offset:0 sc1\n"
    "v_mfma_f32_16x16x32_bf16 v[176:179], a[88:91], v[88:91], v[176:179]\n"
    "ds_read_b64_tr_b16 a[58:59], v8 offset:10240\n"
    "v_mfma_f32_16x16x32_bf16 v[176:179], a[92:95], v[92:95], v[176:179]\n"
    "ds_read_b64_tr_b16 a[60:61], v8 offset:12288\n"
    "buffer_load_dwordx4 v[116:119], v15, s[24:27], s49 offen offset:0 sc1\n"
    "v_mfma_f32_16x16x32_bf16 v[164:167], a[76:79], v[92:95], v[164:167]\n"
    "ds_read_b64_tr_b16 a[62:63], v8 offset:14336\n"
    "buffer_load_dwordx4 v[120:123], v15, s[24:27], s50 offen offset:0 sc1\n"
    "v_mfma_f32_16x16x32_bf16 v[164:167], a[72:75], v[88:91], v[164:167]\n"
    "v_mfma_f32_16x16x32_bf16 v[164:167], a[68:71], v[84:87], v[164:167]\n"
    "buffer_load_dwordx4 v[124:127], v15, s[24:27], s51 offen offset:0 sc1\n"
    "v_mfma_f32_16x16x32_bf16 v[164:167], a[64:67], v[80:83], v[164:167]\n"
    "s_waitcnt vmcnt(16) lgkmcnt(0)\n"
    "s_barrier\n"
    "v_mfma_f32_16x16x32_bf16 v[160:163], a[0:3], a[176:179], v[160:163]\n"
    "ds_read_b64_tr_b16 a[64:65], v5 offset:16384\n"
    "ds_read_b64_tr_b16 a[66:67], v5 offset:18432\n"
    "v_readlane_b32 s39, v16, s43\n"
    "s_add_u32 s43, s43, 1\n"
    "s_add_u32 m0, s40, 0\n"
    "s_add_u32 s45, s39, 0\n"
    "buffer_load_dwordx4 v1, s[12:15], s45 offen lds\n"
    "v_mfma_f32_16x16x32_bf16 v[160:163], a[4:7], a[180:183], v[160:163]\n"
    "ds_read_b64_tr_b16 a[68:69], v5 offset:20480\n"
    "buffer_load_dwordx4 v2, s[12:15], s45 offen offset:1024 lds\n"
    "v_mfma_f32_16x16x32_bf16 v[160:163], a[8:11], a[184:187], v[160:163]\n"
    "ds_read_b64_tr_b16 a[70:71], v5 offset:22528\n"
    "v_mfma_f32_16x16x32_bf16 v[160:163], a[12:15], a[188:191], v[160:163]\n"
    "ds_read_b64_tr_b16 a[72:73], v5 offset:24576\n"
    "buffer_load_dwordx4 v3, s[12:15], s45 offen offset:2048 lds\n"
    "v_mfma_f32_16x16x32_bf16 v[172:175], a[28:31], a[188:191], v[172:175]\n"
    "ds_read_b64_tr_b16 a[74:75], v5 offset:26624\n"
    "buffer_load_dwordx4 v4, s[12:15], s45 offen offset:3072 lds\n"
    "v_mfma_f32_16x16x32_bf16 v[172:175], a[24:27], a[184:187], v[172:175]\n"
    "ds_read_b64_tr_b16 a[76:77], v5 offset:28672\n"
    "v_mfma_f32_16x16x32_bf16 v[172:175], a[20:23], a[180:183], v[172:175]\n"
    "ds_read_b64_tr_b16 a[78:79], v5 offset:30720\n"
    "s_add_u32 m0, s40, 4096\n"
    "s_add_u32 s45, s39, 65536\n"
    "buffer_load_dwordx4 v1, s[12:15], s45 offen lds\n"
    "v_mfma_f32_16x16x32_bf16 v[172:175], a[16:19], a[176:179], v[172:175]\n"
    "ds_read_b64_tr_b16 a[80:81], v6 offset:16384\n"
    "v_mfma_f32_16x16x32_bf16 v[184:187], a[32:35], a[176:179], v[184:187]\n"
    "ds_read_b64_tr_b16 a[82:83], v6 offset:18432\n"
    "buffer_load_dwordx4 v2, s[12:15], s45 offen offset:1024 lds\n"
    "v_mfma_f32_16x16x32_bf16 v[184:187], a[36:39], a[180:183], v[184:187]\n"
    "ds_read_b64_tr_b16 a[84:85], v6 offset:20480\n"
    "ds_read_b64_tr_b16 a[86:87], v6 offset:22528\n"
    "buffer_load_dwordx4 v3, s[12:15], s45 offen offset:2048 lds\n"
    "v_mfma_f32_16x16x32_bf16 v[184:187], a[40:43], a[184:187], v[184:187]\n"
    "ds_read_b64_tr_b16 a[88:89], v6 offset:24576\n"
    "v_mfma_f32_16x16x32_bf16 v[184:187], a[44:47], a[188:191], v[184:187]\n"
    "ds_read_b64_tr_b16 a[90:91], v6 offset:26624\n"
    "buffer_load_dwordx4 v4, s[12:15], s45 offen offset:3072 lds\n"
    "v_mfma_f32_16x16x32_bf16 v[196:199], a[60:63], a[188:191], v[196:199]\n"
    "ds_read_b64_tr_b16 a[92:93], v6 offset:28672\n"
    "s_add_u32 m0, s40, 8192\n"
    "s_add_u32 s45, s39, 131072\n"
    "buffer_load_dwordx4 v1, s[12:15], s45 offen lds\n"
    "v_mfma_f32_16x16x32_bf16 v[196:199], a[56:59], a[184:187], v[196:199]\n"
    "ds_read_b64_tr_b16 a[94:95], v6 offset:30720\n"
    "v_mfma_f32_16x16x32_bf16 v[196:199], a[52:55], a[180:183], v[196:199]\n"
    "ds_read_b64_tr_b16 a[96:97], v7 offset:16384\n"
    "buffer_load_dwordx4 v2, s[12:15], s45 offen offset:1024 lds\n"
    "v_mfma_f32_16x16x32_bf16 v[196:199], a[48:51], a[176:179], v[196:199]\n"
    "s_waitcnt vmcnt(46)\n"
    "ds_read_b64_tr_b16 a[98:99], v7 offset:18432\n"
    "v_mfma_f32_16x16x32_bf16 v[200:203], a[48:51], a[192:195], v[200:203]\n"
    "ds_read_b64_tr_b16 a[100:101], v7 offset:20480\n"
    "buffer_load_dwordx4 v3, s[12:15], s45 offen offset:2048 lds\n"
    "v_mfma_f32_16x16x32_bf16 v[200:203], a[52:55], a[196:199], v[200:203]\n"
    "ds_read_b64_tr_b16 a[102:103], v7 offset:22528\n"
    "buffer_load_dwordx4 v4, s[12:15], s45 offen offset:3072 lds\n"
    "v_mfma_f32_16x16x32_bf16 v[200:203], a[56:59], a[200:203], v[200:203]\n"
    "ds_read_b64_tr_b16 a[104:105], v7 offset:24576\n"
    "v_mfma_f32_16x16x32_bf16 v[200:203], a[60:63], a[204:207], v[200:203]\n"
    "ds_read_b64_tr_b16 a[106:107], v7 offset:26624\n"
    "ds_read_b64_tr_b16 a[108:109], v7 offset:28672\n"
    "s_add_u32 m0, s40, 12288\n"
    "s_add_u32 s45, s39, 196608\n"
    "buffer_load_dwordx4 v1, s[12:15], s45 offen lds\n"
    "v_mfma_f32_16x16x32_bf16 v[188:191], a[44:47], a[204:207], v[188:191]\n"
    "ds_read_b64_tr_b16 a[110:111], v7 offset:30720\n"
    "buffer_load_dwordx4 v2, s[12:15], s45 offen offset:1024 lds\n"
    "v_mfma_f32_16x16x32_bf16 v[188:191], a[40:43], a[200:203], v[188:191]\n"
    "ds_read_b64_tr_b16 a[112:113], v8 offset:16384\n"
    "v_mfma_f32_16x16x32_bf16 v[188:191], a[36:39], a[196:199], v[188:191]\n"
    "ds_read_b64_tr_b16 a[114:115], v8 offset:18432\n"
    "buffer_load_dwordx4 v3, s[12:15], s45 offen offset:2048 lds\n"
    "v_mfma_f32_16x16x32_bf16 v[188:191], a[32:35], a[192:195], v[188:191]\n"
    "ds_read_b64_tr_b16 a[116:117], v8 offset:20480\n"
    "v_mfma_f32_16x16x32_bf16 v[176:179], a[16:19], a[192:195], v[176:179]\n"
    "ds_read_b64_tr_b16 a[118:119], v8 offset:22528\n"
    "buffer_load_dwordx4 v4, s[12:15], s45 offen offset:3072 lds\n"
    "v_mfma_f32_16x16x32_bf16 v[176:179], a[20:23], a[196:199], v[176:179]\n"
    "ds_read_b64_tr_b16 a[120:121], v8 offset:24576\n"
    "s_add_u32 s40, s40, 0x4000\n"
    "s_and_b32 s40, s40, 0x1ffff\n"
    "buffer_load_dwordx4 v[128:131], v15, s[24:27], s48 offen offset:1024 sc1\n"
    "v_mfma_f32_16x16x32_bf16 v[176:179], a[24:27], a[200:203], v[176:179]\n"
    "ds_read_b64_tr_b16 a[122:123], v8 offset:26624\n"
    "v_mfma_f32_16x16x32_bf16 v[176:179], a[28:31], a[204:207], v[176:179]\n"
    "ds_read_b64_tr_b16 a[124:125], v8 offset:28672\n"
    "buffer_load_dwordx4 v[132:135], v15, s[24:27], s49 offen offset:1024 sc1\n"
    "v_mfma_f32_16x16x32_bf16 v[164:167], a[12:15], a[204:207], v[164:167]\n"
    "ds_read_b64_tr_b16 a[126:127], v8 offset:30720\n"
    "buffer_load_dwordx4 v[136:139], v15, s[24:27], s50 offen offset:1024 sc1\n"
    "v_mfma_f32_16x16x32_bf16 v[164:167], a[8:11], a[200:203], v[164:167]\n"
    "v_mfma_f32_16x16x32_bf16 v[164:167], a[4:7], a[196:199], v[164:167]\n"
    "buffer_load_dwordx4 v[140:143], v15, s[24:27], s51 offen offset:1024 sc1\n"
    "v_mfma_f32_16x16x32_bf16 v[164:167], a[0:3], a[192:195], v[164:167]\n"
    "s_waitcnt lgkmcnt(0)\n"
    "v_mfma_f32_16x16x32_bf16 v[208:211], a[64:67], a[128:131], v[208:211]\n"
    "ds_read_b64_tr_b16 a[0:1], v5 offset:32768\n"
    "ds_read_b64_tr_b16 a[2:3], v5 offset:34816\n"
    "s_waitcnt vmcnt(20)\n"
    "v_readlane_b32 s39, v16, s43\n"
    "s_add_u32 s43, s43, 1\n"
    "s_add_u32 m0, s40, 0\n"
    "s_add_u32 s45, s39, 0\n"
    "buffer_load_dwordx4 v1, s[12:15], s45 offen lds\n"
    "v_mfma_f32_16x16x32_bf16 v[208:211], a[68:71], a[132:135], v[208:211]\n"
    "ds_read_b64_tr_b16 a[4:5], v5 offset:36864\n"
    "v_mfma_f32_16x16x32_bf16 v[208:211], a[72:75], a[136:139], v[208:211]\n"
    "ds_read_b64_tr_b16 a[6:7], v5 offset:38912\n"
    "buffer_load_dwordx4 v2, s[12:15], s45 offen offset:1024 lds\n"
    "v_mfma_f32_16x16x32_bf16 v[208:211], a[76:79], a[140:143], v[208:211]\n"
    "ds_read_b64_tr_b16 a[8:9], v5 offset:40960\n"
    "v_mfma_f32_16x16x32_bf16 v[220:223], a[92:95], a[140:143], v[220:223]\n"
    "ds_read_b64_tr_b16 a[10:11], v5 offset:43008\n"
    "buffer_load_dwordx4 v3, s[12:15], s45 offen offset:2048 lds\n"
    "v_cvt_pk_bf16_f32 v160, v160, v161\n"
    "v_cvt_pk_bf16_f32 v161, v162, v163\n"
    "v_cvt_pk_bf16_f32 v162, v172, v173\n"
    "v_cvt_pk_bf16_f32 v163, v174, v175\n"
    "v_cvt_pk_bf16_f32 v184, v184, v185\n"
    "v_cvt_pk_bf16_f32 v185, v186, v187\n"
    "v_cvt_pk_bf16_f32 v186, v196, v197\n"
    "v_cvt_pk_bf16_f32 v187, v198, v199\n"
    "s_add_u32 s46, s55, 0\n"
    "buffer_store_dwordx4 v[160:163], v37, s[24:27], s46 offen offset:0 sc1\n"
    "v_mfma_f32_16x16x32_bf16 v[220:223], a[88:91], a[136:139], v[220:223]\n"
    "ds_read_b64_tr_b16 a[12:13], v5 offset:45056\n"
    "buffer_store_dwordx4 v[184:187], v37, s[24:27], s46 offen offset:1024 sc1\n"
    "v_mfma_f32_16x16x32_bf16 v[220:223], a[84:87], a[132:135], v[220:223]\n"
    "ds_read_b64_tr_b16 a[14:15], v5 offset:47104\n"
    "buffer_load_dwordx4 v4, s[12:15], s45 offen offset:3072 lds\n"
    "v_cvt_pk_bf16_f32 v164, v164, v165\n"
    "v_cvt_pk_bf16_f32 v165, v166, v167\n"
    "v_cvt_pk_bf16_f32 v166, v176, v177\n"
    "v_cvt_pk_bf16_f32 v167, v178, v179\n"
    "v_cvt_pk_bf16_f32 v188, v188, v189\n"
    "v_cvt_pk_bf16_f32 v189, v190, v191\n"
    "v_cvt_pk_bf16_f32 v190, v200, v201\n"
    "v_cvt_pk_bf16_f32 v191, v202, v203\n"
    "s_add_u32 s46, s55, 2048\n"
    "buffer_store_dwordx4 v[164:167], v37, s[24:27], s46 offen offset:0 sc1\n"
    "v_mfma_f32_16x16x32_bf16 v[220:223], a[80:83], a[128:131], v[220:223]\n"
    "ds_read_b64_tr_b16 a[16:17], v6 offset:32768\n"
    "buffer_store_dwordx4 v[188:191], v37, s[24:27], s46 offen offset:1024 sc1\n"
    "v_mfma_f32_16x16x32_bf16 v[232:235], a[96:99], a[128:131], v[232:235]\n"
    "ds_read_b64_tr_b16 a[18:19], v6 offset:34816\n"
    "s_add_u32 m0, s40, 4096\n"
    "s_add_u32 s45, s39, 65536\n"
    "buffer_load_dwordx4 v1, s[12:15], s45 offen lds\n"
    "v_mfma_f32_16x16x32_bf16 v[232:235], a[100:103], a[132:135], v[232:235]\n"
    "ds_read_b64_tr_b16 a[20:21], v6 offset:36864\n"
    "ds_read_b64_tr_b16 a[22:23], v6 offset:38912\n"
    "v_mfma_f32_16x16x32_bf16 v[232:235], a[104:107], a[136:139], v[232:235]\n"
    "ds_read_b64_tr_b16 a[24:25], v6 offset:40960\n"
    "buffer_load_dwordx4 v2, s[12:15], s45 offen offset:1024 lds\n"
    "v_mfma_f32_16x16x32_bf16 v[232:235], a[108:111], a[140:143], v[232:235]\n"
    "ds_read_b64_tr_b16 a[26:27], v6 offset:43008\n"
    "v_mfma_f32_16x16x32_bf16 v[244:247], a[124:127], a[140:143], v[244:247]\n"
    "ds_read_b64_tr_b16 a[28:29], v6 offset:45056\n"
    "buffer_load_dwordx4 v3, s[12:15], s45 offen offset:2048 lds\n"
    "v_mfma_f32_16x16x32_bf16 v[244:247], a[120:123], a[136:139], v[244:247]\n"
    "ds_read_b64_tr_b16 a[30:31], v6 offset:47104\n"
    "v_mfma_f32_16x16x32_bf16 v[244:247], a[116:119], a[132:135], v[244:247]\n"
    "ds_read_b64_tr_b16 a[32:33], v7 offset:32768\n"
    "buffer_load_dwordx4 v4, s[12:15], s45 offen offset:3072 lds\n"
    "v_mfma_f32_16x16x32_bf16 v[244:247], a[112:115], a[128:131], v[244:247]\n"
    "ds_read_b64_tr_b16 a[34:35], v7 offset:34816\n"
    "v_mfma_f32_16x16x32_bf16 v[248:251], a[112:115], a[144:147], v[248:251]\n"
    "ds_read_b64_tr_b16 a[36:37], v7 offset:36864\n"
    "s_add_u32 m0, s40, 8192\n"
    "s_add_u32 s45, s39, 131072\n"
    "buffer_load_dwordx4 v1, s[12:15], s45 offen lds\n"
    "v_mfma_f32_16x16x32_bf16 v[248:251], a[116:119], a[148:151], v[248:251]\n"
    "ds_read_b64_tr_b16 a[38:39], v7 offset:38912\n"
    "v_mfma_f32_16x16x32_bf16 v[248:251], a[120:123], a[152:155], v[248:251]\n"
    "ds_read_b64_tr_b16 a[40:41], v7 offset:40960\n"
    "buffer_load_dwordx4 v2, s[12:15], s45 offen offset:1024 lds\n"
    "v_mfma_f32_16x16x32_bf16 v[248:251], a[124:127], a[156:159], v[248:251]\n"
    "ds_read_b64_tr_b16 a[42:43], v7 offset:43008\n"
    "ds_read_b64_tr_b16 a[44:45], v7 offset:45056\n"
    "v_mfma_f32_16x16x32_bf16 v[236:239], a[108:111], a[156:159], v[236:239]\n"
    "ds_read_b64_tr_b16 a[46:47], v7 offset:47104\n"
    "buffer_load_dwordx4 v3, s[12:15], s45 offen offset:2048 lds\n"
    "v_mfma_f32_16x16x32_bf16 v[236:239], a[104:107], a[152:155], v[236:239]\n"
    "ds_read_b64_tr_b16 a[48:49], v8 offset:32768\n"
    "v_mfma_f32_16x16x32_bf16 v[236:239], a[100:103], a[148:151], v[236:239]\n"
    "ds_read_b64_tr_b16 a[50:51], v8 offset:34816\n"
    "buffer_load_dwordx4 v4, s[12:15], s45 offen offset:3072 lds\n"
    "v_mfma_f32_16x16x32_bf16 v[236:239], a[96:99], a[144:147], v[236:239]\n"
    "ds_read_b64_tr_b16 a[52:53], v8 offset:36864\n"
    "v_mfma_f32_16x16x32_bf16 v[224:227], a[80:83], a[144:147], v[224:227]\n"
    "ds_read_b64_tr_b16 a[54:55], v8 offset:38912\n"
    "s_add_u32 m0, s40, 12288\n"
    "s_add_u32 s45, s39, 196608\n"
    "buffer_load_dwordx4 v1, s[12:15], s45 offen lds\n"
    "v_mfma_f32_16x16x32_bf16 v[224:227], a[84:87], a[148:151], v[224:227]\n"
    "ds_read_b64_tr_b16 a[56:57], v8 offset:40960\n"
    "v_mfma_f32_16x16x32_bf16 v[224:227], a[88:91], a[152:155], v[224:227]\n"
    "ds_read_b64_tr_b16 a[58:59], v8 offset:43008\n"
    "buffer_load_dwordx4 v2, s[12:15], s45 offen offset:1024 lds\n"
    "v_mfma_f32_16x16x32_bf16 v[224:227], a[92:95], a[156:159], v[224:227]\n"
    "ds_read_b64_tr_b16 a[60:61], v8 offset:45056\n"
    "v_mfma_f32_16x16x32_bf16 v[212:215], a[76:79], a[156:159], v[212:215]\n"
    "ds_read_b64_tr_b16 a[62:63], v8 offset:47104\n"
    "buffer_load_dwordx4 v3, s[12:15], s45 offen offset:2048 lds\n"
    "v_mfma_f32_16x16x32_bf16 v[212:215], a[72:75], a[152:155], v[212:215]\n"
    "v_mfma_f32_16x16x32_bf16 v[212:215], a[68:71], a[148:151], v[212:215]\n"
    "buffer_load_dwordx4 v4, s[12:15], s45 offen offset:3072 lds\n"
    "v_mfma_f32_16x16x32_bf16 v[212:215], a[64:67], a[144:147], v[212:215]\n"
    "s_add_u32 s40, s40, 0x4000\n"
    "s_and_b32 s40, s40, 0x1ffff\n"
    "s_waitcnt vmcnt(16) lgkmcnt(0)\n"
    "s_barrier\n"
    "v_mfma_f32_16x16x32_bf16 v[208:211], a[0:3], v[64:67], v[208:211]\n"
    "ds_read_b64_tr_b16 a[64:65], v5 offset:49152\n"
    "ds_read_b64_tr_b16 a[66:67], v5 offset:51200\n"
    "v_readlane_b32 s39, v16, s43\n"
    "s_add_u32 s43, s43, 1\n"
    "s_add_u32 m0, s40, 0\n"
    "s_add_u32 s45, s39, 0\n"
    "buffer_load_dwordx4 v1, s[12:15], s45 offen lds\n"
    "v_mfma_f32_16x16x32_bf16 v[208:211], a[4:7], v[68:71], v[208:211]\n"
    "ds_read_b64_tr_b16 a[68:69], v5 offset:53248\n"
    "v_mfma_f32_16x16x32_bf16 v[208:211], a[8:11], v[72:75], v[208:211]\n"
    "ds_read_b64_tr_b16 a[70:71], v5 offset:55296\n"
    "buffer_load_dwordx4 v2, s[12:15], s45 offen offset:1024 lds\n"
    "v_mfma_f32_16x16x32_bf16 v[208:211], a[12:15], v[76:79], v[208:211]\n"
    "ds_read_b64_tr_b16 a[72:73], v5 offset:57344\n"
    "v_mfma_f32_16x16x32_bf16 v[220:223], a[28:31], v[76:79], v[220:223]\n"
    "ds_read_b64_tr_b16 a[74:75], v5 offset:59392\n"
    "buffer_load_dwordx4 v3, s[12:15], s45 offen offset:2048 lds\n"
    "v_mfma_f32_16x16x32_bf16 v[220:223], a[24:27], v[72:75], v[220:223]\n"
    "ds_read_b64_tr_b16 a[76:77], v5 offset:61440\n"
    "v_mfma_f32_16x16x32_bf16 v[220:223], a[20:23], v[68:71], v[220:223]\n"
    "ds_read_b64_tr_b16 a[78:79], v5 offset:63488\n"
    "v_xor_b32 v5, 0x10000, v5\n"
    "buffer_load_dwordx4 v4, s[12:15], s45 offen offset:3072 lds\n"
    "v_mfma_f32_16x16x32_bf16 v[220:223], a[16:19], v[64:67], v[220:223]\n"
    "ds_read_b64_tr_b16 a[80:81], v6 offset:49152\n"
    "v_mfma_f32_16x16x32_bf16 v[232:235], a[32:35], v[64:67], v[232:235]\n"
    "ds_read_b64_tr_b16 a[82:83], v6 offset:51200\n"
    "s_add_u32 m0, s40, 4096\n"
    "s_add_u32 s45, s39, 65536\n"
    "buffer_load_dwordx4 v1, s[12:15], s45 offen lds\n"
    "v_mfma_f32_16x16x32_bf16 v[232:235], a[36:39], v[68:71], v[232:235]\n"
    "ds_read_b64_tr_b16 a[84:85], v6 offset:53248\n"
    "ds_read_b64_tr_b16 a[86:87], v6 offset:55296\n"
    "v_mfma_f32_16x16x32_bf16 v[232:235], a[40:43], v[72:75], v[232:235]\n"
    "ds_read_b64_tr_b16 a[88:89], v6 offset:57344\n"
    "buffer_load_dwordx4 v2, s[12:15], s45 offen offset:1024 lds\n"
    "v_mfma_f32_16x16x32_bf16 v[232:235], a[44:47], v[76:79], v[232:235]\n"
    "ds_read_b64_tr_b16 a[90:91], v6 offset:59392\n"
    "v_mfma_f32_16x16x32_bf16 v[244:247], a[60:63], v[76:79], v[244:247]\n"
    "ds_read_b64_tr_b16 a[92:93], v6 offset:61440\n"
    "buffer_load_dwordx4 v3, s[12:15], s45 offen offset:2048 lds\n"
    "v_mfma_f32_16x16x32_bf16 v[244:247], a[56:59], v[72:75], v[244:247]\n"
    "ds_read_b64_tr_b16 a[94:95], v6 offset:63488\n"
    "v_xor_b32 v6, 0x10000, v6\n"
    "v_mfma_f32_16x16x32_bf16 v[244:247], a[52:55], v[68:71], v[244:247]\n"
    "ds_read_b64_tr_b16 a[96:97], v7 offset:49152\n"
    "buffer_load_dwordx4 v4, s[12:15], s45 offen offset:3072 lds\n"
    "v_mfma_f32_16x16x32_bf16 v[244:247], a[48:51], v[64:67], v[244:247]\n"
    "ds_read_b64_tr_b16 a[98:99], v7 offset:51200\n"
    "v_mfma_f32_16x16x32_bf16 v[248:251], a[48:51], v[80:83], v[248:251]\n"
    "ds_read_b64_tr_b16 a[100:101], v7 offset:53248\n"
    "s_add_u32 m0, s40, 8192\n"
    "s_add_u32 s45, s39, 131072\n"
    "buffer_load_dwordx4 v1, s[12:15], s45 offen lds\n"
    "v_mfma_f32_16x16x32_bf16 v[248:251], a[52:55], v[84:87], v[248:251]\n"
    "ds_read_b64_tr_b16 a[102:103], v7 offset:55296\n"
    "v_mfma_f32_16x16x32_bf16 v[248:251], a[56:59], v[88:91], v[248:251]\n"
    "ds_read_b64_tr_b16 a[104:105], v7 offset:57344\n"
    "buffer_load_dwordx4 v2, s[12:15], s45 offen offset:1024 lds\n"
    "v_mfma_f32_16x16x32_bf16 v[248:251], a[60:63], v[92:95], v[248:251]\n"
    "ds_read_b64_tr_b16 a[106:107], v7 offset:59392\n"
    "ds_read_b64_tr_b16 a[108:109], v7 offset:61440\n"
    "v_mfma_f32_16x16x32_bf16 v[236:239], a[44:47], v[92:95], v[236:239]\n"
    "ds_read_b64_tr_b16 a[110:111], v7 offset:63488\n"
    "v_xor_b32 v7, 0x10000, v7\n"
    "buffer_load_dwordx4 v3, s[12:15], s45 offen offset:2048 lds\n"
    "v_mfma_f32_16x16x32_bf16 v[236:239], a[40:43], v[88:91], v[236:239]\n"
    "ds_read_b64_tr_b16 a[112:113], v8 offset:49152\n"
    "v_mfma_f32_16x16x32_bf16 v[236:239], a[36:39], v[84:87], v[236:239]\n"
    "ds_read_b64_tr_b16 a[114:115], v8 offset:51200\n"
    "buffer_load_dwordx4 v4, s[12:15], s45 offen offset:3072 lds\n"
    "v_mfma_f32_16x16x32_bf16 v[236:239], a[32:35], v[80:83], v[236:239]\n"
    "ds_read_b64_tr_b16 a[116:117], v8 offset:53248\n"
    "v_mfma_f32_16x16x32_bf16 v[224:227], a[16:19], v[80:83], v[224:227]\n"
    "ds_read_b64_tr_b16 a[118:119], v8 offset:55296\n"
    "s_add_u32 m0, s40, 12288\n"
    "s_add_u32 s45, s39, 196608\n"
    "buffer_load_dwordx4 v1, s[12:15], s45 offen lds\n"
    "v_mfma_f32_16x16x32_bf16 v[224:227], a[20:23], v[84:87], v[224:227]\n"
    "ds_read_b64_tr_b16 a[120:121], v8 offset:57344\n"
    "v_mfma_f32_16x16x32_bf16 v[224:227], a[24:27], v[88:91], v[224:227]\n"
    "ds_read_b64_tr_b16 a[122:123], v8 offset:59392\n"
    "buffer_load_dwordx4 v2, s[12:15], s45 offen offset:1024 lds\n"
    "v_mfma_f32_16x16x32_bf16 v[224:227], a[28:31], v[92:95], v[224:227]\n"
    "ds_read_b64_tr_b16 a[124:125], v8 offset:61440\n"
    "v_mfma_f32_16x16x32_bf16 v[212:215], a[12:15], v[92:95], v[212:215]\n"
    "ds_read_b64_tr_b16 a[126:127], v8 offset:63488\n"
    "v_xor_b32 v8, 0x10000, v8\n"
    "buffer_load_dwordx4 v3, s[12:15], s45 offen offset:2048 lds\n"
    "v_mfma_f32_16x16x32_bf16 v[212:215], a[8:11], v[88:91], v[212:215]\n"
    "v_mfma_f32_16x16x32_bf16 v[212:215], a[4:7], v[84:87], v[212:215]\n"
    "buffer_load_dwordx4 v4, s[12:15], s45 offen offset:3072 lds\n"
    "v_mfma_f32_16x16x32_bf16 v[212:215], a[0:3], v[80:83], v[212:215]\n"
    "s_add_u32 s40, s40, 0x4000\n"
    "s_and_b32 s40, s40, 0x1ffff\n"
    "s_waitcnt lgkmcnt(0)\n"
    "s_waitcnt vmcnt(28)\n"
    "s_xor_b32 s57, s56, 32\n"
    "v_mov_b64 v[62:63], s[62:63]\n"
    "s_mov_b64 exec, 1\n"
    "buffer_store_dwordx2 v[62:63], off, s[32:35], s57 sc1\n"
    "s_mov_b64 exec, -1\n"
    "s_and_b32 s46, %5, 1\n"
    "s_mul_i32 s46, s46, 90112\n"
    "s_add_u32 s55, s55, 45056\n"
    "s_sub_u32 s55, s55, s46\n"
    "v_mfma_f32_16x16x32_bf16 v[208:211], a[64:67], a[176:179], v[208:211]\n"
    "ds_read_b64_tr_b16 a[0:1], v5 offset:0\n"
    "ds_read_b64_tr_b16 a[2:3], v5 offset:2048\n"
    "v_readlane_b32 s39, v16, s43\n"
    "s_add_u32 s43, s43, 1\n"
    "s_add_u32 m0, s40, 0\n"
    "s_add_u32 s45, s39, 0\n"
    "buffer_load_dwordx4 v1, s[12:15], s45 offen lds\n"
    "v_mfma_f32_16x16x32_bf16 v[208:211], a[68:71], a[180:183], v[208:211]\n"
    "ds_read_b64_tr_b16 a[4:5], v5 offset:4096\n"
    "buffer_load_dwordx4 v2, s[12:15], s45 offen offset:1024 lds\n"
    "v_mfma_f32_16x16x32_bf16 v[208:211], a[72:75], a[184:187], v[208:211]\n"
    "ds_read_b64_tr_b16 a[6:7], v5 offset:6144\n"
    "buffer_load_dwordx4 v3, s[12:15], s45 offen offset:2048 lds\n"
    "v_mfma_f32_16x16x32_bf16 v[208:211], a[76:79], a[188:191], v[208:211]\n"
    "ds_read_b64_tr_b16 a[8:9], v5 offset:8192\n"
    "v_mfma_f32_16x16x32_bf16 v[220:223], a[92:95], a[188:191], v[220:223]\n"
    "ds_read_b64_tr_b16 a[10:11], v5 offset:10240\n"
    "buffer_load_dwordx4 v4, s[12:15], s45 offen offset:3072 lds\n"
    "v_mfma_f32_16x16x32_bf16 v[220:223], a[88:91], a[184:187], v[220:223]\n"
    "ds_read_b64_tr_b16 a[12:13], v5 offset:12288\n"
    "s_add_u32 m0, s40, 4096\n"
    "s_add_u32 s45, s39, 65536\n"
    "buffer_load_dwordx4 v1, s[12:15], s45 offen lds\n"
    "v_mfma_f32_16x16x32_bf16 v[220:223], a[84:87], a[180:183], v[220:223]\n"
    "ds_read_b64_tr_b16 a[14:15], v5 offset:14336\n"
    "buffer_load_dwordx4 v2, s[12:15], s45 offen offset:1024 lds\n"
    "v_mfma_f32_16x16x32_bf16 v[220:223], a[80:83], a[176:179], v[220:223]\n"
    "ds_read_b64_tr_b16 a[16:17], v6 offset:0\n"
    "v_mfma_f32_16x16x32_bf16 v[232:235], a[96:99], a[176:179], v[232:235]\n"
    "ds_read_b64_tr_b16 a[18:19], v6 offset:2048\n"
    "buffer_load_dwordx4 v3, s[12:15], s45 offen offset:2048 lds\n"
    "v_mfma_f32_16x16x32_bf16 v[232:235], a[100:103], a[180:183], v[232:235]\n"
    "ds_read_b64_tr_b16 a[20:21], v6 offset:4096\n"
    "ds_read_b64_tr_b16 a[22:23], v6 offset:6144\n"
    "buffer_load_dwordx4 v4, s[12:15], s45 offen offset:3072 lds\n"
    "v_mfma_f32_16x16x32_bf16 v[232:235], a[104:107], a[184:187], v[232:235]\n"
    "ds_read_b64_tr_b16 a[24:25], v6 offset:8192\n"
    "s_add_u32 m0, s40, 8192\n"
    "s_add_u32 s45, s39, 131072\n"
    "buffer_load_dwordx4 v1, s[12:15], s45 offen lds\n"
    "v_mfma_f32_16x16x32_bf16 v[232:235], a[108:111], a[188:191], v[232:235]\n"
    "ds_read_b64_tr_b16 a[26:27], v6 offset:10240\n"
    "v_mfma_f32_16x16x32_bf16 v[244:247], a[124:127], a[188:191], v[244:247]\n"
    "ds_read_b64_tr_b16 a[28:29], v6 offset:12288\n"
    "buffer_load_dwordx4 v2, s[12:15], s45 offen offset:1024 lds\n"
    "v_mfma_f32_16x16x32_bf16 v[244:247], a[120:123], a[184:187], v[244:247]\n"
    "ds_read_b64_tr_b16 a[30:31], v6 offset:14336\n"
    "buffer_load_dwordx4 v3, s[12:15], s45 offen offset:2048 lds\n"
    "v_mfma_f32_16x16x32_bf16 v[244:247], a[116:119], a[180:183], v[244:247]\n"
    "ds_read_b64_tr_b16 a[32:33], v7 offset:0\n"
    "buffer_load_dwordx4 v4, s[12:15], s45 offen offset:3072 lds\n"
    "v_mfma_f32_16x16x32_bf16 v[244:247], a[112:115], a[176:179], v[244:247]\n"
    "ds_read_b64_tr_b16 a[34:35], v7 offset:2048\n"
    "v_mfma_f32_16x16x32_bf16 v[248:251], a[112:115], a[192:195], v[248:251]\n"
    "ds_read_b64_tr_b16 a[36:37], v7 offset:4096\n"
    "s_add_u32 m0, s40, 12288\n"
    "s_add_u32 s45, s39, 196608\n"
    "buffer_load_dwordx4 v1, s[12:15], s45 offen lds\n"
    "v_mfma_f32_16x16x32_bf16 v[248:251], a[116:119], a[196:199], v[248:251]\n"
    "ds_read_b64_tr_b16 a[38:39], v7 offset:6144\n"
    "buffer_load_dwordx4 v2, s[12:15], s45 offen offset:1024 lds\n"
    "v_mfma_f32_16x16x32_bf16 v[248:251], a[120:123], a[200:203], v[248:251]\n"
    "ds_read_b64_tr_b16 a[40:41], v7 offset:8192\n"
    "buffer_load_dwordx4 v3, s[12:15], s45 offen offset:2048 lds\n"
    "v_mfma_f32_16x16x32_bf16 v[248:251], a[124:127], a[204:207], v[248:251]\n"
    "ds_read_b64_tr_b16 a[42:43], v7 offset:10240\n"
    "ds_read_b64_tr_b16 a[44:45], v7 offset:12288\n"
    "v_mfma_f32_16x16x32_bf16 v[236:239], a[108:111], a[204:207], v[236:239]\n"
    "ds_read_b64_tr_b16 a[46:47], v7 offset:14336\n"
    "buffer_load_dwordx4 v4, s[12:15], s45 offen offset:3072 lds\n"
    "v_mfma_f32_16x16x32_bf16 v[236:239], a[104:107], a[200:203], v[236:239]\n"
    "ds_read_b64_tr_b16 a[48:49], v8 offset:0\n"
    "s_add_u32 s40, s40, 0x4000\n"
    "s_and_b32 s40, s40, 0x1ffff\n"
    "v_readlane_b32 s38, v43, s52\n"
    "s_add_u32 s52, s52, 1\n"
    "s_add_u32 s48, s38, 0\n"
    "s_add_u32 s49, s38, 11264\n"
    "s_add_u32 s50, s38, 22528\n"
    "s_add_u32 s51, s38, 33792\n"
    "buffer_load_dwordx4 a[128:131], v15, s[24:27], s48 offen offset:0 sc1\n"
    "v_mfma_f32_16x16x32_bf16 v[236:239], a[100:103], a[196:199], v[236:239]\n"
    "ds_read_b64_tr_b16 a[50:51], v8 offset:2048\n"
    "buffer_load_dwordx4 a[132:135], v15, s[24:27], s49 offen offset:0 sc1\n"
    "v_mfma_f32_16x16x32_bf16 v[236:239], a[96:99], a[192:195], v[236:239]\n"
    "ds_read_b64_tr_b16 a[52:53], v8 offset:4096\n"
    "v_mfma_f32_16x16x32_bf16 v[224:227], a[80:83], a[192:195], v[224:227]\n"
    "ds_read_b64_tr_b16 a[54:55], v8 offset:6144\n"
    "buffer_load_dwordx4 a[136:139], v15, s[24:27], s50 offen offset:0 sc1\n"
    "v_mfma_f32_16x16x32_bf16 v[224:227], a[84:87], a[196:199], v[224:227]\n"
    "ds_read_b64_tr_b16 a[56:57], v8 offset:8192\n"
    "buffer_load_dwordx4 a[140:143], v15, s[24:27], s51 offen offset:0 sc1\n"
    "v_mfma_f32_16x16x32_bf16 v[224:227], a[88:91], a[200:203], v[224:227]\n"
    "ds_read_b64_tr_b16 a[58:59], v8 offset:10240\n"
    "buffer_load_dwordx4 a[144:147], v15, s[24:27], s48 offen offset:1024 sc1\n"
    "v_mfma_f32_16x16x32_bf16 v[224:227], a[92:95], a[204:207], v[224:227]\n"
    "ds_read_b64_tr_b16 a[60:61], v8 offset:12288\n"
    "v_mfma_f32_16x16x32_bf16 v[212:215], a[76:79], a[204:207], v[212:215]\n"
    "ds_read_b64_tr_b16 a[62:63], v8 offset:14336\n"
    "buffer_load_dwordx4 a[148:151], v15, s[24:27], s49 offen offset:1024 sc1\n"
    "v_mfma_f32_16x16x32_bf16 v[212:215], a[72:75], a[200:203], v[212:215]\n"
    "buffer_load_dwordx4 a[152:155], v15, s[24:27], s50 offen offset:1024 sc1\n"
    "v_mfma_f32_16x16x32_bf16 v[212:215], a[68:71], a[196:199], v[212:215]\n"
    "buffer_load_dwordx4 a[156:159], v15, s[24:27], s51 offen offset:1024 sc1\n"
    "v_mfma_f32_16x16x32_bf16 v[212:215], a[64:67], a[192:195], v[212:215]\n"
    "s_waitcnt vmcnt(16) lgkmcnt(0)\n"
    "s_barrier\n"
    "v_mfma_f32_16x16x32_bf16 v[208:211], a[0:3], v[112:115], v[208:211]\n"
    "ds_read_b64_tr_b16 a[64:65], v5 offset:16384\n"
    "ds_read_b64_tr_b16 a[66:67], v5 offset:18432\n"
    "v_readlane_b32 s38, v43, s52\n"
    "s_add_u32 s52, s52, 1\n"
    "s_add_u32 s48, s38, 0\n"
    "s_add_u32 s49, s38, 11264\n"
    "s_add_u32 s50, s38, 22528\n"
    "s_add_u32 s51, s38, 33792\n"
    "buffer_load_dwordx4 v[64:67], v15, s[24:27], s48 offen offset:0 sc1\n"
    "v_mfma_f32_16x16x32_bf16 v[208:211], a[4:7], v[116:119], v[208:211]\n"
    "ds_read_b64_tr_b16 a[68:69], v5 offset:20480\n"
    "v_mfma_f32_16x16x32_bf16 v[208:211], a[8:11], v[120:123], v[208:211]\n"
    "ds_read_b64_tr_b16 a[70:71], v5 offset:22528\n"
    "v_mfma_f32_16x16x32_bf16 v[208:211], a[12:15], v[124:127], v[208:211]\n"
    "ds_read_b64_tr_b16 a[72:73], v5 offset:24576\n"
    "v_mfma_f32_16x16x32_bf16 v[220:223], a[28:31], v[124:127], v[220:223]\n"
    "ds_read_b64_tr_b16 a[74:75], v5 offset:26624\n"
    "buffer_load_dwordx4 v[68:71], v15, s[24:27], s49 offen offset:0 sc1\n"
    "v_mfma_f32_16x16x32_bf16 v[220:223], a[24:27], v[120:123], v[220:223]\n"
    "ds_read_b64_tr_b16 a[76:77], v5 offset:28672\n"
    "v_mfma_f32_16x16x32_bf16 v[220:223], a[20:23], v[116:119], v[220:223]\n"
    "ds_read_b64_tr_b16 a[78:79], v5 offset:30720\n"
    "v_mfma_f32_16x16x32_bf16 v[220:223], a[16:19], v[112:115], v[220:223]\n"
    "ds_read_b64_tr_b16 a[80:81], v6 offset:16384\n"
    "v_mfma_f32_16x16x32_bf16 v[232:235], a[32:35], v[112:115], v[232:235]\n"
    "ds_read_b64_tr_b16 a[82:83], v6 offset:18432\n"
    "buffer_load_dwordx4 v[72:75], v15, s[24:27], s50 offen offset:0 sc1\n"
    "v_mfma_f32_16x16x32_bf16 v[232:235], a[36:39], v[116:119], v[232:235]\n"
    "ds_read_b64_tr_b16 a[84:85], v6 offset:20480\n"
    "ds_read_b64_tr_b16 a[86:87], v6 offset:22528\n"
    "v_mfma_f32_16x16x32_bf16 v[232:235], a[40:43], v[120:123], v[232:235]\n"
    "ds_read_b64_tr_b16 a[88:89], v6 offset:24576\n"
    "v_mfma_f32_16x16x32_bf16 v[232:235], a[44:47], v[124:127], v[232:235]\n"
    "ds_read_b64_tr_b16 a[90:91], v6 offset:26624\n"
    "v_mfma_f32_16x16x32_bf16 v[244:247], a[60:63], v[124:127], v[244:247]\n"
    "ds_read_b64_tr_b16 a[92:93], v6 offset:28672\n"
    "buffer_load_dwordx4 v[76:79], v15, s[24:27], s51 offen offset:0 sc1\n"
    "v_mfma_f32_16x16x32_bf16 v[244:247], a[56:59], v[120:123], v[244:247]\n"
    "ds_read_b64_tr_b16 a[94:95], v6 offset:30720\n"
    "v_mfma_f32_16x16x32_bf16 v[244:247], a[52:55], v[116:119], v[244:247]\n"
    "ds_read_b64_tr_b16 a[96:97], v7 offset:16384\n"
    "v_mfma_f32_16x16x32_bf16 v[244:247], a[48:51], v[112:115], v[244:247]\n"
    "s_waitcnt vmcnt(62)\n"
    "ds_read_b64_tr_b16 a[98:99], v7 offset:18432\n"
    "v_mfma_f32_16x16x32_bf16 v[248:251], a[48:51], v[128:131], v[248:251]\n"
    "ds_read_b64_tr_b16 a[100:101], v7 offset:20480\n"
    "buffer_load_dwordx4 v[80:83], v15, s[24:27], s48 offen offset:1024 sc1\n"
    "v_mfma_f32_16x16x32_bf16 v[248:251], a[52:55], v[132:135], v[248:251]\n"
    "ds_read_b64_tr_b16 a[102:103], v7 offset:22528\n"
    "v_mfma_f32_16x16x32_bf16 v[248:251], a[56:59], v[136:139], v[248:251]\n"
    "ds_read_b64_tr_b16 a[104:105], v7 offset:24576\n"
    "v_mfma_f32_16x16x32_bf16 v[248:251], a[60:63], v[140:143], v[248:251]\n"
    "ds_read_b64_tr_b16 a[106:107], v7 offset:26624\n"
    "ds_read_b64_tr_b16 a[108:109], v7 offset:28672\n"
    "v_mfma_f32_16x16x32_bf16 v[236:239], a[44:47], v[140:143], v[236:239]\n"
    "ds_read_b64_tr_b16 a[110:111], v7 offset:30720\n"
    "buffer_load_dwordx4 v[84:87], v15, s[24:27], s49 offen offset:1024 sc1\n"
    "v_mfma_f32_16x16x32_bf16 v[236:239], a[40:43], v[136:139], v[236:239]\n"
    "ds_read_b64_tr_b16 a[112:113], v8 offset:16384\n"
    "v_mfma_f32_16x16x32_bf16 v[236:239], a[36:39], v[132:135], v[236:239]\n"
    "ds_read_b64_tr_b16 a[114:115], v8 offset:18432\n"
    "v_mfma_f32_16x16x32_bf16 v[236:239], a[32:35], v[128:131], v[236:239]\n"
    "ds_read_b64_tr_b16 a[116:117], v8 offset:20480\n"
    "v_mfma_f32_16x16x32_bf16 v[224:227], a[16:19], v[128:131], v[224:227]\n"
    "ds_read_b64_tr_b16 a[118:119], v8 offset:22528\n"
    "buffer_load_dwordx4 v[88:91], v15, s[24:27], s50 offen offset:1024 sc1\n"
    "v_mfma_f32_16x16x32_bf16 v[224:227], a[20:23], v[132:135], v[224:227]\n"
    "ds_read_b64_tr_b16 a[120:121], v8 offset:24576\n"
    "v_mfma_f32_16x16x32_bf16 v[224:227], a[24:27], v[136:139], v[224:227]\n"
    "ds_read_b64_tr_b16 a[122:123], v8 offset:26624\n"
    "v_mfma_f32_16x16x32_bf16 v[224:227], a[28:31], v[140:143], v[224:227]\n"
    "ds_read_b64_tr_b16 a[124:125], v8 offset:28672\n"
    "v_mfma_f32_16x16x32_bf16 v[212:215], a[12:15], v[140:143], v[212:215]\n"
    "ds_read_b64_tr_b16 a[126:127], v8 offset:30720\n"
    "buffer_load_dwordx4 v[92:95], v15, s[24:27], s51 offen offset:1024 sc1\n"
    "v_mfma_f32_16x16x32_bf16 v[212:215], a[8:11], v[136:139], v[212:215]\n"
    "v_mfma_f32_16x16x32_bf16 v[212:215], a[4:7], v[132:135], v[212:215]\n"
    "v_mfma_f32_16x16x32_bf16 v[212:215], a[0:3], v[128:131], v[212:215]\n"
    "buffer_load_dwordx2 v[40:41], off, s[32:35], s56 sc1\n"
    "s_waitcnt vmcnt(13) lgkmcnt(0)\n"
    "v_mfma_f32_16x16x32_bf16 v[208:211], a[64:67], a[128:131], v[208:211]\n"
    "ds_read_b64_tr_b16 a[0:1], v5 offset:32768\n"
    "ds_read_b64_tr_b16 a[2:3], v5 offset:34816\n"
    "v_readlane_b32 s38, v43, s52\n"
    "s_add_u32 s52, s52, 1\n"
    "s_add_u32 s48, s38, 0\n"
    "s_add_u32 s49, s38, 11264\n"
    "s_add_u32 s50, s38, 22528\n"
    "s_add_u32 s51, s38, 33792\n"
    "buffer_load_dwordx4 a[176:179], v15, s[24:27], s48 offen offset:0 sc1\n"
    "v_mfma_f32_16x16x32_bf16 v[208:211], a[68:71], a[132:135], v[208:211]\n"
    "ds_read_b64_tr_b16 a[4:5], v5 offset:36864\n"
    "v_mfma_f32_16x16x32_bf16 v[208:211], a[72:75], a[136:139], v[208:211]\n"
    "ds_read_b64_tr_b16 a[6:7], v5 offset:38912\n"
    "v_mfma_f32_16x16x32_bf16 v[208:211], a[76:79], a[140:143], v[208:211]\n"
    "ds_read_b64_tr_b16 a[8:9], v5 offset:40960\n"
    "v_mfma_f32_16x16x32_bf16 v[220:223], a[92:95], a[140:143], v[220:223]\n"
    "ds_read_b64_tr_b16 a[10:11], v5 offset:43008\n"
    "buffer_load_dwordx4 a[180:183], v15, s[24:27], s49 offen offset:0 sc1\n"
    "v_mfma_f32_16x16x32_bf16 v[220:223], a[88:91], a[136:139], v[220:223]\n"
    "ds_read_b64_tr_b16 a[12:13], v5 offset:45056\n"
    "v_mfma_f32_16x16x32_bf16 v[220:223], a[84:87], a[132:135], v[220:223]\n"
    "ds_read_b64_tr_b16 a[14:15], v5 offset:47104\n"
    "v_mfma_f32_16x16x32_bf16 v[220:223], a[80:83], a[128:131], v[220:223]\n"
    "ds_read_b64_tr_b16 a[16:17], v6 offset:32768\n"
    "v_mfma_f32_16x16x32_bf16 v[232:235], a[96:99], a[128:131], v[232:235]\n"
    "ds_read_b64_tr_b16 a[18:19], v6 offset:34816\n"
    "buffer_load_dwordx4 a[184:187], v15, s[24:27], s50 offen offset:0 sc1\n"
    "v_mfma_f32_16x16x32_bf16 v[232:235], a[100:103], a[132:135], v[232:235]\n"
    "ds_read_b64_tr_b16 a[20:21], v6 offset:36864\n"
    "ds_read_b64_tr_b16 a[22:23], v6 offset:38912\n"
    "v_mfma_f32_16x16x32_bf16 v[232:235], a[104:107], a[136:139], v[232:235]\n"
    "ds_read_b64_tr_b16 a[24:25], v6 offset:40960\n"
    "v_mfma_f32_16x16x32_bf16 v[232:235], a[108:111], a[140:143], v[232:235]\n"
    "ds_read_b64_tr_b16 a[26:27], v6 offset:43008\n"
    "v_mfma_f32_16x16x32_bf16 v[244:247], a[124:127], a[140:143], v[244:247]\n"
    "ds_read_b64_tr_b16 a[28:29], v6 offset:45056\n"
    "buffer_load_dwordx4 a[188:191], v15, s[24:27], s51 offen offset:0 sc1\n"
    "v_mfma_f32_16x16x32_bf16 v[244:247], a[120:123], a[136:139], v[244:247]\n"
    "ds_read_b64_tr_b16 a[30:31], v6 offset:47104\n"
    "v_mfma_f32_16x16x32_bf16 v[244:247], a[116:119], a[132:135], v[244:247]\n"
    "ds_read_b64_tr_b16 a[32:33], v7 offset:32768\n"
    "v_mfma_f32_16x16x32_bf16 v[244:247], a[112:115], a[128:131], v[244:247]\n"
    "s_waitcnt vmcnt(13)\n"
    "ds_read_b64_tr_b16 a[34:35], v7 offset:34816\n"
    "v_mfma_f32_16x16x32_bf16 v[248:251], a[112:115], a[144:147], v[248:251]\n"
    "ds_read_b64_tr_b16 a[36:37], v7 offset:36864\n"
    "buffer_load_dwordx4 a[192:195], v15, s[24:27], s48 offen offset:1024 sc1\n"
    "v_mfma_f32_16x16x32_bf16 v[248:251], a[116:119], a[148:151], v[248:251]\n"
    "ds_read_b64_tr_b16 a[38:39], v7 offset:38912\n"
    "v_mfma_f32_16x16x32_bf16 v[248:251], a[120:123], a[152:155], v[248:251]\n"
    "ds_read_b64_tr_b16 a[40:41], v7 offset:40960\n"
    "v_mfma_f32_16x16x32_bf16 v[248:251], a[124:127], a[156:159], v[248:251]\n"
    "ds_read_b64_tr_b16 a[42:43], v7 offset:43008\n"
    "ds_read_b64_tr_b16 a[44:45], v7 offset:45056\n"
    "v_mfma_f32_16x16x32_bf16 v[236:239], a[108:111], a[156:159], v[236:239]\n"
    "ds_read_b64_tr_b16 a[46:47], v7 offset:47104\n"
    "buffer_load_dwordx4 a[196:199], v15, s[24:27], s49 offen offset:1024 sc1\n"
    "v_mfma_f32_16x16x32_bf16 v[236:239], a[104:107], a[152:155], v[236:239]\n"
    "ds_read_b64_tr_b16 a[48:49], v8 offset:32768\n"
    "v_mfma_f32_16x16x32_bf16 v[236:239], a[100:103], a[148:151], v[236:239]\n"
    "ds_read_b64_tr_b16 a[50:51], v8 offset:34816\n"
    "v_mfma_f32_16x16x32_bf16 v[236:239], a[96:99], a[144:147], v[236:239]\n"
    "ds_read_b64_tr_b16 a[52:53], v8 offset:36864\n"
    "v_mfma_f32_16x16x32_bf16 v[224:227], a[80:83], a[144:147], v[224:227]\n"
    "ds_read_b64_tr_b16 a[54:55], v8 offset:38912\n"
    "buffer_load_dwordx4 a[200:203], v15, s[24:27], s50 offen offset:1024 sc1\n"
    "v_mfma_f32_16x16x32_bf16 v[224:227], a[84:87], a[148:151], v[224:227]\n"
    "ds_read_b64_tr_b16 a[56:57], v8 offset:40960\n"
    "v_mfma_f32_16x16x32_bf16 v[224:227], a[88:91], a[152:155], v[224:227]\n"
    "ds_read_b64_tr_b16 a[58:59], v8 offset:43008\n"
    "s_waitcnt vmcnt(7)\n"
    "s_mov_b32 s57, 0x40000\n"
    ".L_fspDp_%=:\n"
    "v_cmp_ne_u64 vcc, s[62:63], v[40:41]\n"
    "s_cbranch_vccz .L_fokDp_%=\n"
    "s_sleep 4\n"
    "buffer_load_dwordx2 v[40:41], off, s[32:35], s56 sc1\n"
    "s_sub_u32 s57, s57, 1\n"
    "s_waitcnt vmcnt(0)\n"
    "s_cmp_lg_u32 s57, 0\n"
    "s_cbranch_scc1 .L_fspDp_%=\n"
    ".L_fokDp_%=:\n"
    "s_add_u32 s46, s55, 0\n"
    "buffer_load_dwordx4 v[172:175], v37, s[24:27], s46 offen offset:0 sc1\n"
    "v_mfma_f32_16x16x32_bf16 v[224:227], a[92:95], a[156:159], v[224:227]\n"
    "ds_read_b64_tr_b16 a[60:61], v8 offset:45056\n"
    "buffer_load_dwordx4 v[196:199], v37, s[24:27], s46 offen offset:1024 sc1\n"
    "v_mfma_f32_16x16x32_bf16 v[212:215], a[76:79], a[156:159], v[212:215]\n"
    "ds_read_b64_tr_b16 a[62:63], v8 offset:47104\n"
    "buffer_load_dwordx4 a[204:207], v15, s[24:27], s51 offen offset:1024 sc1\n"
    "v_mfma_f32_16x16x32_bf16 v[212:215], a[72:75], a[152:155], v[212:215]\n"
    "s_add_u32 s46, s55, 2048\n"
    "buffer_load_dwordx4 v[176:179], v37, s[24:27], s46 offen offset:0 sc1\n"
    "v_mfma_f32_16x16x32_bf16 v[212:215], a[68:71], a[148:151], v[212:215]\n"
    "buffer_load_dwordx4 v[200:203], v37, s[24:27], s46 offen offset:1024 sc1\n"
    "v_mfma_f32_16x16x32_bf16 v[212:215], a[64:67], a[144:147], v[212:215]\n"
    "s_waitcnt vmcnt(16) lgkmcnt(0)\n"
    "s_barrier\n"
    "v_mfma_f32_16x16x32_bf16 v[208:211], a[0:3], v[64:67], v[208:211]\n"
    "ds_read_b64_tr_b16 a[64:65], v5 offset:49152\n"
    "ds_read_b64_tr_b16 a[66:67], v5 offset:51200\n"
    "v_mfma_f32_16x16x32_bf16 v[208:211], a[4:7], v[68:71], v[208:211]\n"
    "ds_read_b64_tr_b16 a[68:69], v5 offset:53248\n"
    "v_and_b32 v44, 63, %7\n"
    "v_mfma_f32_16x16x32_bf16 v[208:211], a[8:11], v[72:75], v[208:211]\n"
    "ds_read_b64_tr_b16 a[70:71], v5 offset:55296\n"
    "v_lshrrev_b32 v45, 4, v44\n"
    "v_mfma_f32_16x16x32_bf16 v[208:211], a[12:15], v[76:79], v[208:211]\n"
    "ds_read_b64_tr_b16 a[72:73], v5 offset:57344\n"
    "v_and_b32 v46, 15, v44\n"
    "v_mfma_f32_16x16x32_bf16 v[220:223], a[28:31], v[76:79], v[220:223]\n"
    "ds_read_b64_tr_b16 a[74:75], v5 offset:59392\n"
    "v_bfe_u32 v47, v44, 2, 2\n"
    "v_mfma_f32_16x16x32_bf16 v[220:223], a[24:27], v[72:75], v[220:223]\n"
    "ds_read_b64_tr_b16 a[76:77], v5 offset:61440\n"
    "v_and_b32 v48, 3, v44\n"
    "v_mfma_f32_16x16x32_bf16 v[220:223], a[20:23], v[68:71], v[220:223]\n"
    "ds_read_b64_tr_b16 a[78:79], v5 offset:63488\n"
    "v_xor_b32 v5, 0x10000, v5\n"
    "v_mfma_f32_16x16x32_bf16 v[220:223], a[16:19], v[64:67], v[220:223]\n"
    "ds_read_b64_tr_b16 a[80:81], v6 offset:49152\n"
    "v_and_b32 v49, 1, v45\n"
    "v_mfma_f32_16x16x32_bf16 v[232:235], a[32:35], v[64:67], v[232:235]\n"
    "ds_read_b64_tr_b16 a[82:83], v6 offset:51200\n"
    "v_lshl_or_b32 v49, v49, 2, v47\n"
    "v_mfma_f32_16x16x32_bf16 v[232:235], a[36:39], v[68:71], v[232:235]\n"
    "ds_read_b64_tr_b16 a[84:85], v6 offset:53248\n"
    "ds_read_b64_tr_b16 a[86:87], v6 offset:55296\n"
    "v_mfma_f32_16x16x32_bf16 v[232:235], a[40:43], v[72:75], v[232:235]\n"
    "ds_read_b64_tr_b16 a[88:89], v6 offset:57344\n"
    "v_lshrrev_b32 v50, 3, v44\n"
    "v_mfma_f32_16x16x32_bf16 v[232:235], a[44:47], v[76:79], v[232:235]\n"
    "ds_read_b64_tr_b16 a[90:91], v6 offset:59392\n"
    "v_and_b32 v51, 7, v44\n"
    "s_lshl_b32 s61, s36, 13\n"
    "s_add_u32 s61, s61, 131072\n"
    "v_mfma_f32_16x16x32_bf16 v[244:247], a[60:63], v[76:79], v[244:247]\n"
    "ds_read_b64_tr_b16 a[92:93], v6 offset:61440\n"
    "v_lshrrev_b32 v52, 1, v45\n"
    "v_mfma_f32_16x16x32_bf16 v[244:247], a[56:59], v[72:75], v[244:247]\n"
    "ds_read_b64_tr_b16 a[94:95], v6 offset:63488\n"
    "v_xor_b32 v6, 0x10000, v6\n"
    "v_mfma_f32_16x16x32_bf16 v[244:247], a[52:55], v[68:71], v[244:247]\n"
    "ds_read_b64_tr_b16 a[96:97], v7 offset:49152\n"
    "v_and_b32 v55, 7, v46\n"
    "v_mfma_f32_16x16x32_bf16 v[244:247], a[48:51], v[64:67], v[244:247]\n"
    "s_waitcnt vmcnt(13)\n"
    "ds_read_b64_tr_b16 a[98:99], v7 offset:51200\n"
    "v_xor_b32 v52, v52, v55\n"
    "v_mfma_f32_16x16x32_bf16 v[248:251], a[48:51], v[80:83], v[248:251]\n"
    "ds_read_b64_tr_b16 a[100:101], v7 offset:53248\n"
    "v_lshlrev_b32 v52, 4, v52\n"
    "v_mfma_f32_16x16x32_bf16 v[248:251], a[52:55], v[84:87], v[248:251]\n"
    "ds_read_b64_tr_b16 a[102:103], v7 offset:55296\n"
    "v_and_b32 v55, 1, v45\n"
    "v_mfma_f32_16x16x32_bf16 v[248:251], a[56:59], v[88:91], v[248:251]\n"
    "ds_read_b64_tr_b16 a[104:105], v7 offset:57344\n"
    "v_lshl_add_u32 v52, v55, 3, v52\n"
    "v_mfma_f32_16x16x32_bf16 v[248:251], a[60:63], v[92:95], v[248:251]\n"
    "ds_read_b64_tr_b16 a[106:107], v7 offset:59392\n"
    "ds_read_b64_tr_b16 a[108:109], v7 offset:61440\n"
    "v_mfma_f32_16x16x32_bf16 v[236:239], a[44:47], v[92:95], v[236:239]\n"
    "ds_read_b64_tr_b16 a[110:111], v7 offset:63488\n"
    "v_xor_b32 v7, 0x10000, v7\n"
    "v_mfma_f32_16x16x32_bf16 v[236:239], a[40:43], v[88:91], v[236:239]\n"
    "ds_read_b64_tr_b16 a[112:113], v8 offset:49152\n"
    "v_lshl_add_u32 v52, v46, 7, v52\n"
    "v_mfma_f32_16x16x32_bf16 v[236:239], a[36:39], v[84:87], v[236:239]\n"
    "ds_read_b64_tr_b16 a[114:115], v8 offset:51200\n"
    "v_add_u32 v52, s61, v52\n"
    "v_mfma_f32_16x16x32_bf16 v[236:239], a[32:35], v[80:83], v[236:239]\n"
    "ds_read_b64_tr_b16 a[116:117], v8 offset:53248\n"
    "v_xor_b32 v53, v51, v50\n"
    "v_mfma_f32_16x16x32_bf16 v[224:227], a[16:19], v[80:83], v[224:227]\n"
    "ds_read_b64_tr_b16 a[118:119], v8 offset:55296\n"
    "v_lshlrev_b32 v53, 4, v53\n"
    "v_mfma_f32_16x16x32_bf16 v[224:227], a[20:23], v[84:87], v[224:227]\n"
    "ds_read_b64_tr_b16 a[120:121], v8 offset:57344\n"
    "v_lshl_add_u32 v53, v50, 7, v53\n"
    "v_mfma_f32_16x16x32_bf16 v[224:227], a[24:27], v[88:91], v[224:227]\n"
    "ds_read_b64_tr_b16 a[122:123], v8 offset:59392\n"
    "v_add_u32 v53, s61, v53\n"
    "v_mfma_f32_16x16x32_bf16 v[224:227], a[28:31], v[92:95], v[224:227]\n"
    "ds_read_b64_tr_b16 a[124:125], v8 offset:61440\n"
    "v_lshlrev_b32 v54, 4, v51\n"
    "v_mfma_f32_16x16x32_bf16 v[212:215], a[12:15], v[92:95], v[212:215]\n"
    "ds_read_b64_tr_b16 a[126:127], v8 offset:63488\n"
    "v_xor_b32 v8, 0x10000, v8\n"
    "v_mfma_f32_16x16x32_bf16 v[212:215], a[8:11], v[88:91], v[212:215]\n"
    "v_lshl_add_u32 v54, v50, 12, v54\n"
    "v_xor_b32 v17, 0, v52\n"
    "v_mfma_f32_16x16x32_bf16 v[212:215], a[4:7], v[84:87], v[212:215]\n"
    "v_xor_b32 v18, 32, v52\n"
    "v_xor_b32 v19, 64, v52\n"
    "v_mfma_f32_16x16x32_bf16 v[212:215], a[0:3], v[80:83], v[212:215]\n"
    "v_xor_b32 v20, 96, v52\n"
    "s_waitcnt vmcnt(8) lgkmcnt(0)\n"
    "s_mul_i32 s46, s36, 196608\n"
    "v_mfma_f32_16x16x32_bf16 v[208:211], a[64:67], a[176:179], v[208:211]\n"
    "v_mfma_f32_16x16x32_bf16 v[208:211], a[68:71], a[180:183], v[208:211]\n"
    "v_mfma_f32_16x16x32_bf16 v[208:211], a[72:75], a[184:187], v[208:211]\n"
    "v_mfma_f32_16x16x32_bf16 v[208:211], a[76:79], a[188:191], v[208:211]\n"
    "v_mfma_f32_16x16x32_bf16 v[220:223], a[80:83], a[176:179], v[220:223]\n"
    "v_mfma_f32_16x16x32_bf16 v[220:223], a[84:87], a[180:183], v[220:223]\n"
    "v_mfma_f32_16x16x32_bf16 v[220:223], a[88:91], a[184:187], v[220:223]\n"
    "v_mfma_f32_16x16x32_bf16 v[220:223], a[92:95], a[188:191], v[220:223]\n"
    "v_mfma_f32_16x16x32_bf16 v[232:235], a[96:99], a[176:179], v[232:235]\n"
    "v_mfma_f32_16x16x32_bf16 v[232:235], a[100:103], a[180:183], v[232:235]\n"
    "v_mfma_f32_16x16x32_bf16 v[232:235], a[104:107], a[184:187], v[232:235]\n"
    "v_mfma_f32_16x16x32_bf16 v[232:235], a[108:111], a[188:191], v[232:235]\n"
    "v_mfma_f32_16x16x32_bf16 v[244:247], a[112:115], a[176:179], v[244:247]\n"
    "v_mfma_f32_16x16x32_bf16 v[244:247], a[116:119], a[180:183], v[244:247]\n"
    "v_mfma_f32_16x16x32_bf16 v[244:247], a[120:123], a[184:187], v[244:247]\n"
    "v_mfma_f32_16x16x32_bf16 v[244:247], a[124:127], a[188:191], v[244:247]\n"
    "s_waitcnt vmcnt(0)\n"
    "v_mfma_f32_16x16x32_bf16 v[208:211], a[232:235], v[172:175], v[208:211]\n"
    "v_mfma_f32_16x16x32_bf16 v[220:223], a[236:239], v[172:175], v[220:223]\n"
    "v_mfma_f32_16x16x32_bf16 v[232:235], a[232:235], v[196:199], v[232:235]\n"
    "v_mfma_f32_16x16x32_bf16 v[244:247], a[236:239], v[196:199], v[244:247]\n"
    "v_mfma_f32_16x16x32_bf16 v[212:215], a[64:67], a[192:195], v[212:215]\n"
    "v_mfma_f32_16x16x32_bf16 v[212:215], a[68:71], a[196:199], v[212:215]\n"
    "v_mfma_f32_16x16x32_bf16 v[212:215], a[72:75], a[200:203], v[212:215]\n"
    "v_mfma_f32_16x16x32_bf16 v[212:215], a[76:79], a[204:207], v[212:215]\n"
    "v_mfma_f32_16x16x32_bf16 v[224:227], a[80:83], a[192:195], v[224:227]\n"
    "v_mfma_f32_16x16x32_bf16 v[224:227], a[84:87], a[196:199], v[224:227]\n"
    "v_cvt_pk_bf16_f32 v208, v208, v209\n"
    "v_cvt_pk_bf16_f32 v209, v210, v211\n"
    "v_mfma_f32_16x16x32_bf16 v[224:227], a[88:91], a[200:203], v[224:227]\n"
    "ds_write_b64 v17, v[208:209] offset:0\n"
    "v_cvt_pk_bf16_f32 v220, v220, v221\n"
    "v_mfma_f32_16x16x32_bf16 v[224:227], a[92:95], a[204:207], v[224:227]\n"
    "v_cvt_pk_bf16_f32 v221, v222, v223\n"
    "ds_write_b64 v18, v[220:221] offset:0\n"
    "v_mfma_f32_16x16x32_bf16 v[236:239], a[96:99], a[192:195], v[236:239]\n"
    "v_cvt_pk_bf16_f32 v232, v232, v233\n"
    "v_cvt_pk_bf16_f32 v233, v234, v235\n"
    "v_mfma_f32_16x16x32_bf16 v[236:239], a[100:103], a[196:199], v[236:239]\n"
    "ds_write_b64 v19, v[232:233] offset:0\n"
    "v_cvt_pk_bf16_f32 v244, v244, v245\n"
    "v_mfma_f32_16x16x32_bf16 v[236:239], a[104:107], a[200:203], v[236:239]\n"
    "v_cvt_pk_bf16_f32 v245, v246, v247\n"
    "ds_write_b64 v20, v[244:245] offset:0\n"
    "v_mfma_f32_16x16x32_bf16 v[236:239], a[108:111], a[204:207], v[236:239]\n"
    "ds_read_b128 v[112:115], v53 offset:0\n"
    "v_mfma_f32_16x16x32_bf16 v[248:251], a[112:115], a[192:195], v[248:251]\n"
    "ds_read_b128 v[116:119], v53 offset:1024\n"
    "v_mfma_f32_16x16x32_bf16 v[248:251], a[116:119], a[196:199], v[248:251]\n"
    "v_mfma_f32_16x16x32_bf16 v[248:251], a[120:123], a[200:203], v[248:251]\n"
    "v_mfma_f32_16x16x32_bf16 v[248:251], a[124:127], a[204:207], v[248:251]\n"
    "v_mfma_f32_16x16x32_bf16 v[212:215], a[232:235], v[176:179], v[212:215]\n"
    "v_mfma_f32_16x16x32_bf16 v[224:227], a[236:239], v[176:179], v[224:227]\n"
    "v_mfma_f32_16x16x32_bf16 v[236:239], a[232:235], v[200:203], v[236:239]\n"
    "v_mfma_f32_16x16x32_bf16 v[248:251], a[236:239], v[200:203], v[248:251]\n"
    "s_waitcnt lgkmcnt(0)\n"
    "buffer_store_dwordx4 v[112:115], v54, s[8:11], s46 offen\n"
    "s_add_u32 s46, s46, 32768\n"
    "buffer_store_dwordx4 v[116:119], v54, s[8:11], s46 offen\n"
    "s_add_u32 s46, s46, 32768\n"
    "s_nop 3\n"
    "v_cvt_pk_bf16_f32 v212, v212, v213\n"
    "v_cvt_pk_bf16_f32 v213, v214, v215\n"
    "ds_write_b64 v17, v[212:213] offset:2048\n"
    "v_cvt_pk_bf16_f32 v224, v224, v225\n"
    "v_cvt_pk_bf16_f32 v225, v226, v227\n"
    "ds_write_b64 v18, v[224:225] offset:2048\n"
    "v_cvt_pk_bf16_f32 v236, v236, v237\n"
    "v_cvt_pk_bf16_f32 v237, v238, v239\n"
    "ds_write_b64 v19, v[236:237] offset:2048\n"
    "v_cvt_pk_bf16_f32 v248, v248, v249\n"
    "v_cvt_pk_bf16_f32 v249, v250, v251\n"
    "ds_write_b64 v20, v[248:249] offset:2048\n"
    "ds_read_b128 v[120:123], v53 offset:2048\n"
    "ds_read_b128 v[124:127], v53 offset:3072\n"
    "s_waitcnt lgkmcnt(0)\n"
    "buffer_store_dwordx4 v[120:123], v54, s[8:11], s46 offen\n"
    "s_add_u32 s46, s46, 32768\n"
    "buffer_store_dwordx4 v[124:127], v54, s[8:11], s46 offen\n"
    "s_add_u32 s46, s46, 32768\n"
    "s_cmp_eq_u32 %5, 0\n"
    "s_cbranch_scc0 .L_fste_%=\n"
    "s_mov_b32 s57, 256\n"
    "v_mov_b64 v[62:63], s[62:63]\n"
    "s_mov_b64 exec, 1\n"
    "buffer_store_dwordx2 v[62:63], off, s[32:35], s57 sc1\n"
    "s_mov_b64 exec, -1\n"
    ".L_fste_%=:\n"
    "s_waitcnt vmcnt(0)\n"
    "s_branch .L_end_%=\n"
    ".L_fbD_%=:\n"
    "s_waitcnt vmcnt(0) lgkmcnt(0)\n"
    "s_mov_b32 s44, m0\n"
    "s_mov_b32 m0, s61\n"
    "v_and_b32 v52, 63, %7\n"
    "v_lshrrev_b32 v53, 4, v52\n"
    "v_and_b32 v52, 15, v52\n"
    "v_lshlrev_b32 v52, 8, v52\n"
    "v_lshl_add_u32 v52, v53, 4, v52\n"
    "v_and_b32 v56, 0xfff, v25\n"
    "v_add_u32 v56, s61, v56\n"
    "v_and_b32 v57, 0xfff, v26\n"
    "v_add_u32 v57, s61, v57\n"
    "v_and_b32 v58, 0xfff, v27\n"
    "v_add_u32 v58, s61, v58\n"
    "v_and_b32 v59, 0xfff, v28\n"
    "v_add_u32 v59, s61, v59\n"
    "v_and_b32 v60, 0xfff, v29\n"
    "v_add_u32 v60, s61, v60\n"
    "v_and_b32 v61, 0xfff, v30\n"
    "v_add_u32 v61, s61, v61\n"
    "v_and_b32 v62, 0xfff, v31\n"
    "v_add_u32 v62, s61, v62\n"
    "v_and_b32 v63, 0xfff, v32\n"
    "v_add_u32 v63, s61, v63\n"
    "s_mul_i32 s42, s55, s60\n"
    "s_add_u32 s46, s42, 0x20000\n"
    "v_mov_b32 v232, 0xff800000\n"
    "v_mov_b32 v236, 0xff800000\n"
    ".L_fb1D_%=:\n"
    "s_add_u32 s57, s42, 0\n"
    "buffer_load_dwordx4 a[64:67], v52, s[16:19], s57 offen offset:0\n"
    "buffer_load_dwordx4 a[68:71], v52, s[16:19], s57 offen offset:64\n"
    "buffer_load_dwordx4 a[72:75], v52, s[16:19], s57 offen offset:128\n"
    "buffer_load_dwordx4 a[76:79], v52, s[16:19], s57 offen offset:192\n"
    "s_add_u32 s57, s42, 4096\n"
    "buffer_load_dwordx4 a[80:83], v52, s[16:19], s57 offen offset:0\n"
    "buffer_load_dwordx4 a[84:87], v52, s[16:19], s57 offen offset:64\n"
    "buffer_load_dwordx4 a[88:91], v52, s[16:19], s57 offen offset:128\n"
    "buffer_load_dwordx4 a[92:95], v52, s[16:19], s57 offen offset:192\n"
    "s_waitcnt vmcnt(0)\n"
    "v_mfma_f32_16x16x32_bf16 v[160:163], a[64:67], a[176:179], 0\n"
    "v_mfma_f32_16x16x32_bf16 v[160:163], a[68:71], a[180:183], v[160:163]\n"
    "v_mfma_f32_16x16x32_bf16 v[160:163], a[72:75], a[184:187], v[160:163]\n"
    "v_mfma_f32_16x16x32_bf16 v[160:163], a[76:79], a[188:191], v[160:163]\n"
    "v_mfma_f32_16x16x32_bf16 v[184:187], a[64:67], a[192:195], 0\n"
    "v_mfma_f32_16x16x32_bf16 v[184:187], a[68:71], a[196:199], v[184:187]\n"
    "v_mfma_f32_16x16x32_bf16 v[184:187], a[72:75], a[200:203], v[184:187]\n"
    "v_mfma_f32_16x16x32_bf16 v[184:187], a[76:79], a[204:207], v[184:187]\n"
    "v_mfma_f32_16x16x32_bf16 v[176:179], a[80:83], a[176:179], 0\n"
    "v_mfma_f32_16x16x32_bf16 v[176:179], a[84:87], a[180:183], v[176:179]\n"
    "v_mfma_f32_16x16x32_bf16 v[176:179], a[88:91], a[184:187], v[176:179]\n"
    "v_mfma_f32_16x16x32_bf16 v[176:179], a[92:95], a[188:191], v[176:179]\n"
    "v_mfma_f32_16x16x32_bf16 v[200:203], a[80:83], a[192:195], 0\n"
    "v_mfma_f32_16x16x32_bf16 v[200:203], a[84:87], a[196:199], v[200:203]\n"
    "v_mfma_f32_16x16x32_bf16 v[200:203], a[88:91], a[200:203], v[200:203]\n"
    "v_mfma_f32_16x16x32_bf16 v[200:203], a[92:95], a[204:207], v[200:203]\n"
    "s_nop 7\n"
    "v_max3_f32 v232, v232, v160, v161\n"
    "v_max3_f32 v232, v232, v162, v163\n"
    "v_max3_f32 v232, v232, v176, v177\n"
    "v_max3_f32 v232, v232, v178, v179\n"
    "v_max3_f32 v236, v236, v184, v185\n"
    "v_max3_f32 v236, v236, v186, v187\n"
    "v_max3_f32 v236, v236, v200, v201\n"
    "v_max3_f32 v236, v236, v202, v203\n"
    "s_add_u32 s42, s42, 8192\n"
    "s_cmp_lg_u32 s42, s46\n"
    "s_cbranch_scc1 .L_fb1D_%=\n"
    "v_mov_b32 v44, v232\n"
    "v_mov_b32 v46, v236\n"
    "v_mov_b32 v45, v44\n"
    "v_mov_b32 v47, v46\n"
    "s_nop 1\n"
    "v_permlane32_swap_b32 v44, v45\n"
    "v_permlane32_swap_b32 v46, v47\n"
    "v_max_f32 v44, v44, v45\n"
    "v_max_f32 v46, v46, v47\n"
    "v_mov_b32 v45, v44\n"
    "v_mov_b32 v47, v46\n"
    "s_nop 1\n"
    "v_permlane16_swap_b32 v44, v45\n"
    "v_permlane16_swap_b32 v46, v47\n"
    "v_max_f32 v44, v44, v45\n"
    "v_max_f32 v46, v46, v47\n"
    "v_sub_f32 v232, 0, v44\n"
    "v_sub_f32 v233, 0, v44\n"
    "v_sub_f32 v234, 0, v44\n"
    "v_sub_f32 v235, 0, v44\n"
    "v_sub_f32 v236, 0, v46\n"
    "v_sub_f32 v237, 0, v46\n"
    "v_sub_f32 v238, 0, v46\n"
    "v_sub_f32 v239, 0, v46\n"
    "v_mfma_f32_32x32x16_bf16 v[64:79], a[228:231], a[228:231], 0\n"
    "v_mfma_f32_32x32x16_bf16 v[80:95], a[228:231], a[228:231], 0\n"
    "v_mfma_f32_32x32x16_bf16 v[96:111], a[228:231], a[228:231], 0\n"
    "v_mfma_f32_32x32x16_bf16 v[112:127], a[228:231], a[228:231], 0\n"
    "v_mfma_f32_32x32x16_bf16 v[128:143], a[228:231], a[228:231], 0\n"
    "v_mfma_f32_32x32x16_bf16 v[144:159], a[228:231], a[228:231], 0\n"
    "v_mov_b32 v244, 0\n"
    "v_mov_b32 v245, 0\n"
    "v_mov_b32 v246, 0\n"
    "v_mov_b32 v247, 0\n"
    "v_mov_b32 v248, 0\n"
    "v_mov_b32 v249, 0\n"
    "v_mov_b32 v250, 0\n"
    "v_mov_b32 v251, 0\n"
    "s_sub_u32 s42, s42, 0x20000\n"
    ".L_fb2D_%=:\n"
    "s_add_u32 s57, s42, 0\n"
    "buffer_load_dwordx4 a[64:67], v52, s[16:19], s57 offen offset:0\n"
    "buffer_load_dwordx4 a[68:71], v52, s[16:19], s57 offen offset:64\n"
    "buffer_load_dwordx4 a[72:75], v52, s[16:19], s57 offen offset:128\n"
    "buffer_load_dwordx4 a[76:79], v52, s[16:19], s57 offen offset:192\n"
    "s_add_u32 s57, s42, 4096\n"
    "buffer_load_dwordx4 a[80:83], v52, s[16:19], s57 offen offset:0\n"
    "buffer_load_dwordx4 a[84:87], v52, s[16:19], s57 offen offset:64\n"
    "buffer_load_dwordx4 a[88:91], v52, s[16:19], s57 offen offset:128\n"
    "buffer_load_dwordx4 a[92:95], v52, s[16:19], s57 offen offset:192\n"
    "s_waitcnt lgkmcnt(0)\n"
    "s_add_u32 s57, s42, 0\n"
    "buffer_load_dwordx4 v13, s[20:23], s57 offen lds\n"
    "buffer_load_dwordx4 v14, s[20:23], s57 offen offset:1024 lds\n"
    "buffer_load_dwordx4 v13, s[20:23], s57 offen offset:2048 lds\n"
    "buffer_load_dwordx4 v14, s[20:23], s57 offen offset:3072 lds\n"
    "s_waitcnt vmcnt(0)\n"
    "ds_read_b64_tr_b16 a[96:97], v56\n"
    "ds_read_b64_tr_b16 a[100:101], v57\n"
    "ds_read_b64_tr_b16 a[104:105], v58\n"
    "ds_read_b64_tr_b16 a[108:109], v59\n"
    "ds_read_b64_tr_b16 a[112:113], v60\n"
    "ds_read_b64_tr_b16 a[116:117], v61\n"
    "ds_read_b64_tr_b16 a[120:121], v62\n"
    "ds_read_b64_tr_b16 a[124:125], v63\n"
    "s_waitcnt lgkmcnt(0)\n"
    "s_add_u32 s57, s42, 4096\n"
    "buffer_load_dwordx4 v13, s[20:23], s57 offen lds\n"
    "buffer_load_dwordx4 v14, s[20:23], s57 offen offset:1024 lds\n"
    "buffer_load_dwordx4 v13, s[20:23], s57 offen offset:2048 lds\n"
    "buffer_load_dwordx4 v14, s[20:23], s57 offen offset:3072 lds\n"
    "s_waitcnt vmcnt(0)\n"
    "ds_read_b64_tr_b16 a[98:99], v56\n"
    "ds_read_b64_tr_b16 a[102:103], v57\n"
    "ds_read_b64_tr_b16 a[106:107], v58\n"
    "ds_read_b64_tr_b16 a[110:111], v59\n"
    "ds_read_b64_tr_b16 a[114:115], v60\n"
    "ds_read_b64_tr_b16 a[118:119], v61\n"
    "ds_read_b64_tr_b16 a[122:123], v62\n"
    "ds_read_b64_tr_b16 a[126:127], v63\n"
    "v_mfma_f32_16x16x32_bf16 v[160:163], a[64:67], a[176:179], v[232:235]\n"
    "v_mfma_f32_16x16x32_bf16 v[160:163], a[68:71], a[180:183], v[160:163]\n"
    "v_mfma_f32_16x16x32_bf16 v[160:163], a[72:75], a[184:187], v[160:163]\n"
    "v_mfma_f32_16x16x32_bf16 v[160:163], a[76:79], a[188:191], v[160:163]\n"
    "v_mfma_f32_16x16x32_bf16 v[184:187], a[64:67], a[192:195], v[236:239]\n"
    "v_mfma_f32_16x16x32_bf16 v[184:187], a[68:71], a[196:199], v[184:187]\n"
    "v_mfma_f32_16x16x32_bf16 v[184:187], a[72:75], a[200:203], v[184:187]\n"
    "v_mfma_f32_16x16x32_bf16 v[184:187], a[76:79], a[204:207], v[184:187]\n"
    "v_mfma_f32_16x16x32_bf16 v[176:179], a[80:83], a[176:179], v[232:235]\n"
    "v_mfma_f32_16x16x32_bf16 v[176:179], a[84:87], a[180:183], v[176:179]\n"
    "v_mfma_f32_16x16x32_bf16 v[176:179], a[88:91], a[184:187], v[176:179]\n"
    "v_mfma_f32_16x16x32_bf16 v[176:179], a[92:95], a[188:191], v[176:179]\n"
    "v_mfma_f32_16x16x32_bf16 v[200:203], a[80:83], a[192:195], v[236:239]\n"
    "v_mfma_f32_16x16x32_bf16 v[200:203], a[84:87], a[196:199], v[200:203]\n"
    "v_mfma_f32_16x16x32_bf16 v[200:203], a[88:91], a[200:203], v[200:203]\n"
    "v_mfma_f32_16x16x32_bf16 v[200:203], a[92:95], a[204:207], v[200:203]\n"
    "s_nop 7\n"
    "v_exp_f32 v160, v160\n"
    "v_exp_f32 v161, v161\n"
    "v_exp_f32 v162, v162\n"
    "v_exp_f32 v163, v163\n"
    "v_exp_f32 v176, v176\n"
    "v_exp_f32 v177, v177\n"
    "v_exp_f32 v178, v178\n"
    "v_exp_f32 v179, v179\n"
    "v_exp_f32 v184, v184\n"
    "v_exp_f32 v185, v185\n"
    "v_exp_f32 v186, v186\n"
    "v_exp_f32 v187, v187\n"
    "v_exp_f32 v200, v200\n"
    "v_exp_f32 v201, v201\n"
    "v_exp_f32 v202, v202\n"
    "v_exp_f32 v203, v203\n"
    "s_nop 0\n"
    "v_cvt_pk_bf16_f32 v160, v160, v161\n"
    "v_cvt_pk_bf16_f32 v161, v162, v163\n"
    "v_cvt_pk_bf16_f32 v162, v176, v177\n"
    "v_cvt_pk_bf16_f32 v163, v178, v179\n"
    "v_cvt_pk_bf16_f32 v184, v184, v185\n"
    "v_cvt_pk_bf16_f32 v185, v186, v187\n"
    "v_cvt_pk_bf16_f32 v186, v200, v201\n"
    "v_cvt_pk_bf16_f32 v187, v202, v203\n"
    "s_waitcnt lgkmcnt(0)\n"
    "s_nop 0\n"
    "v_mfma_f32_16x16x32_bf16 v[64:67], a[96:99], v[160:163], v[64:67]\n"
    "v_mfma_f32_16x16x32_bf16 v[68:71], a[96:99], v[184:187], v[68:71]\n"
    "v_mfma_f32_16x16x32_bf16 v[76:79], a[100:103], v[160:163], v[76:79]\n"
    "v_mfma_f32_16x16x32_bf16 v[80:83], a[100:103], v[184:187], v[80:83]\n"
    "v_mfma_f32_16x16x32_bf16 v[88:91], a[104:107], v[160:163], v[88:91]\n"
    "v_mfma_f32_16x16x32_bf16 v[92:95], a[104:107], v[184:187], v[92:95]\n"
    "v_mfma_f32_16x16x32_bf16 v[100:103], a[108:111], v[160:163], v[100:103]\n"
    "v_mfma_f32_16x16x32_bf16 v[104:107], a[108:111], v[184:187], v[104:107]\n"
    "v_mfma_f32_16x16x32_bf16 v[112:115], a[112:115], v[160:163], v[112:115]\n"
    "v_mfma_f32_16x16x32_bf16 v[116:119], a[112:115], v[184:187], v[116:119]\n"
    "v_mfma_f32_16x16x32_bf16 v[124:127], a[116:119], v[160:163], v[124:127]\n"
    "v_mfma_f32_16x16x32_bf16 v[128:131], a[116:119], v[184:187], v[128:131]\n"
    "v_mfma_f32_16x16x32_bf16 v[136:139], a[120:123], v[160:163], v[136:139]\n"
    "v_mfma_f32_16x16x32_bf16 v[140:143], a[120:123], v[184:187], v[140:143]\n"
    "v_mfma_f32_16x16x32_bf16 v[148:151], a[124:127], v[160:163], v[148:151]\n"
    "v_mfma_f32_16x16x32_bf16 v[152:155], a[124:127], v[184:187], v[152:155]\n"
    "v_mfma_f32_16x16x32_bf16 v[244:247], a[224:227], v[160:163], v[244:247]\n"
    "v_mfma_f32_16x16x32_bf16 v[248:251], a[224:227], v[184:187], v[248:251]\n"
    "s_add_u32 s42, s42, 8192\n"
    "s_cmp_lg_u32 s42, s46\n"
    "s_cbranch_scc1 .L_fb2D_%=\n"
    "s_nop 7\n"
    "s_mov_b32 m0, s44\n"
    "s_cmp_eq_u32 s55, 0\n"
    "s_cbranch_scc1 .L_fbretD0_%=\n"
    "s_branch .L_fbretD1_%=\n"
    ".L_pathC_%=:\n"
    "s_load_dwordx2 s[62:63], %4, 0x100\n"
    "s_mov_b64 s[16:17], %2\n"
    "s_and_b32 s17, s17, 0xffff\n"
    "s_mov_b32 s18, 2228224\n"
    "s_mov_b32 s19, 0x20000\n"
    "s_mov_b64 s[20:21], %3\n"
    "s_and_b32 s21, s21, 0xffff\n"
    "s_mov_b32 s22, 2228224\n"
    "s_mov_b32 s23, 0x20000\n"
    "s_mov_b64 s[24:25], %4\n"
    "s_and_b32 s25, s25, 0xffff\n"
    "s_mov_b32 s26, 2162688\n"
    "s_mov_b32 s27, 0x20000\n"
    "s_add_u32 s24, s24, 4096\n"
    "s_addc_u32 s25, s25, 0\n"
    "v_lshlrev_b32 v37, 4, v44\n"
    "v_lshlrev_b32 v52, 1, v45\n"
    "v_xor_b32 v52, v46, v52\n"
    "v_lshlrev_b32 v52, 4, v52\n"
    "v_lshl_add_u32 v13, v45, 8, v52\n"
    "v_xor_b32 v14, 128, v13\n"
    "v_xor_b32 v52, v46, v45\n"
    "v_lshlrev_b32 v52, 4, v52\n"
    "v_lshl_add_u32 v9, v45, 8, v52\n"
    "v_lshl_add_u32 v33, v46, 8, v52\n"
    "v_xor_b32 v10, 64, v9\n"
    "v_xor_b32 v34, 64, v33\n"
    "v_xor_b32 v11, 128, v9\n"
    "v_xor_b32 v35, 128, v33\n"
    "v_xor_b32 v12, 192, v9\n"
    "v_xor_b32 v36, 192, v33\n"
    "s_mul_i32 s45, s36, 6144\n"
    "s_add_u32 s45, s45, 65536\n"
    "v_lshrrev_b32 v52, 1, v46\n"
    "v_xor_b32 v52, v52, v45\n"
    "v_lshlrev_b32 v52, 4, v52\n"
    "v_lshl_add_u32 v52, v46, 7, v52\n"
    "v_add_u32 v15, s45, v52\n"
    "v_xor_b32 v16, 64, v15\n"
    "v_add_u32 v38, 71680, v15\n"
    "v_add_u32 v39, 71680, v16\n"
    "v_lshl_add_u32 v54, v45, 3, v47\n"
    "v_lshlrev_b32 v54, 8, v54\n"
    "v_lshl_add_u32 v54, v48, 3, v54\n"
    "v_lshl_add_u32 v54, v49, 5, v54\n"
    "v_add_u32 v17, 104448, v54\n"
    "v_lshl_add_u32 v55, v45, 2, v47\n"
    "v_lshlrev_b32 v55, 8, v55\n"
    "v_lshl_add_u32 v55, v48, 3, v55\n"
    "v_lshl_add_u32 v55, v49, 5, v55\n"
    "v_add_u32 v25, 65536, v55\n"
    "v_xor_b32 v18, 32, v17\n"
    "v_xor_b32 v26, 32, v25\n"
    "v_xor_b32 v19, 64, v17\n"
    "v_xor_b32 v27, 64, v25\n"
    "v_xor_b32 v20, 96, v17\n"
    "v_xor_b32 v28, 96, v25\n"
    "v_xor_b32 v21, 128, v17\n"
    "v_xor_b32 v29, 128, v25\n"
    "v_xor_b32 v22, 160, v17\n"
    "v_xor_b32 v30, 160, v25\n"
    "v_xor_b32 v23, 192, v17\n"
    "v_xor_b32 v31, 192, v25\n"
    "v_xor_b32 v24, 224, v17\n"
    "v_xor_b32 v32, 224, v25\n"
    "s_mov_b32 s41, 0x4000\n"
    "s_mov_b32 s42, 0xffff8000\n"
    "s_mov_b32 s58, 0x3e0293ee\n"
    "s_mov_b32 s59, 0x3e0293ee\n"
    "s_mov_b32 s38, 0x8000\n"
    "s_mov_b32 s60, 2097152\n"
    "s_lshl_b32 s61, s36, 12\n"
    "s_add_u32 s61, s61, 147456\n"
    "v_mov_b32 v52, 0x3f803f80\n"
    "v_accvgpr_write_b32 a224, v52\n"
    "v_accvgpr_write_b32 a228, 0\n"
    "v_accvgpr_write_b32 a225, v52\n"
    "v_accvgpr_write_b32 a229, 0\n"
    "v_accvgpr_write_b32 a226, v52\n"
    "v_accvgpr_write_b32 a230, 0\n"
    "v_accvgpr_write_b32 a227, v52\n"
    "v_accvgpr_write_b32 a231, 0\n"
    "v_and_b32 v42, 15, v44\n"
    "v_lshlrev_b32 v42, 3, v42\n"
    "s_lshl_b32 s47, s36, 12\n"
    "s_getpc_b64 s[50:51]\n"
    ".L_pc_%=:\n"
    "s_sub_u32 s50, s50, .L_pc_%=-.L_start_%=\n"
    "s_subb_u32 s51, s51, 0\n"
    "s_mul_i32 s45, %6, 3\n"
    "s_add_u32 s45, s45, s36\n"
    "s_mul_i32 s45, s45, 10\n"
    "v_min_u32 v52, 9, v44\n"
    "v_add_u32 v52, s45, v52\n"
    "v_lshlrev_b32 v52, 7, v52\n"
    "v_min_u32 v52, .L_end_%=-.L_start_%=-4, v52\n"
    "v_mov_b32 v3, s51\n"
    "v_add_co_u32 v2, vcc, s50, v52\n"
    "s_nop 1\n"
    "v_addc_co_u32 v3, vcc, 0, v3, vcc\n"
    "global_load_dword v1, v[2:3], off\n"
    "s_mov_b64 s[28:29], %8\n"
    "s_and_b32 s29, s29, 0xffff\n"
    "s_mov_b32 s30, 8388608\n"
    "s_mov_b32 s31, 0x20000\n"
    "v_lshlrev_b32 v52, 7, v44\n"
    "v_mov_b32 v53, 0x7ff00000\n"
    "v_cmp_gt_u32 vcc, 43, v44\n"
    "s_lshr_b32 s52, %11, 8\n"
    "s_lshl_b32 s52, s52, 7\n"
    "v_cndmask_b32 v5, v53, v52, vcc\n"
    "v_mfma_f32_32x32x16_bf16 v[64:79], a[228:231], a[228:231], 0\n"
    "v_mfma_f32_32x32x16_bf16 v[80:95], a[228:231], a[228:231], 0\n"
    "v_mfma_f32_32x32x16_bf16 v[96:111], a[228:231], a[228:231], 0\n"
    "v_mfma_f32_32x32x16_bf16 v[112:127], a[228:231], a[228:231], 0\n"
    "v_mfma_f32_32x32x16_bf16 v[128:143], a[228:231], a[228:231], 0\n"
    "v_mfma_f32_32x32x16_bf16 v[144:159], a[228:231], a[228:231], 0\n"
    "v_mfma_f32_32x32x16_bf16 v[160:175], a[228:231], a[228:231], 0\n"
    "v_mfma_f32_32x32x16_bf16 v[176:191], a[228:231], a[228:231], 0\n"
    "v_mfma_f32_32x32x16_bf16 v[192:207], a[228:231], a[228:231], 0\n"
    "v_mfma_f32_32x32x16_bf16 v[208:223], a[228:231], a[228:231], 0\n"
    "v_mfma_f32_32x32x16_bf16 v[224:239], a[228:231], a[228:231], 0\n"
    "v_mfma_f32_32x32x16_bf16 v[240:255], a[228:231], a[228:231], 0\n"
    "s_barrier\n"
    "ds_read_b64_tr_b16 a[0:1], v17 offset:0\n"
    "ds_read_b64_tr_b16 a[2:3], v17 offset:1024\n"
    "ds_read_b64_tr_b16 a[4:5], v17 offset:8192\n"
    "ds_read_b64_tr_b16 a[6:7], v17 offset:9216\n"
    "ds_read_b64_tr_b16 a[8:9], v18 offset:0\n"
    "ds_read_b64_tr_b16 a[10:11], v18 offset:1024\n"
    "ds_read_b64_tr_b16 a[12:13], v18 offset:8192\n"
    "ds_read_b64_tr_b16 a[14:15], v18 offset:9216\n"
    "ds_read_b64_tr_b16 a[16:17], v19 offset:0\n"
    "ds_read_b64_tr_b16 a[18:19], v19 offset:1024\n"
    "ds_read_b64_tr_b16 a[20:21], v19 offset:8192\n"
    "ds_read_b64_tr_b16 a[22:23], v19 offset:9216\n"
    "ds_read_b64_tr_b16 a[24:25], v20 offset:0\n"
    "ds_read_b64_tr_b16 a[26:27], v20 offset:1024\n"
    "ds_read_b64_tr_b16 a[28:29], v20 offset:8192\n"
    "ds_read_b64_tr_b16 a[30:31], v20 offset:9216\n"
    "ds_read_b64_tr_b16 a[32:33], v21 offset:0\n"
    "ds_read_b64_tr_b16 a[34:35], v21 offset:1024\n"
    "ds_read_b64_tr_b16 a[36:37], v21 offset:8192\n"
    "ds_read_b64_tr_b16 a[38:39], v21 offset:9216\n"
    "ds_read_b64_tr_b16 a[40:41], v22 offset:0\n"
    "ds_read_b64_tr_b16 a[42:43], v22 offset:1024\n"
    "ds_read_b64_tr_b16 a[44:45], v22 offset:8192\n"
    "ds_read_b64_tr_b16 a[46:47], v22 offset:9216\n"
    "ds_read_b64_tr_b16 a[48:49], v23 offset:0\n"
    "ds_read_b64_tr_b16 a[50:51], v23 offset:1024\n"
    "ds_read_b64_tr_b16 a[52:53], v23 offset:8192\n"
    "ds_read_b64_tr_b16 a[54:55], v23 offset:9216\n"
    "ds_read_b64_tr_b16 a[56:57], v24 offset:0\n"
    "ds_read_b64_tr_b16 a[58:59], v24 offset:1024\n"
    "ds_read_b64_tr_b16 a[60:61], v24 offset:8192\n"
    "ds_read_b64_tr_b16 a[62:63], v24 offset:9216\n"
    "ds_read_b128 a[128:131], v15 offset:0\n"
    "ds_read_b128 a[132:135], v16 offset:0\n"
    "ds_read_b128 a[136:139], v15 offset:2048\n"
    "ds_read_b128 a[140:143], v16 offset:2048\n"
    "ds_read_b128 a[144:147], v15 offset:4096\n"
    "ds_read_b128 a[148:151], v16 offset:4096\n"
    "v_add_u32 v17, s41, v17\n"
    "v_add_u32 v18, s41, v18\n"
    "v_add_u32 v19, s41, v19\n"
    "v_add_u32 v20, s41, v20\n"
    "v_add_u32 v21, s41, v21\n"
    "v_add_u32 v22, s41, v22\n"
    "v_add_u32 v23, s41, v23\n"
    "v_add_u32 v24, s41, v24\n"
    "s_mov_b32 s37, 7\n"
    "s_cmp_eq_u32 s36, 2\n"
    "s_cbranch_scc1 .L_qC_%=B\n"
    ".L_qC_%=A:\n"
    "s_waitcnt lgkmcnt(0)\n"
    "s_barrier\n"
    "v_mfma_f32_16x16x32_bf16 v[160:163], a[0:3], a[128:131], v[160:163]\n"
    "s_cmp_eq_u32 s38, 0x8000\n"
    "ds_read_b64_tr_b16 a[64:65], v17 offset:0\n"
    "v_mfma_f32_16x16x32_bf16 v[160:163], a[4:7], a[132:135], v[160:163]\n"
    "s_cselect_b32 s40, s42, s41\n"
    "ds_read_b64_tr_b16 a[66:67], v17 offset:1024\n"
    "v_mfma_f32_16x16x32_bf16 v[164:167], a[4:7], a[140:143], v[164:167]\n"
    "ds_read_b64_tr_b16 a[68:69], v17 offset:8192\n"
    "v_mfma_f32_16x16x32_bf16 v[164:167], a[0:3], a[136:139], v[164:167]\n"
    "ds_read_b64_tr_b16 a[70:71], v17 offset:9216\n"
    "v_add_u32 v17, s40, v17\n"
    "v_mfma_f32_16x16x32_bf16 v[168:171], a[0:3], a[144:147], v[168:171]\n"
    "ds_read_b64_tr_b16 a[72:73], v18 offset:0\n"
    "v_mfma_f32_16x16x32_bf16 v[168:171], a[4:7], a[148:151], v[168:171]\n"
    "ds_read_b64_tr_b16 a[74:75], v18 offset:1024\n"
    "v_mfma_f32_16x16x32_bf16 v[180:183], a[12:15], a[148:151], v[180:183]\n"
    "ds_read_b64_tr_b16 a[76:77], v18 offset:8192\n"
    "v_mfma_f32_16x16x32_bf16 v[180:183], a[8:11], a[144:147], v[180:183]\n"
    "ds_read_b64_tr_b16 a[78:79], v18 offset:9216\n"
    "v_add_u32 v18, s40, v18\n"
    "v_mfma_f32_16x16x32_bf16 v[176:179], a[8:11], a[136:139], v[176:179]\n"
    "ds_read_b64_tr_b16 a[80:81], v19 offset:0\n"
    "v_mfma_f32_16x16x32_bf16 v[176:179], a[12:15], a[140:143], v[176:179]\n"
    "ds_read_b64_tr_b16 a[82:83], v19 offset:1024\n"
    "v_mfma_f32_16x16x32_bf16 v[172:175], a[12:15], a[132:135], v[172:175]\n"
    "ds_read_b64_tr_b16 a[84:85], v19 offset:8192\n"
    "v_mfma_f32_16x16x32_bf16 v[172:175], a[8:11], a[128:131], v[172:175]\n"
    "ds_read_b64_tr_b16 a[86:87], v19 offset:9216\n"
    "v_add_u32 v19, s40, v19\n"
    "v_mfma_f32_16x16x32_bf16 v[184:187], a[16:19], a[128:131], v[184:187]\n"
    "ds_read_b64_tr_b16 a[88:89], v20 offset:0\n"
    "v_mfma_f32_16x16x32_bf16 v[184:187], a[20:23], a[132:135], v[184:187]\n"
    "ds_read_b64_tr_b16 a[90:91], v20 offset:1024\n"
    "v_mfma_f32_16x16x32_bf16 v[188:191], a[20:23], a[140:143], v[188:191]\n"
    "ds_read_b64_tr_b16 a[92:93], v20 offset:8192\n"
    "v_mfma_f32_16x16x32_bf16 v[188:191], a[16:19], a[136:139], v[188:191]\n"
    "ds_read_b64_tr_b16 a[94:95], v20 offset:9216\n"
    "v_add_u32 v20, s40, v20\n"
    "v_mfma_f32_16x16x32_bf16 v[192:195], a[16:19], a[144:147], v[192:195]\n"
    "ds_read_b64_tr_b16 a[96:97], v21 offset:0\n"
    "v_mfma_f32_16x16x32_bf16 v[192:195], a[20:23], a[148:151], v[192:195]\n"
    "ds_read_b64_tr_b16 a[98:99], v21 offset:1024\n"
    "v_mfma_f32_16x16x32_bf16 v[204:207], a[28:31], a[148:151], v[204:207]\n"
    "ds_read_b64_tr_b16 a[100:101], v21 offset:8192\n"
    "v_mfma_f32_16x16x32_bf16 v[204:207], a[24:27], a[144:147], v[204:207]\n"
    "ds_read_b64_tr_b16 a[102:103], v21 offset:9216\n"
    "v_add_u32 v21, s40, v21\n"
    "v_mfma_f32_16x16x32_bf16 v[200:203], a[24:27], a[136:139], v[200:203]\n"
    "ds_read_b64_tr_b16 a[104:105], v22 offset:0\n"
    "v_mfma_f32_16x16x32_bf16 v[200:203], a[28:31], a[140:143], v[200:203]\n"
    "ds_read_b64_tr_b16 a[106:107], v22 offset:1024\n"
    "v_mfma_f32_16x16x32_bf16 v[196:199], a[28:31], a[132:135], v[196:199]\n"
    "ds_read_b64_tr_b16 a[108:109], v22 offset:8192\n"
    "v_mfma_f32_16x16x32_bf16 v[196:199], a[24:27], a[128:131], v[196:199]\n"
    "ds_read_b64_tr_b16 a[110:111], v22 offset:9216\n"
    "v_add_u32 v22, s40, v22\n"
    "v_mfma_f32_16x16x32_bf16 v[208:211], a[32:35], a[128:131], v[208:211]\n"
    "ds_read_b64_tr_b16 a[112:113], v23 offset:0\n"
    "v_mfma_f32_16x16x32_bf16 v[208:211], a[36:39], a[132:135], v[208:211]\n"
    "ds_read_b64_tr_b16 a[114:115], v23 offset:1024\n"
    "v_mfma_f32_16x16x32_bf16 v[212:215], a[36:39], a[140:143], v[212:215]\n"
    "ds_read_b64_tr_b16 a[116:117], v23 offset:8192\n"
    "v_mfma_f32_16x16x32_bf16 v[212:215], a[32:35], a[136:139], v[212:215]\n"
    "ds_read_b64_tr_b16 a[118:119], v23 offset:9216\n"
    "v_add_u32 v23, s40, v23\n"
    "v_mfma_f32_16x16x32_bf16 v[216:219], a[32:35], a[144:147], v[216:219]\n"
    "ds_read_b64_tr_b16 a[120:121], v24 offset:0\n"
    "v_mfma_f32_16x16x32_bf16 v[216:219], a[36:39], a[148:151], v[216:219]\n"
    "ds_read_b64_tr_b16 a[122:123], v24 offset:1024\n"
    "v_mfma_f32_16x16x32_bf16 v[228:231], a[44:47], a[148:151], v[228:231]\n"
    "ds_read_b64_tr_b16 a[124:125], v24 offset:8192\n"
    "v_mfma_f32_16x16x32_bf16 v[228:231], a[40:43], a[144:147], v[228:231]\n"
    "ds_read_b64_tr_b16 a[126:127], v24 offset:9216\n"
    "v_add_u32 v24, s40, v24\n"
    "v_mfma_f32_16x16x32_bf16 v[224:227], a[40:43], a[136:139], v[224:227]\n"
    "v_mfma_f32_16x16x32_bf16 v[224:227], a[44:47], a[140:143], v[224:227]\n"
    "v_mfma_f32_16x16x32_bf16 v[220:223], a[44:47], a[132:135], v[220:223]\n"
    "v_mfma_f32_16x16x32_bf16 v[220:223], a[40:43], a[128:131], v[220:223]\n"
    "v_mfma_f32_16x16x32_bf16 v[232:235], a[48:51], a[128:131], v[232:235]\n"
    "v_mfma_f32_16x16x32_bf16 v[232:235], a[52:55], a[132:135], v[232:235]\n"
    "v_mfma_f32_16x16x32_bf16 v[236:239], a[52:55], a[140:143], v[236:239]\n"
    "v_mfma_f32_16x16x32_bf16 v[236:239], a[48:51], a[136:139], v[236:239]\n"
    "v_mfma_f32_16x16x32_bf16 v[240:243], a[48:51], a[144:147], v[240:243]\n"
    "v_mfma_f32_16x16x32_bf16 v[240:243], a[52:55], a[148:151], v[240:243]\n"
    "s_mov_b32 m0, s47\n"
    "v_mfma_f32_16x16x32_bf16 v[252:255], a[60:63], a[148:151], v[252:255]\n"
    "buffer_load_dwordx4 v9, s[16:19], s47 offen lds\n"
    "v_mfma_f32_16x16x32_bf16 v[252:255], a[56:59], a[144:147], v[252:255]\n"
    "buffer_load_dwordx4 v10, s[16:19], s47 offen offset:1024 lds\n"
    "v_mfma_f32_16x16x32_bf16 v[248:251], a[56:59], a[136:139], v[248:251]\n"
    "buffer_load_dwordx4 v11, s[16:19], s47 offen offset:2048 lds\n"
    "v_mfma_f32_16x16x32_bf16 v[248:251], a[60:63], a[140:143], v[248:251]\n"
    "buffer_load_dwordx4 v12, s[16:19], s47 offen offset:3072 lds\n"
    "v_mfma_f32_16x16x32_bf16 v[244:247], a[60:63], a[132:135], v[244:247]\n"
    "v_mfma_f32_16x16x32_bf16 v[244:247], a[56:59], a[128:131], v[244:247]\n"
    "s_mov_b32 s39, s38\n"
    "s_add_u32 s38, s38, s40\n"
    "s_waitcnt lgkmcnt(0)\n"
    "s_barrier\n"
    "v_mfma_f32_16x16x32_bf16 v[64:67], a[64:67], a[128:131], v[64:67]\n"
    "s_cmp_eq_u32 s38, 0x8000\n"
    "ds_read_b64_tr_b16 a[0:1], v17 offset:0\n"
    "v_mfma_f32_16x16x32_bf16 v[64:67], a[68:71], a[132:135], v[64:67]\n"
    "s_cselect_b32 s40, s42, s41\n"
    "ds_read_b64_tr_b16 a[2:3], v17 offset:1024\n"
    "v_mfma_f32_16x16x32_bf16 v[68:71], a[68:71], a[140:143], v[68:71]\n"
    "ds_read_b64_tr_b16 a[4:5], v17 offset:8192\n"
    "v_mfma_f32_16x16x32_bf16 v[68:71], a[64:67], a[136:139], v[68:71]\n"
    "ds_read_b64_tr_b16 a[6:7], v17 offset:9216\n"
    "v_add_u32 v17, s40, v17\n"
    "v_mfma_f32_16x16x32_bf16 v[72:75], a[64:67], a[144:147], v[72:75]\n"
    "ds_read_b64_tr_b16 a[8:9], v18 offset:0\n"
    "v_mfma_f32_16x16x32_bf16 v[72:75], a[68:71], a[148:151], v[72:75]\n"
    "ds_read_b64_tr_b16 a[10:11], v18 offset:1024\n"
    "v_mfma_f32_16x16x32_bf16 v[84:87], a[76:79], a[148:151], v[84:87]\n"
    "ds_read_b64_tr_b16 a[12:13], v18 offset:8192\n"
    "v_mfma_f32_16x16x32_bf16 v[84:87], a[72:75], a[144:147], v[84:87]\n"
    "ds_read_b64_tr_b16 a[14:15], v18 offset:9216\n"
    "v_add_u32 v18, s40, v18\n"
    "v_mfma_f32_16x16x32_bf16 v[80:83], a[72:75], a[136:139], v[80:83]\n"
    "ds_read_b64_tr_b16 a[16:17], v19 offset:0\n"
    "v_mfma_f32_16x16x32_bf16 v[80:83], a[76:79], a[140:143], v[80:83]\n"
    "ds_read_b64_tr_b16 a[18:19], v19 offset:1024\n"
    "v_mfma_f32_16x16x32_bf16 v[76:79], a[76:79], a[132:135], v[76:79]\n"
    "ds_read_b64_tr_b16 a[20:21], v19 offset:8192\n"
    "v_mfma_f32_16x16x32_bf16 v[76:79], a[72:75], a[128:131], v[76:79]\n"
    "ds_read_b64_tr_b16 a[22:23], v19 offset:9216\n"
    "v_add_u32 v19, s40, v19\n"
    "v_mfma_f32_16x16x32_bf16 v[88:91], a[80:83], a[128:131], v[88:91]\n"
    "ds_read_b64_tr_b16 a[24:25], v20 offset:0\n"
    "v_mfma_f32_16x16x32_bf16 v[88:91], a[84:87], a[132:135], v[88:91]\n"
    "ds_read_b64_tr_b16 a[26:27], v20 offset:1024\n"
    "v_mfma_f32_16x16x32_bf16 v[92:95], a[84:87], a[140:143], v[92:95]\n"
    "ds_read_b64_tr_b16 a[28:29], v20 offset:8192\n"
    "v_mfma_f32_16x16x32_bf16 v[92:95], a[80:83], a[136:139], v[92:95]\n"
    "ds_read_b64_tr_b16 a[30:31], v20 offset:9216\n"
    "v_add_u32 v20, s40, v20\n"
    "v_mfma_f32_16x16x32_bf16 v[96:99], a[80:83], a[144:147], v[96:99]\n"
    "ds_read_b64_tr_b16 a[32:33], v21 offset:0\n"
    "v_mfma_f32_16x16x32_bf16 v[96:99], a[84:87], a[148:151], v[96:99]\n"
    "ds_read_b64_tr_b16 a[34:35], v21 offset:1024\n"
    "v_mfma_f32_16x16x32_bf16 v[108:111], a[92:95], a[148:151], v[108:111]\n"
    "ds_read_b64_tr_b16 a[36:37], v21 offset:8192\n"
    "v_mfma_f32_16x16x32_bf16 v[108:111], a[88:91], a[144:147], v[108:111]\n"
    "ds_read_b64_tr_b16 a[38:39], v21 offset:9216\n"
    "v_add_u32 v21, s40, v21\n"
    "v_mfma_f32_16x16x32_bf16 v[104:107], a[88:91], a[136:139], v[104:107]\n"
    "ds_read_b64_tr_b16 a[40:41], v22 offset:0\n"
    "v_mfma_f32_16x16x32_bf16 v[104:107], a[92:95], a[140:143], v[104:107]\n"
    "ds_read_b64_tr_b16 a[42:43], v22 offset:1024\n"
    "v_mfma_f32_16x16x32_bf16 v[100:103], a[92:95], a[132:135], v[100:103]\n"
    "ds_read_b64_tr_b16 a[44:45], v22 offset:8192\n"
    "v_mfma_f32_16x16x32_bf16 v[100:103], a[88:91], a[128:131], v[100:103]\n"
    "ds_read_b64_tr_b16 a[46:47], v22 offset:9216\n"
    "v_add_u32 v22, s40, v22\n"
    "v_mfma_f32_16x16x32_bf16 v[112:115], a[96:99], a[128:131], v[112:115]\n"
    "ds_read_b64_tr_b16 a[48:49], v23 offset:0\n"
    "v_mfma_f32_16x16x32_bf16 v[112:115], a[100:103], a[132:135], v[112:115]\n"
    "ds_read_b64_tr_b16 a[50:51], v23 offset:1024\n"
    "v_mfma_f32_16x16x32_bf16 v[116:119], a[100:103], a[140:143], v[116:119]\n"
    "ds_read_b64_tr_b16 a[52:53], v23 offset:8192\n"
    "v_mfma_f32_16x16x32_bf16 v[116:119], a[96:99], a[136:139], v[116:119]\n"
    "ds_read_b64_tr_b16 a[54:55], v23 offset:9216\n"
    "v_add_u32 v23, s40, v23\n"
    "v_mfma_f32_16x16x32_bf16 v[120:123], a[96:99], a[144:147], v[120:123]\n"
    "ds_read_b64_tr_b16 a[56:57], v24 offset:0\n"
    "v_mfma_f32_16x16x32_bf16 v[120:123], a[100:103], a[148:151], v[120:123]\n"
    "ds_read_b64_tr_b16 a[58:59], v24 offset:1024\n"
    "v_mfma_f32_16x16x32_bf16 v[132:135], a[108:111], a[148:151], v[132:135]\n"
    "ds_read_b64_tr_b16 a[60:61], v24 offset:8192\n"
    "v_mfma_f32_16x16x32_bf16 v[132:135], a[104:107], a[144:147], v[132:135]\n"
    "ds_read_b64_tr_b16 a[62:63], v24 offset:9216\n"
    "v_add_u32 v24, s40, v24\n"
    "v_mfma_f32_16x16x32_bf16 v[128:131], a[104:107], a[136:139], v[128:131]\n"
    "ds_read_b128 a[152:155], v38 offset:0\n"
    "v_mfma_f32_16x16x32_bf16 v[128:131], a[108:111], a[140:143], v[128:131]\n"
    "ds_read_b128 a[156:159], v39 offset:0\n"
    "v_mfma_f32_16x16x32_bf16 v[124:127], a[108:111], a[132:135], v[124:127]\n"
    "ds_read_b128 a[160:163], v38 offset:2048\n"
    "v_mfma_f32_16x16x32_bf16 v[124:127], a[104:107], a[128:131], v[124:127]\n"
    "ds_read_b128 a[164:167], v39 offset:2048\n"
    "v_mfma_f32_16x16x32_bf16 v[136:139], a[112:115], a[128:131], v[136:139]\n"
    "ds_read_b128 a[168:171], v38 offset:4096\n"
    "v_mfma_f32_16x16x32_bf16 v[136:139], a[116:119], a[132:135], v[136:139]\n"
    "ds_read_b128 a[172:175], v39 offset:4096\n"
    "v_mfma_f32_16x16x32_bf16 v[140:143], a[116:119], a[140:143], v[140:143]\n"
    "v_mfma_f32_16x16x32_bf16 v[140:143], a[112:115], a[136:139], v[140:143]\n"
    "v_mfma_f32_16x16x32_bf16 v[144:147], a[112:115], a[144:147], v[144:147]\n"
    "v_mfma_f32_16x16x32_bf16 v[144:147], a[116:119], a[148:151], v[144:147]\n"
    "s_mov_b32 m0, s47\n"
    "v_mfma_f32_16x16x32_bf16 v[156:159], a[124:127], a[148:151], v[156:159]\n"
    "v_mfma_f32_16x16x32_bf16 v[156:159], a[120:123], a[144:147], v[156:159]\n"
    "v_mfma_f32_16x16x32_bf16 v[152:155], a[120:123], a[136:139], v[152:155]\n"
    "v_mfma_f32_16x16x32_bf16 v[152:155], a[124:127], a[140:143], v[152:155]\n"
    "v_mfma_f32_16x16x32_bf16 v[148:151], a[124:127], a[132:135], v[148:151]\n"
    "v_mfma_f32_16x16x32_bf16 v[148:151], a[120:123], a[128:131], v[148:151]\n"
    "s_mov_b32 s39, s38\n"
    "s_add_u32 s38, s38, s40\n"
    "s_waitcnt lgkmcnt(0)\n"
    "s_barrier\n"
    "v_mfma_f32_16x16x32_bf16 v[160:163], a[0:3], a[152:155], v[160:163]\n"
    "s_cmp_eq_u32 s38, 0x8000\n"
    "ds_read_b64_tr_b16 a[64:65], v17 offset:0\n"
    "v_mfma_f32_16x16x32_bf16 v[160:163], a[4:7], a[156:159], v[160:163]\n"
    "s_cselect_b32 s40, s42, s41\n"
    "ds_read_b64_tr_b16 a[66:67], v17 offset:1024\n"
    "v_mfma_f32_16x16x32_bf16 v[164:167], a[4:7], a[164:167], v[164:167]\n"
    "ds_read_b64_tr_b16 a[68:69], v17 offset:8192\n"
    "v_mfma_f32_16x16x32_bf16 v[164:167], a[0:3], a[160:163], v[164:167]\n"
    "ds_read_b64_tr_b16 a[70:71], v17 offset:9216\n"
    "v_add_u32 v17, s40, v17\n"
    "v_mfma_f32_16x16x32_bf16 v[168:171], a[0:3], a[168:171], v[168:171]\n"
    "ds_read_b64_tr_b16 a[72:73], v18 offset:0\n"
    "v_mfma_f32_16x16x32_bf16 v[168:171], a[4:7], a[172:175], v[168:171]\n"
    "ds_read_b64_tr_b16 a[74:75], v18 offset:1024\n"
    "v_mfma_f32_16x16x32_bf16 v[180:183], a[12:15], a[172:175], v[180:183]\n"
    "ds_read_b64_tr_b16 a[76:77], v18 offset:8192\n"
    "v_mfma_f32_16x16x32_bf16 v[180:183], a[8:11], a[168:171], v[180:183]\n"
    "ds_read_b64_tr_b16 a[78:79], v18 offset:9216\n"
    "v_add_u32 v18, s40, v18\n"
    "v_mfma_f32_16x16x32_bf16 v[176:179], a[8:11], a[160:163], v[176:179]\n"
    "ds_read_b64_tr_b16 a[80:81], v19 offset:0\n"
    "v_mfma_f32_16x16x32_bf16 v[176:179], a[12:15], a[164:167], v[176:179]\n"
    "ds_read_b64_tr_b16 a[82:83], v19 offset:1024\n"
    "v_mfma_f32_16x16x32_bf16 v[172:175], a[12:15], a[156:159], v[172:175]\n"
    "ds_read_b64_tr_b16 a[84:85], v19 offset:8192\n"
    "v_mfma_f32_16x16x32_bf16 v[172:175], a[8:11], a[152:155], v[172:175]\n"
    "ds_read_b64_tr_b16 a[86:87], v19 offset:9216\n"
    "v_add_u32 v19, s40, v19\n"
    "v_mfma_f32_16x16x32_bf16 v[184:187], a[16:19], a[152:155], v[184:187]\n"
    "ds_read_b64_tr_b16 a[88:89], v20 offset:0\n"
    "v_mfma_f32_16x16x32_bf16 v[184:187], a[20:23], a[156:159], v[184:187]\n"
    "ds_read_b64_tr_b16 a[90:91], v20 offset:1024\n"
    "v_mfma_f32_16x16x32_bf16 v[188:191], a[20:23], a[164:167], v[188:191]\n"
    "ds_read_b64_tr_b16 a[92:93], v20 offset:8192\n"
    "v_mfma_f32_16x16x32_bf16 v[188:191], a[16:19], a[160:163], v[188:191]\n"
    "ds_read_b64_tr_b16 a[94:95], v20 offset:9216\n"
    "v_add_u32 v20, s40, v20\n"
    "v_mfma_f32_16x16x32_bf16 v[192:195], a[16:19], a[168:171], v[192:195]\n"
    "ds_read_b64_tr_b16 a[96:97], v21 offset:0\n"
    "v_mfma_f32_16x16x32_bf16 v[192:195], a[20:23], a[172:175], v[192:195]\n"
    "ds_read_b64_tr_b16 a[98:99], v21 offset:1024\n"
    "v_mfma_f32_16x16x32_bf16 v[204:207], a[28:31], a[172:175], v[204:207]\n"
    "ds_read_b64_tr_b16 a[100:101], v21 offset:8192\n"
    "v_mfma_f32_16x16x32_bf16 v[204:207], a[24:27], a[168:171], v[204:207]\n"
    "ds_read_b64_tr_b16 a[102:103], v21 offset:9216\n"
    "v_add_u32 v21, s40, v21\n"
    "v_mfma_f32_16x16x32_bf16 v[200:203], a[24:27], a[160:163], v[200:203]\n"
    "ds_read_b64_tr_b16 a[104:105], v22 offset:0\n"
    "v_mfma_f32_16x16x32_bf16 v[200:203], a[28:31], a[164:167], v[200:203]\n"
    "ds_read_b64_tr_b16 a[106:107], v22 offset:1024\n"
    "v_mfma_f32_16x16x32_bf16 v[196:199], a[28:31], a[156:159], v[196:199]\n"
    "ds_read_b64_tr_b16 a[108:109], v22 offset:8192\n"
    "v_mfma_f32_16x16x32_bf16 v[196:199], a[24:27], a[152:155], v[196:199]\n"
    "ds_read_b64_tr_b16 a[110:111], v22 offset:9216\n"
    "v_add_u32 v22, s40, v22\n"
    "v_mfma_f32_16x16x32_bf16 v[208:211], a[32:35], a[152:155], v[208:211]\n"
    "ds_read_b64_tr_b16 a[112:113], v23 offset:0\n"
    "v_mfma_f32_16x16x32_bf16 v[208:211], a[36:39], a[156:159], v[208:211]\n"
    "ds_read_b64_tr_b16 a[114:115], v23 offset:1024\n"
    "v_mfma_f32_16x16x32_bf16 v[212:215], a[36:39], a[164:167], v[212:215]\n"
    "ds_read_b64_tr_b16 a[116:117], v23 offset:8192\n"
    "v_mfma_f32_16x16x32_bf16 v[212:215], a[32:35], a[160:163], v[212:215]\n"
    "ds_read_b64_tr_b16 a[118:119], v23 offset:9216\n"
    "v_add_u32 v23, s40, v23\n"
    "v_mfma_f32_16x16x32_bf16 v[216:219], a[32:35], a[168:171], v[216:219]\n"
    "ds_read_b64_tr_b16 a[120:121], v24 offset:0\n"
    "v_mfma_f32_16x16x32_bf16 v[216:219], a[36:39], a[172:175], v[216:219]\n"
    "ds_read_b64_tr_b16 a[122:123], v24 offset:1024\n"
    "v_mfma_f32_16x16x32_bf16 v[228:231], a[44:47], a[172:175], v[228:231]\n"
    "ds_read_b64_tr_b16 a[124:125], v24 offset:8192\n"
    "v_mfma_f32_16x16x32_bf16 v[228:231], a[40:43], a[168:171], v[228:231]\n"
    "ds_read_b64_tr_b16 a[126:127], v24 offset:9216\n"
    "v_add_u32 v24, s40, v24\n"
    "v_mfma_f32_16x16x32_bf16 v[224:227], a[40:43], a[160:163], v[224:227]\n"
    "v_mfma_f32_16x16x32_bf16 v[224:227], a[44:47], a[164:167], v[224:227]\n"
    "v_mfma_f32_16x16x32_bf16 v[220:223], a[44:47], a[156:159], v[220:223]\n"
    "v_mfma_f32_16x16x32_bf16 v[220:223], a[40:43], a[152:155], v[220:223]\n"
    "v_mfma_f32_16x16x32_bf16 v[232:235], a[48:51], a[152:155], v[232:235]\n"
    "v_mfma_f32_16x16x32_bf16 v[232:235], a[52:55], a[156:159], v[232:235]\n"
    "v_mfma_f32_16x16x32_bf16 v[236:239], a[52:55], a[164:167], v[236:239]\n"
    "v_mfma_f32_16x16x32_bf16 v[236:239], a[48:51], a[160:163], v[236:239]\n"
    "v_mfma_f32_16x16x32_bf16 v[240:243], a[48:51], a[168:171], v[240:243]\n"
    "v_mfma_f32_16x16x32_bf16 v[240:243], a[52:55], a[172:175], v[240:243]\n"
    "s_mov_b32 m0, s47\n"
    "v_mfma_f32_16x16x32_bf16 v[252:255], a[60:63], a[172:175], v[252:255]\n"
    "v_mfma_f32_16x16x32_bf16 v[252:255], a[56:59], a[168:171], v[252:255]\n"
    "v_mfma_f32_16x16x32_bf16 v[248:251], a[56:59], a[160:163], v[248:251]\n"
    "v_mfma_f32_16x16x32_bf16 v[248:251], a[60:63], a[164:167], v[248:251]\n"
    "v_mfma_f32_16x16x32_bf16 v[244:247], a[60:63], a[156:159], v[244:247]\n"
    "global_load_dword v1, v[2:3], off\n"
    "v_mfma_f32_16x16x32_bf16 v[244:247], a[56:59], a[152:155], v[244:247]\n"
    "s_mov_b32 s39, s38\n"
    "s_add_u32 s38, s38, s40\n"
    "s_cmp_eq_u32 s37, 0\n"
    "s_cbranch_scc1 .L_qC_%=x\n"
    "s_waitcnt lgkmcnt(0)\n"
    "s_barrier\n"
    "v_mfma_f32_16x16x32_bf16 v[64:67], a[64:67], a[152:155], v[64:67]\n"
    "s_cmp_eq_u32 s38, 0x8000\n"
    "ds_read_b64_tr_b16 a[0:1], v17 offset:0\n"
    "v_mfma_f32_16x16x32_bf16 v[64:67], a[68:71], a[156:159], v[64:67]\n"
    "s_cselect_b32 s40, s42, s41\n"
    "ds_read_b64_tr_b16 a[2:3], v17 offset:1024\n"
    "v_mfma_f32_16x16x32_bf16 v[68:71], a[68:71], a[164:167], v[68:71]\n"
    "ds_read_b64_tr_b16 a[4:5], v17 offset:8192\n"
    "v_mfma_f32_16x16x32_bf16 v[68:71], a[64:67], a[160:163], v[68:71]\n"
    "ds_read_b64_tr_b16 a[6:7], v17 offset:9216\n"
    "v_add_u32 v17, s40, v17\n"
    "v_mfma_f32_16x16x32_bf16 v[72:75], a[64:67], a[168:171], v[72:75]\n"
    "ds_read_b64_tr_b16 a[8:9], v18 offset:0\n"
    "v_mfma_f32_16x16x32_bf16 v[72:75], a[68:71], a[172:175], v[72:75]\n"
    "ds_read_b64_tr_b16 a[10:11], v18 offset:1024\n"
    "v_mfma_f32_16x16x32_bf16 v[84:87], a[76:79], a[172:175], v[84:87]\n"
    "ds_read_b64_tr_b16 a[12:13], v18 offset:8192\n"
    "v_mfma_f32_16x16x32_bf16 v[84:87], a[72:75], a[168:171], v[84:87]\n"
    "ds_read_b64_tr_b16 a[14:15], v18 offset:9216\n"
    "v_add_u32 v18, s40, v18\n"
    "v_mfma_f32_16x16x32_bf16 v[80:83], a[72:75], a[160:163], v[80:83]\n"
    "ds_read_b64_tr_b16 a[16:17], v19 offset:0\n"
    "v_mfma_f32_16x16x32_bf16 v[80:83], a[76:79], a[164:167], v[80:83]\n"
    "ds_read_b64_tr_b16 a[18:19], v19 offset:1024\n"
    "v_mfma_f32_16x16x32_bf16 v[76:79], a[76:79], a[156:159], v[76:79]\n"
    "ds_read_b64_tr_b16 a[20:21], v19 offset:8192\n"
    "v_mfma_f32_16x16x32_bf16 v[76:79], a[72:75], a[152:155], v[76:79]\n"
    "ds_read_b64_tr_b16 a[22:23], v19 offset:9216\n"
    "v_add_u32 v19, s40, v19\n"
    "v_mfma_f32_16x16x32_bf16 v[88:91], a[80:83], a[152:155], v[88:91]\n"
    "ds_read_b64_tr_b16 a[24:25], v20 offset:0\n"
    "v_mfma_f32_16x16x32_bf16 v[88:91], a[84:87], a[156:159], v[88:91]\n"
    "ds_read_b64_tr_b16 a[26:27], v20 offset:1024\n"
    "v_mfma_f32_16x16x32_bf16 v[92:95], a[84:87], a[164:167], v[92:95]\n"
    "ds_read_b64_tr_b16 a[28:29], v20 offset:8192\n"
    "v_mfma_f32_16x16x32_bf16 v[92:95], a[80:83], a[160:163], v[92:95]\n"
    "ds_read_b64_tr_b16 a[30:31], v20 offset:9216\n"
    "v_add_u32 v20, s40, v20\n"
    "v_mfma_f32_16x16x32_bf16 v[96:99], a[80:83], a[168:171], v[96:99]\n"
    "ds_read_b64_tr_b16 a[32:33], v21 offset:0\n"
    "v_mfma_f32_16x16x32_bf16 v[96:99], a[84:87], a[172:175], v[96:99]\n"
    "ds_read_b64_tr_b16 a[34:35], v21 offset:1024\n"
    "v_mfma_f32_16x16x32_bf16 v[108:111], a[92:95], a[172:175], v[108:111]\n"
    "ds_read_b64_tr_b16 a[36:37], v21 offset:8192\n"
    "v_mfma_f32_16x16x32_bf16 v[108:111], a[88:91], a[168:171], v[108:111]\n"
    "ds_read_b64_tr_b16 a[38:39], v21 offset:9216\n"
    "v_add_u32 v21, s40, v21\n"
    "v_mfma_f32_16x16x32_bf16 v[104:107], a[88:91], a[160:163], v[104:107]\n"
    "ds_read_b64_tr_b16 a[40:41], v22 offset:0\n"
    "v_mfma_f32_16x16x32_bf16 v[104:107], a[92:95], a[164:167], v[104:107]\n"
    "ds_read_b64_tr_b16 a[42:43], v22 offset:1024\n"
    "v_mfma_f32_16x16x32_bf16 v[100:103], a[92:95], a[156:159], v[100:103]\n"
    "ds_read_b64_tr_b16 a[44:45], v22 offset:8192\n"
    "v_mfma_f32_16x16x32_bf16 v[100:103], a[88:91], a[152:155], v[100:103]\n"
    "ds_read_b64_tr_b16 a[46:47], v22 offset:9216\n"
    "v_add_u32 v22, s40, v22\n"
    "v_mfma_f32_16x16x32_bf16 v[112:115], a[96:99], a[152:155], v[112:115]\n"
    "ds_read_b64_tr_b16 a[48:49], v23 offset:0\n"
    "v_mfma_f32_16x16x32_bf16 v[112:115], a[100:103], a[156:159], v[112:115]\n"
    "ds_read_b64_tr_b16 a[50:51], v23 offset:1024\n"
    "v_mfma_f32_16x16x32_bf16 v[116:119], a[100:103], a[164:167], v[116:119]\n"
    "ds_read_b64_tr_b16 a[52:53], v23 offset:8192\n"
    "v_mfma_f32_16x16x32_bf16 v[116:119], a[96:99], a[160:163], v[116:119]\n"
    "ds_read_b64_tr_b16 a[54:55], v23 offset:9216\n"
    "v_add_u32 v23, s40, v23\n"
    "v_mfma_f32_16x16x32_bf16 v[120:123], a[96:99], a[168:171], v[120:123]\n"
    "ds_read_b64_tr_b16 a[56:57], v24 offset:0\n"
    "v_mfma_f32_16x16x32_bf16 v[120:123], a[100:103], a[172:175], v[120:123]\n"
    "ds_read_b64_tr_b16 a[58:59], v24 offset:1024\n"
    "v_mfma_f32_16x16x32_bf16 v[132:135], a[108:111], a[172:175], v[132:135]\n"
    "ds_read_b64_tr_b16 a[60:61], v24 offset:8192\n"
    "v_mfma_f32_16x16x32_bf16 v[132:135], a[104:107], a[168:171], v[132:135]\n"
    "ds_read_b64_tr_b16 a[62:63], v24 offset:9216\n"
    "v_add_u32 v24, s40, v24\n"
    "v_mfma_f32_16x16x32_bf16 v[128:131], a[104:107], a[160:163], v[128:131]\n"
    "ds_read_b128 a[128:131], v15 offset:0\n"
    "v_mfma_f32_16x16x32_bf16 v[128:131], a[108:111], a[164:167], v[128:131]\n"
    "ds_read_b128 a[132:135], v16 offset:0\n"
    "v_mfma_f32_16x16x32_bf16 v[124:127], a[108:111], a[156:159], v[124:127]\n"
    "ds_read_b128 a[136:139], v15 offset:2048\n"
    "v_mfma_f32_16x16x32_bf16 v[124:127], a[104:107], a[152:155], v[124:127]\n"
    "ds_read_b128 a[140:143], v16 offset:2048\n"
    "v_mfma_f32_16x16x32_bf16 v[136:139], a[112:115], a[152:155], v[136:139]\n"
    "ds_read_b128 a[144:147], v15 offset:4096\n"
    "v_mfma_f32_16x16x32_bf16 v[136:139], a[116:119], a[156:159], v[136:139]\n"
    "ds_read_b128 a[148:151], v16 offset:4096\n"
    "v_mfma_f32_16x16x32_bf16 v[140:143], a[116:119], a[164:167], v[140:143]\n"
    "v_mfma_f32_16x16x32_bf16 v[140:143], a[112:115], a[160:163], v[140:143]\n"
    "v_mfma_f32_16x16x32_bf16 v[144:147], a[112:115], a[168:171], v[144:147]\n"
    "v_mfma_f32_16x16x32_bf16 v[144:147], a[116:119], a[172:175], v[144:147]\n"
    "s_mov_b32 m0, s47\n"
    "v_mfma_f32_16x16x32_bf16 v[156:159], a[124:127], a[172:175], v[156:159]\n"
    "v_mfma_f32_16x16x32_bf16 v[156:159], a[120:123], a[168:171], v[156:159]\n"
    "v_mfma_f32_16x16x32_bf16 v[152:155], a[120:123], a[160:163], v[152:155]\n"
    "v_mfma_f32_16x16x32_bf16 v[152:155], a[124:127], a[164:167], v[152:155]\n"
    "v_mfma_f32_16x16x32_bf16 v[148:151], a[124:127], a[156:159], v[148:151]\n"
    "s_add_u32 s47, s47, 8192\n"
    "v_mfma_f32_16x16x32_bf16 v[148:151], a[120:123], a[152:155], v[148:151]\n"
    "s_mov_b32 s39, s38\n"
    "s_add_u32 s38, s38, s40\n"
    "s_sub_u32 s37, s37, 1\n"
    "s_branch .L_qC_%=A\n"
    ".L_qC_%=B:\n"
    "s_waitcnt lgkmcnt(0)\n"
    "s_barrier\n"
    "v_mfma_f32_16x16x32_bf16 v[160:163], a[0:3], a[128:131], v[160:163]\n"
    "s_cmp_eq_u32 s38, 0x8000\n"
    "ds_read_b64_tr_b16 a[64:65], v17 offset:0\n"
    "v_mfma_f32_16x16x32_bf16 v[160:163], a[4:7], a[132:135], v[160:163]\n"
    "s_cselect_b32 s40, s42, s41\n"
    "ds_read_b64_tr_b16 a[66:67], v17 offset:1024\n"
    "v_mfma_f32_16x16x32_bf16 v[164:167], a[4:7], a[140:143], v[164:167]\n"
    "ds_read_b64_tr_b16 a[68:69], v17 offset:8192\n"
    "v_mfma_f32_16x16x32_bf16 v[164:167], a[0:3], a[136:139], v[164:167]\n"
    "ds_read_b64_tr_b16 a[70:71], v17 offset:9216\n"
    "v_add_u32 v17, s40, v17\n"
    "v_mfma_f32_16x16x32_bf16 v[168:171], a[0:3], a[144:147], v[168:171]\n"
    "ds_read_b64_tr_b16 a[72:73], v18 offset:0\n"
    "v_mfma_f32_16x16x32_bf16 v[168:171], a[4:7], a[148:151], v[168:171]\n"
    "ds_read_b64_tr_b16 a[74:75], v18 offset:1024\n"
    "v_mfma_f32_16x16x32_bf16 v[180:183], a[12:15], a[148:151], v[180:183]\n"
    "ds_read_b64_tr_b16 a[76:77], v18 offset:8192\n"
    "v_mfma_f32_16x16x32_bf16 v[180:183], a[8:11], a[144:147], v[180:183]\n"
    "ds_read_b64_tr_b16 a[78:79], v18 offset:9216\n"
    "v_add_u32 v18, s40, v18\n"
    "v_mfma_f32_16x16x32_bf16 v[176:179], a[8:11], a[136:139], v[176:179]\n"
    "ds_read_b64_tr_b16 a[80:81], v19 offset:0\n"
    "v_mfma_f32_16x16x32_bf16 v[176:179], a[12:15], a[140:143], v[176:179]\n"
    "ds_read_b64_tr_b16 a[82:83], v19 offset:1024\n"
    "v_mfma_f32_16x16x32_bf16 v[172:175], a[12:15], a[132:135], v[172:175]\n"
    "ds_read_b64_tr_b16 a[84:85], v19 offset:8192\n"
    "v_mfma_f32_16x16x32_bf16 v[172:175], a[8:11], a[128:131], v[172:175]\n"
    "ds_read_b64_tr_b16 a[86:87], v19 offset:9216\n"
    "v_add_u32 v19, s40, v19\n"
    "v_mfma_f32_16x16x32_bf16 v[184:187], a[16:19], a[128:131], v[184:187]\n"
    "ds_read_b64_tr_b16 a[88:89], v20 offset:0\n"
    "v_mfma_f32_16x16x32_bf16 v[184:187], a[20:23], a[132:135], v[184:187]\n"
    "ds_read_b64_tr_b16 a[90:91], v20 offset:1024\n"
    "v_mfma_f32_16x16x32_bf16 v[188:191], a[20:23], a[140:143], v[188:191]\n"
    "ds_read_b64_tr_b16 a[92:93], v20 offset:8192\n"
    "v_mfma_f32_16x16x32_bf16 v[188:191], a[16:19], a[136:139], v[188:191]\n"
    "ds_read_b64_tr_b16 a[94:95], v20 offset:9216\n"
    "v_add_u32 v20, s40, v20\n"
    "v_mfma_f32_16x16x32_bf16 v[192:195], a[16:19], a[144:147], v[192:195]\n"
    "ds_read_b64_tr_b16 a[96:97], v21 offset:0\n"
    "v_mfma_f32_16x16x32_bf16 v[192:195], a[20:23], a[148:151], v[192:195]\n"
    "ds_read_b64_tr_b16 a[98:99], v21 offset:1024\n"
    "v_mfma_f32_16x16x32_bf16 v[204:207], a[28:31], a[148:151], v[204:207]\n"
    "ds_read_b64_tr_b16 a[100:101], v21 offset:8192\n"
    "v_mfma_f32_16x16x32_bf16 v[204:207], a[24:27], a[144:147], v[204:207]\n"
    "ds_read_b64_tr_b16 a[102:103], v21 offset:9216\n"
    "v_add_u32 v21, s40, v21\n"
    "v_mfma_f32_16x16x32_bf16 v[200:203], a[24:27], a[136:139], v[200:203]\n"
    "ds_read_b64_tr_b16 a[104:105], v22 offset:0\n"
    "v_mfma_f32_16x16x32_bf16 v[200:203], a[28:31], a[140:143], v[200:203]\n"
    "ds_read_b64_tr_b16 a[106:107], v22 offset:1024\n"
    "v_mfma_f32_16x16x32_bf16 v[196:199], a[28:31], a[132:135], v[196:199]\n"
    "ds_read_b64_tr_b16 a[108:109], v22 offset:8192\n"
    "v_mfma_f32_16x16x32_bf16 v[196:199], a[24:27], a[128:131], v[196:199]\n"
    "ds_read_b64_tr_b16 a[110:111], v22 offset:9216\n"
    "v_add_u32 v22, s40, v22\n"
    "v_mfma_f32_16x16x32_bf16 v[208:211], a[32:35], a[128:131], v[208:211]\n"
    "ds_read_b64_tr_b16 a[112:113], v23 offset:0\n"
    "v_mfma_f32_16x16x32_bf16 v[208:211], a[36:39], a[132:135], v[208:211]\n"
    "ds_read_b64_tr_b16 a[114:115], v23 offset:1024\n"
    "v_mfma_f32_16x16x32_bf16 v[212:215], a[36:39], a[140:143], v[212:215]\n"
    "ds_read_b64_tr_b16 a[116:117], v23 offset:8192\n"
    "v_mfma_f32_16x16x32_bf16 v[212:215], a[32:35], a[136:139], v[212:215]\n"
    "ds_read_b64_tr_b16 a[118:119], v23 offset:9216\n"
    "v_add_u32 v23, s40, v23\n"
    "v_mfma_f32_16x16x32_bf16 v[216:219], a[32:35], a[144:147], v[216:219]\n"
    "ds_read_b64_tr_b16 a[120:121], v24 offset:0\n"
    "v_mfma_f32_16x16x32_bf16 v[216:219], a[36:39], a[148:151], v[216:219]\n"
    "ds_read_b64_tr_b16 a[122:123], v24 offset:1024\n"
    "v_mfma_f32_16x16x32_bf16 v[228:231], a[44:47], a[148:151], v[228:231]\n"
    "ds_read_b64_tr_b16 a[124:125], v24 offset:8192\n"
    "v_mfma_f32_16x16x32_bf16 v[228:231], a[40:43], a[144:147], v[228:231]\n"
    "ds_read_b64_tr_b16 a[126:127], v24 offset:9216\n"
    "v_add_u32 v24, s40, v24\n"
    "v_mfma_f32_16x16x32_bf16 v[224:227], a[40:43], a[136:139], v[224:227]\n"
    "v_mfma_f32_16x16x32_bf16 v[224:227], a[44:47], a[140:143], v[224:227]\n"
    "v_mfma_f32_16x16x32_bf16 v[220:223], a[44:47], a[132:135], v[220:223]\n"
    "v_mfma_f32_16x16x32_bf16 v[220:223], a[40:43], a[128:131], v[220:223]\n"
    "v_mfma_f32_16x16x32_bf16 v[232:235], a[48:51], a[128:131], v[232:235]\n"
    "v_mfma_f32_16x16x32_bf16 v[232:235], a[52:55], a[132:135], v[232:235]\n"
    "v_mfma_f32_16x16x32_bf16 v[236:239], a[52:55], a[140:143], v[236:239]\n"
    "v_mfma_f32_16x16x32_bf16 v[236:239], a[48:51], a[136:139], v[236:239]\n"
    "v_mfma_f32_16x16x32_bf16 v[240:243], a[48:51], a[144:147], v[240:243]\n"
    "v_mfma_f32_16x16x32_bf16 v[240:243], a[52:55], a[148:151], v[240:243]\n"
    "v_mfma_f32_16x16x32_bf16 v[252:255], a[60:63], a[148:151], v[252:255]\n"
    "v_mfma_f32_16x16x32_bf16 v[252:255], a[56:59], a[144:147], v[252:255]\n"
    "v_mfma_f32_16x16x32_bf16 v[248:251], a[56:59], a[136:139], v[248:251]\n"
    "v_mfma_f32_16x16x32_bf16 v[248:251], a[60:63], a[140:143], v[248:251]\n"
    "v_mfma_f32_16x16x32_bf16 v[244:247], a[60:63], a[132:135], v[244:247]\n"
    "v_mfma_f32_16x16x32_bf16 v[244:247], a[56:59], a[128:131], v[244:247]\n"
    "s_mov_b32 s39, s38\n"
    "s_add_u32 s38, s38, s40\n"
    "s_waitcnt lgkmcnt(0)\n"
    "s_barrier\n"
    "v_mfma_f32_16x16x32_bf16 v[64:67], a[64:67], a[128:131], v[64:67]\n"
    "s_cmp_eq_u32 s38, 0x8000\n"
    "ds_read_b64_tr_b16 a[0:1], v17 offset:0\n"
    "v_mfma_f32_16x16x32_bf16 v[64:67], a[68:71], a[132:135], v[64:67]\n"
    "s_cselect_b32 s40, s42, s41\n"
    "ds_read_b64_tr_b16 a[2:3], v17 offset:1024\n"
    "v_mfma_f32_16x16x32_bf16 v[68:71], a[68:71], a[140:143], v[68:71]\n"
    "ds_read_b64_tr_b16 a[4:5], v17 offset:8192\n"
    "v_mfma_f32_16x16x32_bf16 v[68:71], a[64:67], a[136:139], v[68:71]\n"
    "ds_read_b64_tr_b16 a[6:7], v17 offset:9216\n"
    "v_add_u32 v17, s40, v17\n"
    "v_mfma_f32_16x16x32_bf16 v[72:75], a[64:67], a[144:147], v[72:75]\n"
    "ds_read_b64_tr_b16 a[8:9], v18 offset:0\n"
    "v_mfma_f32_16x16x32_bf16 v[72:75], a[68:71], a[148:151], v[72:75]\n"
    "ds_read_b64_tr_b16 a[10:11], v18 offset:1024\n"
    "v_mfma_f32_16x16x32_bf16 v[84:87], a[76:79], a[148:151], v[84:87]\n"
    "ds_read_b64_tr_b16 a[12:13], v18 offset:8192\n"
    "v_mfma_f32_16x16x32_bf16 v[84:87], a[72:75], a[144:147], v[84:87]\n"
    "ds_read_b64_tr_b16 a[14:15], v18 offset:9216\n"
    "v_add_u32 v18, s40, v18\n"
    "v_mfma_f32_16x16x32_bf16 v[80:83], a[72:75], a[136:139], v[80:83]\n"
    "ds_read_b64_tr_b16 a[16:17], v19 offset:0\n"
    "v_mfma_f32_16x16x32_bf16 v[80:83], a[76:79], a[140:143], v[80:83]\n"
    "ds_read_b64_tr_b16 a[18:19], v19 offset:1024\n"
    "v_mfma_f32_16x16x32_bf16 v[76:79], a[76:79], a[132:135], v[76:79]\n"
    "ds_read_b64_tr_b16 a[20:21], v19 offset:8192\n"
    "v_mfma_f32_16x16x32_bf16 v[76:79], a[72:75], a[128:131], v[76:79]\n"
    "ds_read_b64_tr_b16 a[22:23], v19 offset:9216\n"
    "v_add_u32 v19, s40, v19\n"
    "v_mfma_f32_16x16x32_bf16 v[88:91], a[80:83], a[128:131], v[88:91]\n"
    "ds_read_b64_tr_b16 a[24:25], v20 offset:0\n"
    "v_mfma_f32_16x16x32_bf16 v[88:91], a[84:87], a[132:135], v[88:91]\n"
    "ds_read_b64_tr_b16 a[26:27], v20 offset:1024\n"
    "v_mfma_f32_16x16x32_bf16 v[92:95], a[84:87], a[140:143], v[92:95]\n"
    "ds_read_b64_tr_b16 a[28:29], v20 offset:8192\n"
    "v_mfma_f32_16x16x32_bf16 v[92:95], a[80:83], a[136:139], v[92:95]\n"
    "ds_read_b64_tr_b16 a[30:31], v20 offset:9216\n"
    "v_add_u32 v20, s40, v20\n"
    "v_mfma_f32_16x16x32_bf16 v[96:99], a[80:83], a[144:147], v[96:99]\n"
    "ds_read_b64_tr_b16 a[32:33], v21 offset:0\n"
    "v_mfma_f32_16x16x32_bf16 v[96:99], a[84:87], a[148:151], v[96:99]\n"
    "ds_read_b64_tr_b16 a[34:35], v21 offset:1024\n"
    "v_mfma_f32_16x16x32_bf16 v[108:111], a[92:95], a[148:151], v[108:111]\n"
    "ds_read_b64_tr_b16 a[36:37], v21 offset:8192\n"
    "v_mfma_f32_16x16x32_bf16 v[108:111], a[88:91], a[144:147], v[108:111]\n"
    "ds_read_b64_tr_b16 a[38:39], v21 offset:9216\n"
    "v_add_u32 v21, s40, v21\n"
    "v_mfma_f32_16x16x32_bf16 v[104:107], a[88:91], a[136:139], v[104:107]\n"
    "ds_read_b64_tr_b16 a[40:41], v22 offset:0\n"
    "v_mfma_f32_16x16x32_bf16 v[104:107], a[92:95], a[140:143], v[104:107]\n"
    "ds_read_b64_tr_b16 a[42:43], v22 offset:1024\n"
    "v_mfma_f32_16x16x32_bf16 v[100:103], a[92:95], a[132:135], v[100:103]\n"
    "ds_read_b64_tr_b16 a[44:45], v22 offset:8192\n"
    "v_mfma_f32_16x16x32_bf16 v[100:103], a[88:91], a[128:131], v[100:103]\n"
    "ds_read_b64_tr_b16 a[46:47], v22 offset:9216\n"
    "v_add_u32 v22, s40, v22\n"
    "v_mfma_f32_16x16x32_bf16 v[112:115], a[96:99], a[128:131], v[112:115]\n"
    "ds_read_b64_tr_b16 a[48:49], v23 offset:0\n"
    "v_mfma_f32_16x16x32_bf16 v[112:115], a[100:103], a[132:135], v[112:115]\n"
    "ds_read_b64_tr_b16 a[50:51], v23 offset:1024\n"
    "v_mfma_f32_16x16x32_bf16 v[116:119], a[100:103], a[140:143], v[116:119]\n"
    "ds_read_b64_tr_b16 a[52:53], v23 offset:8192\n"
    "v_mfma_f32_16x16x32_bf16 v[116:119], a[96:99], a[136:139], v[116:119]\n"
    "ds_read_b64_tr_b16 a[54:55], v23 offset:9216\n"
    "v_add_u32 v23, s40, v23\n"
    "v_mfma_f32_16x16x32_bf16 v[120:123], a[96:99], a[144:147], v[120:123]\n"
    "ds_read_b64_tr_b16 a[56:57], v24 offset:0\n"
    "v_mfma_f32_16x16x32_bf16 v[120:123], a[100:103], a[148:151], v[120:123]\n"
    "ds_read_b64_tr_b16 a[58:59], v24 offset:1024\n"
    "v_mfma_f32_16x16x32_bf16 v[132:135], a[108:111], a[148:151], v[132:135]\n"
    "ds_read_b64_tr_b16 a[60:61], v24 offset:8192\n"
    "v_mfma_f32_16x16x32_bf16 v[132:135], a[104:107], a[144:147], v[132:135]\n"
    "ds_read_b64_tr_b16 a[62:63], v24 offset:9216\n"
    "v_add_u32 v24, s40, v24\n"
    "v_mfma_f32_16x16x32_bf16 v[128:131], a[104:107], a[136:139], v[128:131]\n"
    "ds_read_b128 a[152:155], v38 offset:0\n"
    "v_mfma_f32_16x16x32_bf16 v[128:131], a[108:111], a[140:143], v[128:131]\n"
    "ds_read_b128 a[156:159], v39 offset:0\n"
    "v_mfma_f32_16x16x32_bf16 v[124:127], a[108:111], a[132:135], v[124:127]\n"
    "ds_read_b128 a[160:163], v38 offset:2048\n"
    "v_mfma_f32_16x16x32_bf16 v[124:127], a[104:107], a[128:131], v[124:127]\n"
    "ds_read_b128 a[164:167], v39 offset:2048\n"
    "v_mfma_f32_16x16x32_bf16 v[136:139], a[112:115], a[128:131], v[136:139]\n"
    "ds_read_b128 a[168:171], v38 offset:4096\n"
    "v_mfma_f32_16x16x32_bf16 v[136:139], a[116:119], a[132:135], v[136:139]\n"
    "ds_read_b128 a[172:175], v39 offset:4096\n"
    "v_mfma_f32_16x16x32_bf16 v[140:143], a[116:119], a[140:143], v[140:143]\n"
    "v_mfma_f32_16x16x32_bf16 v[140:143], a[112:115], a[136:139], v[140:143]\n"
    "v_mfma_f32_16x16x32_bf16 v[144:147], a[112:115], a[144:147], v[144:147]\n"
    "v_mfma_f32_16x16x32_bf16 v[144:147], a[116:119], a[148:151], v[144:147]\n"
    "v_mfma_f32_16x16x32_bf16 v[156:159], a[124:127], a[148:151], v[156:159]\n"
    "v_mfma_f32_16x16x32_bf16 v[156:159], a[120:123], a[144:147], v[156:159]\n"
    "v_mfma_f32_16x16x32_bf16 v[152:155], a[120:123], a[136:139], v[152:155]\n"
    "v_mfma_f32_16x16x32_bf16 v[152:155], a[124:127], a[140:143], v[152:155]\n"
    "v_mfma_f32_16x16x32_bf16 v[148:151], a[124:127], a[132:135], v[148:151]\n"
    "v_mfma_f32_16x16x32_bf16 v[148:151], a[120:123], a[128:131], v[148:151]\n"
    "s_mov_b32 s39, s38\n"
    "s_add_u32 s38, s38, s40\n"
    "s_waitcnt lgkmcnt(0)\n"
    "s_barrier\n"
    "v_mfma_f32_16x16x32_bf16 v[160:163], a[0:3], a[152:155], v[160:163]\n"
    "s_cmp_eq_u32 s38, 0x8000\n"
    "ds_read_b64_tr_b16 a[64:65], v17 offset:0\n"
    "v_mfma_f32_16x16x32_bf16 v[160:163], a[4:7], a[156:159], v[160:163]\n"
    "s_cselect_b32 s40, s42, s41\n"
    "ds_read_b64_tr_b16 a[66:67], v17 offset:1024\n"
    "v_mfma_f32_16x16x32_bf16 v[164:167], a[4:7], a[164:167], v[164:167]\n"
    "ds_read_b64_tr_b16 a[68:69], v17 offset:8192\n"
    "v_mfma_f32_16x16x32_bf16 v[164:167], a[0:3], a[160:163], v[164:167]\n"
    "ds_read_b64_tr_b16 a[70:71], v17 offset:9216\n"
    "v_add_u32 v17, s40, v17\n"
    "v_mfma_f32_16x16x32_bf16 v[168:171], a[0:3], a[168:171], v[168:171]\n"
    "ds_read_b64_tr_b16 a[72:73], v18 offset:0\n"
    "v_mfma_f32_16x16x32_bf16 v[168:171], a[4:7], a[172:175], v[168:171]\n"
    "ds_read_b64_tr_b16 a[74:75], v18 offset:1024\n"
    "v_mfma_f32_16x16x32_bf16 v[180:183], a[12:15], a[172:175], v[180:183]\n"
    "ds_read_b64_tr_b16 a[76:77], v18 offset:8192\n"
    "v_mfma_f32_16x16x32_bf16 v[180:183], a[8:11], a[168:171], v[180:183]\n"
    "ds_read_b64_tr_b16 a[78:79], v18 offset:9216\n"
    "v_add_u32 v18, s40, v18\n"
    "v_mfma_f32_16x16x32_bf16 v[176:179], a[8:11], a[160:163], v[176:179]\n"
    "ds_read_b64_tr_b16 a[80:81], v19 offset:0\n"
    "v_mfma_f32_16x16x32_bf16 v[176:179], a[12:15], a[164:167], v[176:179]\n"
    "ds_read_b64_tr_b16 a[82:83], v19 offset:1024\n"
    "v_mfma_f32_16x16x32_bf16 v[172:175], a[12:15], a[156:159], v[172:175]\n"
    "ds_read_b64_tr_b16 a[84:85], v19 offset:8192\n"
    "v_mfma_f32_16x16x32_bf16 v[172:175], a[8:11], a[152:155], v[172:175]\n"
    "ds_read_b64_tr_b16 a[86:87], v19 offset:9216\n"
    "v_add_u32 v19, s40, v19\n"
    "v_mfma_f32_16x16x32_bf16 v[184:187], a[16:19], a[152:155], v[184:187]\n"
    "ds_read_b64_tr_b16 a[88:89], v20 offset:0\n"
    "v_mfma_f32_16x16x32_bf16 v[184:187], a[20:23], a[156:159], v[184:187]\n"
    "ds_read_b64_tr_b16 a[90:91], v20 offset:1024\n"
    "v_mfma_f32_16x16x32_bf16 v[188:191], a[20:23], a[164:167], v[188:191]\n"
    "ds_read_b64_tr_b16 a[92:93], v20 offset:8192\n"
    "v_mfma_f32_16x16x32_bf16 v[188:191], a[16:19], a[160:163], v[188:191]\n"
    "ds_read_b64_tr_b16 a[94:95], v20 offset:9216\n"
    "v_add_u32 v20, s40, v20\n"
    "v_mfma_f32_16x16x32_bf16 v[192:195], a[16:19], a[168:171], v[192:195]\n"
    "ds_read_b64_tr_b16 a[96:97], v21 offset:0\n"
    "v_mfma_f32_16x16x32_bf16 v[192:195], a[20:23], a[172:175], v[192:195]\n"
    "ds_read_b64_tr_b16 a[98:99], v21 offset:1024\n"
    "v_mfma_f32_16x16x32_bf16 v[204:207], a[28:31], a[172:175], v[204:207]\n"
    "ds_read_b64_tr_b16 a[100:101], v21 offset:8192\n"
    "v_mfma_f32_16x16x32_bf16 v[204:207], a[24:27], a[168:171], v[204:207]\n"
    "ds_read_b64_tr_b16 a[102:103], v21 offset:9216\n"
    "v_add_u32 v21, s40, v21\n"
    "v_mfma_f32_16x16x32_bf16 v[200:203], a[24:27], a[160:163], v[200:203]\n"
    "ds_read_b64_tr_b16 a[104:105], v22 offset:0\n"
    "v_mfma_f32_16x16x32_bf16 v[200:203], a[28:31], a[164:167], v[200:203]\n"
    "ds_read_b64_tr_b16 a[106:107], v22 offset:1024\n"
    "v_mfma_f32_16x16x32_bf16 v[196:199], a[28:31], a[156:159], v[196:199]\n"
    "ds_read_b64_tr_b16 a[108:109], v22 offset:8192\n"
    "v_mfma_f32_16x16x32_bf16 v[196:199], a[24:27], a[152:155], v[196:199]\n"
    "ds_read_b64_tr_b16 a[110:111], v22 offset:9216\n"
    "v_add_u32 v22, s40, v22\n"
    "v_mfma_f32_16x16x32_bf16 v[208:211], a[32:35], a[152:155], v[208:211]\n"
    "ds_read_b64_tr_b16 a[112:113], v23 offset:0\n"
    "v_mfma_f32_16x16x32_bf16 v[208:211], a[36:39], a[156:159], v[208:211]\n"
    "ds_read_b64_tr_b16 a[114:115], v23 offset:1024\n"
    "v_mfma_f32_16x16x32_bf16 v[212:215], a[36:39], a[164:167], v[212:215]\n"
    "ds_read_b64_tr_b16 a[116:117], v23 offset:8192\n"
    "v_mfma_f32_16x16x32_bf16 v[212:215], a[32:35], a[160:163], v[212:215]\n"
    "ds_read_b64_tr_b16 a[118:119], v23 offset:9216\n"
    "v_add_u32 v23, s40, v23\n"
    "v_mfma_f32_16x16x32_bf16 v[216:219], a[32:35], a[168:171], v[216:219]\n"
    "ds_read_b64_tr_b16 a[120:121], v24 offset:0\n"
    "v_mfma_f32_16x16x32_bf16 v[216:219], a[36:39], a[172:175], v[216:219]\n"
    "ds_read_b64_tr_b16 a[122:123], v24 offset:1024\n"
    "v_mfma_f32_16x16x32_bf16 v[228:231], a[44:47], a[172:175], v[228:231]\n"
    "ds_read_b64_tr_b16 a[124:125], v24 offset:8192\n"
    "v_mfma_f32_16x16x32_bf16 v[228:231], a[40:43], a[168:171], v[228:231]\n"
    "ds_read_b64_tr_b16 a[126:127], v24 offset:9216\n"
    "v_add_u32 v24, s40, v24\n"
    "v_mfma_f32_16x16x32_bf16 v[224:227], a[40:43], a[160:163], v[224:227]\n"
    "v_mfma_f32_16x16x32_bf16 v[224:227], a[44:47], a[164:167], v[224:227]\n"
    "v_mfma_f32_16x16x32_bf16 v[220:223], a[44:47], a[156:159], v[220:223]\n"
    "v_mfma_f32_16x16x32_bf16 v[220:223], a[40:43], a[152:155], v[220:223]\n"
    "v_mfma_f32_16x16x32_bf16 v[232:235], a[48:51], a[152:155], v[232:235]\n"
    "v_mfma_f32_16x16x32_bf16 v[232:235], a[52:55], a[156:159], v[232:235]\n"
    "v_mfma_f32_16x16x32_bf16 v[236:239], a[52:55], a[164:167], v[236:239]\n"
    "v_mfma_f32_16x16x32_bf16 v[236:239], a[48:51], a[160:163], v[236:239]\n"
    "v_mfma_f32_16x16x32_bf16 v[240:243], a[48:51], a[168:171], v[240:243]\n"
    "v_mfma_f32_16x16x32_bf16 v[240:243], a[52:55], a[172:175], v[240:243]\n"
    "v_mfma_f32_16x16x32_bf16 v[252:255], a[60:63], a[172:175], v[252:255]\n"
    "buffer_load_dword v1, v5, s[28:31], s52 offen\n"
    "v_mfma_f32_16x16x32_bf16 v[252:255], a[56:59], a[168:171], v[252:255]\n"
    "s_add_u32 s52, s52, 5504\n"
    "v_mfma_f32_16x16x32_bf16 v[248:251], a[56:59], a[160:163], v[248:251]\n"
    "buffer_load_dword v1, v5, s[28:31], s52 offen\n"
    "v_mfma_f32_16x16x32_bf16 v[248:251], a[60:63], a[164:167], v[248:251]\n"
    "s_add_u32 s52, s52, 1043072\n"
    "v_mfma_f32_16x16x32_bf16 v[244:247], a[60:63], a[156:159], v[244:247]\n"
    "global_load_dword v1, v[2:3], off\n"
    "v_mfma_f32_16x16x32_bf16 v[244:247], a[56:59], a[152:155], v[244:247]\n"
    "s_mov_b32 s39, s38\n"
    "s_add_u32 s38, s38, s40\n"
    "s_cmp_eq_u32 s37, 0\n"
    "s_cbranch_scc1 .L_qC_%=x\n"
    "s_waitcnt lgkmcnt(0)\n"
    "s_barrier\n"
    "v_mfma_f32_16x16x32_bf16 v[64:67], a[64:67], a[152:155], v[64:67]\n"
    "s_cmp_eq_u32 s38, 0x8000\n"
    "ds_read_b64_tr_b16 a[0:1], v17 offset:0\n"
    "v_mfma_f32_16x16x32_bf16 v[64:67], a[68:71], a[156:159], v[64:67]\n"
    "s_cselect_b32 s40, s42, s41\n"
    "ds_read_b64_tr_b16 a[2:3], v17 offset:1024\n"
    "v_mfma_f32_16x16x32_bf16 v[68:71], a[68:71], a[164:167], v[68:71]\n"
    "ds_read_b64_tr_b16 a[4:5], v17 offset:8192\n"
    "v_mfma_f32_16x16x32_bf16 v[68:71], a[64:67], a[160:163], v[68:71]\n"
    "ds_read_b64_tr_b16 a[6:7], v17 offset:9216\n"
    "v_add_u32 v17, s40, v17\n"
    "v_mfma_f32_16x16x32_bf16 v[72:75], a[64:67], a[168:171], v[72:75]\n"
    "ds_read_b64_tr_b16 a[8:9], v18 offset:0\n"
    "v_mfma_f32_16x16x32_bf16 v[72:75], a[68:71], a[172:175], v[72:75]\n"
    "ds_read_b64_tr_b16 a[10:11], v18 offset:1024\n"
    "v_mfma_f32_16x16x32_bf16 v[84:87], a[76:79], a[172:175], v[84:87]\n"
    "ds_read_b64_tr_b16 a[12:13], v18 offset:8192\n"
    "v_mfma_f32_16x16x32_bf16 v[84:87], a[72:75], a[168:171], v[84:87]\n"
    "ds_read_b64_tr_b16 a[14:15], v18 offset:9216\n"
    "v_add_u32 v18, s40, v18\n"
    "v_mfma_f32_16x16x32_bf16 v[80:83], a[72:75], a[160:163], v[80:83]\n"
    "ds_read_b64_tr_b16 a[16:17], v19 offset:0\n"
    "v_mfma_f32_16x16x32_bf16 v[80:83], a[76:79], a[164:167], v[80:83]\n"
    "ds_read_b64_tr_b16 a[18:19], v19 offset:1024\n"
    "v_mfma_f32_16x16x32_bf16 v[76:79], a[76:79], a[156:159], v[76:79]\n"
    "ds_read_b64_tr_b16 a[20:21], v19 offset:8192\n"
    "v_mfma_f32_16x16x32_bf16 v[76:79], a[72:75], a[152:155], v[76:79]\n"
    "ds_read_b64_tr_b16 a[22:23], v19 offset:9216\n"
    "v_add_u32 v19, s40, v19\n"
    "v_mfma_f32_16x16x32_bf16 v[88:91], a[80:83], a[152:155], v[88:91]\n"
    "ds_read_b64_tr_b16 a[24:25], v20 offset:0\n"
    "v_mfma_f32_16x16x32_bf16 v[88:91], a[84:87], a[156:159], v[88:91]\n"
    "ds_read_b64_tr_b16 a[26:27], v20 offset:1024\n"
    "v_mfma_f32_16x16x32_bf16 v[92:95], a[84:87], a[164:167], v[92:95]\n"
    "ds_read_b64_tr_b16 a[28:29], v20 offset:8192\n"
    "v_mfma_f32_16x16x32_bf16 v[92:95], a[80:83], a[160:163], v[92:95]\n"
    "ds_read_b64_tr_b16 a[30:31], v20 offset:9216\n"
    "v_add_u32 v20, s40, v20\n"
    "v_mfma_f32_16x16x32_bf16 v[96:99], a[80:83], a[168:171], v[96:99]\n"
    "ds_read_b64_tr_b16 a[32:33], v21 offset:0\n"
    "v_mfma_f32_16x16x32_bf16 v[96:99], a[84:87], a[172:175], v[96:99]\n"
    "ds_read_b64_tr_b16 a[34:35], v21 offset:1024\n"
    "v_mfma_f32_16x16x32_bf16 v[108:111], a[92:95], a[172:175], v[108:111]\n"
    "ds_read_b64_tr_b16 a[36:37], v21 offset:8192\n"
    "v_mfma_f32_16x16x32_bf16 v[108:111], a[88:91], a[168:171], v[108:111]\n"
    "ds_read_b64_tr_b16 a[38:39], v21 offset:9216\n"
    "v_add_u32 v21, s40, v21\n"
    "v_mfma_f32_16x16x32_bf16 v[104:107], a[88:91], a[160:163], v[104:107]\n"
    "ds_read_b64_tr_b16 a[40:41], v22 offset:0\n"
    "v_mfma_f32_16x16x32_bf16 v[104:107], a[92:95], a[164:167], v[104:107]\n"
    "ds_read_b64_tr_b16 a[42:43], v22 offset:1024\n"
    "v_mfma_f32_16x16x32_bf16 v[100:103], a[92:95], a[156:159], v[100:103]\n"
    "ds_read_b64_tr_b16 a[44:45], v22 offset:8192\n"
    "v_mfma_f32_16x16x32_bf16 v[100:103], a[88:91], a[152:155], v[100:103]\n"
    "ds_read_b64_tr_b16 a[46:47], v22 offset:9216\n"
    "v_add_u32 v22, s40, v22\n"
    "v_mfma_f32_16x16x32_bf16 v[112:115], a[96:99], a[152:155], v[112:115]\n"
    "ds_read_b64_tr_b16 a[48:49], v23 offset:0\n"
    "v_mfma_f32_16x16x32_bf16 v[112:115], a[100:103], a[156:159], v[112:115]\n"
    "ds_read_b64_tr_b16 a[50:51], v23 offset:1024\n"
    "v_mfma_f32_16x16x32_bf16 v[116:119], a[100:103], a[164:167], v[116:119]\n"
    "ds_read_b64_tr_b16 a[52:53], v23 offset:8192\n"
    "v_mfma_f32_16x16x32_bf16 v[116:119], a[96:99], a[160:163], v[116:119]\n"
    "ds_read_b64_tr_b16 a[54:55], v23 offset:9216\n"
    "v_add_u32 v23, s40, v23\n"
    "v_mfma_f32_16x16x32_bf16 v[120:123], a[96:99], a[168:171], v[120:123]\n"
    "ds_read_b64_tr_b16 a[56:57], v24 offset:0\n"
    "v_mfma_f32_16x16x32_bf16 v[120:123], a[100:103], a[172:175], v[120:123]\n"
    "ds_read_b64_tr_b16 a[58:59], v24 offset:1024\n"
    "v_mfma_f32_16x16x32_bf16 v[132:135], a[108:111], a[172:175], v[132:135]\n"
    "ds_read_b64_tr_b16 a[60:61], v24 offset:8192\n"
    "v_mfma_f32_16x16x32_bf16 v[132:135], a[104:107], a[168:171], v[132:135]\n"
    "ds_read_b64_tr_b16 a[62:63], v24 offset:9216\n"
    "v_add_u32 v24, s40, v24\n"
    "v_mfma_f32_16x16x32_bf16 v[128:131], a[104:107], a[160:163], v[128:131]\n"
    "ds_read_b128 a[128:131], v15 offset:0\n"
    "v_mfma_f32_16x16x32_bf16 v[128:131], a[108:111], a[164:167], v[128:131]\n"
    "ds_read_b128 a[132:135], v16 offset:0\n"
    "v_mfma_f32_16x16x32_bf16 v[124:127], a[108:111], a[156:159], v[124:127]\n"
    "ds_read_b128 a[136:139], v15 offset:2048\n"
    "v_mfma_f32_16x16x32_bf16 v[124:127], a[104:107], a[152:155], v[124:127]\n"
    "ds_read_b128 a[140:143], v16 offset:2048\n"
    "v_mfma_f32_16x16x32_bf16 v[136:139], a[112:115], a[152:155], v[136:139]\n"
    "ds_read_b128 a[144:147], v15 offset:4096\n"
    "v_mfma_f32_16x16x32_bf16 v[136:139], a[116:119], a[156:159], v[136:139]\n"
    "ds_read_b128 a[148:151], v16 offset:4096\n"
    "v_mfma_f32_16x16x32_bf16 v[140:143], a[116:119], a[164:167], v[140:143]\n"
    "v_mfma_f32_16x16x32_bf16 v[140:143], a[112:115], a[160:163], v[140:143]\n"
    "v_mfma_f32_16x16x32_bf16 v[144:147], a[112:115], a[168:171], v[144:147]\n"
    "v_mfma_f32_16x16x32_bf16 v[144:147], a[116:119], a[172:175], v[144:147]\n"
    "v_mfma_f32_16x16x32_bf16 v[156:159], a[124:127], a[172:175], v[156:159]\n"
    "v_mfma_f32_16x16x32_bf16 v[156:159], a[120:123], a[168:171], v[156:159]\n"
    "v_mfma_f32_16x16x32_bf16 v[152:155], a[120:123], a[160:163], v[152:155]\n"
    "v_mfma_f32_16x16x32_bf16 v[152:155], a[124:127], a[164:167], v[152:155]\n"
    "v_mfma_f32_16x16x32_bf16 v[148:151], a[124:127], a[156:159], v[148:151]\n"
    "v_mfma_f32_16x16x32_bf16 v[148:151], a[120:123], a[152:155], v[148:151]\n"
    "s_mov_b32 s39, s38\n"
    "s_add_u32 s38, s38, s40\n"
    "s_sub_u32 s37, s37, 1\n"
    "s_branch .L_qC_%=B\n"
    ".L_qC_%=x:\n"
    "s_waitcnt vmcnt(16) lgkmcnt(0)\n"
    "s_barrier\n"
    "v_mfma_f32_16x16x32_bf16 v[64:67], a[64:67], a[152:155], v[64:67]\n"
    "ds_read_b128 a[0:3], v33 offset:0\n"
    "v_lshrrev_b32 v52, 1, v45\n"
    "v_mfma_f32_16x16x32_bf16 v[64:67], a[68:71], a[156:159], v[64:67]\n"
    "ds_read_b128 a[4:7], v34 offset:0\n"
    "v_xor_b32 v52, v52, v46\n"
    "v_mfma_f32_16x16x32_bf16 v[68:71], a[68:71], a[164:167], v[68:71]\n"
    "ds_read_b128 a[8:11], v35 offset:0\n"
    "v_lshlrev_b32 v52, 4, v52\n"
    "v_mfma_f32_16x16x32_bf16 v[68:71], a[64:67], a[160:163], v[68:71]\n"
    "ds_read_b128 a[12:15], v36 offset:0\n"
    "v_lshl_add_u32 v52, v46, 8, v52\n"
    "v_mfma_f32_16x16x32_bf16 v[72:75], a[64:67], a[168:171], v[72:75]\n"
    "ds_read_b128 a[16:19], v33 offset:4096\n"
    "v_and_b32 v53, 1, v45\n"
    "v_mfma_f32_16x16x32_bf16 v[72:75], a[68:71], a[172:175], v[72:75]\n"
    "ds_read_b128 a[20:23], v34 offset:4096\n"
    "v_lshl_add_u32 v52, v53, 3, v52\n"
    "v_mfma_f32_16x16x32_bf16 v[84:87], a[76:79], a[172:175], v[84:87]\n"
    "ds_read_b128 a[24:27], v35 offset:4096\n"
    "v_add_u32 v17, s61, v52\n"
    "v_mfma_f32_16x16x32_bf16 v[84:87], a[72:75], a[168:171], v[84:87]\n"
    "ds_read_b128 a[28:31], v36 offset:4096\n"
    "v_xor_b32 v18, 32, v17\n"
    "v_mfma_f32_16x16x32_bf16 v[80:83], a[72:75], a[160:163], v[80:83]\n"
    "ds_read_b128 a[32:35], v33 offset:8192\n"
    "v_xor_b32 v19, 64, v17\n"
    "v_mfma_f32_16x16x32_bf16 v[80:83], a[76:79], a[164:167], v[80:83]\n"
    "ds_read_b128 a[36:39], v34 offset:8192\n"
    "v_xor_b32 v20, 96, v17\n"
    "v_mfma_f32_16x16x32_bf16 v[76:79], a[76:79], a[156:159], v[76:79]\n"
    "ds_read_b128 a[40:43], v35 offset:8192\n"
    "v_xor_b32 v21, 128, v17\n"
    "v_mfma_f32_16x16x32_bf16 v[76:79], a[72:75], a[152:155], v[76:79]\n"
    "ds_read_b128 a[44:47], v36 offset:8192\n"
    "v_xor_b32 v22, 160, v17\n"
    "v_mfma_f32_16x16x32_bf16 v[88:91], a[80:83], a[152:155], v[88:91]\n"
    "ds_read_b128 a[48:51], v33 offset:12288\n"
    "v_xor_b32 v23, 192, v17\n"
    "v_mfma_f32_16x16x32_bf16 v[88:91], a[84:87], a[156:159], v[88:91]\n"
    "ds_read_b128 a[52:55], v34 offset:12288\n"
    "v_xor_b32 v24, 224, v17\n"
    "v_mfma_f32_16x16x32_bf16 v[92:95], a[84:87], a[164:167], v[92:95]\n"
    "ds_read_b128 a[56:59], v35 offset:12288\n"
    "v_add_u32 v52, s61, v33\n"
    "v_mfma_f32_16x16x32_bf16 v[92:95], a[80:83], a[160:163], v[92:95]\n"
    "ds_read_b128 a[60:63], v36 offset:12288\n"
    "v_add_u32 v53, s61, v34\n"
    "v_mfma_f32_16x16x32_bf16 v[96:99], a[80:83], a[168:171], v[96:99]\n"
    "v_add_u32 v54, s61, v35\n"
    "v_add_u32 v55, s61, v36\n"
    "v_mfma_f32_16x16x32_bf16 v[96:99], a[84:87], a[172:175], v[96:99]\n"
    "v_mul_f32 v160, s58, v160\n"
    "v_mul_f32 v161, s58, v161\n"
    "v_mfma_f32_16x16x32_bf16 v[108:111], a[92:95], a[172:175], v[108:111]\n"
    "v_mul_f32 v162, s58, v162\n"
    "v_mul_f32 v163, s58, v163\n"
    "v_mfma_f32_16x16x32_bf16 v[108:111], a[88:91], a[168:171], v[108:111]\n"
    "v_cvt_pk_bf16_f32 v56, v160, v161\n"
    "v_cvt_pk_bf16_f32 v57, v162, v163\n"
    "v_mfma_f32_16x16x32_bf16 v[104:107], a[88:91], a[160:163], v[104:107]\n"
    "ds_write_b64 v17, v[56:57]\n"
    "v_mul_f32 v172, s58, v172\n"
    "v_mfma_f32_16x16x32_bf16 v[104:107], a[92:95], a[164:167], v[104:107]\n"
    "v_mul_f32 v173, s58, v173\n"
    "v_mul_f32 v174, s58, v174\n"
    "v_mfma_f32_16x16x32_bf16 v[100:103], a[92:95], a[156:159], v[100:103]\n"
    "v_mul_f32 v175, s58, v175\n"
    "v_cvt_pk_bf16_f32 v58, v172, v173\n"
    "v_mfma_f32_16x16x32_bf16 v[100:103], a[88:91], a[152:155], v[100:103]\n"
    "v_cvt_pk_bf16_f32 v59, v174, v175\n"
    "ds_write_b64 v18, v[58:59]\n"
    "v_mfma_f32_16x16x32_bf16 v[112:115], a[96:99], a[152:155], v[112:115]\n"
    "v_mul_f32 v184, s58, v184\n"
    "v_mul_f32 v185, s58, v185\n"
    "v_mfma_f32_16x16x32_bf16 v[112:115], a[100:103], a[156:159], v[112:115]\n"
    "v_mul_f32 v186, s58, v186\n"
    "v_mul_f32 v187, s58, v187\n"
    "v_mfma_f32_16x16x32_bf16 v[116:119], a[100:103], a[164:167], v[116:119]\n"
    "v_cvt_pk_bf16_f32 v60, v184, v185\n"
    "v_cvt_pk_bf16_f32 v61, v186, v187\n"
    "v_mfma_f32_16x16x32_bf16 v[116:119], a[96:99], a[160:163], v[116:119]\n"
    "ds_write_b64 v19, v[60:61]\n"
    "v_mul_f32 v196, s58, v196\n"
    "v_mfma_f32_16x16x32_bf16 v[120:123], a[96:99], a[168:171], v[120:123]\n"
    "v_mul_f32 v197, s58, v197\n"
    "v_mul_f32 v198, s58, v198\n"
    "v_mfma_f32_16x16x32_bf16 v[120:123], a[100:103], a[172:175], v[120:123]\n"
    "v_mul_f32 v199, s58, v199\n"
    "v_cvt_pk_bf16_f32 v62, v196, v197\n"
    "v_mfma_f32_16x16x32_bf16 v[132:135], a[108:111], a[172:175], v[132:135]\n"
    "v_cvt_pk_bf16_f32 v63, v198, v199\n"
    "ds_write_b64 v20, v[62:63]\n"
    "v_mfma_f32_16x16x32_bf16 v[132:135], a[104:107], a[168:171], v[132:135]\n"
    "v_mul_f32 v208, s58, v208\n"
    "v_mul_f32 v209, s58, v209\n"
    "v_mfma_f32_16x16x32_bf16 v[128:131], a[104:107], a[160:163], v[128:131]\n"
    "v_mul_f32 v210, s58, v210\n"
    "v_mul_f32 v211, s58, v211\n"
    "v_mfma_f32_16x16x32_bf16 v[128:131], a[108:111], a[164:167], v[128:131]\n"
    "v_cvt_pk_bf16_f32 v56, v208, v209\n"
    "v_cvt_pk_bf16_f32 v57, v210, v211\n"
    "v_mfma_f32_16x16x32_bf16 v[124:127], a[108:111], a[156:159], v[124:127]\n"
    "ds_write_b64 v21, v[56:57]\n"
    "v_mul_f32 v220, s58, v220\n"
    "v_mfma_f32_16x16x32_bf16 v[124:127], a[104:107], a[152:155], v[124:127]\n"
    "v_mul_f32 v221, s58, v221\n"
    "v_mul_f32 v222, s58, v222\n"
    "v_mfma_f32_16x16x32_bf16 v[136:139], a[112:115], a[152:155], v[136:139]\n"
    "v_mul_f32 v223, s58, v223\n"
    "v_cvt_pk_bf16_f32 v58, v220, v221\n"
    "v_mfma_f32_16x16x32_bf16 v[136:139], a[116:119], a[156:159], v[136:139]\n"
    "v_cvt_pk_bf16_f32 v59, v222, v223\n"
    "ds_write_b64 v22, v[58:59]\n"
    "v_mfma_f32_16x16x32_bf16 v[140:143], a[116:119], a[164:167], v[140:143]\n"
    "v_mul_f32 v232, s58, v232\n"
    "v_mul_f32 v233, s58, v233\n"
    "v_mfma_f32_16x16x32_bf16 v[140:143], a[112:115], a[160:163], v[140:143]\n"
    "v_mul_f32 v234, s58, v234\n"
    "v_mul_f32 v235, s58, v235\n"
    "v_mfma_f32_16x16x32_bf16 v[144:147], a[112:115], a[168:171], v[144:147]\n"
    "v_cvt_pk_bf16_f32 v60, v232, v233\n"
    "v_cvt_pk_bf16_f32 v61, v234, v235\n"
    "v_mfma_f32_16x16x32_bf16 v[144:147], a[116:119], a[172:175], v[144:147]\n"
    "ds_write_b64 v23, v[60:61]\n"
    "v_mul_f32 v244, s58, v244\n"
    "v_mfma_f32_16x16x32_bf16 v[156:159], a[124:127], a[172:175], v[156:159]\n"
    "v_mul_f32 v245, s58, v245\n"
    "v_mul_f32 v246, s58, v246\n"
    "v_mfma_f32_16x16x32_bf16 v[156:159], a[120:123], a[168:171], v[156:159]\n"
    "v_mul_f32 v247, s58, v247\n"
    "v_cvt_pk_bf16_f32 v62, v244, v245\n"
    "v_mfma_f32_16x16x32_bf16 v[152:155], a[120:123], a[160:163], v[152:155]\n"
    "v_cvt_pk_bf16_f32 v63, v246, v247\n"
    "ds_write_b64 v24, v[62:63]\n"
    "v_mfma_f32_16x16x32_bf16 v[152:155], a[124:127], a[164:167], v[152:155]\n"
    "ds_read_b128 a[176:179], v52\n"
    "v_mfma_f32_16x16x32_bf16 v[148:151], a[124:127], a[156:159], v[148:151]\n"
    "ds_read_b128 a[180:183], v53\n"
    "v_mfma_f32_16x16x32_bf16 v[148:151], a[120:123], a[152:155], v[148:151]\n"
    "ds_read_b128 a[184:187], v54\n"
    "ds_read_b128 a[188:191], v55\n"
    "v_pk_mul_f32 v[164:165], s[58:59], v[164:165]\n"
    "v_pk_mul_f32 v[166:167], s[58:59], v[166:167]\n"
    "v_cvt_pk_bf16_f32 v56, v164, v165\n"
    "v_cvt_pk_bf16_f32 v57, v166, v167\n"
    "ds_write_b64 v17, v[56:57]\n"
    "v_pk_mul_f32 v[176:177], s[58:59], v[176:177]\n"
    "v_pk_mul_f32 v[178:179], s[58:59], v[178:179]\n"
    "v_cvt_pk_bf16_f32 v58, v176, v177\n"
    "v_cvt_pk_bf16_f32 v59, v178, v179\n"
    "ds_write_b64 v18, v[58:59]\n"
    "v_pk_mul_f32 v[188:189], s[58:59], v[188:189]\n"
    "v_pk_mul_f32 v[190:191], s[58:59], v[190:191]\n"
    "v_cvt_pk_bf16_f32 v60, v188, v189\n"
    "v_cvt_pk_bf16_f32 v61, v190, v191\n"
    "ds_write_b64 v19, v[60:61]\n"
    "v_pk_mul_f32 v[200:201], s[58:59], v[200:201]\n"
    "v_pk_mul_f32 v[202:203], s[58:59], v[202:203]\n"
    "v_cvt_pk_bf16_f32 v62, v200, v201\n"
    "v_cvt_pk_bf16_f32 v63, v202, v203\n"
    "ds_write_b64 v20, v[62:63]\n"
    "v_pk_mul_f32 v[212:213], s[58:59], v[212:213]\n"
    "v_pk_mul_f32 v[214:215], s[58:59], v[214:215]\n"
    "v_cvt_pk_bf16_f32 v56, v212, v213\n"
    "v_cvt_pk_bf16_f32 v57, v214, v215\n"
    "ds_write_b64 v21, v[56:57]\n"
    "v_pk_mul_f32 v[224:225], s[58:59], v[224:225]\n"
    "v_pk_mul_f32 v[226:227], s[58:59], v[226:227]\n"
    "v_cvt_pk_bf16_f32 v58, v224, v225\n"
    "v_cvt_pk_bf16_f32 v59, v226, v227\n"
    "ds_write_b64 v22, v[58:59]\n"
    "v_pk_mul_f32 v[236:237], s[58:59], v[236:237]\n"
    "v_pk_mul_f32 v[238:239], s[58:59], v[238:239]\n"
    "v_cvt_pk_bf16_f32 v60, v236, v237\n"
    "v_cvt_pk_bf16_f32 v61, v238, v239\n"
    "ds_write_b64 v23, v[60:61]\n"
    "v_pk_mul_f32 v[248:249], s[58:59], v[248:249]\n"
    "v_pk_mul_f32 v[250:251], s[58:59], v[250:251]\n"
    "v_cvt_pk_bf16_f32 v62, v248, v249\n"
    "v_cvt_pk_bf16_f32 v63, v250, v251\n"
    "ds_write_b64 v24, v[62:63]\n"
    "ds_read_b128 a[192:195], v52\n"
    "ds_read_b128 a[196:199], v53\n"
    "ds_read_b128 a[200:203], v54\n"
    "ds_read_b128 a[204:207], v55\n"
    "v_pk_mul_f32 v[168:169], s[58:59], v[168:169]\n"
    "v_pk_mul_f32 v[170:171], s[58:59], v[170:171]\n"
    "v_cvt_pk_bf16_f32 v56, v168, v169\n"
    "v_cvt_pk_bf16_f32 v57, v170, v171\n"
    "ds_write_b64 v17, v[56:57]\n"
    "v_pk_mul_f32 v[180:181], s[58:59], v[180:181]\n"
    "v_pk_mul_f32 v[182:183], s[58:59], v[182:183]\n"
    "v_cvt_pk_bf16_f32 v58, v180, v181\n"
    "v_cvt_pk_bf16_f32 v59, v182, v183\n"
    "ds_write_b64 v18, v[58:59]\n"
    "v_pk_mul_f32 v[192:193], s[58:59], v[192:193]\n"
    "v_pk_mul_f32 v[194:195], s[58:59], v[194:195]\n"
    "v_cvt_pk_bf16_f32 v60, v192, v193\n"
    "v_cvt_pk_bf16_f32 v61, v194, v195\n"
    "ds_write_b64 v19, v[60:61]\n"
    "v_pk_mul_f32 v[204:205], s[58:59], v[204:205]\n"
    "v_pk_mul_f32 v[206:207], s[58:59], v[206:207]\n"
    "v_cvt_pk_bf16_f32 v62, v204, v205\n"
    "v_cvt_pk_bf16_f32 v63, v206, v207\n"
    "ds_write_b64 v20, v[62:63]\n"
    "v_pk_mul_f32 v[216:217], s[58:59], v[216:217]\n"
    "v_pk_mul_f32 v[218:219], s[58:59], v[218:219]\n"
    "v_cvt_pk_bf16_f32 v56, v216, v217\n"
    "v_cvt_pk_bf16_f32 v57, v218, v219\n"
    "ds_write_b64 v21, v[56:57]\n"
    "v_pk_mul_f32 v[228:229], s[58:59], v[228:229]\n"
    "v_pk_mul_f32 v[230:231], s[58:59], v[230:231]\n"
    "v_cvt_pk_bf16_f32 v58, v228, v229\n"
    "v_cvt_pk_bf16_f32 v59, v230, v231\n"
    "ds_write_b64 v22, v[58:59]\n"
    "v_pk_mul_f32 v[240:241], s[58:59], v[240:241]\n"
    "v_pk_mul_f32 v[242:243], s[58:59], v[242:243]\n"
    "v_cvt_pk_bf16_f32 v60, v240, v241\n"
    "v_cvt_pk_bf16_f32 v61, v242, v243\n"
    "ds_write_b64 v23, v[60:61]\n"
    "v_pk_mul_f32 v[252:253], s[58:59], v[252:253]\n"
    "v_pk_mul_f32 v[254:255], s[58:59], v[254:255]\n"
    "v_cvt_pk_bf16_f32 v62, v252, v253\n"
    "v_cvt_pk_bf16_f32 v63, v254, v255\n"
    "ds_write_b64 v24, v[62:63]\n"
    "ds_read_b128 a[208:211], v52\n"
    "ds_read_b128 a[212:215], v53\n"
    "ds_read_b128 a[216:219], v54\n"
    "ds_read_b128 a[220:223], v55\n"
    "s_mul_i32 s56, %5, 45056\n"
    "s_mul_i32 s46, s36, 3072\n"
    "s_add_u32 s56, s56, s46\n"
    "s_mov_b32 s55, 0\n"
    "s_mov_b32 s53, 0\n"
    "s_mov_b64 s[32:33], %4\n"
    "s_and_b32 s33, s33, 0xffff\n"
    "s_mov_b32 s34, 1024\n"
    "s_mov_b32 s35, 0x20000\n"
    "s_add_u32 s62, s62, 1\n"
    "s_addc_u32 s63, s63, 0\n"
    "s_waitcnt vmcnt(1) lgkmcnt(0)\n"
    "s_barrier\n"
    "v_mfma_f32_16x16x32_bf16 v[160:163], a[0:3], a[176:179], 0\n"
    "v_mfma_f32_16x16x32_bf16 v[160:163], a[4:7], a[180:183], v[160:163]\n"
    "v_mfma_f32_16x16x32_bf16 v[160:163], a[8:11], a[184:187], v[160:163]\n"
    "v_mul_f32 v64, s58, v64\n"
    "v_mul_f32 v65, s58, v65\n"
    "v_mfma_f32_16x16x32_bf16 v[160:163], a[12:15], a[188:191], v[160:163]\n"
    "v_mul_f32 v66, s58, v66\n"
    "v_mul_f32 v67, s58, v67\n"
    "v_mfma_f32_16x16x32_bf16 v[184:187], a[12:15], a[204:207], 0\n"
    "v_cvt_pk_bf16_f32 v56, v64, v65\n"
    "v_cvt_pk_bf16_f32 v57, v66, v67\n"
    "v_mfma_f32_16x16x32_bf16 v[184:187], a[8:11], a[200:203], v[184:187]\n"
    "ds_write_b64 v17, v[56:57]\n"
    "v_mul_f32 v76, s58, v76\n"
    "v_mfma_f32_16x16x32_bf16 v[184:187], a[4:7], a[196:199], v[184:187]\n"
    "v_mul_f32 v77, s58, v77\n"
    "v_mul_f32 v78, s58, v78\n"
    "v_mfma_f32_16x16x32_bf16 v[184:187], a[0:3], a[192:195], v[184:187]\n"
    "v_mul_f32 v79, s58, v79\n"
    "v_cvt_pk_bf16_f32 v58, v76, v77\n"
    "v_mfma_f32_16x16x32_bf16 v[208:211], a[0:3], a[208:211], 0\n"
    "v_cvt_pk_bf16_f32 v59, v78, v79\n"
    "ds_write_b64 v18, v[58:59]\n"
    "v_mfma_f32_16x16x32_bf16 v[208:211], a[4:7], a[212:215], v[208:211]\n"
    "v_mul_f32 v88, s58, v88\n"
    "v_mul_f32 v89, s58, v89\n"
    "v_mfma_f32_16x16x32_bf16 v[208:211], a[8:11], a[216:219], v[208:211]\n"
    "v_mul_f32 v90, s58, v90\n"
    "v_mul_f32 v91, s58, v91\n"
    "v_mfma_f32_16x16x32_bf16 v[208:211], a[12:15], a[220:223], v[208:211]\n"
    "v_cvt_pk_bf16_f32 v60, v88, v89\n"
    "v_cvt_pk_bf16_f32 v61, v90, v91\n"
    "v_mfma_f32_16x16x32_bf16 v[224:227], a[28:31], a[220:223], 0\n"
    "ds_read_b128 a[0:3], v33 offset:16384\n"
    "v_mfma_f32_16x16x32_bf16 v[224:227], a[24:27], a[216:219], v[224:227]\n"
    "ds_read_b128 a[4:7], v34 offset:16384\n"
    "v_mfma_f32_16x16x32_bf16 v[224:227], a[20:23], a[212:215], v[224:227]\n"
    "ds_read_b128 a[8:11], v35 offset:16384\n"
    "v_mfma_f32_16x16x32_bf16 v[224:227], a[16:19], a[208:211], v[224:227]\n"
    "ds_read_b128 a[12:15], v36 offset:16384\n"
    "v_mfma_f32_16x16x32_bf16 v[200:203], a[16:19], a[192:195], 0\n"
    "ds_write_b64 v19, v[60:61]\n"
    "v_mul_f32 v100, s58, v100\n"
    "v_mfma_f32_16x16x32_bf16 v[200:203], a[20:23], a[196:199], v[200:203]\n"
    "v_mul_f32 v101, s58, v101\n"
    "v_mul_f32 v102, s58, v102\n"
    "v_mfma_f32_16x16x32_bf16 v[200:203], a[24:27], a[200:203], v[200:203]\n"
    "v_mul_f32 v103, s58, v103\n"
    "v_cvt_pk_bf16_f32 v62, v100, v101\n"
    "v_mfma_f32_16x16x32_bf16 v[200:203], a[28:31], a[204:207], v[200:203]\n"
    "v_cvt_pk_bf16_f32 v63, v102, v103\n"
    "ds_write_b64 v20, v[62:63]\n"
    "v_mfma_f32_16x16x32_bf16 v[176:179], a[28:31], a[188:191], 0\n"
    "v_mul_f32 v112, s58, v112\n"
    "v_mul_f32 v113, s58, v113\n"
    "v_mfma_f32_16x16x32_bf16 v[176:179], a[24:27], a[184:187], v[176:179]\n"
    "v_mul_f32 v114, s58, v114\n"
    "v_mul_f32 v115, s58, v115\n"
    "v_mfma_f32_16x16x32_bf16 v[176:179], a[20:23], a[180:183], v[176:179]\n"
    "v_cvt_pk_bf16_f32 v56, v112, v113\n"
    "v_cvt_pk_bf16_f32 v57, v114, v115\n"
    "v_mfma_f32_16x16x32_bf16 v[176:179], a[16:19], a[176:179], v[176:179]\n"
    "ds_write_b64 v21, v[56:57]\n"
    "v_mul_f32 v124, s58, v124\n"
    "v_mfma_f32_16x16x32_bf16 v[168:171], a[32:35], a[176:179], 0\n"
    "ds_read_b128 a[16:19], v33 offset:20480\n"
    "v_mul_f32 v125, s58, v125\n"
    "v_mfma_f32_16x16x32_bf16 v[168:171], a[36:39], a[180:183], v[168:171]\n"
    "ds_read_b128 a[20:23], v34 offset:20480\n"
    "v_mul_f32 v126, s58, v126\n"
    "v_mfma_f32_16x16x32_bf16 v[168:171], a[40:43], a[184:187], v[168:171]\n"
    "ds_read_b128 a[24:27], v35 offset:20480\n"
    "v_mul_f32 v127, s58, v127\n"
    "v_mfma_f32_16x16x32_bf16 v[168:171], a[44:47], a[188:191], v[168:171]\n"
    "ds_read_b128 a[28:31], v36 offset:20480\n"
    "v_cvt_pk_bf16_f32 v58, v124, v125\n"
    "v_mfma_f32_16x16x32_bf16 v[192:195], a[44:47], a[204:207], 0\n"
    "v_cvt_pk_bf16_f32 v59, v126, v127\n"
    "ds_write_b64 v22, v[58:59]\n"
    "v_mfma_f32_16x16x32_bf16 v[192:195], a[40:43], a[200:203], v[192:195]\n"
    "v_mul_f32 v136, s58, v136\n"
    "v_mul_f32 v137, s58, v137\n"
    "v_mfma_f32_16x16x32_bf16 v[192:195], a[36:39], a[196:199], v[192:195]\n"
    "v_mul_f32 v138, s58, v138\n"
    "v_mul_f32 v139, s58, v139\n"
    "v_mfma_f32_16x16x32_bf16 v[192:195], a[32:35], a[192:195], v[192:195]\n"
    "v_cvt_pk_bf16_f32 v60, v136, v137\n"
    "v_cvt_pk_bf16_f32 v61, v138, v139\n"
    "v_mfma_f32_16x16x32_bf16 v[216:219], a[32:35], a[208:211], 0\n"
    "ds_write_b64 v23, v[60:61]\n"
    "v_mul_f32 v148, s58, v148\n"
    "v_mfma_f32_16x16x32_bf16 v[216:219], a[36:39], a[212:215], v[216:219]\n"
    "v_mul_f32 v149, s58, v149\n"
    "v_mul_f32 v150, s58, v150\n"
    "v_mfma_f32_16x16x32_bf16 v[216:219], a[40:43], a[216:219], v[216:219]\n"
    "v_mul_f32 v151, s58, v151\n"
    "v_cvt_pk_bf16_f32 v62, v148, v149\n"
    "v_mfma_f32_16x16x32_bf16 v[216:219], a[44:47], a[220:223], v[216:219]\n"
    "v_cvt_pk_bf16_f32 v63, v150, v151\n"
    "ds_write_b64 v24, v[62:63]\n"
    "v_mfma_f32_16x16x32_bf16 v[228:231], a[60:63], a[220:223], 0\n"
    "ds_read_b128 a[32:35], v33 offset:24576\n"
    "v_mfma_f32_16x16x32_bf16 v[228:231], a[56:59], a[216:219], v[228:231]\n"
    "ds_read_b128 a[36:39], v34 offset:24576\n"
    "v_mfma_f32_16x16x32_bf16 v[228:231], a[52:55], a[212:215], v[228:231]\n"
    "ds_read_b128 a[40:43], v35 offset:24576\n"
    "v_mfma_f32_16x16x32_bf16 v[228:231], a[48:51], a[208:211], v[228:231]\n"
    "ds_read_b128 a[44:47], v36 offset:24576\n"
    "v_mfma_f32_16x16x32_bf16 v[204:207], a[48:51], a[192:195], 0\n"
    "ds_read_b128 a[128:131], v52\n"
    "v_mfma_f32_16x16x32_bf16 v[204:207], a[52:55], a[196:199], v[204:207]\n"
    "ds_read_b128 a[132:135], v53\n"
    "v_mfma_f32_16x16x32_bf16 v[204:207], a[56:59], a[200:203], v[204:207]\n"
    "ds_read_b128 a[136:139], v54\n"
    "v_mfma_f32_16x16x32_bf16 v[204:207], a[60:63], a[204:207], v[204:207]\n"
    "ds_read_b128 a[140:143], v55\n"
    "v_mul_f32 v68, s58, v68\n"
    "v_mfma_f32_16x16x32_bf16 v[180:183], a[60:63], a[188:191], 0\n"
    "v_mul_f32 v69, s58, v69\n"
    "v_mul_f32 v70, s58, v70\n"
    "v_mfma_f32_16x16x32_bf16 v[180:183], a[56:59], a[184:187], v[180:183]\n"
    "v_mul_f32 v71, s58, v71\n"
    "v_cvt_pk_bf16_f32 v56, v68, v69\n"
    "v_mfma_f32_16x16x32_bf16 v[180:183], a[52:55], a[180:183], v[180:183]\n"
    "v_cvt_pk_bf16_f32 v57, v70, v71\n"
    "ds_write_b64 v17, v[56:57]\n"
    "v_mfma_f32_16x16x32_bf16 v[180:183], a[48:51], a[176:179], v[180:183]\n"
    "v_mul_f32 v80, s58, v80\n"
    "v_mul_f32 v81, s58, v81\n"
    "ds_read_b128 a[48:51], v33 offset:28672\n"
    "ds_read_b128 a[52:55], v34 offset:28672\n"
    "ds_read_b128 a[56:59], v35 offset:28672\n"
    "ds_read_b128 a[60:63], v36 offset:28672\n"
    "v_mul_f32 v82, s58, v82\n"
    "v_mul_f32 v83, s58, v83\n"
    "v_cvt_pk_bf16_f32 v58, v80, v81\n"
    "v_cvt_pk_bf16_f32 v59, v82, v83\n"
    "ds_write_b64 v18, v[58:59]\n"
    "v_pk_mul_f32 v[92:93], s[58:59], v[92:93]\n"
    "v_pk_mul_f32 v[94:95], s[58:59], v[94:95]\n"
    "v_cvt_pk_bf16_f32 v60, v92, v93\n"
    "v_cvt_pk_bf16_f32 v61, v94, v95\n"
    "ds_write_b64 v19, v[60:61]\n"
    "v_pk_mul_f32 v[104:105], s[58:59], v[104:105]\n"
    "v_pk_mul_f32 v[106:107], s[58:59], v[106:107]\n"
    "v_cvt_pk_bf16_f32 v62, v104, v105\n"
    "v_cvt_pk_bf16_f32 v63, v106, v107\n"
    "ds_write_b64 v20, v[62:63]\n"
    "v_pk_mul_f32 v[116:117], s[58:59], v[116:117]\n"
    "v_pk_mul_f32 v[118:119], s[58:59], v[118:119]\n"
    "v_cvt_pk_bf16_f32 v56, v116, v117\n"
    "v_cvt_pk_bf16_f32 v57, v118, v119\n"
    "ds_write_b64 v21, v[56:57]\n"
    "v_pk_mul_f32 v[128:129], s[58:59], v[128:129]\n"
    "v_pk_mul_f32 v[130:131], s[58:59], v[130:131]\n"
    "v_cvt_pk_bf16_f32 v58, v128, v129\n"
    "v_cvt_pk_bf16_f32 v59, v130, v131\n"
    "ds_write_b64 v22, v[58:59]\n"
    "v_pk_mul_f32 v[140:141], s[58:59], v[140:141]\n"
    "v_pk_mul_f32 v[142:143], s[58:59], v[142:143]\n"
    "v_cvt_pk_bf16_f32 v60, v140, v141\n"
    "v_cvt_pk_bf16_f32 v61, v142, v143\n"
    "ds_write_b64 v23, v[60:61]\n"
    "v_pk_mul_f32 v[152:153], s[58:59], v[152:153]\n"
    "v_pk_mul_f32 v[154:155], s[58:59], v[154:155]\n"
    "v_cvt_pk_bf16_f32 v62, v152, v153\n"
    "v_cvt_pk_bf16_f32 v63, v154, v155\n"
    "ds_write_b64 v24, v[62:63]\n"
    "ds_read_b128 a[144:147], v52\n"
    "ds_read_b128 a[148:151], v53\n"
    "ds_read_b128 a[152:155], v54\n"
    "ds_read_b128 a[156:159], v55\n"
    "v_pk_mul_f32 v[72:73], s[58:59], v[72:73]\n"
    "v_pk_mul_f32 v[74:75], s[58:59], v[74:75]\n"
    "v_cvt_pk_bf16_f32 v56, v72, v73\n"
    "v_cvt_pk_bf16_f32 v57, v74, v75\n"
    "ds_write_b64 v17, v[56:57]\n"
    "v_pk_mul_f32 v[84:85], s[58:59], v[84:85]\n"
    "v_pk_mul_f32 v[86:87], s[58:59], v[86:87]\n"
    "v_cvt_pk_bf16_f32 v58, v84, v85\n"
    "v_cvt_pk_bf16_f32 v59, v86, v87\n"
    "ds_write_b64 v18, v[58:59]\n"
    "v_pk_mul_f32 v[96:97], s[58:59], v[96:97]\n"
    "v_pk_mul_f32 v[98:99], s[58:59], v[98:99]\n"
    "v_cvt_pk_bf16_f32 v60, v96, v97\n"
    "v_cvt_pk_bf16_f32 v61, v98, v99\n"
    "ds_write_b64 v19, v[60:61]\n"
    "v_pk_mul_f32 v[108:109], s[58:59], v[108:109]\n"
    "v_pk_mul_f32 v[110:111], s[58:59], v[110:111]\n"
    "v_cvt_pk_bf16_f32 v62, v108, v109\n"
    "v_cvt_pk_bf16_f32 v63, v110, v111\n"
    "ds_write_b64 v20, v[62:63]\n"
    "v_pk_mul_f32 v[120:121], s[58:59], v[120:121]\n"
    "v_pk_mul_f32 v[122:123], s[58:59], v[122:123]\n"
    "v_cvt_pk_bf16_f32 v56, v120, v121\n"
    "v_cvt_pk_bf16_f32 v57, v122, v123\n"
    "ds_write_b64 v21, v[56:57]\n"
    "v_pk_mul_f32 v[132:133], s[58:59], v[132:133]\n"
    "v_pk_mul_f32 v[134:135], s[58:59], v[134:135]\n"
    "v_cvt_pk_bf16_f32 v58, v132, v133\n"
    "v_cvt_pk_bf16_f32 v59, v134, v135\n"
    "ds_write_b64 v22, v[58:59]\n"
    "v_pk_mul_f32 v[144:145], s[58:59], v[144:145]\n"
    "v_pk_mul_f32 v[146:147], s[58:59], v[146:147]\n"
    "v_cvt_pk_bf16_f32 v60, v144, v145\n"
    "v_cvt_pk_bf16_f32 v61, v146, v147\n"
    "ds_write_b64 v23, v[60:61]\n"
    "v_pk_mul_f32 v[156:157], s[58:59], v[156:157]\n"
    "v_pk_mul_f32 v[158:159], s[58:59], v[158:159]\n"
    "v_cvt_pk_bf16_f32 v62, v156, v157\n"
    "v_cvt_pk_bf16_f32 v63, v158, v159\n"
    "ds_write_b64 v24, v[62:63]\n"
    "ds_read_b128 a[160:163], v52\n"
    "ds_read_b128 a[164:167], v53\n"
    "ds_read_b128 a[168:171], v54\n"
    "ds_read_b128 a[172:175], v55\n"
    "v_mfma_f32_32x32x16_bf16 v[64:79], a[228:231], a[228:231], 0\n"
    "v_mfma_f32_32x32x16_bf16 v[80:95], a[228:231], a[228:231], 0\n"
    "v_mfma_f32_32x32x16_bf16 v[96:111], a[228:231], a[228:231], 0\n"
    "v_mfma_f32_32x32x16_bf16 v[112:127], a[228:231], a[228:231], 0\n"
    "v_mfma_f32_32x32x16_bf16 v[128:143], a[228:231], a[228:231], 0\n"
    "v_mfma_f32_32x32x16_bf16 v[144:159], a[228:231], a[228:231], 0\n"
    "v_mov_b32 v244, 0\n"
    "v_mov_b32 v245, 0\n"
    "v_mov_b32 v246, 0\n"
    "v_mov_b32 v247, 0\n"
    "v_mov_b32 v248, 0\n"
    "v_mov_b32 v249, 0\n"
    "v_mov_b32 v250, 0\n"
    "v_mov_b32 v251, 0\n"
    "v_mov_b32 v252, 0\n"
    "v_mov_b32 v253, 0\n"
    "v_mov_b32 v254, 0\n"
    "v_mov_b32 v255, 0\n"
    "v_max3_f32 v44, v160, v161, v162\n"
    "v_max3_f32 v44, v44, v163, v176\n"
    "v_max3_f32 v44, v44, v177, v178\n"
    "v_max3_f32 v44, v44, v179, v168\n"
    "v_max3_f32 v44, v44, v169, v170\n"
    "v_max3_f32 v44, v44, v171, v180\n"
    "v_max3_f32 v44, v44, v181, v182\n"
    "v_max_f32 v44, v44, v183\n"
    "v_max3_f32 v46, v184, v185, v186\n"
    "v_max3_f32 v46, v46, v187, v200\n"
    "v_max3_f32 v46, v46, v201, v202\n"
    "v_max3_f32 v46, v46, v203, v192\n"
    "v_max3_f32 v46, v46, v193, v194\n"
    "v_max3_f32 v46, v46, v195, v204\n"
    "v_max3_f32 v46, v46, v205, v206\n"
    "v_max_f32 v46, v46, v207\n"
    "v_max3_f32 v48, v208, v209, v210\n"
    "v_max3_f32 v48, v48, v211, v224\n"
    "v_max3_f32 v48, v48, v225, v226\n"
    "v_max3_f32 v48, v48, v227, v216\n"
    "v_max3_f32 v48, v48, v217, v218\n"
    "v_max3_f32 v48, v48, v219, v228\n"
    "v_max3_f32 v48, v48, v229, v230\n"
    "v_max_f32 v48, v48, v231\n"
    "v_mov_b32 v45, v44\n"
    "v_mov_b32 v47, v46\n"
    "v_mov_b32 v49, v48\n"
    "s_nop 1\n"
    "v_permlane32_swap_b32 v44, v45\n"
    "v_permlane32_swap_b32 v46, v47\n"
    "v_permlane32_swap_b32 v48, v49\n"
    "v_max_f32 v44, v44, v45\n"
    "v_max_f32 v46, v46, v47\n"
    "v_max_f32 v48, v48, v49\n"
    "v_mov_b32 v45, v44\n"
    "v_mov_b32 v47, v46\n"
    "v_mov_b32 v49, v48\n"
    "s_nop 1\n"
    "v_permlane16_swap_b32 v44, v45\n"
    "v_permlane16_swap_b32 v46, v47\n"
    "v_permlane16_swap_b32 v48, v49\n"
    "v_max_f32 v44, v44, v45\n"
    "v_max_f32 v46, v46, v47\n"
    "v_max_f32 v48, v48, v49\n"
    "v_sub_f32 v232, 0, v44\n"
    "v_sub_f32 v233, 0, v44\n"
    "v_sub_f32 v234, 0, v44\n"
    "v_sub_f32 v235, 0, v44\n"
    "v_sub_f32 v236, 0, v46\n"
    "v_sub_f32 v237, 0, v46\n"
    "v_sub_f32 v238, 0, v46\n"
    "v_sub_f32 v239, 0, v46\n"
    "v_sub_f32 v240, 0, v48\n"
    "v_sub_f32 v241, 0, v48\n"
    "v_sub_f32 v242, 0, v48\n"
    "v_sub_f32 v243, 0, v48\n"
    "v_pk_add_f32 v[160:161], v[232:233], v[160:161]\n"
    "v_pk_add_f32 v[162:163], v[232:233], v[162:163]\n"
    "v_pk_add_f32 v[184:185], v[236:237], v[184:185]\n"
    "v_pk_add_f32 v[186:187], v[236:237], v[186:187]\n"
    "v_pk_add_f32 v[208:209], v[240:241], v[208:209]\n"
    "v_pk_add_f32 v[210:211], v[240:241], v[210:211]\n"
    "v_pk_add_f32 v[176:177], v[232:233], v[176:177]\n"
    "v_pk_add_f32 v[178:179], v[232:233], v[178:179]\n"
    "v_pk_add_f32 v[200:201], v[236:237], v[200:201]\n"
    "v_pk_add_f32 v[202:203], v[236:237], v[202:203]\n"
    "v_pk_add_f32 v[224:225], v[240:241], v[224:225]\n"
    "v_pk_add_f32 v[226:227], v[240:241], v[226:227]\n"
    "v_pk_add_f32 v[168:169], v[232:233], v[168:169]\n"
    "v_pk_add_f32 v[170:171], v[232:233], v[170:171]\n"
    "v_pk_add_f32 v[192:193], v[236:237], v[192:193]\n"
    "v_pk_add_f32 v[194:195], v[236:237], v[194:195]\n"
    "v_pk_add_f32 v[216:217], v[240:241], v[216:217]\n"
    "v_pk_add_f32 v[218:219], v[240:241], v[218:219]\n"
    "v_pk_add_f32 v[180:181], v[232:233], v[180:181]\n"
    "v_pk_add_f32 v[182:183], v[232:233], v[182:183]\n"
    "v_pk_add_f32 v[204:205], v[236:237], v[204:205]\n"
    "v_pk_add_f32 v[206:207], v[236:237], v[206:207]\n"
    "v_pk_add_f32 v[228:229], v[240:241], v[228:229]\n"
    "v_pk_add_f32 v[230:231], v[240:241], v[230:231]\n"
    "v_exp_f32 v160, v160\n"
    "v_exp_f32 v161, v161\n"
    "v_exp_f32 v162, v162\n"
    "v_exp_f32 v163, v163\n"
    "v_exp_f32 v184, v184\n"
    "v_exp_f32 v185, v185\n"
    "v_exp_f32 v186, v186\n"
    "v_exp_f32 v187, v187\n"
    "v_exp_f32 v208, v208\n"
    "v_exp_f32 v209, v209\n"
    "v_exp_f32 v210, v210\n"
    "v_exp_f32 v211, v211\n"
    "v_exp_f32 v176, v176\n"
    "v_exp_f32 v177, v177\n"
    "v_exp_f32 v178, v178\n"
    "v_exp_f32 v179, v179\n"
    "v_exp_f32 v200, v200\n"
    "v_exp_f32 v201, v201\n"
    "v_exp_f32 v202, v202\n"
    "v_exp_f32 v203, v203\n"
    "v_exp_f32 v224, v224\n"
    "v_exp_f32 v225, v225\n"
    "v_exp_f32 v226, v226\n"
    "v_exp_f32 v227, v227\n"
    "v_exp_f32 v168, v168\n"
    "v_exp_f32 v169, v169\n"
    "v_exp_f32 v170, v170\n"
    "v_exp_f32 v171, v171\n"
    "v_exp_f32 v192, v192\n"
    "v_exp_f32 v193, v193\n"
    "v_exp_f32 v194, v194\n"
    "v_exp_f32 v195, v195\n"
    "v_exp_f32 v216, v216\n"
    "v_exp_f32 v217, v217\n"
    "v_exp_f32 v218, v218\n"
    "v_exp_f32 v219, v219\n"
    "v_exp_f32 v180, v180\n"
    "v_exp_f32 v181, v181\n"
    "v_exp_f32 v182, v182\n"
    "v_exp_f32 v183, v183\n"
    "v_exp_f32 v204, v204\n"
    "v_exp_f32 v205, v205\n"
    "v_exp_f32 v206, v206\n"
    "v_exp_f32 v207, v207\n"
    "v_exp_f32 v228, v228\n"
    "v_exp_f32 v229, v229\n"
    "v_exp_f32 v230, v230\n"
    "v_exp_f32 v231, v231\n"
    "v_cvt_pk_bf16_f32 v160, v160, v161\n"
    "v_cvt_pk_bf16_f32 v161, v162, v163\n"
    "v_cvt_pk_bf16_f32 v184, v184, v185\n"
    "v_cvt_pk_bf16_f32 v185, v186, v187\n"
    "v_cvt_pk_bf16_f32 v208, v208, v209\n"
    "v_cvt_pk_bf16_f32 v209, v210, v211\n"
    "v_cvt_pk_bf16_f32 v162, v176, v177\n"
    "v_cvt_pk_bf16_f32 v163, v178, v179\n"
    "v_cvt_pk_bf16_f32 v186, v200, v201\n"
    "v_cvt_pk_bf16_f32 v187, v202, v203\n"
    "v_cvt_pk_bf16_f32 v210, v224, v225\n"
    "v_cvt_pk_bf16_f32 v211, v226, v227\n"
    "v_cvt_pk_bf16_f32 v168, v168, v169\n"
    "v_cvt_pk_bf16_f32 v169, v170, v171\n"
    "v_cvt_pk_bf16_f32 v192, v192, v193\n"
    "v_cvt_pk_bf16_f32 v193, v194, v195\n"
    "v_cvt_pk_bf16_f32 v216, v216, v217\n"
    "v_cvt_pk_bf16_f32 v217, v218, v219\n"
    "v_cvt_pk_bf16_f32 v170, v180, v181\n"
    "v_cvt_pk_bf16_f32 v171, v182, v183\n"
    "v_cvt_pk_bf16_f32 v194, v204, v205\n"
    "v_cvt_pk_bf16_f32 v195, v206, v207\n"
    "v_cvt_pk_bf16_f32 v218, v228, v229\n"
    "v_cvt_pk_bf16_f32 v219, v230, v231\n"
    ".L_aC_%=h:\n"
    "s_mov_b32 s37, 3\n"
    ".L_aC_%=:\n"
    "s_waitcnt lgkmcnt(0)\n"
    "s_barrier\n"
    "v_mfma_f32_16x16x32_bf16 v[164:167], a[0:3], a[176:179], v[232:235]\n"
    "s_cmp_eq_u32 s53, 0x10000\n"
    "ds_read_b64_tr_b16 a[64:65], v25 offset:0\n"
    "v_mfma_f32_16x16x32_bf16 v[164:167], a[4:7], a[180:183], v[164:167]\n"
    "s_cselect_b32 s54, 0xffff0000, s41\n"
    "ds_read_b64_tr_b16 a[66:67], v25 offset:4096\n"
    "v_mfma_f32_16x16x32_bf16 v[164:167], a[8:11], a[184:187], v[164:167]\n"
    "s_add_u32 s53, s53, s54\n"
    "ds_read_b64_tr_b16 a[68:69], v25 offset:8192\n"
    "v_mfma_f32_16x16x32_bf16 v[164:167], a[12:15], a[188:191], v[164:167]\n"
    "ds_read_b64_tr_b16 a[70:71], v25 offset:12288\n"
    "v_add_u32 v25, s54, v25\n"
    "v_mfma_f32_16x16x32_bf16 v[188:191], a[12:15], a[204:207], v[236:239]\n"
    "ds_read_b64_tr_b16 a[72:73], v26 offset:0\n"
    "v_mfma_f32_16x16x32_bf16 v[188:191], a[8:11], a[200:203], v[188:191]\n"
    "ds_read_b64_tr_b16 a[74:75], v26 offset:4096\n"
    "global_load_dword v1, v[2:3], off\n"
    "v_mfma_f32_16x16x32_bf16 v[188:191], a[4:7], a[196:199], v[188:191]\n"
    "ds_read_b64_tr_b16 a[76:77], v26 offset:8192\n"
    "v_mfma_f32_16x16x32_bf16 v[188:191], a[0:3], a[192:195], v[188:191]\n"
    "ds_read_b64_tr_b16 a[78:79], v26 offset:12288\n"
    "v_add_u32 v26, s54, v26\n"
    "v_mfma_f32_16x16x32_bf16 v[212:215], a[0:3], a[208:211], v[240:243]\n"
    "ds_read_b64_tr_b16 a[80:81], v27 offset:0\n"
    "v_mfma_f32_16x16x32_bf16 v[212:215], a[4:7], a[212:215], v[212:215]\n"
    "ds_read_b64_tr_b16 a[82:83], v27 offset:4096\n"
    "v_mfma_f32_16x16x32_bf16 v[212:215], a[8:11], a[216:219], v[212:215]\n"
    "ds_read_b64_tr_b16 a[84:85], v27 offset:8192\n"
    "v_mfma_f32_16x16x32_bf16 v[212:215], a[12:15], a[220:223], v[212:215]\n"
    "ds_read_b64_tr_b16 a[86:87], v27 offset:12288\n"
    "v_add_u32 v27, s54, v27\n"
    "v_mfma_f32_16x16x32_bf16 v[224:227], a[28:31], a[220:223], v[240:243]\n"
    "ds_read_b64_tr_b16 a[88:89], v28 offset:0\n"
    "v_xor_b32 v33, 0x8000, v33\n"
    "v_mfma_f32_16x16x32_bf16 v[224:227], a[24:27], a[216:219], v[224:227]\n"
    "ds_read_b64_tr_b16 a[90:91], v28 offset:4096\n"
    "v_mfma_f32_16x16x32_bf16 v[224:227], a[20:23], a[212:215], v[224:227]\n"
    "ds_read_b64_tr_b16 a[92:93], v28 offset:8192\n"
    "v_xor_b32 v34, 0x8000, v34\n"
    "v_mfma_f32_16x16x32_bf16 v[224:227], a[16:19], a[208:211], v[224:227]\n"
    "ds_read_b64_tr_b16 a[94:95], v28 offset:12288\n"
    "v_add_u32 v28, s54, v28\n"
    "v_mfma_f32_16x16x32_bf16 v[200:203], a[16:19], a[192:195], v[236:239]\n"
    "ds_read_b64_tr_b16 a[96:97], v29 offset:0\n"
    "v_xor_b32 v35, 0x8000, v35\n"
    "v_mfma_f32_16x16x32_bf16 v[200:203], a[20:23], a[196:199], v[200:203]\n"
    "ds_read_b64_tr_b16 a[98:99], v29 offset:4096\n"
    "v_mfma_f32_16x16x32_bf16 v[200:203], a[24:27], a[200:203], v[200:203]\n"
    "ds_read_b64_tr_b16 a[100:101], v29 offset:8192\n"
    "v_xor_b32 v36, 0x8000, v36\n"
    "v_mfma_f32_16x16x32_bf16 v[200:203], a[28:31], a[204:207], v[200:203]\n"
    "ds_read_b64_tr_b16 a[102:103], v29 offset:12288\n"
    "v_add_u32 v29, s54, v29\n"
    "v_mfma_f32_16x16x32_bf16 v[176:179], a[28:31], a[188:191], v[232:235]\n"
    "ds_read_b64_tr_b16 a[104:105], v30 offset:0\n"
    "v_mfma_f32_16x16x32_bf16 v[176:179], a[24:27], a[184:187], v[176:179]\n"
    "ds_read_b64_tr_b16 a[106:107], v30 offset:4096\n"
    "v_mfma_f32_16x16x32_bf16 v[176:179], a[20:23], a[180:183], v[176:179]\n"
    "ds_read_b64_tr_b16 a[108:109], v30 offset:8192\n"
    "v_mfma_f32_16x16x32_bf16 v[176:179], a[16:19], a[176:179], v[176:179]\n"
    "ds_read_b64_tr_b16 a[110:111], v30 offset:12288\n"
    "v_add_u32 v30, s54, v30\n"
    "v_mfma_f32_16x16x32_bf16 v[172:175], a[32:35], a[176:179], v[232:235]\n"
    "ds_read_b64_tr_b16 a[112:113], v31 offset:0\n"
    "v_mfma_f32_16x16x32_bf16 v[172:175], a[36:39], a[180:183], v[172:175]\n"
    "ds_read_b64_tr_b16 a[114:115], v31 offset:4096\n"
    "v_mfma_f32_16x16x32_bf16 v[172:175], a[40:43], a[184:187], v[172:175]\n"
    "ds_read_b64_tr_b16 a[116:117], v31 offset:8192\n"
    "v_mfma_f32_16x16x32_bf16 v[172:175], a[44:47], a[188:191], v[172:175]\n"
    "ds_read_b64_tr_b16 a[118:119], v31 offset:12288\n"
    "v_add_u32 v31, s54, v31\n"
    "v_mfma_f32_16x16x32_bf16 v[196:199], a[44:47], a[204:207], v[236:239]\n"
    "ds_read_b64_tr_b16 a[120:121], v32 offset:0\n"
    "v_mfma_f32_16x16x32_bf16 v[196:199], a[40:43], a[200:203], v[196:199]\n"
    "ds_read_b64_tr_b16 a[122:123], v32 offset:4096\n"
    "v_mfma_f32_16x16x32_bf16 v[196:199], a[36:39], a[196:199], v[196:199]\n"
    "ds_read_b64_tr_b16 a[124:125], v32 offset:8192\n"
    "v_mfma_f32_16x16x32_bf16 v[196:199], a[32:35], a[192:195], v[196:199]\n"
    "ds_read_b64_tr_b16 a[126:127], v32 offset:12288\n"
    "v_add_u32 v32, s54, v32\n"
    "v_mfma_f32_16x16x32_bf16 v[220:223], a[32:35], a[208:211], v[240:243]\n"
    "v_exp_f32 v164, v164\n"
    "v_mfma_f32_16x16x32_bf16 v[220:223], a[36:39], a[212:215], v[220:223]\n"
    "v_exp_f32 v165, v165\n"
    "v_mfma_f32_16x16x32_bf16 v[220:223], a[40:43], a[216:219], v[220:223]\n"
    "v_exp_f32 v166, v166\n"
    "v_mfma_f32_16x16x32_bf16 v[220:223], a[44:47], a[220:223], v[220:223]\n"
    "v_exp_f32 v167, v167\n"
    "v_mfma_f32_16x16x32_bf16 v[228:231], a[60:63], a[220:223], v[240:243]\n"
    "v_exp_f32 v188, v188\n"
    "v_mfma_f32_16x16x32_bf16 v[228:231], a[56:59], a[216:219], v[228:231]\n"
    "v_exp_f32 v189, v189\n"
    "v_mfma_f32_16x16x32_bf16 v[228:231], a[52:55], a[212:215], v[228:231]\n"
    "v_exp_f32 v190, v190\n"
    "v_mfma_f32_16x16x32_bf16 v[228:231], a[48:51], a[208:211], v[228:231]\n"
    "v_exp_f32 v191, v191\n"
    "v_mfma_f32_16x16x32_bf16 v[204:207], a[48:51], a[192:195], v[236:239]\n"
    "v_exp_f32 v212, v212\n"
    "v_mfma_f32_16x16x32_bf16 v[204:207], a[52:55], a[196:199], v[204:207]\n"
    "v_exp_f32 v213, v213\n"
    "v_mfma_f32_16x16x32_bf16 v[204:207], a[56:59], a[200:203], v[204:207]\n"
    "v_exp_f32 v214, v214\n"
    "v_mfma_f32_16x16x32_bf16 v[204:207], a[60:63], a[204:207], v[204:207]\n"
    "v_exp_f32 v215, v215\n"
    "v_mfma_f32_16x16x32_bf16 v[180:183], a[60:63], a[188:191], v[232:235]\n"
    "v_exp_f32 v176, v176\n"
    "v_mfma_f32_16x16x32_bf16 v[180:183], a[56:59], a[184:187], v[180:183]\n"
    "v_exp_f32 v177, v177\n"
    "v_mfma_f32_16x16x32_bf16 v[180:183], a[52:55], a[180:183], v[180:183]\n"
    "v_exp_f32 v178, v178\n"
    "v_mfma_f32_16x16x32_bf16 v[180:183], a[48:51], a[176:179], v[180:183]\n"
    "s_waitcnt lgkmcnt(0)\n"
    "v_exp_f32 v179, v179\n"
    "v_mfma_f32_16x16x32_bf16 v[64:67], a[64:67], v[160:163], v[64:67]\n"
    "ds_read_b128 a[0:3], v33 offset:0\n"
    "v_cvt_pk_bf16_f32 v164, v164, v165\n"
    "v_mfma_f32_16x16x32_bf16 v[64:67], a[68:71], v[168:171], v[64:67]\n"
    "v_exp_f32 v200, v200\n"
    "v_mfma_f32_16x16x32_bf16 v[68:71], a[68:71], v[192:195], v[68:71]\n"
    "v_exp_f32 v201, v201\n"
    "v_mfma_f32_16x16x32_bf16 v[68:71], a[64:67], v[184:187], v[68:71]\n"
    "ds_read_b128 a[4:7], v34 offset:0\n"
    "v_cvt_pk_bf16_f32 v165, v166, v167\n"
    "v_mfma_f32_16x16x32_bf16 v[72:75], a[64:67], v[208:211], v[72:75]\n"
    "v_exp_f32 v202, v202\n"
    "v_mfma_f32_16x16x32_bf16 v[72:75], a[68:71], v[216:219], v[72:75]\n"
    "v_exp_f32 v203, v203\n"
    "v_mfma_f32_16x16x32_bf16 v[84:87], a[76:79], v[216:219], v[84:87]\n"
    "ds_read_b128 a[8:11], v35 offset:0\n"
    "v_cvt_pk_bf16_f32 v188, v188, v189\n"
    "v_mfma_f32_16x16x32_bf16 v[84:87], a[72:75], v[208:211], v[84:87]\n"
    "v_exp_f32 v224, v224\n"
    "v_mfma_f32_16x16x32_bf16 v[80:83], a[72:75], v[184:187], v[80:83]\n"
    "v_exp_f32 v225, v225\n"
    "v_mfma_f32_16x16x32_bf16 v[80:83], a[76:79], v[192:195], v[80:83]\n"
    "ds_read_b128 a[12:15], v36 offset:0\n"
    "v_cvt_pk_bf16_f32 v189, v190, v191\n"
    "v_mfma_f32_16x16x32_bf16 v[76:79], a[76:79], v[168:171], v[76:79]\n"
    "v_exp_f32 v226, v226\n"
    "v_mfma_f32_16x16x32_bf16 v[76:79], a[72:75], v[160:163], v[76:79]\n"
    "v_exp_f32 v227, v227\n"
    "v_mfma_f32_16x16x32_bf16 v[88:91], a[80:83], v[160:163], v[88:91]\n"
    "ds_read_b128 a[16:19], v33 offset:4096\n"
    "v_cvt_pk_bf16_f32 v212, v212, v213\n"
    "v_mfma_f32_16x16x32_bf16 v[88:91], a[84:87], v[168:171], v[88:91]\n"
    "v_exp_f32 v172, v172\n"
    "v_mfma_f32_16x16x32_bf16 v[92:95], a[84:87], v[192:195], v[92:95]\n"
    "v_exp_f32 v173, v173\n"
    "v_mfma_f32_16x16x32_bf16 v[92:95], a[80:83], v[184:187], v[92:95]\n"
    "ds_read_b128 a[20:23], v34 offset:4096\n"
    "v_cvt_pk_bf16_f32 v213, v214, v215\n"
    "v_mfma_f32_16x16x32_bf16 v[96:99], a[80:83], v[208:211], v[96:99]\n"
    "v_exp_f32 v174, v174\n"
    "v_mfma_f32_16x16x32_bf16 v[96:99], a[84:87], v[216:219], v[96:99]\n"
    "v_exp_f32 v175, v175\n"
    "v_mfma_f32_16x16x32_bf16 v[108:111], a[92:95], v[216:219], v[108:111]\n"
    "ds_read_b128 a[24:27], v35 offset:4096\n"
    "v_cvt_pk_bf16_f32 v166, v176, v177\n"
    "v_mfma_f32_16x16x32_bf16 v[108:111], a[88:91], v[208:211], v[108:111]\n"
    "v_exp_f32 v196, v196\n"
    "v_mfma_f32_16x16x32_bf16 v[104:107], a[88:91], v[184:187], v[104:107]\n"
    "v_exp_f32 v197, v197\n"
    "v_mfma_f32_16x16x32_bf16 v[104:107], a[92:95], v[192:195], v[104:107]\n"
    "ds_read_b128 a[28:31], v36 offset:4096\n"
    "v_cvt_pk_bf16_f32 v167, v178, v179\n"
    "v_mfma_f32_16x16x32_bf16 v[100:103], a[92:95], v[168:171], v[100:103]\n"
    "v_exp_f32 v198, v198\n"
    "v_mfma_f32_16x16x32_bf16 v[100:103], a[88:91], v[160:163], v[100:103]\n"
    "v_exp_f32 v199, v199\n"
    "v_mfma_f32_16x16x32_bf16 v[112:115], a[96:99], v[160:163], v[112:115]\n"
    "ds_read_b128 a[32:35], v33 offset:8192\n"
    "v_cvt_pk_bf16_f32 v190, v200, v201\n"
    "v_mfma_f32_16x16x32_bf16 v[112:115], a[100:103], v[168:171], v[112:115]\n"
    "v_exp_f32 v220, v220\n"
    "v_mfma_f32_16x16x32_bf16 v[116:119], a[100:103], v[192:195], v[116:119]\n"
    "v_exp_f32 v221, v221\n"
    "v_mfma_f32_16x16x32_bf16 v[116:119], a[96:99], v[184:187], v[116:119]\n"
    "ds_read_b128 a[36:39], v34 offset:8192\n"
    "v_cvt_pk_bf16_f32 v191, v202, v203\n"
    "v_mfma_f32_16x16x32_bf16 v[120:123], a[96:99], v[208:211], v[120:123]\n"
    "v_exp_f32 v222, v222\n"
    "v_mfma_f32_16x16x32_bf16 v[120:123], a[100:103], v[216:219], v[120:123]\n"
    "v_exp_f32 v223, v223\n"
    "v_mfma_f32_16x16x32_bf16 v[132:135], a[108:111], v[216:219], v[132:135]\n"
    "ds_read_b128 a[40:43], v35 offset:8192\n"
    "v_cvt_pk_bf16_f32 v214, v224, v225\n"
    "v_mfma_f32_16x16x32_bf16 v[132:135], a[104:107], v[208:211], v[132:135]\n"
    "v_exp_f32 v180, v180\n"
    "v_mfma_f32_16x16x32_bf16 v[128:131], a[104:107], v[184:187], v[128:131]\n"
    "v_exp_f32 v181, v181\n"
    "v_mfma_f32_16x16x32_bf16 v[128:131], a[108:111], v[192:195], v[128:131]\n"
    "ds_read_b128 a[44:47], v36 offset:8192\n"
    "v_cvt_pk_bf16_f32 v215, v226, v227\n"
    "v_mfma_f32_16x16x32_bf16 v[124:127], a[108:111], v[168:171], v[124:127]\n"
    "v_exp_f32 v182, v182\n"
    "v_mfma_f32_16x16x32_bf16 v[124:127], a[104:107], v[160:163], v[124:127]\n"
    "v_exp_f32 v183, v183\n"
    "v_mfma_f32_16x16x32_bf16 v[136:139], a[112:115], v[160:163], v[136:139]\n"
    "ds_read_b128 a[48:51], v33 offset:12288\n"
    "v_cvt_pk_bf16_f32 v172, v172, v173\n"
    "v_mfma_f32_16x16x32_bf16 v[136:139], a[116:119], v[168:171], v[136:139]\n"
    "v_exp_f32 v204, v204\n"
    "v_mfma_f32_16x16x32_bf16 v[140:143], a[116:119], v[192:195], v[140:143]\n"
    "v_exp_f32 v205, v205\n"
    "v_mfma_f32_16x16x32_bf16 v[140:143], a[112:115], v[184:187], v[140:143]\n"
    "ds_read_b128 a[52:55], v34 offset:12288\n"
    "v_cvt_pk_bf16_f32 v173, v174, v175\n"
    "v_mfma_f32_16x16x32_bf16 v[144:147], a[112:115], v[208:211], v[144:147]\n"
    "v_exp_f32 v206, v206\n"
    "v_mfma_f32_16x16x32_bf16 v[144:147], a[116:119], v[216:219], v[144:147]\n"
    "v_exp_f32 v207, v207\n"
    "v_mfma_f32_16x16x32_bf16 v[156:159], a[124:127], v[216:219], v[156:159]\n"
    "ds_read_b128 a[56:59], v35 offset:12288\n"
    "v_cvt_pk_bf16_f32 v196, v196, v197\n"
    "v_mfma_f32_16x16x32_bf16 v[156:159], a[120:123], v[208:211], v[156:159]\n"
    "v_exp_f32 v228, v228\n"
    "v_mfma_f32_16x16x32_bf16 v[152:155], a[120:123], v[184:187], v[152:155]\n"
    "v_exp_f32 v229, v229\n"
    "v_mfma_f32_16x16x32_bf16 v[152:155], a[124:127], v[192:195], v[152:155]\n"
    "ds_read_b128 a[60:63], v36 offset:12288\n"
    "v_cvt_pk_bf16_f32 v197, v198, v199\n"
    "v_mfma_f32_16x16x32_bf16 v[148:151], a[124:127], v[168:171], v[148:151]\n"
    "v_exp_f32 v230, v230\n"
    "v_mfma_f32_16x16x32_bf16 v[148:151], a[120:123], v[160:163], v[148:151]\n"
    "v_exp_f32 v231, v231\n"
    "v_mfma_f32_16x16x32_bf16 v[244:247], a[224:227], v[160:163], v[244:247]\n"
    "v_cvt_pk_bf16_f32 v220, v220, v221\n"
    "v_cvt_pk_bf16_f32 v221, v222, v223\n"
    "v_mfma_f32_16x16x32_bf16 v[244:247], a[224:227], v[168:171], v[244:247]\n"
    "v_cvt_pk_bf16_f32 v174, v180, v181\n"
    "v_cvt_pk_bf16_f32 v175, v182, v183\n"
    "v_mfma_f32_16x16x32_bf16 v[248:251], a[224:227], v[184:187], v[248:251]\n"
    "v_cvt_pk_bf16_f32 v198, v204, v205\n"
    "v_cvt_pk_bf16_f32 v199, v206, v207\n"
    "v_mfma_f32_16x16x32_bf16 v[248:251], a[224:227], v[192:195], v[248:251]\n"
    "v_cvt_pk_bf16_f32 v222, v228, v229\n"
    "v_cvt_pk_bf16_f32 v223, v230, v231\n"
    "v_mfma_f32_16x16x32_bf16 v[252:255], a[224:227], v[208:211], v[252:255]\n"
    "v_mfma_f32_16x16x32_bf16 v[252:255], a[224:227], v[216:219], v[252:255]\n"
    "s_cmp_eq_u32 s37, s55\n"
    "s_cbranch_scc1 .L_aC_%=x\n"
    "s_waitcnt lgkmcnt(0)\n"
    "s_barrier\n"
    "v_mfma_f32_16x16x32_bf16 v[160:163], a[0:3], a[176:179], v[232:235]\n"
    "s_cmp_eq_u32 s53, 0x10000\n"
    "ds_read_b64_tr_b16 a[64:65], v25 offset:0\n"
    "v_mfma_f32_16x16x32_bf16 v[160:163], a[4:7], a[180:183], v[160:163]\n"
    "s_cselect_b32 s54, 0xffff0000, s41\n"
    "ds_read_b64_tr_b16 a[66:67], v25 offset:4096\n"
    "v_mfma_f32_16x16x32_bf16 v[160:163], a[8:11], a[184:187], v[160:163]\n"
    "s_add_u32 s53, s53, s54\n"
    "ds_read_b64_tr_b16 a[68:69], v25 offset:8192\n"
    "v_mfma_f32_16x16x32_bf16 v[160:163], a[12:15], a[188:191], v[160:163]\n"
    "ds_read_b64_tr_b16 a[70:71], v25 offset:12288\n"
    "v_add_u32 v25, s54, v25\n"
    "v_mfma_f32_16x16x32_bf16 v[184:187], a[12:15], a[204:207], v[236:239]\n"
    "ds_read_b64_tr_b16 a[72:73], v26 offset:0\n"
    "v_mfma_f32_16x16x32_bf16 v[184:187], a[8:11], a[200:203], v[184:187]\n"
    "ds_read_b64_tr_b16 a[74:75], v26 offset:4096\n"
    "v_mfma_f32_16x16x32_bf16 v[184:187], a[4:7], a[196:199], v[184:187]\n"
    "ds_read_b64_tr_b16 a[76:77], v26 offset:8192\n"
    "v_mfma_f32_16x16x32_bf16 v[184:187], a[0:3], a[192:195], v[184:187]\n"
    "ds_read_b64_tr_b16 a[78:79], v26 offset:12288\n"
    "v_add_u32 v26, s54, v26\n"
    "v_mfma_f32_16x16x32_bf16 v[208:211], a[0:3], a[208:211], v[240:243]\n"
    "ds_read_b64_tr_b16 a[80:81], v27 offset:0\n"
    "v_mfma_f32_16x16x32_bf16 v[208:211], a[4:7], a[212:215], v[208:211]\n"
    "ds_read_b64_tr_b16 a[82:83], v27 offset:4096\n"
    "v_mfma_f32_16x16x32_bf16 v[208:211], a[8:11], a[216:219], v[208:211]\n"
    "ds_read_b64_tr_b16 a[84:85], v27 offset:8192\n"
    "v_mfma_f32_16x16x32_bf16 v[208:211], a[12:15], a[220:223], v[208:211]\n"
    "ds_read_b64_tr_b16 a[86:87], v27 offset:12288\n"
    "v_add_u32 v27, s54, v27\n"
    "v_mfma_f32_16x16x32_bf16 v[224:227], a[28:31], a[220:223], v[240:243]\n"
    "ds_read_b64_tr_b16 a[88:89], v28 offset:0\n"
    "v_mfma_f32_16x16x32_bf16 v[224:227], a[24:27], a[216:219], v[224:227]\n"
    "ds_read_b64_tr_b16 a[90:91], v28 offset:4096\n"
    "v_mfma_f32_16x16x32_bf16 v[224:227], a[20:23], a[212:215], v[224:227]\n"
    "ds_read_b64_tr_b16 a[92:93], v28 offset:8192\n"
    "v_mfma_f32_16x16x32_bf16 v[224:227], a[16:19], a[208:211], v[224:227]\n"
    "ds_read_b64_tr_b16 a[94:95], v28 offset:12288\n"
    "v_add_u32 v28, s54, v28\n"
    "v_mfma_f32_16x16x32_bf16 v[200:203], a[16:19], a[192:195], v[236:239]\n"
    "ds_read_b64_tr_b16 a[96:97], v29 offset:0\n"
    "v_mfma_f32_16x16x32_bf16 v[200:203], a[20:23], a[196:199], v[200:203]\n"
    "ds_read_b64_tr_b16 a[98:99], v29 offset:4096\n"
    "v_mfma_f32_16x16x32_bf16 v[200:203], a[24:27], a[200:203], v[200:203]\n"
    "ds_read_b64_tr_b16 a[100:101], v29 offset:8192\n"
    "v_mfma_f32_16x16x32_bf16 v[200:203], a[28:31], a[204:207], v[200:203]\n"
    "ds_read_b64_tr_b16 a[102:103], v29 offset:12288\n"
    "v_add_u32 v29, s54, v29\n"
    "v_mfma_f32_16x16x32_bf16 v[176:179], a[28:31], a[188:191], v[232:235]\n"
    "ds_read_b64_tr_b16 a[104:105], v30 offset:0\n"
    "v_mfma_f32_16x16x32_bf16 v[176:179], a[24:27], a[184:187], v[176:179]\n"
    "ds_read_b64_tr_b16 a[106:107], v30 offset:4096\n"
    "v_mfma_f32_16x16x32_bf16 v[176:179], a[20:23], a[180:183], v[176:179]\n"
    "ds_read_b64_tr_b16 a[108:109], v30 offset:8192\n"
    "v_mfma_f32_16x16x32_bf16 v[176:179], a[16:19], a[176:179], v[176:179]\n"
    "ds_read_b64_tr_b16 a[110:111], v30 offset:12288\n"
    "v_add_u32 v30, s54, v30\n"
    "v_mfma_f32_16x16x32_bf16 v[168:171], a[32:35], a[176:179], v[232:235]\n"
    "ds_read_b64_tr_b16 a[112:113], v31 offset:0\n"
    "v_mfma_f32_16x16x32_bf16 v[168:171], a[36:39], a[180:183], v[168:171]\n"
    "ds_read_b64_tr_b16 a[114:115], v31 offset:4096\n"
    "v_mfma_f32_16x16x32_bf16 v[168:171], a[40:43], a[184:187], v[168:171]\n"
    "ds_read_b64_tr_b16 a[116:117], v31 offset:8192\n"
    "v_mfma_f32_16x16x32_bf16 v[168:171], a[44:47], a[188:191], v[168:171]\n"
    "ds_read_b64_tr_b16 a[118:119], v31 offset:12288\n"
    "v_add_u32 v31, s54, v31\n"
    "v_mfma_f32_16x16x32_bf16 v[192:195], a[44:47], a[204:207], v[236:239]\n"
    "ds_read_b64_tr_b16 a[120:121], v32 offset:0\n"
    "v_mfma_f32_16x16x32_bf16 v[192:195], a[40:43], a[200:203], v[192:195]\n"
    "ds_read_b64_tr_b16 a[122:123], v32 offset:4096\n"
    "v_mfma_f32_16x16x32_bf16 v[192:195], a[36:39], a[196:199], v[192:195]\n"
    "ds_read_b64_tr_b16 a[124:125], v32 offset:8192\n"
    "v_mfma_f32_16x16x32_bf16 v[192:195], a[32:35], a[192:195], v[192:195]\n"
    "ds_read_b64_tr_b16 a[126:127], v32 offset:12288\n"
    "v_add_u32 v32, s54, v32\n"
    "v_mfma_f32_16x16x32_bf16 v[216:219], a[32:35], a[208:211], v[240:243]\n"
    "v_exp_f32 v160, v160\n"
    "v_mfma_f32_16x16x32_bf16 v[216:219], a[36:39], a[212:215], v[216:219]\n"
    "v_exp_f32 v161, v161\n"
    "v_mfma_f32_16x16x32_bf16 v[216:219], a[40:43], a[216:219], v[216:219]\n"
    "v_exp_f32 v162, v162\n"
    "v_mfma_f32_16x16x32_bf16 v[216:219], a[44:47], a[220:223], v[216:219]\n"
    "v_exp_f32 v163, v163\n"
    "v_mfma_f32_16x16x32_bf16 v[228:231], a[60:63], a[220:223], v[240:243]\n"
    "v_exp_f32 v184, v184\n"
    "v_mfma_f32_16x16x32_bf16 v[228:231], a[56:59], a[216:219], v[228:231]\n"
    "v_exp_f32 v185, v185\n"
    "v_mfma_f32_16x16x32_bf16 v[228:231], a[52:55], a[212:215], v[228:231]\n"
    "v_exp_f32 v186, v186\n"
    "v_mfma_f32_16x16x32_bf16 v[228:231], a[48:51], a[208:211], v[228:231]\n"
    "v_exp_f32 v187, v187\n"
    "v_mfma_f32_16x16x32_bf16 v[204:207], a[48:51], a[192:195], v[236:239]\n"
    "v_exp_f32 v208, v208\n"
    "v_mfma_f32_16x16x32_bf16 v[204:207], a[52:55], a[196:199], v[204:207]\n"
    "v_exp_f32 v209, v209\n"
    "v_mfma_f32_16x16x32_bf16 v[204:207], a[56:59], a[200:203], v[204:207]\n"
    "v_exp_f32 v210, v210\n"
    "v_mfma_f32_16x16x32_bf16 v[204:207], a[60:63], a[204:207], v[204:207]\n"
    "v_exp_f32 v211, v211\n"
    "v_mfma_f32_16x16x32_bf16 v[180:183], a[60:63], a[188:191], v[232:235]\n"
    "v_exp_f32 v176, v176\n"
    "v_mfma_f32_16x16x32_bf16 v[180:183], a[56:59], a[184:187], v[180:183]\n"
    "v_exp_f32 v177, v177\n"
    "v_mfma_f32_16x16x32_bf16 v[180:183], a[52:55], a[180:183], v[180:183]\n"
    "v_exp_f32 v178, v178\n"
    "v_mfma_f32_16x16x32_bf16 v[180:183], a[48:51], a[176:179], v[180:183]\n"
    "s_waitcnt lgkmcnt(0)\n"
    "v_exp_f32 v179, v179\n"
    "v_mfma_f32_16x16x32_bf16 v[64:67], a[64:67], v[164:167], v[64:67]\n"
    "ds_read_b128 a[0:3], v33 offset:16384\n"
    "v_cvt_pk_bf16_f32 v160, v160, v161\n"
    "v_mfma_f32_16x16x32_bf16 v[64:67], a[68:71], v[172:175], v[64:67]\n"
    "v_exp_f32 v200, v200\n"
    "v_mfma_f32_16x16x32_bf16 v[68:71], a[68:71], v[196:199], v[68:71]\n"
    "v_exp_f32 v201, v201\n"
    "v_mfma_f32_16x16x32_bf16 v[68:71], a[64:67], v[188:191], v[68:71]\n"
    "ds_read_b128 a[4:7], v34 offset:16384\n"
    "v_cvt_pk_bf16_f32 v161, v162, v163\n"
    "v_mfma_f32_16x16x32_bf16 v[72:75], a[64:67], v[212:215], v[72:75]\n"
    "v_exp_f32 v202, v202\n"
    "v_mfma_f32_16x16x32_bf16 v[72:75], a[68:71], v[220:223], v[72:75]\n"
    "v_exp_f32 v203, v203\n"
    "v_mfma_f32_16x16x32_bf16 v[84:87], a[76:79], v[220:223], v[84:87]\n"
    "ds_read_b128 a[8:11], v35 offset:16384\n"
    "v_cvt_pk_bf16_f32 v184, v184, v185\n"
    "v_mfma_f32_16x16x32_bf16 v[84:87], a[72:75], v[212:215], v[84:87]\n"
    "v_exp_f32 v224, v224\n"
    "v_mfma_f32_16x16x32_bf16 v[80:83], a[72:75], v[188:191], v[80:83]\n"
    "v_exp_f32 v225, v225\n"
    "v_mfma_f32_16x16x32_bf16 v[80:83], a[76:79], v[196:199], v[80:83]\n"
    "ds_read_b128 a[12:15], v36 offset:16384\n"
    "v_cvt_pk_bf16_f32 v185, v186, v187\n"
    "v_mfma_f32_16x16x32_bf16 v[76:79], a[76:79], v[172:175], v[76:79]\n"
    "v_exp_f32 v226, v226\n"
    "v_mfma_f32_16x16x32_bf16 v[76:79], a[72:75], v[164:167], v[76:79]\n"
    "v_exp_f32 v227, v227\n"
    "v_mfma_f32_16x16x32_bf16 v[88:91], a[80:83], v[164:167], v[88:91]\n"
    "ds_read_b128 a[16:19], v33 offset:20480\n"
    "v_cvt_pk_bf16_f32 v208, v208, v209\n"
    "v_mfma_f32_16x16x32_bf16 v[88:91], a[84:87], v[172:175], v[88:91]\n"
    "v_exp_f32 v168, v168\n"
    "v_mfma_f32_16x16x32_bf16 v[92:95], a[84:87], v[196:199], v[92:95]\n"
    "v_exp_f32 v169, v169\n"
    "v_mfma_f32_16x16x32_bf16 v[92:95], a[80:83], v[188:191], v[92:95]\n"
    "ds_read_b128 a[20:23], v34 offset:20480\n"
    "v_cvt_pk_bf16_f32 v209, v210, v211\n"
    "v_mfma_f32_16x16x32_bf16 v[96:99], a[80:83], v[212:215], v[96:99]\n"
    "v_exp_f32 v170, v170\n"
    "v_mfma_f32_16x16x32_bf16 v[96:99], a[84:87], v[220:223], v[96:99]\n"
    "v_exp_f32 v171, v171\n"
    "v_mfma_f32_16x16x32_bf16 v[108:111], a[92:95], v[220:223], v[108:111]\n"
    "ds_read_b128 a[24:27], v35 offset:20480\n"
    "v_cvt_pk_bf16_f32 v162, v176, v177\n"
    "v_mfma_f32_16x16x32_bf16 v[108:111], a[88:91], v[212:215], v[108:111]\n"
    "v_exp_f32 v192, v192\n"
    "v_mfma_f32_16x16x32_bf16 v[104:107], a[88:91], v[188:191], v[104:107]\n"
    "v_exp_f32 v193, v193\n"
    "v_mfma_f32_16x16x32_bf16 v[104:107], a[92:95], v[196:199], v[104:107]\n"
    "ds_read_b128 a[28:31], v36 offset:20480\n"
    "v_cvt_pk_bf16_f32 v163, v178, v179\n"
    "v_mfma_f32_16x16x32_bf16 v[100:103], a[92:95], v[172:175], v[100:103]\n"
    "v_exp_f32 v194, v194\n"
    "v_mfma_f32_16x16x32_bf16 v[100:103], a[88:91], v[164:167], v[100:103]\n"
    "v_exp_f32 v195, v195\n"
    "v_mfma_f32_16x16x32_bf16 v[112:115], a[96:99], v[164:167], v[112:115]\n"
    "ds_read_b128 a[32:35], v33 offset:24576\n"
    "v_cvt_pk_bf16_f32 v186, v200, v201\n"
    "v_mfma_f32_16x16x32_bf16 v[112:115], a[100:103], v[172:175], v[112:115]\n"
    "v_exp_f32 v216, v216\n"
    "v_mfma_f32_16x16x32_bf16 v[116:119], a[100:103], v[196:199], v[116:119]\n"
    "v_exp_f32 v217, v217\n"
    "v_mfma_f32_16x16x32_bf16 v[116:119], a[96:99], v[188:191], v[116:119]\n"
    "ds_read_b128 a[36:39], v34 offset:24576\n"
    "v_cvt_pk_bf16_f32 v187, v202, v203\n"
    "v_mfma_f32_16x16x32_bf16 v[120:123], a[96:99], v[212:215], v[120:123]\n"
    "v_exp_f32 v218, v218\n"
    "v_mfma_f32_16x16x32_bf16 v[120:123], a[100:103], v[220:223], v[120:123]\n"
    "v_exp_f32 v219, v219\n"
    "v_mfma_f32_16x16x32_bf16 v[132:135], a[108:111], v[220:223], v[132:135]\n"
    "ds_read_b128 a[40:43], v35 offset:24576\n"
    "v_cvt_pk_bf16_f32 v210, v224, v225\n"
    "v_mfma_f32_16x16x32_bf16 v[132:135], a[104:107], v[212:215], v[132:135]\n"
    "v_exp_f32 v180, v180\n"
    "v_mfma_f32_16x16x32_bf16 v[128:131], a[104:107], v[188:191], v[128:131]\n"
    "v_exp_f32 v181, v181\n"
    "v_mfma_f32_16x16x32_bf16 v[128:131], a[108:111], v[196:199], v[128:131]\n"
    "ds_read_b128 a[44:47], v36 offset:24576\n"
    "v_cvt_pk_bf16_f32 v211, v226, v227\n"
    "v_mfma_f32_16x16x32_bf16 v[124:127], a[108:111], v[172:175], v[124:127]\n"
    "v_exp_f32 v182, v182\n"
    "v_mfma_f32_16x16x32_bf16 v[124:127], a[104:107], v[164:167], v[124:127]\n"
    "v_exp_f32 v183, v183\n"
    "v_mfma_f32_16x16x32_bf16 v[136:139], a[112:115], v[164:167], v[136:139]\n"
    "ds_read_b128 a[48:51], v33 offset:28672\n"
    "v_cvt_pk_bf16_f32 v168, v168, v169\n"
    "v_mfma_f32_16x16x32_bf16 v[136:139], a[116:119], v[172:175], v[136:139]\n"
    "v_exp_f32 v204, v204\n"
    "v_mfma_f32_16x16x32_bf16 v[140:143], a[116:119], v[196:199], v[140:143]\n"
    "v_exp_f32 v205, v205\n"
    "v_mfma_f32_16x16x32_bf16 v[140:143], a[112:115], v[188:191], v[140:143]\n"
    "ds_read_b128 a[52:55], v34 offset:28672\n"
    "v_cvt_pk_bf16_f32 v169, v170, v171\n"
    "v_mfma_f32_16x16x32_bf16 v[144:147], a[112:115], v[212:215], v[144:147]\n"
    "v_exp_f32 v206, v206\n"
    "v_mfma_f32_16x16x32_bf16 v[144:147], a[116:119], v[220:223], v[144:147]\n"
    "v_exp_f32 v207, v207\n"
    "v_mfma_f32_16x16x32_bf16 v[156:159], a[124:127], v[220:223], v[156:159]\n"
    "ds_read_b128 a[56:59], v35 offset:28672\n"
    "v_cvt_pk_bf16_f32 v192, v192, v193\n"
    "v_mfma_f32_16x16x32_bf16 v[156:159], a[120:123], v[212:215], v[156:159]\n"
    "v_exp_f32 v228, v228\n"
    "v_mfma_f32_16x16x32_bf16 v[152:155], a[120:123], v[188:191], v[152:155]\n"
    "v_exp_f32 v229, v229\n"
    "v_mfma_f32_16x16x32_bf16 v[152:155], a[124:127], v[196:199], v[152:155]\n"
    "ds_read_b128 a[60:63], v36 offset:28672\n"
    "v_cvt_pk_bf16_f32 v193, v194, v195\n"
    "v_mfma_f32_16x16x32_bf16 v[148:151], a[124:127], v[172:175], v[148:151]\n"
    "v_exp_f32 v230, v230\n"
    "v_mfma_f32_16x16x32_bf16 v[148:151], a[120:123], v[164:167], v[148:151]\n"
    "v_exp_f32 v231, v231\n"
    "v_mfma_f32_16x16x32_bf16 v[244:247], a[224:227], v[164:167], v[244:247]\n"
    "v_cvt_pk_bf16_f32 v216, v216, v217\n"
    "v_cvt_pk_bf16_f32 v217, v218, v219\n"
    "v_mfma_f32_16x16x32_bf16 v[244:247], a[224:227], v[172:175], v[244:247]\n"
    "v_cvt_pk_bf16_f32 v170, v180, v181\n"
    "v_cvt_pk_bf16_f32 v171, v182, v183\n"
    "v_mfma_f32_16x16x32_bf16 v[248:251], a[224:227], v[188:191], v[248:251]\n"
    "v_cvt_pk_bf16_f32 v194, v204, v205\n"
    "v_cvt_pk_bf16_f32 v195, v206, v207\n"
    "v_mfma_f32_16x16x32_bf16 v[248:251], a[224:227], v[196:199], v[248:251]\n"
    "v_cvt_pk_bf16_f32 v218, v228, v229\n"
    "v_cvt_pk_bf16_f32 v219, v230, v231\n"
    "v_mfma_f32_16x16x32_bf16 v[252:255], a[224:227], v[212:215], v[252:255]\n"
    "v_mfma_f32_16x16x32_bf16 v[252:255], a[224:227], v[220:223], v[252:255]\n"
    "s_sub_u32 s37, s37, 1\n"
    "s_branch .L_aC_%=\n"
    ".L_aC_%=x:\n"
    "s_cmp_lg_u32 s55, 0\n"
    "s_cbranch_scc1 .L_aC_%=y\n"
    "s_waitcnt lgkmcnt(0)\n"
    "s_barrier\n"
    "ds_read_b64_tr_b16 a[64:65], v25 offset:0\n"
    "ds_read_b64_tr_b16 a[66:67], v25 offset:4096\n"
    "ds_read_b64_tr_b16 a[68:69], v25 offset:8192\n"
    "ds_read_b64_tr_b16 a[70:71], v25 offset:12288\n"
    "v_mfma_f32_16x16x32_bf16 v[244:247], a[224:227], v[164:167], v[244:247]\n"
    "ds_read_b64_tr_b16 a[72:73], v26 offset:0\n"
    "v_mfma_f32_16x16x32_bf16 v[244:247], a[224:227], v[172:175], v[244:247]\n"
    "ds_read_b64_tr_b16 a[74:75], v26 offset:4096\n"
    "v_mfma_f32_16x16x32_bf16 v[248:251], a[224:227], v[188:191], v[248:251]\n"
    "ds_read_b64_tr_b16 a[76:77], v26 offset:8192\n"
    "v_mfma_f32_16x16x32_bf16 v[248:251], a[224:227], v[196:199], v[248:251]\n"
    "ds_read_b64_tr_b16 a[78:79], v26 offset:12288\n"
    "v_mfma_f32_16x16x32_bf16 v[252:255], a[224:227], v[212:215], v[252:255]\n"
    "ds_read_b64_tr_b16 a[80:81], v27 offset:0\n"
    "v_mfma_f32_16x16x32_bf16 v[252:255], a[224:227], v[220:223], v[252:255]\n"
    "s_waitcnt lgkmcnt(5)\n"
    "ds_read_b64_tr_b16 a[82:83], v27 offset:4096\n"
    "v_mfma_f32_16x16x32_bf16 v[64:67], a[64:67], v[164:167], v[64:67]\n"
    "ds_read_b64_tr_b16 a[84:85], v27 offset:8192\n"
    "v_mfma_f32_16x16x32_bf16 v[64:67], a[68:71], v[172:175], v[64:67]\n"
    "ds_read_b64_tr_b16 a[86:87], v27 offset:12288\n"
    "v_mfma_f32_16x16x32_bf16 v[68:71], a[68:71], v[196:199], v[68:71]\n"
    "ds_read_b64_tr_b16 a[88:89], v28 offset:0\n"
    "v_mfma_f32_16x16x32_bf16 v[68:71], a[64:67], v[188:191], v[68:71]\n"
    "ds_read_b64_tr_b16 a[90:91], v28 offset:4096\n"
    "v_mfma_f32_16x16x32_bf16 v[72:75], a[64:67], v[212:215], v[72:75]\n"
    "ds_read_b64_tr_b16 a[92:93], v28 offset:8192\n"
    "v_mfma_f32_16x16x32_bf16 v[72:75], a[68:71], v[220:223], v[72:75]\n"
    "s_waitcnt lgkmcnt(7)\n"
    "ds_read_b64_tr_b16 a[94:95], v28 offset:12288\n"
    "v_mfma_f32_16x16x32_bf16 v[84:87], a[76:79], v[220:223], v[84:87]\n"
    "ds_read_b64_tr_b16 a[96:97], v29 offset:0\n"
    "v_mfma_f32_16x16x32_bf16 v[84:87], a[72:75], v[212:215], v[84:87]\n"
    "ds_read_b64_tr_b16 a[98:99], v29 offset:4096\n"
    "v_mfma_f32_16x16x32_bf16 v[80:83], a[72:75], v[188:191], v[80:83]\n"
    "ds_read_b64_tr_b16 a[100:101], v29 offset:8192\n"
    "v_mfma_f32_16x16x32_bf16 v[80:83], a[76:79], v[196:199], v[80:83]\n"
    "ds_read_b64_tr_b16 a[102:103], v29 offset:12288\n"
    "v_mfma_f32_16x16x32_bf16 v[76:79], a[76:79], v[172:175], v[76:79]\n"
    "ds_read_b64_tr_b16 a[104:105], v30 offset:0\n"
    "v_mfma_f32_16x16x32_bf16 v[76:79], a[72:75], v[164:167], v[76:79]\n"
    "s_waitcnt lgkmcnt(9)\n"
    "ds_read_b64_tr_b16 a[106:107], v30 offset:4096\n"
    "v_mfma_f32_16x16x32_bf16 v[88:91], a[80:83], v[164:167], v[88:91]\n"
    "ds_read_b64_tr_b16 a[108:109], v30 offset:8192\n"
    "v_mfma_f32_16x16x32_bf16 v[88:91], a[84:87], v[172:175], v[88:91]\n"
    "ds_read_b64_tr_b16 a[110:111], v30 offset:12288\n"
    "v_mfma_f32_16x16x32_bf16 v[92:95], a[84:87], v[196:199], v[92:95]\n"
    "ds_read_b64_tr_b16 a[112:113], v31 offset:0\n"
    "v_mfma_f32_16x16x32_bf16 v[92:95], a[80:83], v[188:191], v[92:95]\n"
    "ds_read_b64_tr_b16 a[114:115], v31 offset:4096\n"
    "v_mfma_f32_16x16x32_bf16 v[96:99], a[80:83], v[212:215], v[96:99]\n"
    "ds_read_b64_tr_b16 a[116:117], v31 offset:8192\n"
    "v_mfma_f32_16x16x32_bf16 v[96:99], a[84:87], v[220:223], v[96:99]\n"
    "s_waitcnt lgkmcnt(11)\n"
    "ds_read_b64_tr_b16 a[118:119], v31 offset:12288\n"
    "v_mfma_f32_16x16x32_bf16 v[108:111], a[92:95], v[220:223], v[108:111]\n"
    "ds_read_b64_tr_b16 a[120:121], v32 offset:0\n"
    "v_mfma_f32_16x16x32_bf16 v[108:111], a[88:91], v[212:215], v[108:111]\n"
    "ds_read_b64_tr_b16 a[122:123], v32 offset:4096\n"
    "v_mfma_f32_16x16x32_bf16 v[104:107], a[88:91], v[188:191], v[104:107]\n"
    "ds_read_b64_tr_b16 a[124:125], v32 offset:8192\n"
    "v_mfma_f32_16x16x32_bf16 v[104:107], a[92:95], v[196:199], v[104:107]\n"
    "ds_read_b64_tr_b16 a[126:127], v32 offset:12288\n"
    "s_cmp_eq_u32 s53, 0x10000\n"
    "s_cselect_b32 s54, 0xffff0000, s41\n"
    "s_add_u32 s53, s53, s54\n"
    "v_mfma_f32_16x16x32_bf16 v[100:103], a[92:95], v[172:175], v[100:103]\n"
    "v_add_u32 v25, s54, v25\n"
    "v_add_u32 v26, s54, v26\n"
    "v_mfma_f32_16x16x32_bf16 v[100:103], a[88:91], v[164:167], v[100:103]\n"
    "s_waitcnt lgkmcnt(12)\n"
    "v_add_u32 v27, s54, v27\n"
    "v_add_u32 v28, s54, v28\n"
    "v_mfma_f32_16x16x32_bf16 v[112:115], a[96:99], v[164:167], v[112:115]\n"
    "v_add_u32 v29, s54, v29\n"
    "v_add_u32 v30, s54, v30\n"
    "v_mfma_f32_16x16x32_bf16 v[112:115], a[100:103], v[172:175], v[112:115]\n"
    "v_add_u32 v31, s54, v31\n"
    "v_add_u32 v32, s54, v32\n"
    "v_mfma_f32_16x16x32_bf16 v[116:119], a[100:103], v[196:199], v[116:119]\n"
    "v_mfma_f32_16x16x32_bf16 v[116:119], a[96:99], v[188:191], v[116:119]\n"
    "v_mfma_f32_16x16x32_bf16 v[120:123], a[96:99], v[212:215], v[120:123]\n"
    "v_mfma_f32_16x16x32_bf16 v[120:123], a[100:103], v[220:223], v[120:123]\n"
    "s_waitcnt lgkmcnt(8)\n"
    "v_mfma_f32_16x16x32_bf16 v[132:135], a[108:111], v[220:223], v[132:135]\n"
    "v_mfma_f32_16x16x32_bf16 v[132:135], a[104:107], v[212:215], v[132:135]\n"
    "v_mfma_f32_16x16x32_bf16 v[128:131], a[104:107], v[188:191], v[128:131]\n"
    "v_mfma_f32_16x16x32_bf16 v[128:131], a[108:111], v[196:199], v[128:131]\n"
    "v_mfma_f32_16x16x32_bf16 v[124:127], a[108:111], v[172:175], v[124:127]\n"
    "v_mfma_f32_16x16x32_bf16 v[124:127], a[104:107], v[164:167], v[124:127]\n"
    "s_waitcnt lgkmcnt(4)\n"
    "v_mfma_f32_16x16x32_bf16 v[136:139], a[112:115], v[164:167], v[136:139]\n"
    "v_mfma_f32_16x16x32_bf16 v[136:139], a[116:119], v[172:175], v[136:139]\n"
    "v_mfma_f32_16x16x32_bf16 v[140:143], a[116:119], v[196:199], v[140:143]\n"
    "v_mfma_f32_16x16x32_bf16 v[140:143], a[112:115], v[188:191], v[140:143]\n"
    "v_mfma_f32_16x16x32_bf16 v[144:147], a[112:115], v[212:215], v[144:147]\n"
    "v_mfma_f32_16x16x32_bf16 v[144:147], a[116:119], v[220:223], v[144:147]\n"
    "s_waitcnt lgkmcnt(0)\n"
    "v_mfma_f32_16x16x32_bf16 v[156:159], a[124:127], v[220:223], v[156:159]\n"
    "v_mfma_f32_16x16x32_bf16 v[156:159], a[120:123], v[212:215], v[156:159]\n"
    "v_mfma_f32_16x16x32_bf16 v[152:155], a[120:123], v[188:191], v[152:155]\n"
    "v_mfma_f32_16x16x32_bf16 v[152:155], a[124:127], v[196:199], v[152:155]\n"
    "v_mfma_f32_16x16x32_bf16 v[148:151], a[124:127], v[172:175], v[148:151]\n"
    "v_mfma_f32_16x16x32_bf16 v[148:151], a[120:123], v[164:167], v[148:151]\n"
    "s_nop 7\n"
    "s_mov_b32 s57, 0x53800000\n"
    "v_cmp_nge_f32 vcc, s57, v244\n"
    "v_cmp_nge_f32 s[50:51], s57, v248\n"
    "s_or_b64 vcc, vcc, s[50:51]\n"
    "v_cmp_nge_f32 s[50:51], s57, v252\n"
    "s_or_b64 vcc, vcc, s[50:51]\n"
    "s_cbranch_vccnz .L_fbC_%=\n"
    ".L_fbretC0_%=:\n"
    "v_accvgpr_mov_b32 a176, a128\n"
    "v_accvgpr_mov_b32 a177, a129\n"
    "v_accvgpr_mov_b32 a178, a130\n"
    "v_accvgpr_mov_b32 a179, a131\n"
    "v_accvgpr_mov_b32 a180, a132\n"
    "v_accvgpr_mov_b32 a181, a133\n"
    "v_accvgpr_mov_b32 a182, a134\n"
    "v_accvgpr_mov_b32 a183, a135\n"
    "v_accvgpr_mov_b32 a184, a136\n"
    "v_accvgpr_mov_b32 a185, a137\n"
    "v_accvgpr_mov_b32 a186, a138\n"
    "v_accvgpr_mov_b32 a187, a139\n"
    "v_accvgpr_mov_b32 a188, a140\n"
    "v_accvgpr_mov_b32 a189, a141\n"
    "v_accvgpr_mov_b32 a190, a142\n"
    "v_accvgpr_mov_b32 a191, a143\n"
    "v_accvgpr_mov_b32 a192, a144\n"
    "v_accvgpr_mov_b32 a193, a145\n"
    "v_accvgpr_mov_b32 a194, a146\n"
    "v_accvgpr_mov_b32 a195, a147\n"
    "v_accvgpr_mov_b32 a196, a148\n"
    "v_accvgpr_mov_b32 a197, a149\n"
    "v_accvgpr_mov_b32 a198, a150\n"
    "v_accvgpr_mov_b32 a199, a151\n"
    "v_accvgpr_mov_b32 a200, a152\n"
    "v_accvgpr_mov_b32 a201, a153\n"
    "v_accvgpr_mov_b32 a202, a154\n"
    "v_accvgpr_mov_b32 a203, a155\n"
    "v_accvgpr_mov_b32 a204, a156\n"
    "v_accvgpr_mov_b32 a205, a157\n"
    "v_accvgpr_mov_b32 a206, a158\n"
    "v_accvgpr_mov_b32 a207, a159\n"
    "v_accvgpr_mov_b32 a208, a160\n"
    "v_accvgpr_mov_b32 a209, a161\n"
    "v_accvgpr_mov_b32 a210, a162\n"
    "v_accvgpr_mov_b32 a211, a163\n"
    "v_accvgpr_mov_b32 a212, a164\n"
    "v_accvgpr_mov_b32 a213, a165\n"
    "v_accvgpr_mov_b32 a214, a166\n"
    "v_accvgpr_mov_b32 a215, a167\n"
    "v_accvgpr_mov_b32 a216, a168\n"
    "v_accvgpr_mov_b32 a217, a169\n"
    "v_accvgpr_mov_b32 a218, a170\n"
    "v_accvgpr_mov_b32 a219, a171\n"
    "v_accvgpr_mov_b32 a220, a172\n"
    "v_accvgpr_mov_b32 a221, a173\n"
    "v_accvgpr_mov_b32 a222, a174\n"
    "v_accvgpr_mov_b32 a223, a175\n"
    "v_rcp_f32 v44, v244\n"
    "v_rcp_f32 v45, v244\n"
    "v_rcp_f32 v46, v248\n"
    "v_rcp_f32 v47, v248\n"
    "v_rcp_f32 v48, v252\n"
    "v_rcp_f32 v49, v252\n"
    "v_pk_mul_f32 v[64:65], v[44:45], v[64:65]\n"
    "v_pk_mul_f32 v[66:67], v[44:45], v[66:67]\n"
    "v_pk_mul_f32 v[76:77], v[44:45], v[76:77]\n"
    "v_pk_mul_f32 v[78:79], v[44:45], v[78:79]\n"
    "v_cvt_pk_bf16_f32 v64, v64, v65\n"
    "v_cvt_pk_bf16_f32 v65, v66, v67\n"
    "v_cvt_pk_bf16_f32 v66, v76, v77\n"
    "v_cvt_pk_bf16_f32 v67, v78, v79\n"
    "v_accvgpr_write_b32 a128, v64\n"
    "v_accvgpr_write_b32 a129, v65\n"
    "v_accvgpr_write_b32 a130, v66\n"
    "v_accvgpr_write_b32 a131, v67\n"
    "v_pk_mul_f32 v[68:69], v[46:47], v[68:69]\n"
    "v_pk_mul_f32 v[70:71], v[46:47], v[70:71]\n"
    "v_pk_mul_f32 v[80:81], v[46:47], v[80:81]\n"
    "v_pk_mul_f32 v[82:83], v[46:47], v[82:83]\n"
    "v_cvt_pk_bf16_f32 v68, v68, v69\n"
    "v_cvt_pk_bf16_f32 v69, v70, v71\n"
    "v_cvt_pk_bf16_f32 v70, v80, v81\n"
    "v_cvt_pk_bf16_f32 v71, v82, v83\n"
    "v_accvgpr_write_b32 a144, v68\n"
    "v_accvgpr_write_b32 a145, v69\n"
    "v_accvgpr_write_b32 a146, v70\n"
    "v_accvgpr_write_b32 a147, v71\n"
    "v_pk_mul_f32 v[72:73], v[48:49], v[72:73]\n"
    "v_pk_mul_f32 v[74:75], v[48:49], v[74:75]\n"
    "v_pk_mul_f32 v[84:85], v[48:49], v[84:85]\n"
    "v_pk_mul_f32 v[86:87], v[48:49], v[86:87]\n"
    "v_cvt_pk_bf16_f32 v72, v72, v73\n"
    "v_cvt_pk_bf16_f32 v73, v74, v75\n"
    "v_cvt_pk_bf16_f32 v74, v84, v85\n"
    "v_cvt_pk_bf16_f32 v75, v86, v87\n"
    "v_accvgpr_write_b32 a160, v72\n"
    "v_accvgpr_write_b32 a161, v73\n"
    "v_accvgpr_write_b32 a162, v74\n"
    "v_accvgpr_write_b32 a163, v75\n"
    "v_pk_mul_f32 v[88:89], v[44:45], v[88:89]\n"
    "v_pk_mul_f32 v[90:91], v[44:45], v[90:91]\n"
    "v_pk_mul_f32 v[100:101], v[44:45], v[100:101]\n"
    "v_pk_mul_f32 v[102:103], v[44:45], v[102:103]\n"
    "v_cvt_pk_bf16_f32 v88, v88, v89\n"
    "v_cvt_pk_bf16_f32 v89, v90, v91\n"
    "v_cvt_pk_bf16_f32 v90, v100, v101\n"
    "v_cvt_pk_bf16_f32 v91, v102, v103\n"
    "v_accvgpr_write_b32 a132, v88\n"
    "v_accvgpr_write_b32 a133, v89\n"
    "v_accvgpr_write_b32 a134, v90\n"
    "v_accvgpr_write_b32 a135, v91\n"
    "v_pk_mul_f32 v[92:93], v[46:47], v[92:93]\n"
    "v_pk_mul_f32 v[94:95], v[46:47], v[94:95]\n"
    "v_pk_mul_f32 v[104:105], v[46:47], v[104:105]\n"
    "v_pk_mul_f32 v[106:107], v[46:47], v[106:107]\n"
    "v_cvt_pk_bf16_f32 v92, v92, v93\n"
    "v_cvt_pk_bf16_f32 v93, v94, v95\n"
    "v_cvt_pk_bf16_f32 v94, v104, v105\n"
    "v_cvt_pk_bf16_f32 v95, v106, v107\n"
    "v_accvgpr_write_b32 a148, v92\n"
    "v_accvgpr_write_b32 a149, v93\n"
    "v_accvgpr_write_b32 a150, v94\n"
    "v_accvgpr_write_b32 a151, v95\n"
    "v_pk_mul_f32 v[96:97], v[48:49], v[96:97]\n"
    "v_pk_mul_f32 v[98:99], v[48:49], v[98:99]\n"
    "v_pk_mul_f32 v[108:109], v[48:49], v[108:109]\n"
    "v_pk_mul_f32 v[110:111], v[48:49], v[110:111]\n"
    "v_cvt_pk_bf16_f32 v96, v96, v97\n"
    "v_cvt_pk_bf16_f32 v97, v98, v99\n"
    "v_cvt_pk_bf16_f32 v98, v108, v109\n"
    "v_cvt_pk_bf16_f32 v99, v110, v111\n"
    "v_accvgpr_write_b32 a164, v96\n"
    "v_accvgpr_write_b32 a165, v97\n"
    "v_accvgpr_write_b32 a166, v98\n"
    "v_accvgpr_write_b32 a167, v99\n"
    "v_pk_mul_f32 v[112:113], v[44:45], v[112:113]\n"
    "v_pk_mul_f32 v[114:115], v[44:45], v[114:115]\n"
    "v_pk_mul_f32 v[124:125], v[44:45], v[124:125]\n"
    "v_pk_mul_f32 v[126:127], v[44:45], v[126:127]\n"
    "v_cvt_pk_bf16_f32 v112, v112, v113\n"
    "v_cvt_pk_bf16_f32 v113, v114, v115\n"
    "v_cvt_pk_bf16_f32 v114, v124, v125\n"
    "v_cvt_pk_bf16_f32 v115, v126, v127\n"
    "v_accvgpr_write_b32 a136, v112\n"
    "v_accvgpr_write_b32 a137, v113\n"
    "v_accvgpr_write_b32 a138, v114\n"
    "v_accvgpr_write_b32 a139, v115\n"
    "v_pk_mul_f32 v[116:117], v[46:47], v[116:117]\n"
    "v_pk_mul_f32 v[118:119], v[46:47], v[118:119]\n"
    "v_pk_mul_f32 v[128:129], v[46:47], v[128:129]\n"
    "v_pk_mul_f32 v[130:131], v[46:47], v[130:131]\n"
    "v_cvt_pk_bf16_f32 v116, v116, v117\n"
    "v_cvt_pk_bf16_f32 v117, v118, v119\n"
    "v_cvt_pk_bf16_f32 v118, v128, v129\n"
    "v_cvt_pk_bf16_f32 v119, v130, v131\n"
    "v_accvgpr_write_b32 a152, v116\n"
    "v_accvgpr_write_b32 a153, v117\n"
    "v_accvgpr_write_b32 a154, v118\n"
    "v_accvgpr_write_b32 a155, v119\n"
    "v_pk_mul_f32 v[120:121], v[48:49], v[120:121]\n"
    "v_pk_mul_f32 v[122:123], v[48:49], v[122:123]\n"
    "v_pk_mul_f32 v[132:133], v[48:49], v[132:133]\n"
    "v_pk_mul_f32 v[134:135], v[48:49], v[134:135]\n"
    "v_cvt_pk_bf16_f32 v120, v120, v121\n"
    "v_cvt_pk_bf16_f32 v121, v122, v123\n"
    "v_cvt_pk_bf16_f32 v122, v132, v133\n"
    "v_cvt_pk_bf16_f32 v123, v134, v135\n"
    "v_accvgpr_write_b32 a168, v120\n"
    "v_accvgpr_write_b32 a169, v121\n"
    "v_accvgpr_write_b32 a170, v122\n"
    "v_accvgpr_write_b32 a171, v123\n"
    "v_pk_mul_f32 v[136:137], v[44:45], v[136:137]\n"
    "v_pk_mul_f32 v[138:139], v[44:45], v[138:139]\n"
    "v_pk_mul_f32 v[148:149], v[44:45], v[148:149]\n"
    "v_pk_mul_f32 v[150:151], v[44:45], v[150:151]\n"
    "v_cvt_pk_bf16_f32 v136, v136, v137\n"
    "v_cvt_pk_bf16_f32 v137, v138, v139\n"
    "v_cvt_pk_bf16_f32 v138, v148, v149\n"
    "v_cvt_pk_bf16_f32 v139, v150, v151\n"
    "v_accvgpr_write_b32 a140, v136\n"
    "v_accvgpr_write_b32 a141, v137\n"
    "v_accvgpr_write_b32 a142, v138\n"
    "v_accvgpr_write_b32 a143, v139\n"
    "v_pk_mul_f32 v[140:141], v[46:47], v[140:141]\n"
    "v_pk_mul_f32 v[142:143], v[46:47], v[142:143]\n"
    "v_pk_mul_f32 v[152:153], v[46:47], v[152:153]\n"
    "v_pk_mul_f32 v[154:155], v[46:47], v[154:155]\n"
    "v_cvt_pk_bf16_f32 v140, v140, v141\n"
    "v_cvt_pk_bf16_f32 v141, v142, v143\n"
    "v_cvt_pk_bf16_f32 v142, v152, v153\n"
    "v_cvt_pk_bf16_f32 v143, v154, v155\n"
    "v_accvgpr_write_b32 a156, v140\n"
    "v_accvgpr_write_b32 a157, v141\n"
    "v_accvgpr_write_b32 a158, v142\n"
    "v_accvgpr_write_b32 a159, v143\n"
    "v_pk_mul_f32 v[144:145], v[48:49], v[144:145]\n"
    "v_pk_mul_f32 v[146:147], v[48:49], v[146:147]\n"
    "v_pk_mul_f32 v[156:157], v[48:49], v[156:157]\n"
    "v_pk_mul_f32 v[158:159], v[48:49], v[158:159]\n"
    "v_cvt_pk_bf16_f32 v144, v144, v145\n"
    "v_cvt_pk_bf16_f32 v145, v146, v147\n"
    "v_cvt_pk_bf16_f32 v146, v156, v157\n"
    "v_cvt_pk_bf16_f32 v147, v158, v159\n"
    "v_accvgpr_write_b32 a172, v144\n"
    "v_accvgpr_write_b32 a173, v145\n"
    "v_accvgpr_write_b32 a174, v146\n"
    "v_accvgpr_write_b32 a175, v147\n"
    "s_mov_b32 s55, 1\n"
    "v_mfma_f32_16x16x32_bf16 v[160:163], a[0:3], a[176:179], 0\n"
    "v_and_b32 v44, 63, %7\n"
    "v_lshrrev_b32 v45, 4, v44\n"
    "v_mfma_f32_16x16x32_bf16 v[160:163], a[4:7], a[180:183], v[160:163]\n"
    "v_and_b32 v46, 15, v44\n"
    "v_bfe_u32 v47, v44, 2, 2\n"
    "v_mfma_f32_16x16x32_bf16 v[160:163], a[8:11], a[184:187], v[160:163]\n"
    "v_and_b32 v48, 3, v44\n"
    "v_and_b32 v49, 1, v45\n"
    "v_mfma_f32_16x16x32_bf16 v[160:163], a[12:15], a[188:191], v[160:163]\n"
    "v_lshl_or_b32 v49, v49, 2, v47\n"
    "v_lshrrev_b32 v50, 3, v44\n"
    "v_mfma_f32_16x16x32_bf16 v[184:187], a[12:15], a[204:207], 0\n"
    "v_and_b32 v51, 7, v44\n"
    "s_lshr_b32 s46, %5, 1\n"
    "s_lshl_b32 s46, s46, 8\n"
    "s_sub_u32 s46, 8388608, s46\n"
    "s_mov_b64 s[12:13], %9\n"
    "s_and_b32 s13, s13, 0xffff\n"
    "s_mov_b32 s14, s46\n"
    "s_mov_b32 s15, 0x20000\n"
    "s_mov_b64 s[8:9], %10\n"
    "s_and_b32 s9, s9, 0xffff\n"
    "s_mov_b32 s10, 716928\n"
    "s_mov_b32 s11, 0x20000\n"
    "v_lshl_add_u32 v54, v45, 2, v47\n"
    "v_mfma_f32_16x16x32_bf16 v[184:187], a[8:11], a[200:203], v[184:187]\n"
    "v_bfe_u32 v52, v54, 1, 2\n"
    "v_lshlrev_b32 v54, 7, v54\n"
    "v_mfma_f32_16x16x32_bf16 v[184:187], a[4:7], a[196:199], v[184:187]\n"
    "v_lshl_add_u32 v54, v48, 3, v54\n"
    "v_lshl_add_u32 v5, v52, 5, v54\n"
    "v_mfma_f32_16x16x32_bf16 v[184:187], a[0:3], a[192:195], v[184:187]\n"
    "v_xor_b32 v6, 32, v5\n"
    "v_xor_b32 v7, 64, v5\n"
    "v_mfma_f32_16x16x32_bf16 v[208:211], a[0:3], a[208:211], 0\n"
    "v_xor_b32 v8, 96, v5\n"
    "s_mul_i32 s46, s36, 3072\n"
    "v_add_u32 v15, s46, v37\n"
    "s_mov_b32 s40, 0\n"
    "s_mov_b32 s43, 8\n"
    "s_mov_b32 s52, 2\n"
    "s_and_b32 s46, %5, 1\n"
    "s_lshr_b32 s57, %5, 1\n"
    "s_mov_b32 s58, 0x46b1a200\n"
    "s_mov_b32 s59, 0x36b634\n"
    "v_mfma_f32_16x16x32_bf16 v[208:211], a[4:7], a[212:215], v[208:211]\n"
    "v_mul_u32_u24 v54, 3, v44\n"
    "v_lshrrev_b64 v[52:53], v54, s[58:59]\n"
    "v_mfma_f32_16x16x32_bf16 v[208:211], a[8:11], a[216:219], v[208:211]\n"
    "v_and_b32 v52, 7, v52\n"
    "v_cmp_le_u32 vcc, s57, v52\n"
    "s_nop 1\n"
    "v_mfma_f32_16x16x32_bf16 v[208:211], a[12:15], a[220:223], v[208:211]\n"
    "v_addc_co_u32 v52, vcc, 0, v52, vcc\n"
    "v_mov_b32 v54, s57\n"
    "v_mfma_f32_16x16x32_bf16 v[224:227], a[28:31], a[220:223], 0\n"
    "ds_read_b128 a[0:3], v33 offset:16384\n"
    "v_cmp_gt_u32 vcc, 2, v44\n"
    "s_nop 1\n"
    "v_mfma_f32_16x16x32_bf16 v[224:227], a[24:27], a[216:219], v[224:227]\n"
    "ds_read_b128 a[4:7], v34 offset:16384\n"
    "v_cndmask_b32 v52, v52, v54, vcc\n"
    "v_mfma_f32_16x16x32_bf16 v[224:227], a[20:23], a[212:215], v[224:227]\n"
    "ds_read_b128 a[8:11], v35 offset:16384\n"
    "v_mov_b32 v54, 0x3fe02\n"
    "v_mfma_f32_16x16x32_bf16 v[224:227], a[16:19], a[208:211], v[224:227]\n"
    "ds_read_b128 a[12:15], v36 offset:16384\n"
    "v_bfe_u32 v54, v54, v44, 1\n"
    "v_mfma_f32_16x16x32_bf16 v[200:203], a[16:19], a[192:195], 0\n"
    "v_lshl_add_u32 v52, v54, 3, v52\n"
    "v_and_b32 v53, 1, v44\n"
    "v_mfma_f32_16x16x32_bf16 v[200:203], a[20:23], a[196:199], v[200:203]\n"
    "v_mov_b32 v54, s46\n"
    "v_cmp_gt_u32 vcc, 14, v44\n"
    "s_nop 1\n"
    "v_mfma_f32_16x16x32_bf16 v[200:203], a[24:27], a[200:203], v[200:203]\n"
    "v_cndmask_b32 v54, v53, v54, vcc\n"
    "v_lshl_add_u32 v43, v52, 1, v54\n"
    "v_mfma_f32_16x16x32_bf16 v[200:203], a[28:31], a[204:207], v[200:203]\n"
    "v_mul_u32_u24 v43, 45056, v43\n"
    "v_and_b32 v52, 1, v48\n"
    "v_mfma_f32_16x16x32_bf16 v[176:179], a[28:31], a[188:191], 0\n"
    "v_lshlrev_b32 v52, 4, v52\n"
    "v_mov_b32 v54, 0x3f80\n"
    "v_mfma_f32_16x16x32_bf16 v[176:179], a[24:27], a[184:187], v[176:179]\n"
    "v_lshlrev_b32 v52, v52, v54\n"
    "v_cmp_eq_u32 vcc, v45, v47\n"
    "s_nop 1\n"
    "v_mfma_f32_16x16x32_bf16 v[176:179], a[20:23], a[180:183], v[176:179]\n"
    "v_cndmask_b32 v52, 0, v52, vcc\n"
    "v_cmp_gt_u32 vcc, 2, v48\n"
    "s_nop 1\n"
    "v_mfma_f32_16x16x32_bf16 v[176:179], a[16:19], a[176:179], v[176:179]\n"
    "v_cndmask_b32 v54, 0, v52, vcc\n"
    "v_sub_u32 v52, v52, v54\n"
    "v_mfma_f32_16x16x32_bf16 v[168:171], a[32:35], a[176:179], 0\n"
    "ds_read_b128 a[16:19], v33 offset:20480\n"
    "v_accvgpr_write_b32 a232, v54\n"
    "v_mfma_f32_16x16x32_bf16 v[168:171], a[36:39], a[180:183], v[168:171]\n"
    "ds_read_b128 a[20:23], v34 offset:20480\n"
    "v_accvgpr_write_b32 a233, v52\n"
    "v_mfma_f32_16x16x32_bf16 v[168:171], a[40:43], a[184:187], v[168:171]\n"
    "ds_read_b128 a[24:27], v35 offset:20480\n"
    "v_accvgpr_write_b32 a234, 0\n"
    "v_mfma_f32_16x16x32_bf16 v[168:171], a[44:47], a[188:191], v[168:171]\n"
    "ds_read_b128 a[28:31], v36 offset:20480\n"
    "v_accvgpr_write_b32 a235, 0\n"
    "v_mfma_f32_16x16x32_bf16 v[192:195], a[44:47], a[204:207], 0\n"
    "v_accvgpr_write_b32 a236, 0\n"
    "v_accvgpr_write_b32 a237, 0\n"
    "v_mfma_f32_16x16x32_bf16 v[192:195], a[40:43], a[200:203], v[192:195]\n"
    "v_accvgpr_write_b32 a238, v54\n"
    "v_accvgpr_write_b32 a239, v52\n"
    "v_mfma_f32_16x16x32_bf16 v[192:195], a[36:39], a[196:199], v[192:195]\n"
    "v_mfma_f32_16x16x32_bf16 v[192:195], a[32:35], a[192:195], v[192:195]\n"
    "v_mfma_f32_16x16x32_bf16 v[216:219], a[32:35], a[208:211], 0\n"
    "v_mfma_f32_16x16x32_bf16 v[216:219], a[36:39], a[212:215], v[216:219]\n"
    "v_mfma_f32_16x16x32_bf16 v[216:219], a[40:43], a[216:219], v[216:219]\n"
    "v_mfma_f32_16x16x32_bf16 v[216:219], a[44:47], a[220:223], v[216:219]\n"
    "v_mfma_f32_16x16x32_bf16 v[228:231], a[60:63], a[220:223], 0\n"
    "ds_read_b128 a[32:35], v33 offset:24576\n"
    "v_mfma_f32_16x16x32_bf16 v[228:231], a[56:59], a[216:219], v[228:231]\n"
    "ds_read_b128 a[36:39], v34 offset:24576\n"
    "v_mfma_f32_16x16x32_bf16 v[228:231], a[52:55], a[212:215], v[228:231]\n"
    "ds_read_b128 a[40:43], v35 offset:24576\n"
    "v_mfma_f32_16x16x32_bf16 v[228:231], a[48:51], a[208:211], v[228:231]\n"
    "ds_read_b128 a[44:47], v36 offset:24576\n"
    "v_mfma_f32_16x16x32_bf16 v[204:207], a[48:51], a[192:195], 0\n"
    "v_mfma_f32_16x16x32_bf16 v[204:207], a[52:55], a[196:199], v[204:207]\n"
    "v_mfma_f32_16x16x32_bf16 v[204:207], a[56:59], a[200:203], v[204:207]\n"
    "v_mfma_f32_16x16x32_bf16 v[204:207], a[60:63], a[204:207], v[204:207]\n"
    "v_mfma_f32_16x16x32_bf16 v[180:183], a[60:63], a[188:191], 0\n"
    "v_mfma_f32_16x16x32_bf16 v[180:183], a[56:59], a[184:187], v[180:183]\n"
    "v_mfma_f32_16x16x32_bf16 v[180:183], a[52:55], a[180:183], v[180:183]\n"
    "v_mfma_f32_16x16x32_bf16 v[180:183], a[48:51], a[176:179], v[180:183]\n"
    "ds_read_b128 a[48:51], v33 offset:28672\n"
    "ds_read_b128 a[52:55], v34 offset:28672\n"
    "ds_read_b128 a[56:59], v35 offset:28672\n"
    "ds_read_b128 a[60:63], v36 offset:28672\n"
    "v_mfma_f32_32x32x16_bf16 v[64:79], a[228:231], a[228:231], 0\n"
    "v_mfma_f32_32x32x16_bf16 v[80:95], a[228:231], a[228:231], 0\n"
    "v_mfma_f32_32x32x16_bf16 v[96:111], a[228:231], a[228:231], 0\n"
    "v_mfma_f32_32x32x16_bf16 v[112:127], a[228:231], a[228:231], 0\n"
    "v_mfma_f32_32x32x16_bf16 v[128:143], a[228:231], a[228:231], 0\n"
    "v_mfma_f32_32x32x16_bf16 v[144:159], a[228:231], a[228:231], 0\n"
    "v_mov_b32 v244, 0\n"
    "v_mov_b32 v245, 0\n"
    "v_mov_b32 v246, 0\n"
    "v_mov_b32 v247, 0\n"
    "v_mov_b32 v248, 0\n"
    "v_mov_b32 v249, 0\n"
    "v_mov_b32 v250, 0\n"
    "v_mov_b32 v251, 0\n"
    "v_mov_b32 v252, 0\n"
    "v_mov_b32 v253, 0\n"
    "v_mov_b32 v254, 0\n"
    "v_mov_b32 v255, 0\n"
    "v_max3_f32 v44, v160, v161, v162\n"
    "v_max3_f32 v44, v44, v163, v176\n"
    "v_max3_f32 v44, v44, v177, v178\n"
    "v_max3_f32 v44, v44, v179, v168\n"
    "v_max3_f32 v44, v44, v169, v170\n"
    "v_max3_f32 v44, v44, v171, v180\n"
    "v_max3_f32 v44, v44, v181, v182\n"
    "v_max_f32 v44, v44, v183\n"
    "v_max3_f32 v46, v184, v185, v186\n"
    "v_max3_f32 v46, v46, v187, v200\n"
    "v_max3_f32 v46, v46, v201, v202\n"
    "v_max3_f32 v46, v46, v203, v192\n"
    "v_max3_f32 v46, v46, v193, v194\n"
    "v_max3_f32 v46, v46, v195, v204\n"
    "v_max3_f32 v46, v46, v205, v206\n"
    "v_max_f32 v46, v46, v207\n"
    "v_max3_f32 v48, v208, v209, v210\n"
    "v_max3_f32 v48, v48, v211, v224\n"
    "v_max3_f32 v48, v48, v225, v226\n"
    "v_max3_f32 v48, v48, v227, v216\n"
    "v_max3_f32 v48, v48, v217, v218\n"
    "v_max3_f32 v48, v48, v219, v228\n"
    "v_max3_f32 v48, v48, v229, v230\n"
    "v_max_f32 v48, v48, v231\n"
    "v_mov_b32 v45, v44\n"
    "v_mov_b32 v47, v46\n"
    "v_mov_b32 v49, v48\n"
    "s_nop 1\n"
    "v_permlane32_swap_b32 v44, v45\n"
    "v_permlane32_swap_b32 v46, v47\n"
    "v_permlane32_swap_b32 v48, v49\n"
    "v_max_f32 v44, v44, v45\n"
    "v_max_f32 v46, v46, v47\n"
    "v_max_f32 v48, v48, v49\n"
    "v_mov_b32 v45, v44\n"
    "v_mov_b32 v47, v46\n"
    "v_mov_b32 v49, v48\n"
    "s_nop 1\n"
    "v_permlane16_swap_b32 v44, v45\n"
    "v_permlane16_swap_b32 v46, v47\n"
    "v_permlane16_swap_b32 v48, v49\n"
    "v_max_f32 v44, v44, v45\n"
    "v_max_f32 v46, v46, v47\n"
    "v_max_f32 v48, v48, v49\n"
    "v_sub_f32 v232, 0, v44\n"
    "v_sub_f32 v233, 0, v44\n"
    "v_sub_f32 v234, 0, v44\n"
    "v_sub_f32 v235, 0, v44\n"
    "v_sub_f32 v236, 0, v46\n"
    "v_sub_f32 v237, 0, v46\n"
    "v_sub_f32 v238, 0, v46\n"
    "v_sub_f32 v239, 0, v46\n"
    "v_sub_f32 v240, 0, v48\n"
    "v_sub_f32 v241, 0, v48\n"
    "v_sub_f32 v242, 0, v48\n"
    "v_sub_f32 v243, 0, v48\n"
    "v_pk_add_f32 v[160:161], v[232:233], v[160:161]\n"
    "v_pk_add_f32 v[162:163], v[232:233], v[162:163]\n"
    "v_pk_add_f32 v[184:185], v[236:237], v[184:185]\n"
    "v_pk_add_f32 v[186:187], v[236:237], v[186:187]\n"
    "v_pk_add_f32 v[208:209], v[240:241], v[208:209]\n"
    "v_pk_add_f32 v[210:211], v[240:241], v[210:211]\n"
    "v_pk_add_f32 v[176:177], v[232:233], v[176:177]\n"
    "v_pk_add_f32 v[178:179], v[232:233], v[178:179]\n"
    "v_pk_add_f32 v[200:201], v[236:237], v[200:201]\n"
    "v_pk_add_f32 v[202:203], v[236:237], v[202:203]\n"
    "v_pk_add_f32 v[224:225], v[240:241], v[224:225]\n"
    "v_pk_add_f32 v[226:227], v[240:241], v[226:227]\n"
    "v_pk_add_f32 v[168:169], v[232:233], v[168:169]\n"
    "v_pk_add_f32 v[170:171], v[232:233], v[170:171]\n"
    "v_pk_add_f32 v[192:193], v[236:237], v[192:193]\n"
    "v_pk_add_f32 v[194:195], v[236:237], v[194:195]\n"
    "v_pk_add_f32 v[216:217], v[240:241], v[216:217]\n"
    "v_pk_add_f32 v[218:219], v[240:241], v[218:219]\n"
    "v_pk_add_f32 v[180:181], v[232:233], v[180:181]\n"
    "v_pk_add_f32 v[182:183], v[232:233], v[182:183]\n"
    "v_pk_add_f32 v[204:205], v[236:237], v[204:205]\n"
    "v_pk_add_f32 v[206:207], v[236:237], v[206:207]\n"
    "v_pk_add_f32 v[228:229], v[240:241], v[228:229]\n"
    "v_pk_add_f32 v[230:231], v[240:241], v[230:231]\n"
    "v_exp_f32 v160, v160\n"
    "v_exp_f32 v161, v161\n"
    "v_exp_f32 v162, v162\n"
    "v_exp_f32 v163, v163\n"
    "v_exp_f32 v184, v184\n"
    "v_exp_f32 v185, v185\n"
    "v_exp_f32 v186, v186\n"
    "v_exp_f32 v187, v187\n"
    "v_exp_f32 v208, v208\n"
    "v_exp_f32 v209, v209\n"
    "v_exp_f32 v210, v210\n"
    "v_exp_f32 v211, v211\n"
    "v_exp_f32 v176, v176\n"
    "v_exp_f32 v177, v177\n"
    "v_exp_f32 v178, v178\n"
    "v_exp_f32 v179, v179\n"
    "v_exp_f32 v200, v200\n"
    "v_exp_f32 v201, v201\n"
    "v_exp_f32 v202, v202\n"
    "v_exp_f32 v203, v203\n"
    "v_exp_f32 v224, v224\n"
    "v_exp_f32 v225, v225\n"
    "v_exp_f32 v226, v226\n"
    "v_exp_f32 v227, v227\n"
    "v_exp_f32 v168, v168\n"
    "v_exp_f32 v169, v169\n"
    "v_exp_f32 v170, v170\n"
    "v_exp_f32 v171, v171\n"
    "v_exp_f32 v192, v192\n"
    "v_exp_f32 v193, v193\n"
    "v_exp_f32 v194, v194\n"
    "v_exp_f32 v195, v195\n"
    "v_exp_f32 v216, v216\n"
    "v_exp_f32 v217, v217\n"
    "v_exp_f32 v218, v218\n"
    "v_exp_f32 v219, v219\n"
    "v_exp_f32 v180, v180\n"
    "v_exp_f32 v181, v181\n"
    "v_exp_f32 v182, v182\n"
    "v_exp_f32 v183, v183\n"
    "v_exp_f32 v204, v204\n"
    "v_exp_f32 v205, v205\n"
    "v_exp_f32 v206, v206\n"
    "v_exp_f32 v207, v207\n"
    "v_exp_f32 v228, v228\n"
    "v_exp_f32 v229, v229\n"
    "v_exp_f32 v230, v230\n"
    "v_exp_f32 v231, v231\n"
    "v_cvt_pk_bf16_f32 v160, v160, v161\n"
    "v_cvt_pk_bf16_f32 v161, v162, v163\n"
    "v_cvt_pk_bf16_f32 v184, v184, v185\n"
    "v_cvt_pk_bf16_f32 v185, v186, v187\n"
    "v_cvt_pk_bf16_f32 v208, v208, v209\n"
    "v_cvt_pk_bf16_f32 v209, v210, v211\n"
    "v_cvt_pk_bf16_f32 v162, v176, v177\n"
    "v_cvt_pk_bf16_f32 v163, v178, v179\n"
    "v_cvt_pk_bf16_f32 v186, v200, v201\n"
    "v_cvt_pk_bf16_f32 v187, v202, v203\n"
    "v_cvt_pk_bf16_f32 v210, v224, v225\n"
    "v_cvt_pk_bf16_f32 v211, v226, v227\n"
    "v_cvt_pk_bf16_f32 v168, v168, v169\n"
    "v_cvt_pk_bf16_f32 v169, v170, v171\n"
    "v_cvt_pk_bf16_f32 v192, v192, v193\n"
    "v_cvt_pk_bf16_f32 v193, v194, v195\n"
    "v_cvt_pk_bf16_f32 v216, v216, v217\n"
    "v_cvt_pk_bf16_f32 v217, v218, v219\n"
    "v_cvt_pk_bf16_f32 v170, v180, v181\n"
    "v_cvt_pk_bf16_f32 v171, v182, v183\n"
    "v_cvt_pk_bf16_f32 v194, v204, v205\n"
    "v_cvt_pk_bf16_f32 v195, v206, v207\n"
    "v_cvt_pk_bf16_f32 v218, v228, v229\n"
    "v_cvt_pk_bf16_f32 v219, v230, v231\n"
    "s_branch .L_aC_%=h\n"
    ".L_aC_%=y:\n"
    "s_waitcnt lgkmcnt(0)\n"
    "s_barrier\n"
    "v_mfma_f32_16x16x32_bf16 v[160:163], a[0:3], a[176:179], v[232:235]\n"
    "s_cmp_eq_u32 s53, 0x10000\n"
    "ds_read_b64_tr_b16 a[64:65], v25 offset:0\n"
    "v_mfma_f32_16x16x32_bf16 v[160:163], a[4:7], a[180:183], v[160:163]\n"
    "s_cselect_b32 s54, 0xffff0000, s41\n"
    "ds_read_b64_tr_b16 a[66:67], v25 offset:4096\n"
    "v_mfma_f32_16x16x32_bf16 v[160:163], a[8:11], a[184:187], v[160:163]\n"
    "s_add_u32 s53, s53, s54\n"
    "ds_read_b64_tr_b16 a[68:69], v25 offset:8192\n"
    "s_add_u32 s46, s56, 0\n"
    "buffer_store_dwordx4 a[128:131], v37, s[24:27], s46 offen offset:0 sc1\n"
    "v_mfma_f32_16x16x32_bf16 v[160:163], a[12:15], a[188:191], v[160:163]\n"
    "ds_read_b64_tr_b16 a[70:71], v25 offset:12288\n"
    "v_add_u32 v25, s54, v25\n"
    "v_mfma_f32_16x16x32_bf16 v[184:187], a[12:15], a[204:207], v[236:239]\n"
    "ds_read_b64_tr_b16 a[72:73], v26 offset:0\n"
    "s_add_u32 s46, s56, 0\n"
    "buffer_store_dwordx4 a[144:147], v37, s[24:27], s46 offen offset:1024 sc1\n"
    "v_mfma_f32_16x16x32_bf16 v[184:187], a[8:11], a[200:203], v[184:187]\n"
    "ds_read_b64_tr_b16 a[74:75], v26 offset:4096\n"
    "s_add_u32 s46, s56, 0\n"
    "buffer_store_dwordx4 a[160:163], v37, s[24:27], s46 offen offset:2048 sc1\n"
    "v_mfma_f32_16x16x32_bf16 v[184:187], a[4:7], a[196:199], v[184:187]\n"
    "ds_read_b64_tr_b16 a[76:77], v26 offset:8192\n"
    "v_mfma_f32_16x16x32_bf16 v[184:187], a[0:3], a[192:195], v[184:187]\n"
    "ds_read_b64_tr_b16 a[78:79], v26 offset:12288\n"
    "v_add_u32 v26, s54, v26\n"
    "v_mfma_f32_16x16x32_bf16 v[208:211], a[0:3], a[208:211], v[240:243]\n"
    "ds_read_b64_tr_b16 a[80:81], v27 offset:0\n"
    "s_add_u32 s46, s56, 11264\n"
    "buffer_store_dwordx4 a[132:135], v37, s[24:27], s46 offen offset:0 sc1\n"
    "v_mfma_f32_16x16x32_bf16 v[208:211], a[4:7], a[212:215], v[208:211]\n"
    "ds_read_b64_tr_b16 a[82:83], v27 offset:4096\n"
    "v_mfma_f32_16x16x32_bf16 v[208:211], a[8:11], a[216:219], v[208:211]\n"
    "ds_read_b64_tr_b16 a[84:85], v27 offset:8192\n"
    "s_add_u32 s46, s56, 11264\n"
    "buffer_store_dwordx4 a[148:151], v37, s[24:27], s46 offen offset:1024 sc1\n"
    "v_mfma_f32_16x16x32_bf16 v[208:211], a[12:15], a[220:223], v[208:211]\n"
    "ds_read_b64_tr_b16 a[86:87], v27 offset:12288\n"
    "v_add_u32 v27, s54, v27\n"
    "v_mfma_f32_16x16x32_bf16 v[224:227], a[28:31], a[220:223], v[240:243]\n"
    "ds_read_b64_tr_b16 a[88:89], v28 offset:0\n"
    "s_add_u32 s46, s56, 11264\n"
    "buffer_store_dwordx4 a[164:167], v37, s[24:27], s46 offen offset:2048 sc1\n"
    "v_mfma_f32_16x16x32_bf16 v[224:227], a[24:27], a[216:219], v[224:227]\n"
    "ds_read_b64_tr_b16 a[90:91], v28 offset:4096\n"
    "v_mfma_f32_16x16x32_bf16 v[224:227], a[20:23], a[212:215], v[224:227]\n"
    "ds_read_b64_tr_b16 a[92:93], v28 offset:8192\n"
    "v_mfma_f32_16x16x32_bf16 v[224:227], a[16:19], a[208:211], v[224:227]\n"
    "ds_read_b64_tr_b16 a[94:95], v28 offset:12288\n"
    "v_add_u32 v28, s54, v28\n"
    "v_mfma_f32_16x16x32_bf16 v[200:203], a[16:19], a[192:195], v[236:239]\n"
    "ds_read_b64_tr_b16 a[96:97], v29 offset:0\n"
    "s_add_u32 s46, s56, 22528\n"
    "buffer_store_dwordx4 a[136:139], v37, s[24:27], s46 offen offset:0 sc1\n"
    "v_mfma_f32_16x16x32_bf16 v[200:203], a[20:23], a[196:199], v[200:203]\n"
    "ds_read_b64_tr_b16 a[98:99], v29 offset:4096\n"
    "s_add_u32 s46, s56, 22528\n"
    "buffer_store_dwordx4 a[152:155], v37, s[24:27], s46 offen offset:1024 sc1\n"
    "v_mfma_f32_16x16x32_bf16 v[200:203], a[24:27], a[200:203], v[200:203]\n"
    "ds_read_b64_tr_b16 a[100:101], v29 offset:8192\n"
    "v_mfma_f32_16x16x32_bf16 v[200:203], a[28:31], a[204:207], v[200:203]\n"
    "ds_read_b64_tr_b16 a[102:103], v29 offset:12288\n"
    "v_add_u32 v29, s54, v29\n"
    "v_mfma_f32_16x16x32_bf16 v[176:179], a[28:31], a[188:191], v[232:235]\n"
    "ds_read_b64_tr_b16 a[104:105], v30 offset:0\n"
    "s_add_u32 s46, s56, 22528\n"
    "buffer_store_dwordx4 a[168:171], v37, s[24:27], s46 offen offset:2048 sc1\n"
    "v_mfma_f32_16x16x32_bf16 v[176:179], a[24:27], a[184:187], v[176:179]\n"
    "ds_read_b64_tr_b16 a[106:107], v30 offset:4096\n"
    "v_mfma_f32_16x16x32_bf16 v[176:179], a[20:23], a[180:183], v[176:179]\n"
    "ds_read_b64_tr_b16 a[108:109], v30 offset:8192\n"
    "s_add_u32 s46, s56, 33792\n"
    "buffer_store_dwordx4 a[140:143], v37, s[24:27], s46 offen offset:0 sc1\n"
    "v_mfma_f32_16x16x32_bf16 v[176:179], a[16:19], a[176:179], v[176:179]\n"
    "ds_read_b64_tr_b16 a[110:111], v30 offset:12288\n"
    "v_add_u32 v30, s54, v30\n"
    "v_mfma_f32_16x16x32_bf16 v[168:171], a[32:35], a[176:179], v[232:235]\n"
    "ds_read_b64_tr_b16 a[112:113], v31 offset:0\n"
    "v_mfma_f32_16x16x32_bf16 v[168:171], a[36:39], a[180:183], v[168:171]\n"
    "ds_read_b64_tr_b16 a[114:115], v31 offset:4096\n"
    "s_add_u32 s46, s56, 33792\n"
    "buffer_store_dwordx4 a[156:159], v37, s[24:27], s46 offen offset:1024 sc1\n"
    "v_mfma_f32_16x16x32_bf16 v[168:171], a[40:43], a[184:187], v[168:171]\n"
    "ds_read_b64_tr_b16 a[116:117], v31 offset:8192\n"
    "v_mfma_f32_16x16x32_bf16 v[168:171], a[44:47], a[188:191], v[168:171]\n"
    "ds_read_b64_tr_b16 a[118:119], v31 offset:12288\n"
    "v_add_u32 v31, s54, v31\n"
    "v_mfma_f32_16x16x32_bf16 v[192:195], a[44:47], a[204:207], v[236:239]\n"
    "ds_read_b64_tr_b16 a[120:121], v32 offset:0\n"
    "s_add_u32 s46, s56, 33792\n"
    "buffer_store_dwordx4 a[172:175], v37, s[24:27], s46 offen offset:2048 sc1\n"
    "v_mfma_f32_16x16x32_bf16 v[192:195], a[40:43], a[200:203], v[192:195]\n"
    "ds_read_b64_tr_b16 a[122:123], v32 offset:4096\n"
    "v_mfma_f32_16x16x32_bf16 v[192:195], a[36:39], a[196:199], v[192:195]\n"
    "ds_read_b64_tr_b16 a[124:125], v32 offset:8192\n"
    "v_mfma_f32_16x16x32_bf16 v[192:195], a[32:35], a[192:195], v[192:195]\n"
    "ds_read_b64_tr_b16 a[126:127], v32 offset:12288\n"
    "v_add_u32 v32, s54, v32\n"
    "v_mfma_f32_16x16x32_bf16 v[216:219], a[32:35], a[208:211], v[240:243]\n"
    "v_exp_f32 v160, v160\n"
    "v_mfma_f32_16x16x32_bf16 v[216:219], a[36:39], a[212:215], v[216:219]\n"
    "v_exp_f32 v161, v161\n"
    "v_mfma_f32_16x16x32_bf16 v[216:219], a[40:43], a[216:219], v[216:219]\n"
    "v_exp_f32 v162, v162\n"
    "v_mfma_f32_16x16x32_bf16 v[216:219], a[44:47], a[220:223], v[216:219]\n"
    "v_exp_f32 v163, v163\n"
    "v_mfma_f32_16x16x32_bf16 v[228:231], a[60:63], a[220:223], v[240:243]\n"
    "v_exp_f32 v184, v184\n"
    "v_mfma_f32_16x16x32_bf16 v[228:231], a[56:59], a[216:219], v[228:231]\n"
    "v_exp_f32 v185, v185\n"
    "v_mfma_f32_16x16x32_bf16 v[228:231], a[52:55], a[212:215], v[228:231]\n"
    "v_exp_f32 v186, v186\n"
    "v_mfma_f32_16x16x32_bf16 v[228:231], a[48:51], a[208:211], v[228:231]\n"
    "v_exp_f32 v187, v187\n"
    "v_mfma_f32_16x16x32_bf16 v[204:207], a[48:51], a[192:195], v[236:239]\n"
    "v_exp_f32 v208, v208\n"
    "v_mfma_f32_16x16x32_bf16 v[204:207], a[52:55], a[196:199], v[204:207]\n"
    "v_exp_f32 v209, v209\n"
    "v_mfma_f32_16x16x32_bf16 v[204:207], a[56:59], a[200:203], v[204:207]\n"
    "v_exp_f32 v210, v210\n"
    "v_mfma_f32_16x16x32_bf16 v[204:207], a[60:63], a[204:207], v[204:207]\n"
    "v_exp_f32 v211, v211\n"
    "v_mfma_f32_16x16x32_bf16 v[180:183], a[60:63], a[188:191], v[232:235]\n"
    "v_exp_f32 v176, v176\n"
    "v_mfma_f32_16x16x32_bf16 v[180:183], a[56:59], a[184:187], v[180:183]\n"
    "v_exp_f32 v177, v177\n"
    "v_mfma_f32_16x16x32_bf16 v[180:183], a[52:55], a[180:183], v[180:183]\n"
    "v_exp_f32 v178, v178\n"
    "v_mfma_f32_16x16x32_bf16 v[180:183], a[48:51], a[176:179], v[180:183]\n"
    "s_waitcnt lgkmcnt(0)\n"
    "v_exp_f32 v179, v179\n"
    "v_mfma_f32_16x16x32_bf16 v[64:67], a[64:67], v[164:167], v[64:67]\n"
    "ds_read_b128 a[0:3], v33 offset:16384\n"
    "v_cvt_pk_bf16_f32 v160, v160, v161\n"
    "v_mfma_f32_16x16x32_bf16 v[64:67], a[68:71], v[172:175], v[64:67]\n"
    "v_exp_f32 v200, v200\n"
    "v_mfma_f32_16x16x32_bf16 v[68:71], a[68:71], v[196:199], v[68:71]\n"
    "v_exp_f32 v201, v201\n"
    "v_mfma_f32_16x16x32_bf16 v[68:71], a[64:67], v[188:191], v[68:71]\n"
    "ds_read_b128 a[4:7], v34 offset:16384\n"
    "v_cvt_pk_bf16_f32 v161, v162, v163\n"
    "v_mfma_f32_16x16x32_bf16 v[72:75], a[64:67], v[212:215], v[72:75]\n"
    "v_exp_f32 v202, v202\n"
    "v_mfma_f32_16x16x32_bf16 v[72:75], a[68:71], v[220:223], v[72:75]\n"
    "v_exp_f32 v203, v203\n"
    "v_mfma_f32_16x16x32_bf16 v[84:87], a[76:79], v[220:223], v[84:87]\n"
    "ds_read_b128 a[8:11], v35 offset:16384\n"
    "v_cvt_pk_bf16_f32 v184, v184, v185\n"
    "v_mfma_f32_16x16x32_bf16 v[84:87], a[72:75], v[212:215], v[84:87]\n"
    "v_exp_f32 v224, v224\n"
    "v_mfma_f32_16x16x32_bf16 v[80:83], a[72:75], v[188:191], v[80:83]\n"
    "v_exp_f32 v225, v225\n"
    "v_mfma_f32_16x16x32_bf16 v[80:83], a[76:79], v[196:199], v[80:83]\n"
    "ds_read_b128 a[12:15], v36 offset:16384\n"
    "v_cvt_pk_bf16_f32 v185, v186, v187\n"
    "v_mfma_f32_16x16x32_bf16 v[76:79], a[76:79], v[172:175], v[76:79]\n"
    "v_exp_f32 v226, v226\n"
    "v_mfma_f32_16x16x32_bf16 v[76:79], a[72:75], v[164:167], v[76:79]\n"
    "v_exp_f32 v227, v227\n"
    "v_mfma_f32_16x16x32_bf16 v[88:91], a[80:83], v[164:167], v[88:91]\n"
    "ds_read_b128 a[16:19], v33 offset:20480\n"
    "v_cvt_pk_bf16_f32 v208, v208, v209\n"
    "v_mfma_f32_16x16x32_bf16 v[88:91], a[84:87], v[172:175], v[88:91]\n"
    "v_exp_f32 v168, v168\n"
    "v_mfma_f32_16x16x32_bf16 v[92:95], a[84:87], v[196:199], v[92:95]\n"
    "v_exp_f32 v169, v169\n"
    "v_mfma_f32_16x16x32_bf16 v[92:95], a[80:83], v[188:191], v[92:95]\n"
    "ds_read_b128 a[20:23], v34 offset:20480\n"
    "v_cvt_pk_bf16_f32 v209, v210, v211\n"
    "v_mfma_f32_16x16x32_bf16 v[96:99], a[80:83], v[212:215], v[96:99]\n"
    "v_exp_f32 v170, v170\n"
    "v_mfma_f32_16x16x32_bf16 v[96:99], a[84:87], v[220:223], v[96:99]\n"
    "v_exp_f32 v171, v171\n"
    "v_mfma_f32_16x16x32_bf16 v[108:111], a[92:95], v[220:223], v[108:111]\n"
    "ds_read_b128 a[24:27], v35 offset:20480\n"
    "v_cvt_pk_bf16_f32 v162, v176, v177\n"
    "v_mfma_f32_16x16x32_bf16 v[108:111], a[88:91], v[212:215], v[108:111]\n"
    "v_exp_f32 v192, v192\n"
    "v_mfma_f32_16x16x32_bf16 v[104:107], a[88:91], v[188:191], v[104:107]\n"
    "v_exp_f32 v193, v193\n"
    "v_mfma_f32_16x16x32_bf16 v[104:107], a[92:95], v[196:199], v[104:107]\n"
    "ds_read_b128 a[28:31], v36 offset:20480\n"
    "v_cvt_pk_bf16_f32 v163, v178, v179\n"
    "v_mfma_f32_16x16x32_bf16 v[100:103], a[92:95], v[172:175], v[100:103]\n"
    "v_exp_f32 v194, v194\n"
    "v_mfma_f32_16x16x32_bf16 v[100:103], a[88:91], v[164:167], v[100:103]\n"
    "v_exp_f32 v195, v195\n"
    "v_mfma_f32_16x16x32_bf16 v[112:115], a[96:99], v[164:167], v[112:115]\n"
    "ds_read_b128 a[32:35], v33 offset:24576\n"
    "v_cvt_pk_bf16_f32 v186, v200, v201\n"
    "v_mfma_f32_16x16x32_bf16 v[112:115], a[100:103], v[172:175], v[112:115]\n"
    "v_exp_f32 v216, v216\n"
    "v_mfma_f32_16x16x32_bf16 v[116:119], a[100:103], v[196:199], v[116:119]\n"
    "v_exp_f32 v217, v217\n"
    "v_mfma_f32_16x16x32_bf16 v[116:119], a[96:99], v[188:191], v[116:119]\n"
    "ds_read_b128 a[36:39], v34 offset:24576\n"
    "v_cvt_pk_bf16_f32 v187, v202, v203\n"
    "v_mfma_f32_16x16x32_bf16 v[120:123], a[96:99], v[212:215], v[120:123]\n"
    "v_exp_f32 v218, v218\n"
    "v_mfma_f32_16x16x32_bf16 v[120:123], a[100:103], v[220:223], v[120:123]\n"
    "v_exp_f32 v219, v219\n"
    "v_mfma_f32_16x16x32_bf16 v[132:135], a[108:111], v[220:223], v[132:135]\n"
    "ds_read_b128 a[40:43], v35 offset:24576\n"
    "v_cvt_pk_bf16_f32 v210, v224, v225\n"
    "v_mfma_f32_16x16x32_bf16 v[132:135], a[104:107], v[212:215], v[132:135]\n"
    "v_exp_f32 v180, v180\n"
    "v_mfma_f32_16x16x32_bf16 v[128:131], a[104:107], v[188:191], v[128:131]\n"
    "v_exp_f32 v181, v181\n"
    "v_mfma_f32_16x16x32_bf16 v[128:131], a[108:111], v[196:199], v[128:131]\n"
    "ds_read_b128 a[44:47], v36 offset:24576\n"
    "v_cvt_pk_bf16_f32 v211, v226, v227\n"
    "v_mfma_f32_16x16x32_bf16 v[124:127], a[108:111], v[172:175], v[124:127]\n"
    "v_exp_f32 v182, v182\n"
    "v_mfma_f32_16x16x32_bf16 v[124:127], a[104:107], v[164:167], v[124:127]\n"
    "v_exp_f32 v183, v183\n"
    "v_mfma_f32_16x16x32_bf16 v[136:139], a[112:115], v[164:167], v[136:139]\n"
    "ds_read_b128 a[48:51], v33 offset:28672\n"
    "v_cvt_pk_bf16_f32 v168, v168, v169\n"
    "v_mfma_f32_16x16x32_bf16 v[136:139], a[116:119], v[172:175], v[136:139]\n"
    "v_exp_f32 v204, v204\n"
    "v_mfma_f32_16x16x32_bf16 v[140:143], a[116:119], v[196:199], v[140:143]\n"
    "v_exp_f32 v205, v205\n"
    "v_mfma_f32_16x16x32_bf16 v[140:143], a[112:115], v[188:191], v[140:143]\n"
    "ds_read_b128 a[52:55], v34 offset:28672\n"
    "v_cvt_pk_bf16_f32 v169, v170, v171\n"
    "v_mfma_f32_16x16x32_bf16 v[144:147], a[112:115], v[212:215], v[144:147]\n"
    "v_exp_f32 v206, v206\n"
    "v_mfma_f32_16x16x32_bf16 v[144:147], a[116:119], v[220:223], v[144:147]\n"
    "v_exp_f32 v207, v207\n"
    "v_mfma_f32_16x16x32_bf16 v[156:159], a[124:127], v[220:223], v[156:159]\n"
    "ds_read_b128 a[56:59], v35 offset:28672\n"
    "v_cvt_pk_bf16_f32 v192, v192, v193\n"
    "v_mfma_f32_16x16x32_bf16 v[156:159], a[120:123], v[212:215], v[156:159]\n"
    "v_exp_f32 v228, v228\n"
    "v_mfma_f32_16x16x32_bf16 v[152:155], a[120:123], v[188:191], v[152:155]\n"
    "v_exp_f32 v229, v229\n"
    "v_mfma_f32_16x16x32_bf16 v[152:155], a[124:127], v[196:199], v[152:155]\n"
    "ds_read_b128 a[60:63], v36 offset:28672\n"
    "v_cvt_pk_bf16_f32 v193, v194, v195\n"
    "v_mfma_f32_16x16x32_bf16 v[148:151], a[124:127], v[172:175], v[148:151]\n"
    "v_exp_f32 v230, v230\n"
    "v_mfma_f32_16x16x32_bf16 v[148:151], a[120:123], v[164:167], v[148:151]\n"
    "v_exp_f32 v231, v231\n"
    "v_mfma_f32_16x16x32_bf16 v[244:247], a[224:227], v[164:167], v[244:247]\n"
    "v_cvt_pk_bf16_f32 v216, v216, v217\n"
    "v_cvt_pk_bf16_f32 v217, v218, v219\n"
    "v_mfma_f32_16x16x32_bf16 v[244:247], a[224:227], v[172:175], v[244:247]\n"
    "v_cvt_pk_bf16_f32 v170, v180, v181\n"
    "v_cvt_pk_bf16_f32 v171, v182, v183\n"
    "v_mfma_f32_16x16x32_bf16 v[248:251], a[224:227], v[188:191], v[248:251]\n"
    "v_cvt_pk_bf16_f32 v194, v204, v205\n"
    "v_cvt_pk_bf16_f32 v195, v206, v207\n"
    "v_mfma_f32_16x16x32_bf16 v[248:251], a[224:227], v[196:199], v[248:251]\n"
    "v_cvt_pk_bf16_f32 v218, v228, v229\n"
    "v_cvt_pk_bf16_f32 v219, v230, v231\n"
    "v_mfma_f32_16x16x32_bf16 v[252:255], a[224:227], v[212:215], v[252:255]\n"
    "v_mfma_f32_16x16x32_bf16 v[252:255], a[224:227], v[220:223], v[252:255]\n"
    "s_waitcnt vmcnt(0) lgkmcnt(0)\n"
    "s_barrier\n"
    "v_mfma_f32_16x16x32_bf16 v[164:167], a[0:3], a[176:179], v[232:235]\n"
    "s_cmp_eq_u32 s53, 0x10000\n"
    "ds_read_b64_tr_b16 a[64:65], v25 offset:0\n"
    "v_mfma_f32_16x16x32_bf16 v[164:167], a[4:7], a[180:183], v[164:167]\n"
    "s_cselect_b32 s54, 0xffff0000, s41\n"
    "ds_read_b64_tr_b16 a[66:67], v25 offset:4096\n"
    "v_mfma_f32_16x16x32_bf16 v[164:167], a[8:11], a[184:187], v[164:167]\n"
    "s_add_u32 s53, s53, s54\n"
    "ds_read_b64_tr_b16 a[68:69], v25 offset:8192\n"
    "v_mfma_f32_16x16x32_bf16 v[164:167], a[12:15], a[188:191], v[164:167]\n"
    "ds_read_b64_tr_b16 a[70:71], v25 offset:12288\n"
    "v_add_u32 v25, s54, v25\n"
    "v_mfma_f32_16x16x32_bf16 v[188:191], a[12:15], a[204:207], v[236:239]\n"
    "ds_read_b64_tr_b16 a[72:73], v26 offset:0\n"
    "v_mfma_f32_16x16x32_bf16 v[188:191], a[8:11], a[200:203], v[188:191]\n"
    "ds_read_b64_tr_b16 a[74:75], v26 offset:4096\n"
    "global_load_dword v1, v[2:3], off\n"
    "v_mfma_f32_16x16x32_bf16 v[188:191], a[4:7], a[196:199], v[188:191]\n"
    "ds_read_b64_tr_b16 a[76:77], v26 offset:8192\n"
    "v_mfma_f32_16x16x32_bf16 v[188:191], a[0:3], a[192:195], v[188:191]\n"
    "ds_read_b64_tr_b16 a[78:79], v26 offset:12288\n"
    "v_add_u32 v26, s54, v26\n"
    "v_mfma_f32_16x16x32_bf16 v[212:215], a[0:3], a[208:211], v[240:243]\n"
    "ds_read_b64_tr_b16 a[80:81], v27 offset:0\n"
    "v_mfma_f32_16x16x32_bf16 v[212:215], a[4:7], a[212:215], v[212:215]\n"
    "ds_read_b64_tr_b16 a[82:83], v27 offset:4096\n"
    "v_mfma_f32_16x16x32_bf16 v[212:215], a[8:11], a[216:219], v[212:215]\n"
    "ds_read_b64_tr_b16 a[84:85], v27 offset:8192\n"
    "v_mfma_f32_16x16x32_bf16 v[212:215], a[12:15], a[220:223], v[212:215]\n"
    "ds_read_b64_tr_b16 a[86:87], v27 offset:12288\n"
    "v_add_u32 v27, s54, v27\n"
    "v_mfma_f32_16x16x32_bf16 v[224:227], a[28:31], a[220:223], v[240:243]\n"
    "ds_read_b64_tr_b16 a[88:89], v28 offset:0\n"
    "v_xor_b32 v33, 0x8000, v33\n"
    "v_mfma_f32_16x16x32_bf16 v[224:227], a[24:27], a[216:219], v[224:227]\n"
    "ds_read_b64_tr_b16 a[90:91], v28 offset:4096\n"
    "v_mfma_f32_16x16x32_bf16 v[224:227], a[20:23], a[212:215], v[224:227]\n"
    "ds_read_b64_tr_b16 a[92:93], v28 offset:8192\n"
    "v_xor_b32 v34, 0x8000, v34\n"
    "v_mfma_f32_16x16x32_bf16 v[224:227], a[16:19], a[208:211], v[224:227]\n"
    "ds_read_b64_tr_b16 a[94:95], v28 offset:12288\n"
    "v_add_u32 v28, s54, v28\n"
    "v_mfma_f32_16x16x32_bf16 v[200:203], a[16:19], a[192:195], v[236:239]\n"
    "ds_read_b64_tr_b16 a[96:97], v29 offset:0\n"
    "v_xor_b32 v35, 0x8000, v35\n"
    "v_mfma_f32_16x16x32_bf16 v[200:203], a[20:23], a[196:199], v[200:203]\n"
    "ds_read_b64_tr_b16 a[98:99], v29 offset:4096\n"
    "v_mfma_f32_16x16x32_bf16 v[200:203], a[24:27], a[200:203], v[200:203]\n"
    "ds_read_b64_tr_b16 a[100:101], v29 offset:8192\n"
    "v_xor_b32 v36, 0x8000, v36\n"
    "v_mfma_f32_16x16x32_bf16 v[200:203], a[28:31], a[204:207], v[200:203]\n"
    "ds_read_b64_tr_b16 a[102:103], v29 offset:12288\n"
    "v_add_u32 v29, s54, v29\n"
    "v_mfma_f32_16x16x32_bf16 v[176:179], a[28:31], a[188:191], v[232:235]\n"
    "ds_read_b64_tr_b16 a[104:105], v30 offset:0\n"
    "v_mfma_f32_16x16x32_bf16 v[176:179], a[24:27], a[184:187], v[176:179]\n"
    "ds_read_b64_tr_b16 a[106:107], v30 offset:4096\n"
    "v_mfma_f32_16x16x32_bf16 v[176:179], a[20:23], a[180:183], v[176:179]\n"
    "ds_read_b64_tr_b16 a[108:109], v30 offset:8192\n"
    "v_mfma_f32_16x16x32_bf16 v[176:179], a[16:19], a[176:179], v[176:179]\n"
    "ds_read_b64_tr_b16 a[110:111], v30 offset:12288\n"
    "v_add_u32 v30, s54, v30\n"
    "v_mfma_f32_16x16x32_bf16 v[172:175], a[32:35], a[176:179], v[232:235]\n"
    "ds_read_b64_tr_b16 a[112:113], v31 offset:0\n"
    "v_mfma_f32_16x16x32_bf16 v[172:175], a[36:39], a[180:183], v[172:175]\n"
    "ds_read_b64_tr_b16 a[114:115], v31 offset:4096\n"
    "v_mfma_f32_16x16x32_bf16 v[172:175], a[40:43], a[184:187], v[172:175]\n"
    "ds_read_b64_tr_b16 a[116:117], v31 offset:8192\n"
    "v_mfma_f32_16x16x32_bf16 v[172:175], a[44:47], a[188:191], v[172:175]\n"
    "ds_read_b64_tr_b16 a[118:119], v31 offset:12288\n"
    "v_add_u32 v31, s54, v31\n"
    "v_mfma_f32_16x16x32_bf16 v[196:199], a[44:47], a[204:207], v[236:239]\n"
    "ds_read_b64_tr_b16 a[120:121], v32 offset:0\n"
    "v_mfma_f32_16x16x32_bf16 v[196:199], a[40:43], a[200:203], v[196:199]\n"
    "ds_read_b64_tr_b16 a[122:123], v32 offset:4096\n"
    "v_mfma_f32_16x16x32_bf16 v[196:199], a[36:39], a[196:199], v[196:199]\n"
    "ds_read_b64_tr_b16 a[124:125], v32 offset:8192\n"
    "v_mfma_f32_16x16x32_bf16 v[196:199], a[32:35], a[192:195], v[196:199]\n"
    "ds_read_b64_tr_b16 a[126:127], v32 offset:12288\n"
    "v_add_u32 v32, s54, v32\n"
    "v_mfma_f32_16x16x32_bf16 v[220:223], a[32:35], a[208:211], v[240:243]\n"
    "v_exp_f32 v164, v164\n"
    "v_mfma_f32_16x16x32_bf16 v[220:223], a[36:39], a[212:215], v[220:223]\n"
    "v_exp_f32 v165, v165\n"
    "v_mfma_f32_16x16x32_bf16 v[220:223], a[40:43], a[216:219], v[220:223]\n"
    "v_exp_f32 v166, v166\n"
    "v_mfma_f32_16x16x32_bf16 v[220:223], a[44:47], a[220:223], v[220:223]\n"
    "v_exp_f32 v167, v167\n"
    "v_mfma_f32_16x16x32_bf16 v[228:231], a[60:63], a[220:223], v[240:243]\n"
    "v_exp_f32 v188, v188\n"
    "v_mfma_f32_16x16x32_bf16 v[228:231], a[56:59], a[216:219], v[228:231]\n"
    "v_exp_f32 v189, v189\n"
    "v_mfma_f32_16x16x32_bf16 v[228:231], a[52:55], a[212:215], v[228:231]\n"
    "v_exp_f32 v190, v190\n"
    "v_mfma_f32_16x16x32_bf16 v[228:231], a[48:51], a[208:211], v[228:231]\n"
    "v_exp_f32 v191, v191\n"
    "v_mfma_f32_16x16x32_bf16 v[204:207], a[48:51], a[192:195], v[236:239]\n"
    "v_exp_f32 v212, v212\n"
    "v_mfma_f32_16x16x32_bf16 v[204:207], a[52:55], a[196:199], v[204:207]\n"
    "v_exp_f32 v213, v213\n"
    "v_mfma_f32_16x16x32_bf16 v[204:207], a[56:59], a[200:203], v[204:207]\n"
    "v_exp_f32 v214, v214\n"
    "v_mfma_f32_16x16x32_bf16 v[204:207], a[60:63], a[204:207], v[204:207]\n"
    "v_exp_f32 v215, v215\n"
    "v_mfma_f32_16x16x32_bf16 v[180:183], a[60:63], a[188:191], v[232:235]\n"
    "v_exp_f32 v176, v176\n"
    "v_mfma_f32_16x16x32_bf16 v[180:183], a[56:59], a[184:187], v[180:183]\n"
    "v_exp_f32 v177, v177\n"
    "v_mfma_f32_16x16x32_bf16 v[180:183], a[52:55], a[180:183], v[180:183]\n"
    "v_exp_f32 v178, v178\n"
    "v_mfma_f32_16x16x32_bf16 v[180:183], a[48:51], a[176:179], v[180:183]\n"
    "s_waitcnt lgkmcnt(0)\n"
    "v_exp_f32 v179, v179\n"
    "v_mfma_f32_16x16x32_bf16 v[64:67], a[64:67], v[160:163], v[64:67]\n"
    "v_cvt_pk_bf16_f32 v164, v164, v165\n"
    "v_mfma_f32_16x16x32_bf16 v[64:67], a[68:71], v[168:171], v[64:67]\n"
    "v_exp_f32 v200, v200\n"
    "v_mfma_f32_16x16x32_bf16 v[68:71], a[68:71], v[192:195], v[68:71]\n"
    "v_exp_f32 v201, v201\n"
    "v_mfma_f32_16x16x32_bf16 v[68:71], a[64:67], v[184:187], v[68:71]\n"
    "v_cvt_pk_bf16_f32 v165, v166, v167\n"
    "v_mfma_f32_16x16x32_bf16 v[72:75], a[64:67], v[208:211], v[72:75]\n"
    "v_exp_f32 v202, v202\n"
    "v_mfma_f32_16x16x32_bf16 v[72:75], a[68:71], v[216:219], v[72:75]\n"
    "v_exp_f32 v203, v203\n"
    "v_mfma_f32_16x16x32_bf16 v[84:87], a[76:79], v[216:219], v[84:87]\n"
    "v_cvt_pk_bf16_f32 v188, v188, v189\n"
    "v_mfma_f32_16x16x32_bf16 v[84:87], a[72:75], v[208:211], v[84:87]\n"
    "v_exp_f32 v224, v224\n"
    "v_mfma_f32_16x16x32_bf16 v[80:83], a[72:75], v[184:187], v[80:83]\n"
    "v_exp_f32 v225, v225\n"
    "v_mfma_f32_16x16x32_bf16 v[80:83], a[76:79], v[192:195], v[80:83]\n"
    "v_cvt_pk_bf16_f32 v189, v190, v191\n"
    "v_mfma_f32_16x16x32_bf16 v[76:79], a[76:79], v[168:171], v[76:79]\n"
    "v_exp_f32 v226, v226\n"
    "v_mfma_f32_16x16x32_bf16 v[76:79], a[72:75], v[160:163], v[76:79]\n"
    "v_exp_f32 v227, v227\n"
    "v_mfma_f32_16x16x32_bf16 v[88:91], a[80:83], v[160:163], v[88:91]\n"
    "v_cvt_pk_bf16_f32 v212, v212, v213\n"
    "v_mfma_f32_16x16x32_bf16 v[88:91], a[84:87], v[168:171], v[88:91]\n"
    "v_exp_f32 v172, v172\n"
    "v_mfma_f32_16x16x32_bf16 v[92:95], a[84:87], v[192:195], v[92:95]\n"
    "v_exp_f32 v173, v173\n"
    "v_mfma_f32_16x16x32_bf16 v[92:95], a[80:83], v[184:187], v[92:95]\n"
    "v_cvt_pk_bf16_f32 v213, v214, v215\n"
    "v_mfma_f32_16x16x32_bf16 v[96:99], a[80:83], v[208:211], v[96:99]\n"
    "v_exp_f32 v174, v174\n"
    "v_mfma_f32_16x16x32_bf16 v[96:99], a[84:87], v[216:219], v[96:99]\n"
    "v_exp_f32 v175, v175\n"
    "v_mfma_f32_16x16x32_bf16 v[108:111], a[92:95], v[216:219], v[108:111]\n"
    "v_cvt_pk_bf16_f32 v166, v176, v177\n"
    "v_mfma_f32_16x16x32_bf16 v[108:111], a[88:91], v[208:211], v[108:111]\n"
    "v_exp_f32 v196, v196\n"
    "v_mfma_f32_16x16x32_bf16 v[104:107], a[88:91], v[184:187], v[104:107]\n"
    "v_exp_f32 v197, v197\n"
    "v_mfma_f32_16x16x32_bf16 v[104:107], a[92:95], v[192:195], v[104:107]\n"
    "v_cvt_pk_bf16_f32 v167, v178, v179\n"
    "v_mfma_f32_16x16x32_bf16 v[100:103], a[92:95], v[168:171], v[100:103]\n"
    "v_exp_f32 v198, v198\n"
    "v_mfma_f32_16x16x32_bf16 v[100:103], a[88:91], v[160:163], v[100:103]\n"
    "v_exp_f32 v199, v199\n"
    "v_mfma_f32_16x16x32_bf16 v[112:115], a[96:99], v[160:163], v[112:115]\n"
    "v_cvt_pk_bf16_f32 v190, v200, v201\n"
    "v_mfma_f32_16x16x32_bf16 v[112:115], a[100:103], v[168:171], v[112:115]\n"
    "v_exp_f32 v220, v220\n"
    "v_mfma_f32_16x16x32_bf16 v[116:119], a[100:103], v[192:195], v[116:119]\n"
    "v_exp_f32 v221, v221\n"
    "v_mfma_f32_16x16x32_bf16 v[116:119], a[96:99], v[184:187], v[116:119]\n"
    "v_cvt_pk_bf16_f32 v191, v202, v203\n"
    "v_mfma_f32_16x16x32_bf16 v[120:123], a[96:99], v[208:211], v[120:123]\n"
    "v_exp_f32 v222, v222\n"
    "v_mfma_f32_16x16x32_bf16 v[120:123], a[100:103], v[216:219], v[120:123]\n"
    "v_exp_f32 v223, v223\n"
    "v_mfma_f32_16x16x32_bf16 v[132:135], a[108:111], v[216:219], v[132:135]\n"
    "v_cvt_pk_bf16_f32 v214, v224, v225\n"
    "v_mfma_f32_16x16x32_bf16 v[132:135], a[104:107], v[208:211], v[132:135]\n"
    "v_exp_f32 v180, v180\n"
    "v_mfma_f32_16x16x32_bf16 v[128:131], a[104:107], v[184:187], v[128:131]\n"
    "v_exp_f32 v181, v181\n"
    "v_mfma_f32_16x16x32_bf16 v[128:131], a[108:111], v[192:195], v[128:131]\n"
    "v_cvt_pk_bf16_f32 v215, v226, v227\n"
    "v_mfma_f32_16x16x32_bf16 v[124:127], a[108:111], v[168:171], v[124:127]\n"
    "v_exp_f32 v182, v182\n"
    "v_mfma_f32_16x16x32_bf16 v[124:127], a[104:107], v[160:163], v[124:127]\n"
    "v_exp_f32 v183, v183\n"
    "v_mfma_f32_16x16x32_bf16 v[136:139], a[112:115], v[160:163], v[136:139]\n"
    "v_cvt_pk_bf16_f32 v172, v172, v173\n"
    "v_mfma_f32_16x16x32_bf16 v[136:139], a[116:119], v[168:171], v[136:139]\n"
    "v_exp_f32 v204, v204\n"
    "v_mfma_f32_16x16x32_bf16 v[140:143], a[116:119], v[192:195], v[140:143]\n"
    "v_exp_f32 v205, v205\n"
    "v_mfma_f32_16x16x32_bf16 v[140:143], a[112:115], v[184:187], v[140:143]\n"
    "v_cvt_pk_bf16_f32 v173, v174, v175\n"
    "v_mfma_f32_16x16x32_bf16 v[144:147], a[112:115], v[208:211], v[144:147]\n"
    "v_exp_f32 v206, v206\n"
    "v_mfma_f32_16x16x32_bf16 v[144:147], a[116:119], v[216:219], v[144:147]\n"
    "v_exp_f32 v207, v207\n"
    "v_mfma_f32_16x16x32_bf16 v[156:159], a[124:127], v[216:219], v[156:159]\n"
    "v_cvt_pk_bf16_f32 v196, v196, v197\n"
    "v_mfma_f32_16x16x32_bf16 v[156:159], a[120:123], v[208:211], v[156:159]\n"
    "v_exp_f32 v228, v228\n"
    "v_mfma_f32_16x16x32_bf16 v[152:155], a[120:123], v[184:187], v[152:155]\n"
    "v_exp_f32 v229, v229\n"
    "v_mfma_f32_16x16x32_bf16 v[152:155], a[124:127], v[192:195], v[152:155]\n"
    "v_cvt_pk_bf16_f32 v197, v198, v199\n"
    "v_mfma_f32_16x16x32_bf16 v[148:151], a[124:127], v[168:171], v[148:151]\n"
    "v_exp_f32 v230, v230\n"
    "v_mfma_f32_16x16x32_bf16 v[148:151], a[120:123], v[160:163], v[148:151]\n"
    "v_exp_f32 v231, v231\n"
    "v_mfma_f32_16x16x32_bf16 v[244:247], a[224:227], v[160:163], v[244:247]\n"
    "v_cvt_pk_bf16_f32 v220, v220, v221\n"
    "v_cvt_pk_bf16_f32 v221, v222, v223\n"
    "v_mfma_f32_16x16x32_bf16 v[244:247], a[224:227], v[168:171], v[244:247]\n"
    "v_cvt_pk_bf16_f32 v174, v180, v181\n"
    "v_cvt_pk_bf16_f32 v175, v182, v183\n"
    "v_mfma_f32_16x16x32_bf16 v[248:251], a[224:227], v[184:187], v[248:251]\n"
    "v_cvt_pk_bf16_f32 v198, v204, v205\n"
    "v_cvt_pk_bf16_f32 v199, v206, v207\n"
    "v_mfma_f32_16x16x32_bf16 v[248:251], a[224:227], v[192:195], v[248:251]\n"
    "v_cvt_pk_bf16_f32 v222, v228, v229\n"
    "v_cvt_pk_bf16_f32 v223, v230, v231\n"
    "v_mfma_f32_16x16x32_bf16 v[252:255], a[224:227], v[208:211], v[252:255]\n"
    "v_mfma_f32_16x16x32_bf16 v[252:255], a[224:227], v[216:219], v[252:255]\n"
    "s_add_u32 s56, s56, 720896\n"
    "s_waitcnt lgkmcnt(0)\n"
    "s_barrier\n"
    "ds_read_b64_tr_b16 a[64:65], v25 offset:0\n"
    "ds_read_b64_tr_b16 a[66:67], v25 offset:4096\n"
    "ds_read_b64_tr_b16 a[68:69], v25 offset:8192\n"
    "ds_read_b64_tr_b16 a[70:71], v25 offset:12288\n"
    "v_mfma_f32_16x16x32_bf16 v[244:247], a[224:227], v[164:167], v[244:247]\n"
    "ds_read_b64_tr_b16 a[72:73], v26 offset:0\n"
    "v_mfma_f32_16x16x32_bf16 v[244:247], a[224:227], v[172:175], v[244:247]\n"
    "ds_read_b64_tr_b16 a[74:75], v26 offset:4096\n"
    "v_mfma_f32_16x16x32_bf16 v[248:251], a[224:227], v[188:191], v[248:251]\n"
    "ds_read_b64_tr_b16 a[76:77], v26 offset:8192\n"
    "v_mfma_f32_16x16x32_bf16 v[248:251], a[224:227], v[196:199], v[248:251]\n"
    "ds_read_b64_tr_b16 a[78:79], v26 offset:12288\n"
    "v_mfma_f32_16x16x32_bf16 v[252:255], a[224:227], v[212:215], v[252:255]\n"
    "ds_read_b64_tr_b16 a[80:81], v27 offset:0\n"
    "v_mfma_f32_16x16x32_bf16 v[252:255], a[224:227], v[220:223], v[252:255]\n"
    "s_waitcnt lgkmcnt(5)\n"
    "ds_read_b64_tr_b16 a[82:83], v27 offset:4096\n"
    "v_mfma_f32_16x16x32_bf16 v[64:67], a[64:67], v[164:167], v[64:67]\n"
    "ds_read_b64_tr_b16 a[84:85], v27 offset:8192\n"
    "v_mfma_f32_16x16x32_bf16 v[64:67], a[68:71], v[172:175], v[64:67]\n"
    "ds_read_b64_tr_b16 a[86:87], v27 offset:12288\n"
    "v_mfma_f32_16x16x32_bf16 v[68:71], a[68:71], v[196:199], v[68:71]\n"
    "ds_read_b64_tr_b16 a[88:89], v28 offset:0\n"
    "v_mfma_f32_16x16x32_bf16 v[68:71], a[64:67], v[188:191], v[68:71]\n"
    "ds_read_b64_tr_b16 a[90:91], v28 offset:4096\n"
    "v_mfma_f32_16x16x32_bf16 v[72:75], a[64:67], v[212:215], v[72:75]\n"
    "ds_read_b64_tr_b16 a[92:93], v28 offset:8192\n"
    "v_mfma_f32_16x16x32_bf16 v[72:75], a[68:71], v[220:223], v[72:75]\n"
    "s_waitcnt lgkmcnt(7)\n"
    "ds_read_b64_tr_b16 a[94:95], v28 offset:12288\n"
    "v_mfma_f32_16x16x32_bf16 v[84:87], a[76:79], v[220:223], v[84:87]\n"
    "ds_read_b64_tr_b16 a[96:97], v29 offset:0\n"
    "v_mfma_f32_16x16x32_bf16 v[84:87], a[72:75], v[212:215], v[84:87]\n"
    "ds_read_b64_tr_b16 a[98:99], v29 offset:4096\n"
    "v_mfma_f32_16x16x32_bf16 v[80:83], a[72:75], v[188:191], v[80:83]\n"
    "ds_read_b64_tr_b16 a[100:101], v29 offset:8192\n"
    "v_mfma_f32_16x16x32_bf16 v[80:83], a[76:79], v[196:199], v[80:83]\n"
    "ds_read_b64_tr_b16 a[102:103], v29 offset:12288\n"
    "v_mfma_f32_16x16x32_bf16 v[76:79], a[76:79], v[172:175], v[76:79]\n"
    "ds_read_b64_tr_b16 a[104:105], v30 offset:0\n"
    "v_mfma_f32_16x16x32_bf16 v[76:79], a[72:75], v[164:167], v[76:79]\n"
    "s_waitcnt lgkmcnt(9)\n"
    "ds_read_b64_tr_b16 a[106:107], v30 offset:4096\n"
    "v_mfma_f32_16x16x32_bf16 v[88:91], a[80:83], v[164:167], v[88:91]\n"
    "ds_read_b64_tr_b16 a[108:109], v30 offset:8192\n"
    "v_mfma_f32_16x16x32_bf16 v[88:91], a[84:87], v[172:175], v[88:91]\n"
    "ds_read_b64_tr_b16 a[110:111], v30 offset:12288\n"
    "v_mfma_f32_16x16x32_bf16 v[92:95], a[84:87], v[196:199], v[92:95]\n"
    "ds_read_b64_tr_b16 a[112:113], v31 offset:0\n"
    "v_mfma_f32_16x16x32_bf16 v[92:95], a[80:83], v[188:191], v[92:95]\n"
    "ds_read_b64_tr_b16 a[114:115], v31 offset:4096\n"
    "v_mfma_f32_16x16x32_bf16 v[96:99], a[80:83], v[212:215], v[96:99]\n"
    "ds_read_b64_tr_b16 a[116:117], v31 offset:8192\n"
    "v_mfma_f32_16x16x32_bf16 v[96:99], a[84:87], v[220:223], v[96:99]\n"
    "s_waitcnt lgkmcnt(11)\n"
    "ds_read_b64_tr_b16 a[118:119], v31 offset:12288\n"
    "v_mfma_f32_16x16x32_bf16 v[108:111], a[92:95], v[220:223], v[108:111]\n"
    "ds_read_b64_tr_b16 a[120:121], v32 offset:0\n"
    "v_mfma_f32_16x16x32_bf16 v[108:111], a[88:91], v[212:215], v[108:111]\n"
    "ds_read_b64_tr_b16 a[122:123], v32 offset:4096\n"
    "v_mfma_f32_16x16x32_bf16 v[104:107], a[88:91], v[188:191], v[104:107]\n"
    "ds_read_b64_tr_b16 a[124:125], v32 offset:8192\n"
    "v_mfma_f32_16x16x32_bf16 v[104:107], a[92:95], v[196:199], v[104:107]\n"
    "ds_read_b64_tr_b16 a[126:127], v32 offset:12288\n"
    "v_mfma_f32_16x16x32_bf16 v[100:103], a[92:95], v[172:175], v[100:103]\n"
    "ds_read_b64_tr_b16 a[0:1], v5 offset:0\n"
    "v_mfma_f32_16x16x32_bf16 v[100:103], a[88:91], v[164:167], v[100:103]\n"
    "s_waitcnt lgkmcnt(13)\n"
    "ds_read_b64_tr_b16 a[2:3], v5 offset:2048\n"
    "v_mfma_f32_16x16x32_bf16 v[112:115], a[96:99], v[164:167], v[112:115]\n"
    "ds_read_b64_tr_b16 a[4:5], v5 offset:4096\n"
    "v_mfma_f32_16x16x32_bf16 v[112:115], a[100:103], v[172:175], v[112:115]\n"
    "ds_read_b64_tr_b16 a[6:7], v5 offset:6144\n"
    "v_mfma_f32_16x16x32_bf16 v[116:119], a[100:103], v[196:199], v[116:119]\n"
    "ds_read_b64_tr_b16 a[8:9], v5 offset:8192\n"
    "v_mfma_f32_16x16x32_bf16 v[116:119], a[96:99], v[188:191], v[116:119]\n"
    "ds_read_b64_tr_b16 a[10:11], v5 offset:10240\n"
    "v_mfma_f32_16x16x32_bf16 v[120:123], a[96:99], v[212:215], v[120:123]\n"
    "ds_read_b64_tr_b16 a[12:13], v5 offset:12288\n"
    "v_mfma_f32_16x16x32_bf16 v[120:123], a[100:103], v[220:223], v[120:123]\n"
    "s_waitcnt lgkmcnt(15)\n"
    "ds_read_b64_tr_b16 a[14:15], v5 offset:14336\n"
    "v_mfma_f32_16x16x32_bf16 v[132:135], a[108:111], v[220:223], v[132:135]\n"
    "ds_read_b64_tr_b16 a[16:17], v6 offset:0\n"
    "v_mfma_f32_16x16x32_bf16 v[132:135], a[104:107], v[212:215], v[132:135]\n"
    "ds_read_b64_tr_b16 a[18:19], v6 offset:2048\n"
    "v_mfma_f32_16x16x32_bf16 v[128:131], a[104:107], v[188:191], v[128:131]\n"
    "ds_read_b64_tr_b16 a[20:21], v6 offset:4096\n"
    "v_mfma_f32_16x16x32_bf16 v[128:131], a[108:111], v[196:199], v[128:131]\n"
    "ds_read_b64_tr_b16 a[22:23], v6 offset:6144\n"
    "v_mfma_f32_16x16x32_bf16 v[124:127], a[108:111], v[172:175], v[124:127]\n"
    "ds_read_b64_tr_b16 a[24:25], v6 offset:8192\n"
    "v_mfma_f32_16x16x32_bf16 v[124:127], a[104:107], v[164:167], v[124:127]\n"
    "s_waitcnt lgkmcnt(15)\n"
    "ds_read_b64_tr_b16 a[26:27], v6 offset:10240\n"
    "v_mfma_f32_16x16x32_bf16 v[136:139], a[112:115], v[164:167], v[136:139]\n"
    "ds_read_b64_tr_b16 a[28:29], v6 offset:12288\n"
    "v_mfma_f32_16x16x32_bf16 v[136:139], a[116:119], v[172:175], v[136:139]\n"
    "ds_read_b64_tr_b16 a[30:31], v6 offset:14336\n"
    "v_mfma_f32_16x16x32_bf16 v[140:143], a[116:119], v[196:199], v[140:143]\n"
    "ds_read_b64_tr_b16 a[32:33], v7 offset:0\n"
    "v_mfma_f32_16x16x32_bf16 v[140:143], a[112:115], v[188:191], v[140:143]\n"
    "ds_read_b64_tr_b16 a[34:35], v7 offset:2048\n"
    "v_mfma_f32_16x16x32_bf16 v[144:147], a[112:115], v[212:215], v[144:147]\n"
    "ds_read_b64_tr_b16 a[36:37], v7 offset:4096\n"
    "v_mfma_f32_16x16x32_bf16 v[144:147], a[116:119], v[220:223], v[144:147]\n"
    "s_waitcnt lgkmcnt(15)\n"
    "ds_read_b64_tr_b16 a[38:39], v7 offset:6144\n"
    "v_mfma_f32_16x16x32_bf16 v[156:159], a[124:127], v[220:223], v[156:159]\n"
    "ds_read_b64_tr_b16 a[40:41], v7 offset:8192\n"
    "v_mfma_f32_16x16x32_bf16 v[156:159], a[120:123], v[212:215], v[156:159]\n"
    "ds_read_b64_tr_b16 a[42:43], v7 offset:10240\n"
    "v_mfma_f32_16x16x32_bf16 v[152:155], a[120:123], v[188:191], v[152:155]\n"
    "ds_read_b64_tr_b16 a[44:45], v7 offset:12288\n"
    "v_mfma_f32_16x16x32_bf16 v[152:155], a[124:127], v[196:199], v[152:155]\n"
    "ds_read_b64_tr_b16 a[46:47], v7 offset:14336\n"
    "v_mfma_f32_16x16x32_bf16 v[148:151], a[124:127], v[172:175], v[148:151]\n"
    "ds_read_b64_tr_b16 a[48:49], v8 offset:0\n"
    "v_mfma_f32_16x16x32_bf16 v[148:151], a[120:123], v[164:167], v[148:151]\n"
    "ds_read_b64_tr_b16 a[50:51], v8 offset:2048\n"
    "ds_read_b64_tr_b16 a[52:53], v8 offset:4096\n"
    "ds_read_b64_tr_b16 a[54:55], v8 offset:6144\n"
    "ds_read_b64_tr_b16 a[56:57], v8 offset:8192\n"
    "ds_read_b64_tr_b16 a[58:59], v8 offset:10240\n"
    "ds_read_b64_tr_b16 a[60:61], v8 offset:12288\n"
    "ds_read_b64_tr_b16 a[62:63], v8 offset:14336\n"
    "s_nop 1\n"
    "s_mov_b32 s57, 0x53800000\n"
    "v_cmp_nge_f32 vcc, s57, v244\n"
    "v_cmp_nge_f32 s[50:51], s57, v248\n"
    "s_or_b64 vcc, vcc, s[50:51]\n"
    "v_cmp_nge_f32 s[50:51], s57, v252\n"
    "s_or_b64 vcc, vcc, s[50:51]\n"
    "s_cbranch_vccnz .L_fbC_%=\n"
    ".L_fbretC1_%=:\n"
    "v_rcp_f32 v44, v244\n"
    "v_rcp_f32 v46, v248\n"
    "v_rcp_f32 v48, v252\n"
    "s_add_u32 s16, s56, 0\n"
    "s_add_u32 s17, s56, 11264\n"
    "s_add_u32 s18, s56, 22528\n"
    "s_add_u32 s19, s56, 33792\n"
    "s_waitcnt vmcnt(40) lgkmcnt(0)\n"
    "s_barrier\n"
    "v_mfma_f32_16x16x32_bf16 v[160:163], a[0:3], a[128:131], 0\n"
    "ds_read_b64_tr_b16 a[64:65], v5 offset:16384\n"
    "v_readlane_b32 s38, v43, s52\n"
    "v_mfma_f32_16x16x32_bf16 v[160:163], a[4:7], a[132:135], v[160:163]\n"
    "s_add_u32 s52, s52, 1\n"
    "s_add_u32 s48, s38, 0\n"
    "s_add_u32 s49, s38, 11264\n"
    "s_add_u32 s50, s38, 22528\n"
    "s_add_u32 s51, s38, 33792\n"
    "ds_read_b64_tr_b16 a[66:67], v5 offset:18432\n"
    "buffer_load_dwordx4 a[176:179], v15, s[24:27], s48 offen offset:0 sc1\n"
    "v_mfma_f32_16x16x32_bf16 v[160:163], a[8:11], a[136:139], v[160:163]\n"
    "buffer_load_dwordx4 a[180:183], v15, s[24:27], s49 offen offset:0 sc1\n"
    "v_mul_f32 v64, v44, v64\n"
    "v_mfma_f32_16x16x32_bf16 v[160:163], a[12:15], a[140:143], v[160:163]\n"
    "ds_read_b64_tr_b16 a[68:69], v5 offset:20480\n"
    "buffer_load_dwordx4 a[184:187], v15, s[24:27], s50 offen offset:0 sc1\n"
    "v_mfma_f32_16x16x32_bf16 v[172:175], a[28:31], a[140:143], 0\n"
    "ds_read_b64_tr_b16 a[70:71], v5 offset:22528\n"
    "buffer_load_dwordx4 a[188:191], v15, s[24:27], s51 offen offset:0 sc1\n"
    "v_mfma_f32_16x16x32_bf16 v[172:175], a[24:27], a[136:139], v[172:175]\n"
    "ds_read_b64_tr_b16 a[72:73], v5 offset:24576\n"
    "buffer_load_dwordx4 a[192:195], v15, s[24:27], s48 offen offset:1024 sc1\n"
    "v_mfma_f32_16x16x32_bf16 v[172:175], a[20:23], a[132:135], v[172:175]\n"
    "ds_read_b64_tr_b16 a[74:75], v5 offset:26624\n"
    "buffer_load_dwordx4 a[196:199], v15, s[24:27], s49 offen offset:1024 sc1\n"
    "v_mfma_f32_16x16x32_bf16 v[172:175], a[16:19], a[128:131], v[172:175]\n"
    "ds_read_b64_tr_b16 a[76:77], v5 offset:28672\n"
    "buffer_load_dwordx4 a[200:203], v15, s[24:27], s50 offen offset:1024 sc1\n"
    "v_mfma_f32_16x16x32_bf16 v[184:187], a[32:35], a[128:131], 0\n"
    "ds_read_b64_tr_b16 a[78:79], v5 offset:30720\n"
    "buffer_load_dwordx4 a[204:207], v15, s[24:27], s51 offen offset:1024 sc1\n"
    "v_mfma_f32_16x16x32_bf16 v[184:187], a[36:39], a[132:135], v[184:187]\n"
    "buffer_load_dwordx4 a[208:211], v15, s[24:27], s48 offen offset:2048 sc1\n"
    "v_mul_f32 v65, v44, v65\n"
    "v_mfma_f32_16x16x32_bf16 v[184:187], a[40:43], a[136:139], v[184:187]\n"
    "ds_read_b64_tr_b16 a[80:81], v6 offset:16384\n"
    "buffer_load_dwordx4 a[212:215], v15, s[24:27], s49 offen offset:2048 sc1\n"
    "v_mfma_f32_16x16x32_bf16 v[184:187], a[44:47], a[140:143], v[184:187]\n"
    "ds_read_b64_tr_b16 a[82:83], v6 offset:18432\n"
    "buffer_load_dwordx4 a[216:219], v15, s[24:27], s50 offen offset:2048 sc1\n"
    "v_mfma_f32_16x16x32_bf16 v[196:199], a[60:63], a[140:143], 0\n"
    "ds_read_b64_tr_b16 a[84:85], v6 offset:20480\n"
    "buffer_load_dwordx4 a[220:223], v15, s[24:27], s51 offen offset:2048 sc1\n"
    "v_mfma_f32_16x16x32_bf16 v[196:199], a[56:59], a[136:139], v[196:199]\n"
    "ds_read_b64_tr_b16 a[86:87], v6 offset:22528\n"
    "v_mul_f32 v66, v44, v66\n"
    "v_mfma_f32_16x16x32_bf16 v[196:199], a[52:55], a[132:135], v[196:199]\n"
    "ds_read_b64_tr_b16 a[88:89], v6 offset:24576\n"
    "v_mul_f32 v67, v44, v67\n"
    "v_mfma_f32_16x16x32_bf16 v[196:199], a[48:51], a[128:131], v[196:199]\n"
    "ds_read_b64_tr_b16 a[90:91], v6 offset:26624\n"
    "v_mul_f32 v76, v44, v76\n"
    "v_mfma_f32_16x16x32_bf16 v[200:203], a[48:51], a[144:147], 0\n"
    "ds_read_b64_tr_b16 a[92:93], v6 offset:28672\n"
    "v_mul_f32 v77, v44, v77\n"
    "v_mfma_f32_16x16x32_bf16 v[200:203], a[52:55], a[148:151], v[200:203]\n"
    "ds_read_b64_tr_b16 a[94:95], v6 offset:30720\n"
    "v_mul_f32 v78, v44, v78\n"
    "v_mfma_f32_16x16x32_bf16 v[200:203], a[56:59], a[152:155], v[200:203]\n"
    "ds_read_b64_tr_b16 a[96:97], v7 offset:16384\n"
    "v_mul_f32 v79, v44, v79\n"
    "v_mfma_f32_16x16x32_bf16 v[200:203], a[60:63], a[156:159], v[200:203]\n"
    "ds_read_b64_tr_b16 a[98:99], v7 offset:18432\n"
    "v_cvt_pk_bf16_f32 v64, v64, v65\n"
    "v_mfma_f32_16x16x32_bf16 v[188:191], a[44:47], a[156:159], 0\n"
    "v_cvt_pk_bf16_f32 v65, v66, v67\n"
    "v_cvt_pk_bf16_f32 v66, v76, v77\n"
    "v_mfma_f32_16x16x32_bf16 v[188:191], a[40:43], a[152:155], v[188:191]\n"
    "ds_read_b64_tr_b16 a[100:101], v7 offset:20480\n"
    "v_cvt_pk_bf16_f32 v67, v78, v79\n"
    "v_mfma_f32_16x16x32_bf16 v[188:191], a[36:39], a[148:151], v[188:191]\n"
    "ds_read_b64_tr_b16 a[102:103], v7 offset:22528\n"
    "buffer_store_dwordx4 v[64:67], v37, s[24:27], s16 offen offset:0 sc1\n"
    "v_mfma_f32_16x16x32_bf16 v[188:191], a[32:35], a[144:147], v[188:191]\n"
    "ds_read_b64_tr_b16 a[104:105], v7 offset:24576\n"
    "v_mul_f32 v88, v44, v88\n"
    "v_mfma_f32_16x16x32_bf16 v[176:179], a[16:19], a[144:147], 0\n"
    "ds_read_b64_tr_b16 a[106:107], v7 offset:26624\n"
    "v_mul_f32 v89, v44, v89\n"
    "v_mfma_f32_16x16x32_bf16 v[176:179], a[20:23], a[148:151], v[176:179]\n"
    "ds_read_b64_tr_b16 a[108:109], v7 offset:28672\n"
    "v_mul_f32 v90, v44, v90\n"
    "v_mfma_f32_16x16x32_bf16 v[176:179], a[24:27], a[152:155], v[176:179]\n"
    "ds_read_b64_tr_b16 a[110:111], v7 offset:30720\n"
    "v_mul_f32 v91, v44, v91\n"
    "v_mfma_f32_16x16x32_bf16 v[176:179], a[28:31], a[156:159], v[176:179]\n"
    "v_mul_f32 v100, v44, v100\n"
    "v_mul_f32 v101, v44, v101\n"
    "v_mfma_f32_16x16x32_bf16 v[164:167], a[12:15], a[156:159], 0\n"
    "ds_read_b64_tr_b16 a[112:113], v8 offset:16384\n"
    "v_mul_f32 v102, v44, v102\n"
    "v_mfma_f32_16x16x32_bf16 v[164:167], a[8:11], a[152:155], v[164:167]\n"
    "ds_read_b64_tr_b16 a[114:115], v8 offset:18432\n"
    "v_mul_f32 v103, v44, v103\n"
    "v_mfma_f32_16x16x32_bf16 v[164:167], a[4:7], a[148:151], v[164:167]\n"
    "ds_read_b64_tr_b16 a[116:117], v8 offset:20480\n"
    "v_cvt_pk_bf16_f32 v88, v88, v89\n"
    "v_mfma_f32_16x16x32_bf16 v[164:167], a[0:3], a[144:147], v[164:167]\n"
    "ds_read_b64_tr_b16 a[118:119], v8 offset:22528\n"
    "v_cvt_pk_bf16_f32 v89, v90, v91\n"
    "v_mfma_f32_16x16x32_bf16 v[168:171], a[0:3], a[160:163], 0\n"
    "ds_read_b64_tr_b16 a[120:121], v8 offset:24576\n"
    "v_cvt_pk_bf16_f32 v90, v100, v101\n"
    "v_mfma_f32_16x16x32_bf16 v[168:171], a[4:7], a[164:167], v[168:171]\n"
    "ds_read_b64_tr_b16 a[122:123], v8 offset:26624\n"
    "v_cvt_pk_bf16_f32 v91, v102, v103\n"
    "v_mfma_f32_16x16x32_bf16 v[168:171], a[8:11], a[168:171], v[168:171]\n"
    "buffer_store_dwordx4 v[88:91], v37, s[24:27], s17 offen offset:0 sc1\n"
    "v_mul_f32 v112, v44, v112\n"
    "v_mfma_f32_16x16x32_bf16 v[168:171], a[12:15], a[172:175], v[168:171]\n"
    "ds_read_b64_tr_b16 a[124:125], v8 offset:28672\n"
    "v_mul_f32 v113, v44, v113\n"
    "v_mfma_f32_16x16x32_bf16 v[180:183], a[28:31], a[172:175], 0\n"
    "ds_read_b64_tr_b16 a[126:127], v8 offset:30720\n"
    "v_mul_f32 v114, v44, v114\n"
    "v_mfma_f32_16x16x32_bf16 v[180:183], a[24:27], a[168:171], v[180:183]\n"
    "v_mul_f32 v115, v44, v115\n"
    "v_mul_f32 v124, v44, v124\n"
    "v_mfma_f32_16x16x32_bf16 v[180:183], a[20:23], a[164:167], v[180:183]\n"
    "v_mul_f32 v125, v44, v125\n"
    "v_mul_f32 v126, v44, v126\n"
    "v_mfma_f32_16x16x32_bf16 v[180:183], a[16:19], a[160:163], v[180:183]\n"
    "v_mul_f32 v127, v44, v127\n"
    "v_cvt_pk_bf16_f32 v112, v112, v113\n"
    "v_mfma_f32_16x16x32_bf16 v[192:195], a[32:35], a[160:163], 0\n"
    "v_cvt_pk_bf16_f32 v113, v114, v115\n"
    "v_cvt_pk_bf16_f32 v114, v124, v125\n"
    "v_mfma_f32_16x16x32_bf16 v[192:195], a[36:39], a[164:167], v[192:195]\n"
    "v_cvt_pk_bf16_f32 v115, v126, v127\n"
    "buffer_store_dwordx4 v[112:115], v37, s[24:27], s18 offen offset:0 sc1\n"
    "v_mfma_f32_16x16x32_bf16 v[192:195], a[40:43], a[168:171], v[192:195]\n"
    "v_mul_f32 v136, v44, v136\n"
    "v_mul_f32 v137, v44, v137\n"
    "v_mfma_f32_16x16x32_bf16 v[192:195], a[44:47], a[172:175], v[192:195]\n"
    "v_mul_f32 v138, v44, v138\n"
    "v_mul_f32 v139, v44, v139\n"
    "v_mfma_f32_16x16x32_bf16 v[204:207], a[60:63], a[172:175], 0\n"
    "v_mul_f32 v148, v44, v148\n"
    "v_mul_f32 v149, v44, v149\n"
    "v_mfma_f32_16x16x32_bf16 v[204:207], a[56:59], a[168:171], v[204:207]\n"
    "v_mul_f32 v150, v44, v150\n"
    "v_mul_f32 v151, v44, v151\n"
    "v_mfma_f32_16x16x32_bf16 v[204:207], a[52:55], a[164:167], v[204:207]\n"
    "v_cvt_pk_bf16_f32 v136, v136, v137\n"
    "v_cvt_pk_bf16_f32 v137, v138, v139\n"
    "v_mfma_f32_16x16x32_bf16 v[204:207], a[48:51], a[160:163], v[204:207]\n"
    "v_cvt_pk_bf16_f32 v138, v148, v149\n"
    "v_cvt_pk_bf16_f32 v139, v150, v151\n"
    "s_waitcnt lgkmcnt(0)\n"
    "v_mfma_f32_16x16x32_bf16 v[208:211], a[64:67], a[128:131], 0\n"
    "ds_read_b64_tr_b16 a[0:1], v5 offset:32768\n"
    "buffer_store_dwordx4 v[136:139], v37, s[24:27], s19 offen offset:0 sc1\n"
    "v_mfma_f32_16x16x32_bf16 v[208:211], a[68:71], a[132:135], v[208:211]\n"
    "ds_read_b64_tr_b16 a[2:3], v5 offset:34816\n"
    "v_mul_f32 v68, v46, v68\n"
    "v_mfma_f32_16x16x32_bf16 v[208:211], a[72:75], a[136:139], v[208:211]\n"
    "v_mul_f32 v69, v46, v69\n"
    "v_mul_f32 v70, v46, v70\n"
    "v_mfma_f32_16x16x32_bf16 v[208:211], a[76:79], a[140:143], v[208:211]\n"
    "ds_read_b64_tr_b16 a[4:5], v5 offset:36864\n"
    "v_mul_f32 v71, v46, v71\n"
    "v_mfma_f32_16x16x32_bf16 v[220:223], a[92:95], a[140:143], 0\n"
    "ds_read_b64_tr_b16 a[6:7], v5 offset:38912\n"
    "v_mul_f32 v80, v46, v80\n"
    "v_mfma_f32_16x16x32_bf16 v[220:223], a[88:91], a[136:139], v[220:223]\n"
    "ds_read_b64_tr_b16 a[8:9], v5 offset:40960\n"
    "v_mul_f32 v81, v46, v81\n"
    "v_mfma_f32_16x16x32_bf16 v[220:223], a[84:87], a[132:135], v[220:223]\n"
    "ds_read_b64_tr_b16 a[10:11], v5 offset:43008\n"
    "v_mul_f32 v82, v46, v82\n"
    "v_mfma_f32_16x16x32_bf16 v[220:223], a[80:83], a[128:131], v[220:223]\n"
    "ds_read_b64_tr_b16 a[12:13], v5 offset:45056\n"
    "v_mul_f32 v83, v46, v83\n"
    "v_mfma_f32_16x16x32_bf16 v[232:235], a[96:99], a[128:131], 0\n"
    "ds_read_b64_tr_b16 a[14:15], v5 offset:47104\n"
    "v_cvt_pk_bf16_f32 v68, v68, v69\n"
    "v_mfma_f32_16x16x32_bf16 v[232:235], a[100:103], a[132:135], v[232:235]\n"
    "v_cvt_pk_bf16_f32 v69, v70, v71\n"
    "v_cvt_pk_bf16_f32 v70, v80, v81\n"
    "v_mfma_f32_16x16x32_bf16 v[232:235], a[104:107], a[136:139], v[232:235]\n"
    "ds_read_b64_tr_b16 a[16:17], v6 offset:32768\n"
    "v_cvt_pk_bf16_f32 v71, v82, v83\n"
    "v_mfma_f32_16x16x32_bf16 v[232:235], a[108:111], a[140:143], v[232:235]\n"
    "ds_read_b64_tr_b16 a[18:19], v6 offset:34816\n"
    "buffer_store_dwordx4 v[68:71], v37, s[24:27], s16 offen offset:1024 sc1\n"
    "v_mfma_f32_16x16x32_bf16 v[244:247], a[124:127], a[140:143], 0\n"
    "ds_read_b64_tr_b16 a[20:21], v6 offset:36864\n"
    "v_mul_f32 v92, v46, v92\n"
    "v_mfma_f32_16x16x32_bf16 v[244:247], a[120:123], a[136:139], v[244:247]\n"
    "ds_read_b64_tr_b16 a[22:23], v6 offset:38912\n"
    "v_mul_f32 v93, v46, v93\n"
    "v_mfma_f32_16x16x32_bf16 v[244:247], a[116:119], a[132:135], v[244:247]\n"
    "ds_read_b64_tr_b16 a[24:25], v6 offset:40960\n"
    "v_mul_f32 v94, v46, v94\n"
    "v_mfma_f32_16x16x32_bf16 v[244:247], a[112:115], a[128:131], v[244:247]\n"
    "ds_read_b64_tr_b16 a[26:27], v6 offset:43008\n"
    "v_mul_f32 v95, v46, v95\n"
    "v_mfma_f32_16x16x32_bf16 v[248:251], a[112:115], a[144:147], 0\n"
    "ds_read_b64_tr_b16 a[28:29], v6 offset:45056\n"
    "v_mul_f32 v104, v46, v104\n"
    "v_mfma_f32_16x16x32_bf16 v[248:251], a[116:119], a[148:151], v[248:251]\n"
    "ds_read_b64_tr_b16 a[30:31], v6 offset:47104\n"
    "v_mul_f32 v105, v46, v105\n"
    "v_mfma_f32_16x16x32_bf16 v[248:251], a[120:123], a[152:155], v[248:251]\n"
    "ds_read_b64_tr_b16 a[32:33], v7 offset:32768\n"
    "v_mul_f32 v106, v46, v106\n"
    "v_mfma_f32_16x16x32_bf16 v[248:251], a[124:127], a[156:159], v[248:251]\n"
    "ds_read_b64_tr_b16 a[34:35], v7 offset:34816\n"
    "v_mul_f32 v107, v46, v107\n"
    "v_mfma_f32_16x16x32_bf16 v[236:239], a[108:111], a[156:159], 0\n"
    "v_cvt_pk_bf16_f32 v92, v92, v93\n"
    "v_cvt_pk_bf16_f32 v93, v94, v95\n"
    "v_mfma_f32_16x16x32_bf16 v[236:239], a[104:107], a[152:155], v[236:239]\n"
    "ds_read_b64_tr_b16 a[36:37], v7 offset:36864\n"
    "v_cvt_pk_bf16_f32 v94, v104, v105\n"
    "v_mfma_f32_16x16x32_bf16 v[236:239], a[100:103], a[148:151], v[236:239]\n"
    "ds_read_b64_tr_b16 a[38:39], v7 offset:38912\n"
    "v_cvt_pk_bf16_f32 v95, v106, v107\n"
    "v_mfma_f32_16x16x32_bf16 v[236:239], a[96:99], a[144:147], v[236:239]\n"
    "ds_read_b64_tr_b16 a[40:41], v7 offset:40960\n"
    "buffer_store_dwordx4 v[92:95], v37, s[24:27], s17 offen offset:1024 sc1\n"
    "v_mfma_f32_16x16x32_bf16 v[224:227], a[80:83], a[144:147], 0\n"
    "ds_read_b64_tr_b16 a[42:43], v7 offset:43008\n"
    "v_mul_f32 v116, v46, v116\n"
    "v_mfma_f32_16x16x32_bf16 v[224:227], a[84:87], a[148:151], v[224:227]\n"
    "ds_read_b64_tr_b16 a[44:45], v7 offset:45056\n"
    "v_mul_f32 v117, v46, v117\n"
    "v_mfma_f32_16x16x32_bf16 v[224:227], a[88:91], a[152:155], v[224:227]\n"
    "ds_read_b64_tr_b16 a[46:47], v7 offset:47104\n"
    "v_mul_f32 v118, v46, v118\n"
    "v_mfma_f32_16x16x32_bf16 v[224:227], a[92:95], a[156:159], v[224:227]\n"
    "v_mul_f32 v119, v46, v119\n"
    "v_mul_f32 v128, v46, v128\n"
    "v_mfma_f32_16x16x32_bf16 v[212:215], a[76:79], a[156:159], 0\n"
    "ds_read_b64_tr_b16 a[48:49], v8 offset:32768\n"
    "v_mul_f32 v129, v46, v129\n"
    "v_mfma_f32_16x16x32_bf16 v[212:215], a[72:75], a[152:155], v[212:215]\n"
    "ds_read_b64_tr_b16 a[50:51], v8 offset:34816\n"
    "v_mul_f32 v130, v46, v130\n"
    "v_mfma_f32_16x16x32_bf16 v[212:215], a[68:71], a[148:151], v[212:215]\n"
    "ds_read_b64_tr_b16 a[52:53], v8 offset:36864\n"
    "v_mul_f32 v131, v46, v131\n"
    "v_mfma_f32_16x16x32_bf16 v[212:215], a[64:67], a[144:147], v[212:215]\n"
    "ds_read_b64_tr_b16 a[54:55], v8 offset:38912\n"
    "v_cvt_pk_bf16_f32 v116, v116, v117\n"
    "v_mfma_f32_16x16x32_bf16 v[216:219], a[64:67], a[160:163], 0\n"
    "ds_read_b64_tr_b16 a[56:57], v8 offset:40960\n"
    "v_cvt_pk_bf16_f32 v117, v118, v119\n"
    "v_mfma_f32_16x16x32_bf16 v[216:219], a[68:71], a[164:167], v[216:219]\n"
    "ds_read_b64_tr_b16 a[58:59], v8 offset:43008\n"
    "v_cvt_pk_bf16_f32 v118, v128, v129\n"
    "v_mfma_f32_16x16x32_bf16 v[216:219], a[72:75], a[168:171], v[216:219]\n"
    "v_cvt_pk_bf16_f32 v119, v130, v131\n"
    "buffer_store_dwordx4 v[116:119], v37, s[24:27], s18 offen offset:1024 sc1\n"
    "v_mfma_f32_16x16x32_bf16 v[216:219], a[76:79], a[172:175], v[216:219]\n"
    "ds_read_b64_tr_b16 a[60:61], v8 offset:45056\n"
    "v_mul_f32 v140, v46, v140\n"
    "v_mfma_f32_16x16x32_bf16 v[228:231], a[92:95], a[172:175], 0\n"
    "ds_read_b64_tr_b16 a[62:63], v8 offset:47104\n"
    "v_mul_f32 v141, v46, v141\n"
    "v_mfma_f32_16x16x32_bf16 v[228:231], a[88:91], a[168:171], v[228:231]\n"
    "v_mul_f32 v142, v46, v142\n"
    "v_mul_f32 v143, v46, v143\n"
    "v_mfma_f32_16x16x32_bf16 v[228:231], a[84:87], a[164:167], v[228:231]\n"
    "v_mul_f32 v152, v46, v152\n"
    "v_mul_f32 v153, v46, v153\n"
    "v_mfma_f32_16x16x32_bf16 v[228:231], a[80:83], a[160:163], v[228:231]\n"
    "v_mul_f32 v154, v46, v154\n"
    "v_mul_f32 v155, v46, v155\n"
    "v_mfma_f32_16x16x32_bf16 v[240:243], a[96:99], a[160:163], 0\n"
    "v_cvt_pk_bf16_f32 v140, v140, v141\n"
    "v_cvt_pk_bf16_f32 v141, v142, v143\n"
    "v_mfma_f32_16x16x32_bf16 v[240:243], a[100:103], a[164:167], v[240:243]\n"
    "v_cvt_pk_bf16_f32 v142, v152, v153\n"
    "v_cvt_pk_bf16_f32 v143, v154, v155\n"
    "v_mfma_f32_16x16x32_bf16 v[240:243], a[104:107], a[168:171], v[240:243]\n"
    "buffer_store_dwordx4 v[140:143], v37, s[24:27], s19 offen offset:1024 sc1\n"
    "v_mul_f32 v72, v48, v72\n"
    "v_mfma_f32_16x16x32_bf16 v[240:243], a[108:111], a[172:175], v[240:243]\n"
    "v_mul_f32 v73, v48, v73\n"
    "v_mul_f32 v74, v48, v74\n"
    "v_mfma_f32_16x16x32_bf16 v[252:255], a[124:127], a[172:175], 0\n"
    "v_mul_f32 v75, v48, v75\n"
    "v_mul_f32 v84, v48, v84\n"
    "v_mfma_f32_16x16x32_bf16 v[252:255], a[120:123], a[168:171], v[252:255]\n"
    "v_mul_f32 v85, v48, v85\n"
    "v_mul_f32 v86, v48, v86\n"
    "v_mfma_f32_16x16x32_bf16 v[252:255], a[116:119], a[164:167], v[252:255]\n"
    "v_mul_f32 v87, v48, v87\n"
    "v_cvt_pk_bf16_f32 v72, v72, v73\n"
    "v_mfma_f32_16x16x32_bf16 v[252:255], a[112:115], a[160:163], v[252:255]\n"
    "v_cvt_pk_bf16_f32 v73, v74, v75\n"
    "v_cvt_pk_bf16_f32 v74, v84, v85\n"
    "v_cvt_pk_bf16_f32 v75, v86, v87\n"
    "buffer_store_dwordx4 v[72:75], v37, s[24:27], s16 offen offset:2048 sc1\n"
    "v_mul_f32 v96, v48, v96\n"
    "v_mul_f32 v97, v48, v97\n"
    "v_mul_f32 v98, v48, v98\n"
    "v_mul_f32 v99, v48, v99\n"
    "v_mul_f32 v108, v48, v108\n"
    "v_mul_f32 v109, v48, v109\n"
    "v_mul_f32 v110, v48, v110\n"
    "v_mul_f32 v111, v48, v111\n"
    "v_cvt_pk_bf16_f32 v96, v96, v97\n"
    "v_cvt_pk_bf16_f32 v97, v98, v99\n"
    "v_cvt_pk_bf16_f32 v98, v108, v109\n"
    "v_cvt_pk_bf16_f32 v99, v110, v111\n"
    "buffer_store_dwordx4 v[96:99], v37, s[24:27], s17 offen offset:2048 sc1\n"
    "s_waitcnt vmcnt(40) lgkmcnt(0)\n"
    "s_barrier\n"
    "v_mfma_f32_16x16x32_bf16 v[160:163], a[0:3], v[64:67], v[160:163]\n"
    "ds_read_b64_tr_b16 a[64:65], v5 offset:49152\n"
    "v_mul_f32 v120, v48, v120\n"
    "v_mfma_f32_16x16x32_bf16 v[160:163], a[4:7], v[88:91], v[160:163]\n"
    "ds_read_b64_tr_b16 a[66:67], v5 offset:51200\n"
    "v_mul_f32 v121, v48, v121\n"
    "v_mfma_f32_16x16x32_bf16 v[160:163], a[8:11], v[112:115], v[160:163]\n"
    "v_mul_f32 v122, v48, v122\n"
    "v_mul_f32 v123, v48, v123\n"
    "v_mfma_f32_16x16x32_bf16 v[160:163], a[12:15], v[136:139], v[160:163]\n"
    "ds_read_b64_tr_b16 a[68:69], v5 offset:53248\n"
    "v_mul_f32 v132, v48, v132\n"
    "v_mfma_f32_16x16x32_bf16 v[172:175], a[28:31], v[136:139], v[172:175]\n"
    "ds_read_b64_tr_b16 a[70:71], v5 offset:55296\n"
    "v_mul_f32 v133, v48, v133\n"
    "v_mfma_f32_16x16x32_bf16 v[172:175], a[24:27], v[112:115], v[172:175]\n"
    "ds_read_b64_tr_b16 a[72:73], v5 offset:57344\n"
    "v_mul_f32 v134, v48, v134\n"
    "v_mfma_f32_16x16x32_bf16 v[172:175], a[20:23], v[88:91], v[172:175]\n"
    "ds_read_b64_tr_b16 a[74:75], v5 offset:59392\n"
    "v_mul_f32 v135, v48, v135\n"
    "v_mfma_f32_16x16x32_bf16 v[172:175], a[16:19], v[64:67], v[172:175]\n"
    "ds_read_b64_tr_b16 a[76:77], v5 offset:61440\n"
    "v_cvt_pk_bf16_f32 v120, v120, v121\n"
    "v_mfma_f32_16x16x32_bf16 v[184:187], a[32:35], v[64:67], v[184:187]\n"
    "ds_read_b64_tr_b16 a[78:79], v5 offset:63488\n"
    "v_xor_b32 v5, 0x10000, v5\n"
    "v_mfma_f32_16x16x32_bf16 v[184:187], a[36:39], v[88:91], v[184:187]\n"
    "v_cvt_pk_bf16_f32 v121, v122, v123\n"
    "v_cvt_pk_bf16_f32 v122, v132, v133\n"
    "v_mfma_f32_16x16x32_bf16 v[184:187], a[40:43], v[112:115], v[184:187]\n"
    "ds_read_b64_tr_b16 a[80:81], v6 offset:49152\n"
    "v_cvt_pk_bf16_f32 v123, v134, v135\n"
    "v_mfma_f32_16x16x32_bf16 v[184:187], a[44:47], v[136:139], v[184:187]\n"
    "ds_read_b64_tr_b16 a[82:83], v6 offset:51200\n"
    "buffer_store_dwordx4 v[120:123], v37, s[24:27], s18 offen offset:2048 sc1\n"
    "v_mfma_f32_16x16x32_bf16 v[196:199], a[60:63], v[136:139], v[196:199]\n"
    "ds_read_b64_tr_b16 a[84:85], v6 offset:53248\n"
    "v_mul_f32 v144, v48, v144\n"
    "v_mfma_f32_16x16x32_bf16 v[196:199], a[56:59], v[112:115], v[196:199]\n"
    "ds_read_b64_tr_b16 a[86:87], v6 offset:55296\n"
    "v_mul_f32 v145, v48, v145\n"
    "v_mfma_f32_16x16x32_bf16 v[196:199], a[52:55], v[88:91], v[196:199]\n"
    "ds_read_b64_tr_b16 a[88:89], v6 offset:57344\n"
    "v_mul_f32 v146, v48, v146\n"
    "v_mfma_f32_16x16x32_bf16 v[196:199], a[48:51], v[64:67], v[196:199]\n"
    "ds_read_b64_tr_b16 a[90:91], v6 offset:59392\n"
    "v_mul_f32 v147, v48, v147\n"
    "v_mfma_f32_16x16x32_bf16 v[200:203], a[48:51], v[68:71], v[200:203]\n"
    "ds_read_b64_tr_b16 a[92:93], v6 offset:61440\n"
    "v_mul_f32 v156, v48, v156\n"
    "v_mfma_f32_16x16x32_bf16 v[200:203], a[52:55], v[92:95], v[200:203]\n"
    "ds_read_b64_tr_b16 a[94:95], v6 offset:63488\n"
    "v_xor_b32 v6, 0x10000, v6\n"
    "v_mfma_f32_16x16x32_bf16 v[200:203], a[56:59], v[116:119], v[200:203]\n"
    "ds_read_b64_tr_b16 a[96:97], v7 offset:49152\n"
    "v_mul_f32 v157, v48, v157\n"
    "v_mfma_f32_16x16x32_bf16 v[200:203], a[60:63], v[140:143], v[200:203]\n"
    "ds_read_b64_tr_b16 a[98:99], v7 offset:51200\n"
    "v_mul_f32 v158, v48, v158\n"
    "v_mfma_f32_16x16x32_bf16 v[188:191], a[44:47], v[140:143], v[188:191]\n"
    "v_mul_f32 v159, v48, v159\n"
    "v_cvt_pk_bf16_f32 v144, v144, v145\n"
    "v_mfma_f32_16x16x32_bf16 v[188:191], a[40:43], v[116:119], v[188:191]\n"
    "ds_read_b64_tr_b16 a[100:101], v7 offset:53248\n"
    "v_cvt_pk_bf16_f32 v145, v146, v147\n"
    "v_mfma_f32_16x16x32_bf16 v[188:191], a[36:39], v[92:95], v[188:191]\n"
    "ds_read_b64_tr_b16 a[102:103], v7 offset:55296\n"
    "v_cvt_pk_bf16_f32 v146, v156, v157\n"
    "v_mfma_f32_16x16x32_bf16 v[188:191], a[32:35], v[68:71], v[188:191]\n"
    "ds_read_b64_tr_b16 a[104:105], v7 offset:57344\n"
    "v_cvt_pk_bf16_f32 v147, v158, v159\n"
    "v_mfma_f32_16x16x32_bf16 v[176:179], a[16:19], v[68:71], v[176:179]\n"
    "ds_read_b64_tr_b16 a[106:107], v7 offset:59392\n"
    "buffer_store_dwordx4 v[144:147], v37, s[24:27], s19 offen offset:2048 sc1\n"
    "v_mfma_f32_16x16x32_bf16 v[176:179], a[20:23], v[92:95], v[176:179]\n"
    "ds_read_b64_tr_b16 a[108:109], v7 offset:61440\n"
    "v_mfma_f32_16x16x32_bf16 v[176:179], a[24:27], v[116:119], v[176:179]\n"
    "ds_read_b64_tr_b16 a[110:111], v7 offset:63488\n"
    "v_xor_b32 v7, 0x10000, v7\n"
    "v_mfma_f32_16x16x32_bf16 v[176:179], a[28:31], v[140:143], v[176:179]\n"
    "v_mfma_f32_16x16x32_bf16 v[164:167], a[12:15], v[140:143], v[164:167]\n"
    "ds_read_b64_tr_b16 a[112:113], v8 offset:49152\n"
    "v_mfma_f32_16x16x32_bf16 v[164:167], a[8:11], v[116:119], v[164:167]\n"
    "ds_read_b64_tr_b16 a[114:115], v8 offset:51200\n"
    "v_mfma_f32_16x16x32_bf16 v[164:167], a[4:7], v[92:95], v[164:167]\n"
    "ds_read_b64_tr_b16 a[116:117], v8 offset:53248\n"
    "v_mfma_f32_16x16x32_bf16 v[164:167], a[0:3], v[68:71], v[164:167]\n"
    "ds_read_b64_tr_b16 a[118:119], v8 offset:55296\n"
    "v_mfma_f32_16x16x32_bf16 v[168:171], a[0:3], v[72:75], v[168:171]\n"
    "ds_read_b64_tr_b16 a[120:121], v8 offset:57344\n"
    "v_mfma_f32_16x16x32_bf16 v[168:171], a[4:7], v[96:99], v[168:171]\n"
    "ds_read_b64_tr_b16 a[122:123], v8 offset:59392\n"
    "v_mfma_f32_16x16x32_bf16 v[168:171], a[8:11], v[120:123], v[168:171]\n"
    "v_mfma_f32_16x16x32_bf16 v[168:171], a[12:15], v[144:147], v[168:171]\n"
    "ds_read_b64_tr_b16 a[124:125], v8 offset:61440\n"
    "v_mfma_f32_16x16x32_bf16 v[180:183], a[28:31], v[144:147], v[180:183]\n"
    "ds_read_b64_tr_b16 a[126:127], v8 offset:63488\n"
    "v_xor_b32 v8, 0x10000, v8\n"
    "v_mfma_f32_16x16x32_bf16 v[180:183], a[24:27], v[120:123], v[180:183]\n"
    "v_mfma_f32_16x16x32_bf16 v[180:183], a[20:23], v[96:99], v[180:183]\n"
    "v_mfma_f32_16x16x32_bf16 v[180:183], a[16:19], v[72:75], v[180:183]\n"
    "v_mfma_f32_16x16x32_bf16 v[192:195], a[32:35], v[72:75], v[192:195]\n"
    "v_mfma_f32_16x16x32_bf16 v[192:195], a[36:39], v[96:99], v[192:195]\n"
    "v_mfma_f32_16x16x32_bf16 v[192:195], a[40:43], v[120:123], v[192:195]\n"
    "v_mfma_f32_16x16x32_bf16 v[192:195], a[44:47], v[144:147], v[192:195]\n"
    "v_mfma_f32_16x16x32_bf16 v[204:207], a[60:63], v[144:147], v[204:207]\n"
    "v_mfma_f32_16x16x32_bf16 v[204:207], a[56:59], v[120:123], v[204:207]\n"
    "v_mfma_f32_16x16x32_bf16 v[204:207], a[52:55], v[96:99], v[204:207]\n"
    "v_mfma_f32_16x16x32_bf16 v[204:207], a[48:51], v[72:75], v[204:207]\n"
    "s_waitcnt lgkmcnt(0)\n"
    "v_mfma_f32_16x16x32_bf16 v[208:211], a[64:67], v[64:67], v[208:211]\n"
    "ds_read_b64_tr_b16 a[0:1], v5 offset:0\n"
    "v_mfma_f32_16x16x32_bf16 v[208:211], a[68:71], v[88:91], v[208:211]\n"
    "ds_read_b64_tr_b16 a[2:3], v5 offset:2048\n"
    "v_mfma_f32_16x16x32_bf16 v[208:211], a[72:75], v[112:115], v[208:211]\n"
    "v_mfma_f32_16x16x32_bf16 v[208:211], a[76:79], v[136:139], v[208:211]\n"
    "ds_read_b64_tr_b16 a[4:5], v5 offset:4096\n"
    "v_mfma_f32_16x16x32_bf16 v[220:223], a[92:95], v[136:139], v[220:223]\n"
    "ds_read_b64_tr_b16 a[6:7], v5 offset:6144\n"
    "v_mfma_f32_16x16x32_bf16 v[220:223], a[88:91], v[112:115], v[220:223]\n"
    "ds_read_b64_tr_b16 a[8:9], v5 offset:8192\n"
    "v_mfma_f32_16x16x32_bf16 v[220:223], a[84:87], v[88:91], v[220:223]\n"
    "ds_read_b64_tr_b16 a[10:11], v5 offset:10240\n"
    "v_mfma_f32_16x16x32_bf16 v[220:223], a[80:83], v[64:67], v[220:223]\n"
    "ds_read_b64_tr_b16 a[12:13], v5 offset:12288\n"
    "v_mfma_f32_16x16x32_bf16 v[232:235], a[96:99], v[64:67], v[232:235]\n"
    "ds_read_b64_tr_b16 a[14:15], v5 offset:14336\n"
    "v_mfma_f32_16x16x32_bf16 v[232:235], a[100:103], v[88:91], v[232:235]\n"
    "v_mfma_f32_16x16x32_bf16 v[232:235], a[104:107], v[112:115], v[232:235]\n"
    "ds_read_b64_tr_b16 a[16:17], v6 offset:0\n"
    "v_mfma_f32_16x16x32_bf16 v[232:235], a[108:111], v[136:139], v[232:235]\n"
    "ds_read_b64_tr_b16 a[18:19], v6 offset:2048\n"
    "v_mfma_f32_16x16x32_bf16 v[244:247], a[124:127], v[136:139], v[244:247]\n"
    "ds_read_b64_tr_b16 a[20:21], v6 offset:4096\n"
    "v_mfma_f32_16x16x32_bf16 v[244:247], a[120:123], v[112:115], v[244:247]\n"
    "ds_read_b64_tr_b16 a[22:23], v6 offset:6144\n"
    "v_mfma_f32_16x16x32_bf16 v[244:247], a[116:119], v[88:91], v[244:247]\n"
    "ds_read_b64_tr_b16 a[24:25], v6 offset:8192\n"
    "v_mfma_f32_16x16x32_bf16 v[244:247], a[112:115], v[64:67], v[244:247]\n"
    "ds_read_b64_tr_b16 a[26:27], v6 offset:10240\n"
    "v_mfma_f32_16x16x32_bf16 v[248:251], a[112:115], v[68:71], v[248:251]\n"
    "ds_read_b64_tr_b16 a[28:29], v6 offset:12288\n"
    "v_mfma_f32_16x16x32_bf16 v[248:251], a[116:119], v[92:95], v[248:251]\n"
    "ds_read_b64_tr_b16 a[30:31], v6 offset:14336\n"
    "v_mfma_f32_16x16x32_bf16 v[248:251], a[120:123], v[116:119], v[248:251]\n"
    "ds_read_b64_tr_b16 a[32:33], v7 offset:0\n"
    "v_mfma_f32_16x16x32_bf16 v[248:251], a[124:127], v[140:143], v[248:251]\n"
    "ds_read_b64_tr_b16 a[34:35], v7 offset:2048\n"
    "v_readlane_b32 s38, v43, s52\n"
    "v_mfma_f32_16x16x32_bf16 v[236:239], a[108:111], v[140:143], v[236:239]\n"
    "s_add_u32 s52, s52, 1\n"
    "s_add_u32 s48, s38, 0\n"
    "s_add_u32 s49, s38, 11264\n"
    "s_add_u32 s50, s38, 22528\n"
    "s_add_u32 s51, s38, 33792\n"
    "buffer_load_dwordx4 a[128:131], v15, s[24:27], s48 offen offset:0 sc1\n"
    "v_mfma_f32_16x16x32_bf16 v[236:239], a[104:107], v[116:119], v[236:239]\n"
    "ds_read_b64_tr_b16 a[36:37], v7 offset:4096\n"
    "v_mfma_f32_16x16x32_bf16 v[236:239], a[100:103], v[92:95], v[236:239]\n"
    "ds_read_b64_tr_b16 a[38:39], v7 offset:6144\n"
    "v_mfma_f32_16x16x32_bf16 v[236:239], a[96:99], v[68:71], v[236:239]\n"
    "ds_read_b64_tr_b16 a[40:41], v7 offset:8192\n"
    "v_mfma_f32_16x16x32_bf16 v[224:227], a[80:83], v[68:71], v[224:227]\n"
    "ds_read_b64_tr_b16 a[42:43], v7 offset:10240\n"
    "v_mfma_f32_16x16x32_bf16 v[224:227], a[84:87], v[92:95], v[224:227]\n"
    "ds_read_b64_tr_b16 a[44:45], v7 offset:12288\n"
    "v_mfma_f32_16x16x32_bf16 v[224:227], a[88:91], v[116:119], v[224:227]\n"
    "ds_read_b64_tr_b16 a[46:47], v7 offset:14336\n"
    "v_mfma_f32_16x16x32_bf16 v[224:227], a[92:95], v[140:143], v[224:227]\n"
    "buffer_load_dwordx4 a[132:135], v15, s[24:27], s49 offen offset:0 sc1\n"
    "v_mfma_f32_16x16x32_bf16 v[212:215], a[76:79], v[140:143], v[212:215]\n"
    "ds_read_b64_tr_b16 a[48:49], v8 offset:0\n"
    "v_mfma_f32_16x16x32_bf16 v[212:215], a[72:75], v[116:119], v[212:215]\n"
    "ds_read_b64_tr_b16 a[50:51], v8 offset:2048\n"
    "v_mfma_f32_16x16x32_bf16 v[212:215], a[68:71], v[92:95], v[212:215]\n"
    "ds_read_b64_tr_b16 a[52:53], v8 offset:4096\n"
    "v_mfma_f32_16x16x32_bf16 v[212:215], a[64:67], v[68:71], v[212:215]\n"
    "ds_read_b64_tr_b16 a[54:55], v8 offset:6144\n"
    "v_mfma_f32_16x16x32_bf16 v[216:219], a[64:67], v[72:75], v[216:219]\n"
    "ds_read_b64_tr_b16 a[56:57], v8 offset:8192\n"
    "v_mfma_f32_16x16x32_bf16 v[216:219], a[68:71], v[96:99], v[216:219]\n"
    "ds_read_b64_tr_b16 a[58:59], v8 offset:10240\n"
    "v_mfma_f32_16x16x32_bf16 v[216:219], a[72:75], v[120:123], v[216:219]\n"
    "buffer_load_dwordx4 a[136:139], v15, s[24:27], s50 offen offset:0 sc1\n"
    "v_mfma_f32_16x16x32_bf16 v[216:219], a[76:79], v[144:147], v[216:219]\n"
    "ds_read_b64_tr_b16 a[60:61], v8 offset:12288\n"
    "v_mfma_f32_16x16x32_bf16 v[228:231], a[92:95], v[144:147], v[228:231]\n"
    "ds_read_b64_tr_b16 a[62:63], v8 offset:14336\n"
    "v_mfma_f32_16x16x32_bf16 v[228:231], a[88:91], v[120:123], v[228:231]\n"
    "v_mfma_f32_16x16x32_bf16 v[228:231], a[84:87], v[96:99], v[228:231]\n"
    "v_mfma_f32_16x16x32_bf16 v[228:231], a[80:83], v[72:75], v[228:231]\n"
    "v_mfma_f32_16x16x32_bf16 v[240:243], a[96:99], v[72:75], v[240:243]\n"
    "v_mfma_f32_16x16x32_bf16 v[240:243], a[100:103], v[96:99], v[240:243]\n"
    "buffer_load_dwordx4 a[140:143], v15, s[24:27], s51 offen offset:0 sc1\n"
    "v_mfma_f32_16x16x32_bf16 v[240:243], a[104:107], v[120:123], v[240:243]\n"
    "v_mfma_f32_16x16x32_bf16 v[240:243], a[108:111], v[144:147], v[240:243]\n"
    "v_mfma_f32_16x16x32_bf16 v[252:255], a[124:127], v[144:147], v[252:255]\n"
    "v_mfma_f32_16x16x32_bf16 v[252:255], a[120:123], v[120:123], v[252:255]\n"
    "v_mfma_f32_16x16x32_bf16 v[252:255], a[116:119], v[96:99], v[252:255]\n"
    "v_mfma_f32_16x16x32_bf16 v[252:255], a[112:115], v[72:75], v[252:255]\n"
    "s_add_u32 s55, %5, 32\n"
    "s_mul_i32 s55, s55, 45056\n"
    "s_mul_i32 s46, s36, 6144\n"
    "s_add_u32 s55, s55, s46\n"
    "s_xor_b32 s56, %5, 1\n"
    "s_lshl_b32 s56, s56, 2\n"
    "s_add_u32 s56, s56, s36\n"
    "s_lshl_b32 s56, s56, 3\n"
    "s_add_u32 s56, s56, 512\n"
    "s_mov_b32 s37, 4\n"
    ".L_oC_%=:\n"
    "s_waitcnt vmcnt(13) lgkmcnt(0)\n"
    "s_barrier\n"
    "v_mfma_f32_16x16x32_bf16 v[160:163], a[0:3], a[176:179], v[160:163]\n"
    "ds_read_b64_tr_b16 a[64:65], v5 offset:16384\n"
    "v_mfma_f32_16x16x32_bf16 v[160:163], a[4:7], a[180:183], v[160:163]\n"
    "ds_read_b64_tr_b16 a[66:67], v5 offset:18432\n"
    "v_mfma_f32_16x16x32_bf16 v[160:163], a[8:11], a[184:187], v[160:163]\n"
    "buffer_load_dwordx4 a[144:147], v15, s[24:27], s48 offen offset:1024 sc1\n"
    "v_mfma_f32_16x16x32_bf16 v[160:163], a[12:15], a[188:191], v[160:163]\n"
    "ds_read_b64_tr_b16 a[68:69], v5 offset:20480\n"
    "v_mfma_f32_16x16x32_bf16 v[172:175], a[28:31], a[188:191], v[172:175]\n"
    "ds_read_b64_tr_b16 a[70:71], v5 offset:22528\n"
    "v_mfma_f32_16x16x32_bf16 v[172:175], a[24:27], a[184:187], v[172:175]\n"
    "ds_read_b64_tr_b16 a[72:73], v5 offset:24576\n"
    "v_mfma_f32_16x16x32_bf16 v[172:175], a[20:23], a[180:183], v[172:175]\n"
    "ds_read_b64_tr_b16 a[74:75], v5 offset:26624\n"
    "v_mfma_f32_16x16x32_bf16 v[172:175], a[16:19], a[176:179], v[172:175]\n"
    "ds_read_b64_tr_b16 a[76:77], v5 offset:28672\n"
    "v_mfma_f32_16x16x32_bf16 v[184:187], a[32:35], a[176:179], v[184:187]\n"
    "ds_read_b64_tr_b16 a[78:79], v5 offset:30720\n"
    "v_mfma_f32_16x16x32_bf16 v[184:187], a[36:39], a[180:183], v[184:187]\n"
    "buffer_load_dwordx4 a[148:151], v15, s[24:27], s49 offen offset:1024 sc1\n"
    "v_mfma_f32_16x16x32_bf16 v[184:187], a[40:43], a[184:187], v[184:187]\n"
    "ds_read_b64_tr_b16 a[80:81], v6 offset:16384\n"
    "v_mfma_f32_16x16x32_bf16 v[184:187], a[44:47], a[188:191], v[184:187]\n"
    "ds_read_b64_tr_b16 a[82:83], v6 offset:18432\n"
    "v_mfma_f32_16x16x32_bf16 v[196:199], a[60:63], a[188:191], v[196:199]\n"
    "ds_read_b64_tr_b16 a[84:85], v6 offset:20480\n"
    "v_mfma_f32_16x16x32_bf16 v[196:199], a[56:59], a[184:187], v[196:199]\n"
    "ds_read_b64_tr_b16 a[86:87], v6 offset:22528\n"
    "v_mfma_f32_16x16x32_bf16 v[196:199], a[52:55], a[180:183], v[196:199]\n"
    "ds_read_b64_tr_b16 a[88:89], v6 offset:24576\n"
    "v_mfma_f32_16x16x32_bf16 v[196:199], a[48:51], a[176:179], v[196:199]\n"
    "s_waitcnt vmcnt(10)\n"
    "ds_read_b64_tr_b16 a[90:91], v6 offset:26624\n"
    "v_mfma_f32_16x16x32_bf16 v[200:203], a[48:51], a[192:195], v[200:203]\n"
    "ds_read_b64_tr_b16 a[92:93], v6 offset:28672\n"
    "v_mfma_f32_16x16x32_bf16 v[200:203], a[52:55], a[196:199], v[200:203]\n"
    "ds_read_b64_tr_b16 a[94:95], v6 offset:30720\n"
    "v_mfma_f32_16x16x32_bf16 v[200:203], a[56:59], a[200:203], v[200:203]\n"
    "ds_read_b64_tr_b16 a[96:97], v7 offset:16384\n"
    "v_mfma_f32_16x16x32_bf16 v[200:203], a[60:63], a[204:207], v[200:203]\n"
    "ds_read_b64_tr_b16 a[98:99], v7 offset:18432\n"
    "v_mfma_f32_16x16x32_bf16 v[188:191], a[44:47], a[204:207], v[188:191]\n"
    "buffer_load_dwordx4 a[152:155], v15, s[24:27], s50 offen offset:1024 sc1\n"
    "v_mfma_f32_16x16x32_bf16 v[188:191], a[40:43], a[200:203], v[188:191]\n"
    "ds_read_b64_tr_b16 a[100:101], v7 offset:20480\n"
    "v_mfma_f32_16x16x32_bf16 v[188:191], a[36:39], a[196:199], v[188:191]\n"
    "ds_read_b64_tr_b16 a[102:103], v7 offset:22528\n"
    "v_mfma_f32_16x16x32_bf16 v[188:191], a[32:35], a[192:195], v[188:191]\n"
    "ds_read_b64_tr_b16 a[104:105], v7 offset:24576\n"
    "v_mfma_f32_16x16x32_bf16 v[176:179], a[16:19], a[192:195], v[176:179]\n"
    "ds_read_b64_tr_b16 a[106:107], v7 offset:26624\n"
    "v_mfma_f32_16x16x32_bf16 v[176:179], a[20:23], a[196:199], v[176:179]\n"
    "ds_read_b64_tr_b16 a[108:109], v7 offset:28672\n"
    "v_mfma_f32_16x16x32_bf16 v[176:179], a[24:27], a[200:203], v[176:179]\n"
    "ds_read_b64_tr_b16 a[110:111], v7 offset:30720\n"
    "v_mfma_f32_16x16x32_bf16 v[176:179], a[28:31], a[204:207], v[176:179]\n"
    "buffer_load_dwordx4 a[156:159], v15, s[24:27], s51 offen offset:1024 sc1\n"
    "v_mfma_f32_16x16x32_bf16 v[164:167], a[12:15], a[204:207], v[164:167]\n"
    "ds_read_b64_tr_b16 a[112:113], v8 offset:16384\n"
    "v_mfma_f32_16x16x32_bf16 v[164:167], a[8:11], a[200:203], v[164:167]\n"
    "ds_read_b64_tr_b16 a[114:115], v8 offset:18432\n"
    "v_mfma_f32_16x16x32_bf16 v[164:167], a[4:7], a[196:199], v[164:167]\n"
    "ds_read_b64_tr_b16 a[116:117], v8 offset:20480\n"
    "v_mfma_f32_16x16x32_bf16 v[164:167], a[0:3], a[192:195], v[164:167]\n"
    "s_waitcnt vmcnt(8)\n"
    "ds_read_b64_tr_b16 a[118:119], v8 offset:22528\n"
    "v_mfma_f32_16x16x32_bf16 v[168:171], a[0:3], a[208:211], v[168:171]\n"
    "ds_read_b64_tr_b16 a[120:121], v8 offset:24576\n"
    "v_mfma_f32_16x16x32_bf16 v[168:171], a[4:7], a[212:215], v[168:171]\n"
    "ds_read_b64_tr_b16 a[122:123], v8 offset:26624\n"
    "v_mfma_f32_16x16x32_bf16 v[168:171], a[8:11], a[216:219], v[168:171]\n"
    "buffer_load_dwordx4 a[160:163], v15, s[24:27], s48 offen offset:2048 sc1\n"
    "v_mfma_f32_16x16x32_bf16 v[168:171], a[12:15], a[220:223], v[168:171]\n"
    "ds_read_b64_tr_b16 a[124:125], v8 offset:28672\n"
    "v_mfma_f32_16x16x32_bf16 v[180:183], a[28:31], a[220:223], v[180:183]\n"
    "ds_read_b64_tr_b16 a[126:127], v8 offset:30720\n"
    "v_mfma_f32_16x16x32_bf16 v[180:183], a[24:27], a[216:219], v[180:183]\n"
    "v_mfma_f32_16x16x32_bf16 v[180:183], a[20:23], a[212:215], v[180:183]\n"
    "v_mfma_f32_16x16x32_bf16 v[180:183], a[16:19], a[208:211], v[180:183]\n"
    "v_mfma_f32_16x16x32_bf16 v[192:195], a[32:35], a[208:211], v[192:195]\n"
    "v_mfma_f32_16x16x32_bf16 v[192:195], a[36:39], a[212:215], v[192:195]\n"
    "buffer_load_dwordx4 a[164:167], v15, s[24:27], s49 offen offset:2048 sc1\n"
    "v_mfma_f32_16x16x32_bf16 v[192:195], a[40:43], a[216:219], v[192:195]\n"
    "v_mfma_f32_16x16x32_bf16 v[192:195], a[44:47], a[220:223], v[192:195]\n"
    "v_mfma_f32_16x16x32_bf16 v[204:207], a[60:63], a[220:223], v[204:207]\n"
    "v_mfma_f32_16x16x32_bf16 v[204:207], a[56:59], a[216:219], v[204:207]\n"
    "v_mfma_f32_16x16x32_bf16 v[204:207], a[52:55], a[212:215], v[204:207]\n"
    "v_mfma_f32_16x16x32_bf16 v[204:207], a[48:51], a[208:211], v[204:207]\n"
    "s_waitcnt lgkmcnt(0)\n"
    "v_mfma_f32_16x16x32_bf16 v[208:211], a[64:67], a[176:179], v[208:211]\n"
    "ds_read_b64_tr_b16 a[0:1], v5 offset:32768\n"
    "v_mfma_f32_16x16x32_bf16 v[208:211], a[68:71], a[180:183], v[208:211]\n"
    "ds_read_b64_tr_b16 a[2:3], v5 offset:34816\n"
    "v_mfma_f32_16x16x32_bf16 v[208:211], a[72:75], a[184:187], v[208:211]\n"
    "buffer_load_dwordx4 a[168:171], v15, s[24:27], s50 offen offset:2048 sc1\n"
    "v_mfma_f32_16x16x32_bf16 v[208:211], a[76:79], a[188:191], v[208:211]\n"
    "ds_read_b64_tr_b16 a[4:5], v5 offset:36864\n"
    "v_mfma_f32_16x16x32_bf16 v[220:223], a[92:95], a[188:191], v[220:223]\n"
    "ds_read_b64_tr_b16 a[6:7], v5 offset:38912\n"
    "v_mfma_f32_16x16x32_bf16 v[220:223], a[88:91], a[184:187], v[220:223]\n"
    "ds_read_b64_tr_b16 a[8:9], v5 offset:40960\n"
    "v_mfma_f32_16x16x32_bf16 v[220:223], a[84:87], a[180:183], v[220:223]\n"
    "ds_read_b64_tr_b16 a[10:11], v5 offset:43008\n"
    "v_mfma_f32_16x16x32_bf16 v[220:223], a[80:83], a[176:179], v[220:223]\n"
    "ds_read_b64_tr_b16 a[12:13], v5 offset:45056\n"
    "v_mfma_f32_16x16x32_bf16 v[232:235], a[96:99], a[176:179], v[232:235]\n"
    "ds_read_b64_tr_b16 a[14:15], v5 offset:47104\n"
    "v_mfma_f32_16x16x32_bf16 v[232:235], a[100:103], a[180:183], v[232:235]\n"
    "buffer_load_dwordx4 a[172:175], v15, s[24:27], s51 offen offset:2048 sc1\n"
    "v_mfma_f32_16x16x32_bf16 v[232:235], a[104:107], a[184:187], v[232:235]\n"
    "ds_read_b64_tr_b16 a[16:17], v6 offset:32768\n"
    "v_mfma_f32_16x16x32_bf16 v[232:235], a[108:111], a[188:191], v[232:235]\n"
    "ds_read_b64_tr_b16 a[18:19], v6 offset:34816\n"
    "v_mfma_f32_16x16x32_bf16 v[244:247], a[124:127], a[188:191], v[244:247]\n"
    "ds_read_b64_tr_b16 a[20:21], v6 offset:36864\n"
    "v_mfma_f32_16x16x32_bf16 v[244:247], a[120:123], a[184:187], v[244:247]\n"
    "ds_read_b64_tr_b16 a[22:23], v6 offset:38912\n"
    "v_readlane_b32 s38, v43, s52\n"
    "v_mfma_f32_16x16x32_bf16 v[244:247], a[116:119], a[180:183], v[244:247]\n"
    "ds_read_b64_tr_b16 a[24:25], v6 offset:40960\n"
    "v_mfma_f32_16x16x32_bf16 v[244:247], a[112:115], a[176:179], v[244:247]\n"
    "s_add_u32 s52, s52, 1\n"
    "ds_read_b64_tr_b16 a[26:27], v6 offset:43008\n"
    "v_mfma_f32_16x16x32_bf16 v[248:251], a[112:115], a[192:195], v[248:251]\n"
    "s_add_u32 s48, s38, 0\n"
    "ds_read_b64_tr_b16 a[28:29], v6 offset:45056\n"
    "v_mfma_f32_16x16x32_bf16 v[248:251], a[116:119], a[196:199], v[248:251]\n"
    "s_add_u32 s49, s38, 11264\n"
    "ds_read_b64_tr_b16 a[30:31], v6 offset:47104\n"
    "v_mfma_f32_16x16x32_bf16 v[248:251], a[120:123], a[200:203], v[248:251]\n"
    "s_add_u32 s50, s38, 22528\n"
    "ds_read_b64_tr_b16 a[32:33], v7 offset:32768\n"
    "v_mfma_f32_16x16x32_bf16 v[248:251], a[124:127], a[204:207], v[248:251]\n"
    "s_add_u32 s51, s38, 33792\n"
    "ds_read_b64_tr_b16 a[34:35], v7 offset:34816\n"
    "v_mfma_f32_16x16x32_bf16 v[236:239], a[108:111], a[204:207], v[236:239]\n"
    "buffer_load_dwordx4 a[176:179], v15, s[24:27], s48 offen offset:0 sc1\n"
    "v_mfma_f32_16x16x32_bf16 v[236:239], a[104:107], a[200:203], v[236:239]\n"
    "ds_read_b64_tr_b16 a[36:37], v7 offset:36864\n"
    "v_mfma_f32_16x16x32_bf16 v[236:239], a[100:103], a[196:199], v[236:239]\n"
    "ds_read_b64_tr_b16 a[38:39], v7 offset:38912\n"
    "v_mfma_f32_16x16x32_bf16 v[236:239], a[96:99], a[192:195], v[236:239]\n"
    "ds_read_b64_tr_b16 a[40:41], v7 offset:40960\n"
    "v_mfma_f32_16x16x32_bf16 v[224:227], a[80:83], a[192:195], v[224:227]\n"
    "ds_read_b64_tr_b16 a[42:43], v7 offset:43008\n"
    "v_mfma_f32_16x16x32_bf16 v[224:227], a[84:87], a[196:199], v[224:227]\n"
    "ds_read_b64_tr_b16 a[44:45], v7 offset:45056\n"
    "v_mfma_f32_16x16x32_bf16 v[224:227], a[88:91], a[200:203], v[224:227]\n"
    "ds_read_b64_tr_b16 a[46:47], v7 offset:47104\n"
    "v_mfma_f32_16x16x32_bf16 v[224:227], a[92:95], a[204:207], v[224:227]\n"
    "buffer_load_dwordx4 a[180:183], v15, s[24:27], s49 offen offset:0 sc1\n"
    "v_mfma_f32_16x16x32_bf16 v[212:215], a[76:79], a[204:207], v[212:215]\n"
    "ds_read_b64_tr_b16 a[48:49], v8 offset:32768\n"
    "v_mfma_f32_16x16x32_bf16 v[212:215], a[72:75], a[200:203], v[212:215]\n"
    "ds_read_b64_tr_b16 a[50:51], v8 offset:34816\n"
    "v_mfma_f32_16x16x32_bf16 v[212:215], a[68:71], a[196:199], v[212:215]\n"
    "ds_read_b64_tr_b16 a[52:53], v8 offset:36864\n"
    "v_mfma_f32_16x16x32_bf16 v[212:215], a[64:67], a[192:195], v[212:215]\n"
    "ds_read_b64_tr_b16 a[54:55], v8 offset:38912\n"
    "v_mfma_f32_16x16x32_bf16 v[216:219], a[64:67], a[208:211], v[216:219]\n"
    "ds_read_b64_tr_b16 a[56:57], v8 offset:40960\n"
    "v_mfma_f32_16x16x32_bf16 v[216:219], a[68:71], a[212:215], v[216:219]\n"
    "ds_read_b64_tr_b16 a[58:59], v8 offset:43008\n"
    "v_mfma_f32_16x16x32_bf16 v[216:219], a[72:75], a[216:219], v[216:219]\n"
    "buffer_load_dwordx4 a[184:187], v15, s[24:27], s50 offen offset:0 sc1\n"
    "v_mfma_f32_16x16x32_bf16 v[216:219], a[76:79], a[220:223], v[216:219]\n"
    "ds_read_b64_tr_b16 a[60:61], v8 offset:45056\n"
    "v_mfma_f32_16x16x32_bf16 v[228:231], a[92:95], a[220:223], v[228:231]\n"
    "ds_read_b64_tr_b16 a[62:63], v8 offset:47104\n"
    "v_mfma_f32_16x16x32_bf16 v[228:231], a[88:91], a[216:219], v[228:231]\n"
    "v_mfma_f32_16x16x32_bf16 v[228:231], a[84:87], a[212:215], v[228:231]\n"
    "v_mfma_f32_16x16x32_bf16 v[228:231], a[80:83], a[208:211], v[228:231]\n"
    "v_mfma_f32_16x16x32_bf16 v[240:243], a[96:99], a[208:211], v[240:243]\n"
    "v_mfma_f32_16x16x32_bf16 v[240:243], a[100:103], a[212:215], v[240:243]\n"
    "buffer_load_dwordx4 a[188:191], v15, s[24:27], s51 offen offset:0 sc1\n"
    "v_mfma_f32_16x16x32_bf16 v[240:243], a[104:107], a[216:219], v[240:243]\n"
    "v_mfma_f32_16x16x32_bf16 v[240:243], a[108:111], a[220:223], v[240:243]\n"
    "v_mfma_f32_16x16x32_bf16 v[252:255], a[124:127], a[220:223], v[252:255]\n"
    "global_load_dword v1, v[2:3], off\n"
    "v_mfma_f32_16x16x32_bf16 v[252:255], a[120:123], a[216:219], v[252:255]\n"
    "v_mfma_f32_16x16x32_bf16 v[252:255], a[116:119], a[212:215], v[252:255]\n"
    "v_mfma_f32_16x16x32_bf16 v[252:255], a[112:115], a[208:211], v[252:255]\n"
    "s_waitcnt vmcnt(13) lgkmcnt(0)\n"
    "s_barrier\n"
    "v_mfma_f32_16x16x32_bf16 v[160:163], a[0:3], a[128:131], v[160:163]\n"
    "ds_read_b64_tr_b16 a[64:65], v5 offset:49152\n"
    "v_mfma_f32_16x16x32_bf16 v[160:163], a[4:7], a[132:135], v[160:163]\n"
    "ds_read_b64_tr_b16 a[66:67], v5 offset:51200\n"
    "v_mfma_f32_16x16x32_bf16 v[160:163], a[8:11], a[136:139], v[160:163]\n"
    "buffer_load_dwordx4 a[192:195], v15, s[24:27], s48 offen offset:1024 sc1\n"
    "v_mfma_f32_16x16x32_bf16 v[160:163], a[12:15], a[140:143], v[160:163]\n"
    "ds_read_b64_tr_b16 a[68:69], v5 offset:53248\n"
    "v_mfma_f32_16x16x32_bf16 v[172:175], a[28:31], a[140:143], v[172:175]\n"
    "ds_read_b64_tr_b16 a[70:71], v5 offset:55296\n"
    "v_mfma_f32_16x16x32_bf16 v[172:175], a[24:27], a[136:139], v[172:175]\n"
    "ds_read_b64_tr_b16 a[72:73], v5 offset:57344\n"
    "v_mfma_f32_16x16x32_bf16 v[172:175], a[20:23], a[132:135], v[172:175]\n"
    "ds_read_b64_tr_b16 a[74:75], v5 offset:59392\n"
    "v_mfma_f32_16x16x32_bf16 v[172:175], a[16:19], a[128:131], v[172:175]\n"
    "ds_read_b64_tr_b16 a[76:77], v5 offset:61440\n"
    "v_mfma_f32_16x16x32_bf16 v[184:187], a[32:35], a[128:131], v[184:187]\n"
    "ds_read_b64_tr_b16 a[78:79], v5 offset:63488\n"
    "v_xor_b32 v5, 0x10000, v5\n"
    "v_mfma_f32_16x16x32_bf16 v[184:187], a[36:39], a[132:135], v[184:187]\n"
    "buffer_load_dwordx4 a[196:199], v15, s[24:27], s49 offen offset:1024 sc1\n"
    "v_mfma_f32_16x16x32_bf16 v[184:187], a[40:43], a[136:139], v[184:187]\n"
    "ds_read_b64_tr_b16 a[80:81], v6 offset:49152\n"
    "v_mfma_f32_16x16x32_bf16 v[184:187], a[44:47], a[140:143], v[184:187]\n"
    "ds_read_b64_tr_b16 a[82:83], v6 offset:51200\n"
    "v_mfma_f32_16x16x32_bf16 v[196:199], a[60:63], a[140:143], v[196:199]\n"
    "ds_read_b64_tr_b16 a[84:85], v6 offset:53248\n"
    "v_mfma_f32_16x16x32_bf16 v[196:199], a[56:59], a[136:139], v[196:199]\n"
    "ds_read_b64_tr_b16 a[86:87], v6 offset:55296\n"
    "v_mfma_f32_16x16x32_bf16 v[196:199], a[52:55], a[132:135], v[196:199]\n"
    "ds_read_b64_tr_b16 a[88:89], v6 offset:57344\n"
    "v_mfma_f32_16x16x32_bf16 v[196:199], a[48:51], a[128:131], v[196:199]\n"
    "s_waitcnt vmcnt(11)\n"
    "ds_read_b64_tr_b16 a[90:91], v6 offset:59392\n"
    "v_mfma_f32_16x16x32_bf16 v[200:203], a[48:51], a[144:147], v[200:203]\n"
    "ds_read_b64_tr_b16 a[92:93], v6 offset:61440\n"
    "v_mfma_f32_16x16x32_bf16 v[200:203], a[52:55], a[148:151], v[200:203]\n"
    "ds_read_b64_tr_b16 a[94:95], v6 offset:63488\n"
    "v_xor_b32 v6, 0x10000, v6\n"
    "v_mfma_f32_16x16x32_bf16 v[200:203], a[56:59], a[152:155], v[200:203]\n"
    "ds_read_b64_tr_b16 a[96:97], v7 offset:49152\n"
    "v_mfma_f32_16x16x32_bf16 v[200:203], a[60:63], a[156:159], v[200:203]\n"
    "ds_read_b64_tr_b16 a[98:99], v7 offset:51200\n"
    "v_mfma_f32_16x16x32_bf16 v[188:191], a[44:47], a[156:159], v[188:191]\n"
    "buffer_load_dwordx4 a[200:203], v15, s[24:27], s50 offen offset:1024 sc1\n"
    "v_mfma_f32_16x16x32_bf16 v[188:191], a[40:43], a[152:155], v[188:191]\n"
    "ds_read_b64_tr_b16 a[100:101], v7 offset:53248\n"
    "v_mfma_f32_16x16x32_bf16 v[188:191], a[36:39], a[148:151], v[188:191]\n"
    "ds_read_b64_tr_b16 a[102:103], v7 offset:55296\n"
    "v_mfma_f32_16x16x32_bf16 v[188:191], a[32:35], a[144:147], v[188:191]\n"
    "ds_read_b64_tr_b16 a[104:105], v7 offset:57344\n"
    "v_mfma_f32_16x16x32_bf16 v[176:179], a[16:19], a[144:147], v[176:179]\n"
    "ds_read_b64_tr_b16 a[106:107], v7 offset:59392\n"
    "v_mfma_f32_16x16x32_bf16 v[176:179], a[20:23], a[148:151], v[176:179]\n"
    "ds_read_b64_tr_b16 a[108:109], v7 offset:61440\n"
    "v_mfma_f32_16x16x32_bf16 v[176:179], a[24:27], a[152:155], v[176:179]\n"
    "ds_read_b64_tr_b16 a[110:111], v7 offset:63488\n"
    "v_xor_b32 v7, 0x10000, v7\n"
    "v_mfma_f32_16x16x32_bf16 v[176:179], a[28:31], a[156:159], v[176:179]\n"
    "buffer_load_dwordx4 a[204:207], v15, s[24:27], s51 offen offset:1024 sc1\n"
    "v_mfma_f32_16x16x32_bf16 v[164:167], a[12:15], a[156:159], v[164:167]\n"
    "ds_read_b64_tr_b16 a[112:113], v8 offset:49152\n"
    "v_mfma_f32_16x16x32_bf16 v[164:167], a[8:11], a[152:155], v[164:167]\n"
    "ds_read_b64_tr_b16 a[114:115], v8 offset:51200\n"
    "v_mfma_f32_16x16x32_bf16 v[164:167], a[4:7], a[148:151], v[164:167]\n"
    "ds_read_b64_tr_b16 a[116:117], v8 offset:53248\n"
    "v_mfma_f32_16x16x32_bf16 v[164:167], a[0:3], a[144:147], v[164:167]\n"
    "s_waitcnt vmcnt(9)\n"
    "ds_read_b64_tr_b16 a[118:119], v8 offset:55296\n"
    "v_mfma_f32_16x16x32_bf16 v[168:171], a[0:3], a[160:163], v[168:171]\n"
    "ds_read_b64_tr_b16 a[120:121], v8 offset:57344\n"
    "v_mfma_f32_16x16x32_bf16 v[168:171], a[4:7], a[164:167], v[168:171]\n"
    "ds_read_b64_tr_b16 a[122:123], v8 offset:59392\n"
    "v_mfma_f32_16x16x32_bf16 v[168:171], a[8:11], a[168:171], v[168:171]\n"
    "buffer_load_dwordx4 a[208:211], v15, s[24:27], s48 offen offset:2048 sc1\n"
    "v_mfma_f32_16x16x32_bf16 v[168:171], a[12:15], a[172:175], v[168:171]\n"
    "ds_read_b64_tr_b16 a[124:125], v8 offset:61440\n"
    "v_mfma_f32_16x16x32_bf16 v[180:183], a[28:31], a[172:175], v[180:183]\n"
    "ds_read_b64_tr_b16 a[126:127], v8 offset:63488\n"
    "v_xor_b32 v8, 0x10000, v8\n"
    "v_mfma_f32_16x16x32_bf16 v[180:183], a[24:27], a[168:171], v[180:183]\n"
    "v_mfma_f32_16x16x32_bf16 v[180:183], a[20:23], a[164:167], v[180:183]\n"
    "v_mfma_f32_16x16x32_bf16 v[180:183], a[16:19], a[160:163], v[180:183]\n"
    "v_mfma_f32_16x16x32_bf16 v[192:195], a[32:35], a[160:163], v[192:195]\n"
    "v_mfma_f32_16x16x32_bf16 v[192:195], a[36:39], a[164:167], v[192:195]\n"
    "buffer_load_dwordx4 a[212:215], v15, s[24:27], s49 offen offset:2048 sc1\n"
    "v_mfma_f32_16x16x32_bf16 v[192:195], a[40:43], a[168:171], v[192:195]\n"
    "v_mfma_f32_16x16x32_bf16 v[192:195], a[44:47], a[172:175], v[192:195]\n"
    "v_mfma_f32_16x16x32_bf16 v[204:207], a[60:63], a[172:175], v[204:207]\n"
    "v_mfma_f32_16x16x32_bf16 v[204:207], a[56:59], a[168:171], v[204:207]\n"
    "v_mfma_f32_16x16x32_bf16 v[204:207], a[52:55], a[164:167], v[204:207]\n"
    "v_mfma_f32_16x16x32_bf16 v[204:207], a[48:51], a[160:163], v[204:207]\n"
    "s_waitcnt lgkmcnt(0)\n"
    "v_mfma_f32_16x16x32_bf16 v[208:211], a[64:67], a[128:131], v[208:211]\n"
    "ds_read_b64_tr_b16 a[0:1], v5 offset:0\n"
    "v_mfma_f32_16x16x32_bf16 v[208:211], a[68:71], a[132:135], v[208:211]\n"
    "ds_read_b64_tr_b16 a[2:3], v5 offset:2048\n"
    "v_mfma_f32_16x16x32_bf16 v[208:211], a[72:75], a[136:139], v[208:211]\n"
    "buffer_load_dwordx4 a[216:219], v15, s[24:27], s50 offen offset:2048 sc1\n"
    "v_mfma_f32_16x16x32_bf16 v[208:211], a[76:79], a[140:143], v[208:211]\n"
    "ds_read_b64_tr_b16 a[4:5], v5 offset:4096\n"
    "v_mfma_f32_16x16x32_bf16 v[220:223], a[92:95], a[140:143], v[220:223]\n"
    "ds_read_b64_tr_b16 a[6:7], v5 offset:6144\n"
    "v_mfma_f32_16x16x32_bf16 v[220:223], a[88:91], a[136:139], v[220:223]\n"
    "ds_read_b64_tr_b16 a[8:9], v5 offset:8192\n"
    "v_mfma_f32_16x16x32_bf16 v[220:223], a[84:87], a[132:135], v[220:223]\n"
    "ds_read_b64_tr_b16 a[10:11], v5 offset:10240\n"
    "v_mfma_f32_16x16x32_bf16 v[220:223], a[80:83], a[128:131], v[220:223]\n"
    "ds_read_b64_tr_b16 a[12:13], v5 offset:12288\n"
    "v_mfma_f32_16x16x32_bf16 v[232:235], a[96:99], a[128:131], v[232:235]\n"
    "ds_read_b64_tr_b16 a[14:15], v5 offset:14336\n"
    "v_mfma_f32_16x16x32_bf16 v[232:235], a[100:103], a[132:135], v[232:235]\n"
    "buffer_load_dwordx4 a[220:223], v15, s[24:27], s51 offen offset:2048 sc1\n"
    "v_mfma_f32_16x16x32_bf16 v[232:235], a[104:107], a[136:139], v[232:235]\n"
    "ds_read_b64_tr_b16 a[16:17], v6 offset:0\n"
    "v_mfma_f32_16x16x32_bf16 v[232:235], a[108:111], a[140:143], v[232:235]\n"
    "ds_read_b64_tr_b16 a[18:19], v6 offset:2048\n"
    "v_mfma_f32_16x16x32_bf16 v[244:247], a[124:127], a[140:143], v[244:247]\n"
    "ds_read_b64_tr_b16 a[20:21], v6 offset:4096\n"
    "v_mfma_f32_16x16x32_bf16 v[244:247], a[120:123], a[136:139], v[244:247]\n"
    "ds_read_b64_tr_b16 a[22:23], v6 offset:6144\n"
    "v_readlane_b32 s38, v43, s52\n"
    "v_mfma_f32_16x16x32_bf16 v[244:247], a[116:119], a[132:135], v[244:247]\n"
    "ds_read_b64_tr_b16 a[24:25], v6 offset:8192\n"
    "v_mfma_f32_16x16x32_bf16 v[244:247], a[112:115], a[128:131], v[244:247]\n"
    "s_add_u32 s52, s52, 1\n"
    "ds_read_b64_tr_b16 a[26:27], v6 offset:10240\n"
    "v_mfma_f32_16x16x32_bf16 v[248:251], a[112:115], a[144:147], v[248:251]\n"
    "s_add_u32 s48, s38, 0\n"
    "ds_read_b64_tr_b16 a[28:29], v6 offset:12288\n"
    "v_mfma_f32_16x16x32_bf16 v[248:251], a[116:119], a[148:151], v[248:251]\n"
    "s_add_u32 s49, s38, 11264\n"
    "ds_read_b64_tr_b16 a[30:31], v6 offset:14336\n"
    "v_mfma_f32_16x16x32_bf16 v[248:251], a[120:123], a[152:155], v[248:251]\n"
    "s_add_u32 s50, s38, 22528\n"
    "ds_read_b64_tr_b16 a[32:33], v7 offset:0\n"
    "v_mfma_f32_16x16x32_bf16 v[248:251], a[124:127], a[156:159], v[248:251]\n"
    "s_add_u32 s51, s38, 33792\n"
    "ds_read_b64_tr_b16 a[34:35], v7 offset:2048\n"
    "v_mfma_f32_16x16x32_bf16 v[236:239], a[108:111], a[156:159], v[236:239]\n"
    "buffer_load_dwordx4 a[128:131], v15, s[24:27], s48 offen offset:0 sc1\n"
    "v_mfma_f32_16x16x32_bf16 v[236:239], a[104:107], a[152:155], v[236:239]\n"
    "ds_read_b64_tr_b16 a[36:37], v7 offset:4096\n"
    "v_mfma_f32_16x16x32_bf16 v[236:239], a[100:103], a[148:151], v[236:239]\n"
    "ds_read_b64_tr_b16 a[38:39], v7 offset:6144\n"
    "v_mfma_f32_16x16x32_bf16 v[236:239], a[96:99], a[144:147], v[236:239]\n"
    "ds_read_b64_tr_b16 a[40:41], v7 offset:8192\n"
    "v_mfma_f32_16x16x32_bf16 v[224:227], a[80:83], a[144:147], v[224:227]\n"
    "ds_read_b64_tr_b16 a[42:43], v7 offset:10240\n"
    "v_mfma_f32_16x16x32_bf16 v[224:227], a[84:87], a[148:151], v[224:227]\n"
    "ds_read_b64_tr_b16 a[44:45], v7 offset:12288\n"
    "v_mfma_f32_16x16x32_bf16 v[224:227], a[88:91], a[152:155], v[224:227]\n"
    "ds_read_b64_tr_b16 a[46:47], v7 offset:14336\n"
    "v_mfma_f32_16x16x32_bf16 v[224:227], a[92:95], a[156:159], v[224:227]\n"
    "buffer_load_dwordx4 a[132:135], v15, s[24:27], s49 offen offset:0 sc1\n"
    "v_mfma_f32_16x16x32_bf16 v[212:215], a[76:79], a[156:159], v[212:215]\n"
    "ds_read_b64_tr_b16 a[48:49], v8 offset:0\n"
    "v_mfma_f32_16x16x32_bf16 v[212:215], a[72:75], a[152:155], v[212:215]\n"
    "ds_read_b64_tr_b16 a[50:51], v8 offset:2048\n"
    "v_mfma_f32_16x16x32_bf16 v[212:215], a[68:71], a[148:151], v[212:215]\n"
    "ds_read_b64_tr_b16 a[52:53], v8 offset:4096\n"
    "v_mfma_f32_16x16x32_bf16 v[212:215], a[64:67], a[144:147], v[212:215]\n"
    "ds_read_b64_tr_b16 a[54:55], v8 offset:6144\n"
    "v_mfma_f32_16x16x32_bf16 v[216:219], a[64:67], a[160:163], v[216:219]\n"
    "ds_read_b64_tr_b16 a[56:57], v8 offset:8192\n"
    "v_mfma_f32_16x16x32_bf16 v[216:219], a[68:71], a[164:167], v[216:219]\n"
    "ds_read_b64_tr_b16 a[58:59], v8 offset:10240\n"
    "v_mfma_f32_16x16x32_bf16 v[216:219], a[72:75], a[168:171], v[216:219]\n"
    "buffer_load_dwordx4 a[136:139], v15, s[24:27], s50 offen offset:0 sc1\n"
    "v_mfma_f32_16x16x32_bf16 v[216:219], a[76:79], a[172:175], v[216:219]\n"
    "ds_read_b64_tr_b16 a[60:61], v8 offset:12288\n"
    "v_mfma_f32_16x16x32_bf16 v[228:231], a[92:95], a[172:175], v[228:231]\n"
    "ds_read_b64_tr_b16 a[62:63], v8 offset:14336\n"
    "v_mfma_f32_16x16x32_bf16 v[228:231], a[88:91], a[168:171], v[228:231]\n"
    "v_mfma_f32_16x16x32_bf16 v[228:231], a[84:87], a[164:167], v[228:231]\n"
    "v_mfma_f32_16x16x32_bf16 v[228:231], a[80:83], a[160:163], v[228:231]\n"
    "v_mfma_f32_16x16x32_bf16 v[240:243], a[96:99], a[160:163], v[240:243]\n"
    "v_mfma_f32_16x16x32_bf16 v[240:243], a[100:103], a[164:167], v[240:243]\n"
    "buffer_load_dwordx4 a[140:143], v15, s[24:27], s51 offen offset:0 sc1\n"
    "v_mfma_f32_16x16x32_bf16 v[240:243], a[104:107], a[168:171], v[240:243]\n"
    "v_mfma_f32_16x16x32_bf16 v[240:243], a[108:111], a[172:175], v[240:243]\n"
    "v_mfma_f32_16x16x32_bf16 v[252:255], a[124:127], a[172:175], v[252:255]\n"
    "v_mfma_f32_16x16x32_bf16 v[252:255], a[120:123], a[168:171], v[252:255]\n"
    "v_mfma_f32_16x16x32_bf16 v[252:255], a[116:119], a[164:167], v[252:255]\n"
    "v_mfma_f32_16x16x32_bf16 v[252:255], a[112:115], a[160:163], v[252:255]\n"
    "s_sub_u32 s37, s37, 1\n"
    "s_cmp_lg_u32 s37, 0\n"
    "s_cbranch_scc1 .L_oC_%=\n"
    "s_waitcnt vmcnt(13) lgkmcnt(0)\n"
    "s_barrier\n"
    "v_mfma_f32_16x16x32_bf16 v[160:163], a[0:3], a[176:179], v[160:163]\n"
    "ds_read_b64_tr_b16 a[64:65], v5 offset:16384\n"
    "v_mfma_f32_16x16x32_bf16 v[160:163], a[4:7], a[180:183], v[160:163]\n"
    "ds_read_b64_tr_b16 a[66:67], v5 offset:18432\n"
    "buffer_load_dwordx4 a[144:147], v15, s[24:27], s48 offen offset:1024 sc1\n"
    "v_mfma_f32_16x16x32_bf16 v[160:163], a[8:11], a[184:187], v[160:163]\n"
    "v_mfma_f32_16x16x32_bf16 v[160:163], a[12:15], a[188:191], v[160:163]\n"
    "ds_read_b64_tr_b16 a[68:69], v5 offset:20480\n"
    "v_mfma_f32_16x16x32_bf16 v[172:175], a[28:31], a[188:191], v[172:175]\n"
    "ds_read_b64_tr_b16 a[70:71], v5 offset:22528\n"
    "v_mfma_f32_16x16x32_bf16 v[172:175], a[24:27], a[184:187], v[172:175]\n"
    "ds_read_b64_tr_b16 a[72:73], v5 offset:24576\n"
    "buffer_load_dwordx4 a[148:151], v15, s[24:27], s49 offen offset:1024 sc1\n"
    "v_mfma_f32_16x16x32_bf16 v[172:175], a[20:23], a[180:183], v[172:175]\n"
    "ds_read_b64_tr_b16 a[74:75], v5 offset:26624\n"
    "v_mfma_f32_16x16x32_bf16 v[172:175], a[16:19], a[176:179], v[172:175]\n"
    "ds_read_b64_tr_b16 a[76:77], v5 offset:28672\n"
    "v_mfma_f32_16x16x32_bf16 v[184:187], a[32:35], a[176:179], v[184:187]\n"
    "ds_read_b64_tr_b16 a[78:79], v5 offset:30720\n"
    "v_mfma_f32_16x16x32_bf16 v[184:187], a[36:39], a[180:183], v[184:187]\n"
    "v_mfma_f32_16x16x32_bf16 v[184:187], a[40:43], a[184:187], v[184:187]\n"
    "ds_read_b64_tr_b16 a[80:81], v6 offset:16384\n"
    "buffer_load_dwordx4 a[152:155], v15, s[24:27], s50 offen offset:1024 sc1\n"
    "v_mfma_f32_16x16x32_bf16 v[184:187], a[44:47], a[188:191], v[184:187]\n"
    "ds_read_b64_tr_b16 a[82:83], v6 offset:18432\n"
    "v_mfma_f32_16x16x32_bf16 v[196:199], a[60:63], a[188:191], v[196:199]\n"
    "ds_read_b64_tr_b16 a[84:85], v6 offset:20480\n"
    "v_mfma_f32_16x16x32_bf16 v[196:199], a[56:59], a[184:187], v[196:199]\n"
    "ds_read_b64_tr_b16 a[86:87], v6 offset:22528\n"
    "v_mfma_f32_16x16x32_bf16 v[196:199], a[52:55], a[180:183], v[196:199]\n"
    "ds_read_b64_tr_b16 a[88:89], v6 offset:24576\n"
    "buffer_load_dwordx4 a[156:159], v15, s[24:27], s51 offen offset:1024 sc1\n"
    "v_mfma_f32_16x16x32_bf16 v[196:199], a[48:51], a[176:179], v[196:199]\n"
    "s_waitcnt vmcnt(12)\n"
    "ds_read_b64_tr_b16 a[90:91], v6 offset:26624\n"
    "v_mfma_f32_16x16x32_bf16 v[200:203], a[48:51], a[192:195], v[200:203]\n"
    "ds_read_b64_tr_b16 a[92:93], v6 offset:28672\n"
    "v_mfma_f32_16x16x32_bf16 v[200:203], a[52:55], a[196:199], v[200:203]\n"
    "ds_read_b64_tr_b16 a[94:95], v6 offset:30720\n"
    "v_mfma_f32_16x16x32_bf16 v[200:203], a[56:59], a[200:203], v[200:203]\n"
    "ds_read_b64_tr_b16 a[96:97], v7 offset:16384\n"
    "v_mfma_f32_16x16x32_bf16 v[200:203], a[60:63], a[204:207], v[200:203]\n"
    "ds_read_b64_tr_b16 a[98:99], v7 offset:18432\n"
    "buffer_load_dwordx4 a[160:163], v15, s[24:27], s48 offen offset:2048 sc1\n"
    "v_mfma_f32_16x16x32_bf16 v[188:191], a[44:47], a[204:207], v[188:191]\n"
    "v_mfma_f32_16x16x32_bf16 v[188:191], a[40:43], a[200:203], v[188:191]\n"
    "ds_read_b64_tr_b16 a[100:101], v7 offset:20480\n"
    "v_mfma_f32_16x16x32_bf16 v[188:191], a[36:39], a[196:199], v[188:191]\n"
    "ds_read_b64_tr_b16 a[102:103], v7 offset:22528\n"
    "v_mfma_f32_16x16x32_bf16 v[188:191], a[32:35], a[192:195], v[188:191]\n"
    "ds_read_b64_tr_b16 a[104:105], v7 offset:24576\n"
    "buffer_load_dwordx4 a[164:167], v15, s[24:27], s49 offen offset:2048 sc1\n"
    "v_mfma_f32_16x16x32_bf16 v[176:179], a[16:19], a[192:195], v[176:179]\n"
    "ds_read_b64_tr_b16 a[106:107], v7 offset:26624\n"
    "v_mfma_f32_16x16x32_bf16 v[176:179], a[20:23], a[196:199], v[176:179]\n"
    "ds_read_b64_tr_b16 a[108:109], v7 offset:28672\n"
    "v_mfma_f32_16x16x32_bf16 v[176:179], a[24:27], a[200:203], v[176:179]\n"
    "ds_read_b64_tr_b16 a[110:111], v7 offset:30720\n"
    "v_mfma_f32_16x16x32_bf16 v[176:179], a[28:31], a[204:207], v[176:179]\n"
    "v_mfma_f32_16x16x32_bf16 v[164:167], a[12:15], a[204:207], v[164:167]\n"
    "ds_read_b64_tr_b16 a[112:113], v8 offset:16384\n"
    "buffer_load_dwordx4 a[168:171], v15, s[24:27], s50 offen offset:2048 sc1\n"
    "v_mfma_f32_16x16x32_bf16 v[164:167], a[8:11], a[200:203], v[164:167]\n"
    "ds_read_b64_tr_b16 a[114:115], v8 offset:18432\n"
    "v_mfma_f32_16x16x32_bf16 v[164:167], a[4:7], a[196:199], v[164:167]\n"
    "ds_read_b64_tr_b16 a[116:117], v8 offset:20480\n"
    "v_mfma_f32_16x16x32_bf16 v[164:167], a[0:3], a[192:195], v[164:167]\n"
    "s_waitcnt vmcnt(11)\n"
    "ds_read_b64_tr_b16 a[118:119], v8 offset:22528\n"
    "v_mfma_f32_16x16x32_bf16 v[168:171], a[0:3], a[208:211], v[168:171]\n"
    "ds_read_b64_tr_b16 a[120:121], v8 offset:24576\n"
    "buffer_load_dwordx4 a[172:175], v15, s[24:27], s51 offen offset:2048 sc1\n"
    "v_mfma_f32_16x16x32_bf16 v[168:171], a[4:7], a[212:215], v[168:171]\n"
    "ds_read_b64_tr_b16 a[122:123], v8 offset:26624\n"
    "v_mfma_f32_16x16x32_bf16 v[168:171], a[8:11], a[216:219], v[168:171]\n"
    "v_mfma_f32_16x16x32_bf16 v[168:171], a[12:15], a[220:223], v[168:171]\n"
    "ds_read_b64_tr_b16 a[124:125], v8 offset:28672\n"
    "v_mfma_f32_16x16x32_bf16 v[180:183], a[28:31], a[220:223], v[180:183]\n"
    "ds_read_b64_tr_b16 a[126:127], v8 offset:30720\n"
    "v_readlane_b32 s38, v43, s52\n"
    "v_mfma_f32_16x16x32_bf16 v[180:183], a[24:27], a[216:219], v[180:183]\n"
    "s_add_u32 s52, s52, 1\n"
    "s_add_u32 s48, s38, 0\n"
    "s_add_u32 s49, s38, 11264\n"
    "s_add_u32 s50, s38, 22528\n"
    "s_add_u32 s51, s38, 33792\n"
    "buffer_load_dwordx4 v[64:67], v15, s[24:27], s48 offen offset:0 sc1\n"
    "v_mfma_f32_16x16x32_bf16 v[180:183], a[20:23], a[212:215], v[180:183]\n"
    "v_mfma_f32_16x16x32_bf16 v[180:183], a[16:19], a[208:211], v[180:183]\n"
    "v_mfma_f32_16x16x32_bf16 v[192:195], a[32:35], a[208:211], v[192:195]\n"
    "v_mfma_f32_16x16x32_bf16 v[192:195], a[36:39], a[212:215], v[192:195]\n"
    "buffer_load_dwordx4 v[68:71], v15, s[24:27], s49 offen offset:0 sc1\n"
    "v_mfma_f32_16x16x32_bf16 v[192:195], a[40:43], a[216:219], v[192:195]\n"
    "v_mfma_f32_16x16x32_bf16 v[192:195], a[44:47], a[220:223], v[192:195]\n"
    "v_mfma_f32_16x16x32_bf16 v[204:207], a[60:63], a[220:223], v[204:207]\n"
    "v_mfma_f32_16x16x32_bf16 v[204:207], a[56:59], a[216:219], v[204:207]\n"
    "v_mfma_f32_16x16x32_bf16 v[204:207], a[52:55], a[212:215], v[204:207]\n"
    "v_mfma_f32_16x16x32_bf16 v[204:207], a[48:51], a[208:211], v[204:207]\n"
    "s_waitcnt lgkmcnt(0)\n"
    "v_mfma_f32_16x16x32_bf16 v[208:211], a[64:67], a[176:179], v[208:211]\n"
    "ds_read_b64_tr_b16 a[0:1], v5 offset:32768\n"
    "v_mfma_f32_16x16x32_bf16 v[208:211], a[68:71], a[180:183], v[208:211]\n"
    "ds_read_b64_tr_b16 a[2:3], v5 offset:34816\n"
    "buffer_load_dwordx4 v[72:75], v15, s[24:27], s50 offen offset:0 sc1\n"
    "v_mfma_f32_16x16x32_bf16 v[208:211], a[72:75], a[184:187], v[208:211]\n"
    "v_mfma_f32_16x16x32_bf16 v[208:211], a[76:79], a[188:191], v[208:211]\n"
    "ds_read_b64_tr_b16 a[4:5], v5 offset:36864\n"
    "v_mfma_f32_16x16x32_bf16 v[220:223], a[92:95], a[188:191], v[220:223]\n"
    "ds_read_b64_tr_b16 a[6:7], v5 offset:38912\n"
    "v_mfma_f32_16x16x32_bf16 v[220:223], a[88:91], a[184:187], v[220:223]\n"
    "ds_read_b64_tr_b16 a[8:9], v5 offset:40960\n"
    "buffer_load_dwordx4 v[76:79], v15, s[24:27], s51 offen offset:0 sc1\n"
    "v_mfma_f32_16x16x32_bf16 v[220:223], a[84:87], a[180:183], v[220:223]\n"
    "ds_read_b64_tr_b16 a[10:11], v5 offset:43008\n"
    "v_mfma_f32_16x16x32_bf16 v[220:223], a[80:83], a[176:179], v[220:223]\n"
    "ds_read_b64_tr_b16 a[12:13], v5 offset:45056\n"
    "v_mfma_f32_16x16x32_bf16 v[232:235], a[96:99], a[176:179], v[232:235]\n"
    "ds_read_b64_tr_b16 a[14:15], v5 offset:47104\n"
    "v_mfma_f32_16x16x32_bf16 v[232:235], a[100:103], a[180:183], v[232:235]\n"
    "v_mfma_f32_16x16x32_bf16 v[232:235], a[104:107], a[184:187], v[232:235]\n"
    "ds_read_b64_tr_b16 a[16:17], v6 offset:32768\n"
    "buffer_load_dwordx4 v[80:83], v15, s[24:27], s48 offen offset:1024 sc1\n"
    "v_mfma_f32_16x16x32_bf16 v[232:235], a[108:111], a[188:191], v[232:235]\n"
    "ds_read_b64_tr_b16 a[18:19], v6 offset:34816\n"
    "v_mfma_f32_16x16x32_bf16 v[244:247], a[124:127], a[188:191], v[244:247]\n"
    "ds_read_b64_tr_b16 a[20:21], v6 offset:36864\n"
    "v_mfma_f32_16x16x32_bf16 v[244:247], a[120:123], a[184:187], v[244:247]\n"
    "ds_read_b64_tr_b16 a[22:23], v6 offset:38912\n"
    "v_mfma_f32_16x16x32_bf16 v[244:247], a[116:119], a[180:183], v[244:247]\n"
    "ds_read_b64_tr_b16 a[24:25], v6 offset:40960\n"
    "buffer_load_dwordx4 v[84:87], v15, s[24:27], s49 offen offset:1024 sc1\n"
    "v_mfma_f32_16x16x32_bf16 v[244:247], a[112:115], a[176:179], v[244:247]\n"
    "ds_read_b64_tr_b16 a[26:27], v6 offset:43008\n"
    "v_mfma_f32_16x16x32_bf16 v[248:251], a[112:115], a[192:195], v[248:251]\n"
    "ds_read_b64_tr_b16 a[28:29], v6 offset:45056\n"
    "v_mfma_f32_16x16x32_bf16 v[248:251], a[116:119], a[196:199], v[248:251]\n"
    "ds_read_b64_tr_b16 a[30:31], v6 offset:47104\n"
    "v_mfma_f32_16x16x32_bf16 v[248:251], a[120:123], a[200:203], v[248:251]\n"
    "ds_read_b64_tr_b16 a[32:33], v7 offset:32768\n"
    "v_mfma_f32_16x16x32_bf16 v[248:251], a[124:127], a[204:207], v[248:251]\n"
    "ds_read_b64_tr_b16 a[34:35], v7 offset:34816\n"
    "buffer_load_dwordx4 v[88:91], v15, s[24:27], s50 offen offset:1024 sc1\n"
    "v_mfma_f32_16x16x32_bf16 v[236:239], a[108:111], a[204:207], v[236:239]\n"
    "v_mfma_f32_16x16x32_bf16 v[236:239], a[104:107], a[200:203], v[236:239]\n"
    "ds_read_b64_tr_b16 a[36:37], v7 offset:36864\n"
    "v_mfma_f32_16x16x32_bf16 v[236:239], a[100:103], a[196:199], v[236:239]\n"
    "ds_read_b64_tr_b16 a[38:39], v7 offset:38912\n"
    "v_mfma_f32_16x16x32_bf16 v[236:239], a[96:99], a[192:195], v[236:239]\n"
    "ds_read_b64_tr_b16 a[40:41], v7 offset:40960\n"
    "buffer_load_dwordx4 v[92:95], v15, s[24:27], s51 offen offset:1024 sc1\n"
    "v_mfma_f32_16x16x32_bf16 v[224:227], a[80:83], a[192:195], v[224:227]\n"
    "ds_read_b64_tr_b16 a[42:43], v7 offset:43008\n"
    "v_mfma_f32_16x16x32_bf16 v[224:227], a[84:87], a[196:199], v[224:227]\n"
    "ds_read_b64_tr_b16 a[44:45], v7 offset:45056\n"
    "v_mfma_f32_16x16x32_bf16 v[224:227], a[88:91], a[200:203], v[224:227]\n"
    "ds_read_b64_tr_b16 a[46:47], v7 offset:47104\n"
    "v_mfma_f32_16x16x32_bf16 v[224:227], a[92:95], a[204:207], v[224:227]\n"
    "v_mfma_f32_16x16x32_bf16 v[212:215], a[76:79], a[204:207], v[212:215]\n"
    "ds_read_b64_tr_b16 a[48:49], v8 offset:32768\n"
    "buffer_load_dwordx4 v[96:99], v15, s[24:27], s48 offen offset:2048 sc1\n"
    "v_mfma_f32_16x16x32_bf16 v[212:215], a[72:75], a[200:203], v[212:215]\n"
    "ds_read_b64_tr_b16 a[50:51], v8 offset:34816\n"
    "v_mfma_f32_16x16x32_bf16 v[212:215], a[68:71], a[196:199], v[212:215]\n"
    "ds_read_b64_tr_b16 a[52:53], v8 offset:36864\n"
    "v_mfma_f32_16x16x32_bf16 v[212:215], a[64:67], a[192:195], v[212:215]\n"
    "ds_read_b64_tr_b16 a[54:55], v8 offset:38912\n"
    "v_mfma_f32_16x16x32_bf16 v[216:219], a[64:67], a[208:211], v[216:219]\n"
    "ds_read_b64_tr_b16 a[56:57], v8 offset:40960\n"
    "buffer_load_dwordx4 v[100:103], v15, s[24:27], s49 offen offset:2048 sc1\n"
    "v_mfma_f32_16x16x32_bf16 v[216:219], a[68:71], a[212:215], v[216:219]\n"
    "ds_read_b64_tr_b16 a[58:59], v8 offset:43008\n"
    "v_mfma_f32_16x16x32_bf16 v[216:219], a[72:75], a[216:219], v[216:219]\n"
    "v_mfma_f32_16x16x32_bf16 v[216:219], a[76:79], a[220:223], v[216:219]\n"
    "ds_read_b64_tr_b16 a[60:61], v8 offset:45056\n"
    "v_mfma_f32_16x16x32_bf16 v[228:231], a[92:95], a[220:223], v[228:231]\n"
    "ds_read_b64_tr_b16 a[62:63], v8 offset:47104\n"
    "v_mfma_f32_16x16x32_bf16 v[228:231], a[88:91], a[216:219], v[228:231]\n"
    "buffer_load_dwordx4 v[104:107], v15, s[24:27], s50 offen offset:2048 sc1\n"
    "v_mfma_f32_16x16x32_bf16 v[228:231], a[84:87], a[212:215], v[228:231]\n"
    "v_mfma_f32_16x16x32_bf16 v[228:231], a[80:83], a[208:211], v[228:231]\n"
    "v_mfma_f32_16x16x32_bf16 v[240:243], a[96:99], a[208:211], v[240:243]\n"
    "v_mfma_f32_16x16x32_bf16 v[240:243], a[100:103], a[212:215], v[240:243]\n"
    "buffer_load_dwordx4 v[108:111], v15, s[24:27], s51 offen offset:2048 sc1\n"
    "v_mfma_f32_16x16x32_bf16 v[240:243], a[104:107], a[216:219], v[240:243]\n"
    "v_mfma_f32_16x16x32_bf16 v[240:243], a[108:111], a[220:223], v[240:243]\n"
    "v_mfma_f32_16x16x32_bf16 v[252:255], a[124:127], a[220:223], v[252:255]\n"
    "v_mfma_f32_16x16x32_bf16 v[252:255], a[120:123], a[216:219], v[252:255]\n"
    "v_mfma_f32_16x16x32_bf16 v[252:255], a[116:119], a[212:215], v[252:255]\n"
    "v_mfma_f32_16x16x32_bf16 v[252:255], a[112:115], a[208:211], v[252:255]\n"
    "s_waitcnt vmcnt(20) lgkmcnt(0)\n"
    "s_barrier\n"
    "v_mfma_f32_16x16x32_bf16 v[160:163], a[0:3], a[128:131], v[160:163]\n"
    "ds_read_b64_tr_b16 a[64:65], v5 offset:49152\n"
    "v_readlane_b32 s38, v43, s52\n"
    "v_mfma_f32_16x16x32_bf16 v[160:163], a[4:7], a[132:135], v[160:163]\n"
    "s_add_u32 s52, s52, 1\n"
    "s_add_u32 s48, s38, 0\n"
    "s_add_u32 s49, s38, 11264\n"
    "s_add_u32 s50, s38, 22528\n"
    "s_add_u32 s51, s38, 33792\n"
    "ds_read_b64_tr_b16 a[66:67], v5 offset:51200\n"
    "buffer_load_dwordx4 a[176:179], v15, s[24:27], s48 offen offset:0 sc1\n"
    "v_mfma_f32_16x16x32_bf16 v[160:163], a[8:11], a[136:139], v[160:163]\n"
    "v_mfma_f32_16x16x32_bf16 v[160:163], a[12:15], a[140:143], v[160:163]\n"
    "ds_read_b64_tr_b16 a[68:69], v5 offset:53248\n"
    "v_mfma_f32_16x16x32_bf16 v[172:175], a[28:31], a[140:143], v[172:175]\n"
    "ds_read_b64_tr_b16 a[70:71], v5 offset:55296\n"
    "buffer_load_dwordx4 a[180:183], v15, s[24:27], s49 offen offset:0 sc1\n"
    "v_mfma_f32_16x16x32_bf16 v[172:175], a[24:27], a[136:139], v[172:175]\n"
    "ds_read_b64_tr_b16 a[72:73], v5 offset:57344\n"
    "v_mfma_f32_16x16x32_bf16 v[172:175], a[20:23], a[132:135], v[172:175]\n"
    "ds_read_b64_tr_b16 a[74:75], v5 offset:59392\n"
    "v_mfma_f32_16x16x32_bf16 v[172:175], a[16:19], a[128:131], v[172:175]\n"
    "ds_read_b64_tr_b16 a[76:77], v5 offset:61440\n"
    "v_mfma_f32_16x16x32_bf16 v[184:187], a[32:35], a[128:131], v[184:187]\n"
    "ds_read_b64_tr_b16 a[78:79], v5 offset:63488\n"
    "v_xor_b32 v5, 0x10000, v5\n"
    "buffer_load_dwordx4 a[184:187], v15, s[24:27], s50 offen offset:0 sc1\n"
    "v_mfma_f32_16x16x32_bf16 v[184:187], a[36:39], a[132:135], v[184:187]\n"
    "v_mfma_f32_16x16x32_bf16 v[184:187], a[40:43], a[136:139], v[184:187]\n"
    "ds_read_b64_tr_b16 a[80:81], v6 offset:49152\n"
    "v_mfma_f32_16x16x32_bf16 v[184:187], a[44:47], a[140:143], v[184:187]\n"
    "ds_read_b64_tr_b16 a[82:83], v6 offset:51200\n"
    "v_mfma_f32_16x16x32_bf16 v[196:199], a[60:63], a[140:143], v[196:199]\n"
    "ds_read_b64_tr_b16 a[84:85], v6 offset:53248\n"
    "buffer_load_dwordx4 a[188:191], v15, s[24:27], s51 offen offset:0 sc1\n"
    "v_mfma_f32_16x16x32_bf16 v[196:199], a[56:59], a[136:139], v[196:199]\n"
    "ds_read_b64_tr_b16 a[86:87], v6 offset:55296\n"
    "v_mfma_f32_16x16x32_bf16 v[196:199], a[52:55], a[132:135], v[196:199]\n"
    "ds_read_b64_tr_b16 a[88:89], v6 offset:57344\n"
    "v_mfma_f32_16x16x32_bf16 v[196:199], a[48:51], a[128:131], v[196:199]\n"
    "s_waitcnt vmcnt(20)\n"
    "ds_read_b64_tr_b16 a[90:91], v6 offset:59392\n"
    "v_mfma_f32_16x16x32_bf16 v[200:203], a[48:51], a[144:147], v[200:203]\n"
    "ds_read_b64_tr_b16 a[92:93], v6 offset:61440\n"
    "buffer_load_dwordx4 a[192:195], v15, s[24:27], s48 offen offset:1024 sc1\n"
    "v_mfma_f32_16x16x32_bf16 v[200:203], a[52:55], a[148:151], v[200:203]\n"
    "ds_read_b64_tr_b16 a[94:95], v6 offset:63488\n"
    "v_xor_b32 v6, 0x10000, v6\n"
    "v_mfma_f32_16x16x32_bf16 v[200:203], a[56:59], a[152:155], v[200:203]\n"
    "ds_read_b64_tr_b16 a[96:97], v7 offset:49152\n"
    "v_mfma_f32_16x16x32_bf16 v[200:203], a[60:63], a[156:159], v[200:203]\n"
    "ds_read_b64_tr_b16 a[98:99], v7 offset:51200\n"
    "buffer_load_dwordx4 a[196:199], v15, s[24:27], s49 offen offset:1024 sc1\n"
    "v_mfma_f32_16x16x32_bf16 v[188:191], a[44:47], a[156:159], v[188:191]\n"
    "v_mfma_f32_16x16x32_bf16 v[188:191], a[40:43], a[152:155], v[188:191]\n"
    "ds_read_b64_tr_b16 a[100:101], v7 offset:53248\n"
    "v_mfma_f32_16x16x32_bf16 v[188:191], a[36:39], a[148:151], v[188:191]\n"
    "ds_read_b64_tr_b16 a[102:103], v7 offset:55296\n"
    "v_mfma_f32_16x16x32_bf16 v[188:191], a[32:35], a[144:147], v[188:191]\n"
    "ds_read_b64_tr_b16 a[104:105], v7 offset:57344\n"
    "buffer_load_dwordx4 a[200:203], v15, s[24:27], s50 offen offset:1024 sc1\n"
    "v_mfma_f32_16x16x32_bf16 v[176:179], a[16:19], a[144:147], v[176:179]\n"
    "ds_read_b64_tr_b16 a[106:107], v7 offset:59392\n"
    "v_mfma_f32_16x16x32_bf16 v[176:179], a[20:23], a[148:151], v[176:179]\n"
    "ds_read_b64_tr_b16 a[108:109], v7 offset:61440\n"
    "v_mfma_f32_16x16x32_bf16 v[176:179], a[24:27], a[152:155], v[176:179]\n"
    "ds_read_b64_tr_b16 a[110:111], v7 offset:63488\n"
    "v_xor_b32 v7, 0x10000, v7\n"
    "v_mfma_f32_16x16x32_bf16 v[176:179], a[28:31], a[156:159], v[176:179]\n"
    "buffer_load_dwordx4 a[204:207], v15, s[24:27], s51 offen offset:1024 sc1\n"
    "v_mfma_f32_16x16x32_bf16 v[164:167], a[12:15], a[156:159], v[164:167]\n"
    "ds_read_b64_tr_b16 a[112:113], v8 offset:49152\n"
    "v_mfma_f32_16x16x32_bf16 v[164:167], a[8:11], a[152:155], v[164:167]\n"
    "ds_read_b64_tr_b16 a[114:115], v8 offset:51200\n"
    "v_mfma_f32_16x16x32_bf16 v[164:167], a[4:7], a[148:151], v[164:167]\n"
    "ds_read_b64_tr_b16 a[116:117], v8 offset:53248\n"
    "v_mfma_f32_16x16x32_bf16 v[164:167], a[0:3], a[144:147], v[164:167]\n"
    "s_waitcnt vmcnt(20)\n"
    "ds_read_b64_tr_b16 a[118:119], v8 offset:55296\n"
    "buffer_load_dwordx4 a[208:211], v15, s[24:27], s48 offen offset:2048 sc1\n"
    "v_mfma_f32_16x16x32_bf16 v[168:171], a[0:3], a[160:163], v[168:171]\n"
    "ds_read_b64_tr_b16 a[120:121], v8 offset:57344\n"
    "v_mfma_f32_16x16x32_bf16 v[168:171], a[4:7], a[164:167], v[168:171]\n"
    "ds_read_b64_tr_b16 a[122:123], v8 offset:59392\n"
    "v_mfma_f32_16x16x32_bf16 v[168:171], a[8:11], a[168:171], v[168:171]\n"
    "buffer_load_dwordx4 a[212:215], v15, s[24:27], s49 offen offset:2048 sc1\n"
    "v_mfma_f32_16x16x32_bf16 v[168:171], a[12:15], a[172:175], v[168:171]\n"
    "ds_read_b64_tr_b16 a[124:125], v8 offset:61440\n"
    "v_mfma_f32_16x16x32_bf16 v[180:183], a[28:31], a[172:175], v[180:183]\n"
    "ds_read_b64_tr_b16 a[126:127], v8 offset:63488\n"
    "v_xor_b32 v8, 0x10000, v8\n"
    "v_mfma_f32_16x16x32_bf16 v[180:183], a[24:27], a[168:171], v[180:183]\n"
    "v_mfma_f32_16x16x32_bf16 v[180:183], a[20:23], a[164:167], v[180:183]\n"
    "buffer_load_dwordx4 a[216:219], v15, s[24:27], s50 offen offset:2048 sc1\n"
    "v_mfma_f32_16x16x32_bf16 v[180:183], a[16:19], a[160:163], v[180:183]\n"
    "v_mfma_f32_16x16x32_bf16 v[192:195], a[32:35], a[160:163], v[192:195]\n"
    "v_mfma_f32_16x16x32_bf16 v[192:195], a[36:39], a[164:167], v[192:195]\n"
    "v_mfma_f32_16x16x32_bf16 v[192:195], a[40:43], a[168:171], v[192:195]\n"
    "buffer_load_dwordx4 a[220:223], v15, s[24:27], s51 offen offset:2048 sc1\n"
    "v_mfma_f32_16x16x32_bf16 v[192:195], a[44:47], a[172:175], v[192:195]\n"
    "v_mfma_f32_16x16x32_bf16 v[204:207], a[60:63], a[172:175], v[204:207]\n"
    "v_mfma_f32_16x16x32_bf16 v[204:207], a[56:59], a[168:171], v[204:207]\n"
    "v_mfma_f32_16x16x32_bf16 v[204:207], a[52:55], a[164:167], v[204:207]\n"
    "v_mfma_f32_16x16x32_bf16 v[204:207], a[48:51], a[160:163], v[204:207]\n"
    "s_waitcnt vmcnt(20) lgkmcnt(0)\n"
    "v_mfma_f32_16x16x32_bf16 v[160:163], a[64:67], v[64:67], v[160:163]\n"
    "ds_read_b64_tr_b16 a[0:1], v5 offset:0\n"
    "v_mfma_f32_16x16x32_bf16 v[160:163], a[68:71], v[68:71], v[160:163]\n"
    "ds_read_b64_tr_b16 a[2:3], v5 offset:2048\n"
    "v_readlane_b32 s38, v43, s52\n"
    "v_mfma_f32_16x16x32_bf16 v[160:163], a[72:75], v[72:75], v[160:163]\n"
    "s_add_u32 s52, s52, 1\n"
    "s_add_u32 s48, s38, 0\n"
    "s_add_u32 s49, s38, 11264\n"
    "s_add_u32 s50, s38, 22528\n"
    "s_add_u32 s51, s38, 33792\n"
    "buffer_load_dwordx4 v[112:115], v15, s[24:27], s48 offen offset:0 sc1\n"
    "v_mfma_f32_16x16x32_bf16 v[160:163], a[76:79], v[76:79], v[160:163]\n"
    "ds_read_b64_tr_b16 a[4:5], v5 offset:4096\n"
    "v_mfma_f32_16x16x32_bf16 v[172:175], a[92:95], v[76:79], v[172:175]\n"
    "ds_read_b64_tr_b16 a[6:7], v5 offset:6144\n"
    "v_mfma_f32_16x16x32_bf16 v[172:175], a[88:91], v[72:75], v[172:175]\n"
    "ds_read_b64_tr_b16 a[8:9], v5 offset:8192\n"
    "v_mfma_f32_16x16x32_bf16 v[172:175], a[84:87], v[68:71], v[172:175]\n"
    "ds_read_b64_tr_b16 a[10:11], v5 offset:10240\n"
    "v_mfma_f32_16x16x32_bf16 v[172:175], a[80:83], v[64:67], v[172:175]\n"
    "ds_read_b64_tr_b16 a[12:13], v5 offset:12288\n"
    "v_mfma_f32_16x16x32_bf16 v[184:187], a[96:99], v[64:67], v[184:187]\n"
    "ds_read_b64_tr_b16 a[14:15], v5 offset:14336\n"
    "v_mfma_f32_16x16x32_bf16 v[184:187], a[100:103], v[68:71], v[184:187]\n"
    "buffer_load_dwordx4 v[116:119], v15, s[24:27], s49 offen offset:0 sc1\n"
    "v_mfma_f32_16x16x32_bf16 v[184:187], a[104:107], v[72:75], v[184:187]\n"
    "ds_read_b64_tr_b16 a[16:17], v6 offset:0\n"
    "v_mfma_f32_16x16x32_bf16 v[184:187], a[108:111], v[76:79], v[184:187]\n"
    "ds_read_b64_tr_b16 a[18:19], v6 offset:2048\n"
    "v_mfma_f32_16x16x32_bf16 v[196:199], a[124:127], v[76:79], v[196:199]\n"
    "ds_read_b64_tr_b16 a[20:21], v6 offset:4096\n"
    "v_mfma_f32_16x16x32_bf16 v[196:199], a[120:123], v[72:75], v[196:199]\n"
    "ds_read_b64_tr_b16 a[22:23], v6 offset:6144\n"
    "v_mfma_f32_16x16x32_bf16 v[196:199], a[116:119], v[68:71], v[196:199]\n"
    "ds_read_b64_tr_b16 a[24:25], v6 offset:8192\n"
    "v_mfma_f32_16x16x32_bf16 v[196:199], a[112:115], v[64:67], v[196:199]\n"
    "s_waitcnt vmcnt(18)\n"
    "ds_read_b64_tr_b16 a[26:27], v6 offset:10240\n"
    "v_mfma_f32_16x16x32_bf16 v[200:203], a[112:115], v[80:83], v[200:203]\n"
    "ds_read_b64_tr_b16 a[28:29], v6 offset:12288\n"
    "v_mfma_f32_16x16x32_bf16 v[200:203], a[116:119], v[84:87], v[200:203]\n"
    "ds_read_b64_tr_b16 a[30:31], v6 offset:14336\n"
    "v_mfma_f32_16x16x32_bf16 v[200:203], a[120:123], v[88:91], v[200:203]\n"
    "ds_read_b64_tr_b16 a[32:33], v7 offset:0\n"
    "v_mfma_f32_16x16x32_bf16 v[200:203], a[124:127], v[92:95], v[200:203]\n"
    "ds_read_b64_tr_b16 a[34:35], v7 offset:2048\n"
    "v_mfma_f32_16x16x32_bf16 v[188:191], a[108:111], v[92:95], v[188:191]\n"
    "buffer_load_dwordx4 v[120:123], v15, s[24:27], s50 offen offset:0 sc1\n"
    "v_mfma_f32_16x16x32_bf16 v[188:191], a[104:107], v[88:91], v[188:191]\n"
    "ds_read_b64_tr_b16 a[36:37], v7 offset:4096\n"
    "v_mfma_f32_16x16x32_bf16 v[188:191], a[100:103], v[84:87], v[188:191]\n"
    "ds_read_b64_tr_b16 a[38:39], v7 offset:6144\n"
    "v_mfma_f32_16x16x32_bf16 v[188:191], a[96:99], v[80:83], v[188:191]\n"
    "ds_read_b64_tr_b16 a[40:41], v7 offset:8192\n"
    "v_mfma_f32_16x16x32_bf16 v[176:179], a[80:83], v[80:83], v[176:179]\n"
    "ds_read_b64_tr_b16 a[42:43], v7 offset:10240\n"
    "v_mfma_f32_16x16x32_bf16 v[176:179], a[84:87], v[84:87], v[176:179]\n"
    "ds_read_b64_tr_b16 a[44:45], v7 offset:12288\n"
    "v_mfma_f32_16x16x32_bf16 v[176:179], a[88:91], v[88:91], v[176:179]\n"
    "ds_read_b64_tr_b16 a[46:47], v7 offset:14336\n"
    "v_mfma_f32_16x16x32_bf16 v[176:179], a[92:95], v[92:95], v[176:179]\n"
    "buffer_load_dwordx4 v[124:127], v15, s[24:27], s51 offen offset:0 sc1\n"
    "v_mfma_f32_16x16x32_bf16 v[164:167], a[76:79], v[92:95], v[164:167]\n"
    "ds_read_b64_tr_b16 a[48:49], v8 offset:0\n"
    "v_mfma_f32_16x16x32_bf16 v[164:167], a[72:75], v[88:91], v[164:167]\n"
    "ds_read_b64_tr_b16 a[50:51], v8 offset:2048\n"
    "v_mfma_f32_16x16x32_bf16 v[164:167], a[68:71], v[84:87], v[164:167]\n"
    "ds_read_b64_tr_b16 a[52:53], v8 offset:4096\n"
    "v_mfma_f32_16x16x32_bf16 v[164:167], a[64:67], v[80:83], v[164:167]\n"
    "s_waitcnt vmcnt(16)\n"
    "ds_read_b64_tr_b16 a[54:55], v8 offset:6144\n"
    "v_mfma_f32_16x16x32_bf16 v[168:171], a[64:67], v[96:99], v[168:171]\n"
    "ds_read_b64_tr_b16 a[56:57], v8 offset:8192\n"
    "v_mfma_f32_16x16x32_bf16 v[168:171], a[68:71], v[100:103], v[168:171]\n"
    "ds_read_b64_tr_b16 a[58:59], v8 offset:10240\n"
    "v_mfma_f32_16x16x32_bf16 v[168:171], a[72:75], v[104:107], v[168:171]\n"
    "buffer_load_dwordx4 v[128:131], v15, s[24:27], s48 offen offset:1024 sc1\n"
    "v_mfma_f32_16x16x32_bf16 v[168:171], a[76:79], v[108:111], v[168:171]\n"
    "ds_read_b64_tr_b16 a[60:61], v8 offset:12288\n"
    "v_mfma_f32_16x16x32_bf16 v[180:183], a[92:95], v[108:111], v[180:183]\n"
    "ds_read_b64_tr_b16 a[62:63], v8 offset:14336\n"
    "v_mfma_f32_16x16x32_bf16 v[180:183], a[88:91], v[104:107], v[180:183]\n"
    "v_mfma_f32_16x16x32_bf16 v[180:183], a[84:87], v[100:103], v[180:183]\n"
    "v_mfma_f32_16x16x32_bf16 v[180:183], a[80:83], v[96:99], v[180:183]\n"
    "v_mfma_f32_16x16x32_bf16 v[192:195], a[96:99], v[96:99], v[192:195]\n"
    "v_mfma_f32_16x16x32_bf16 v[192:195], a[100:103], v[100:103], v[192:195]\n"
    "buffer_load_dwordx4 v[132:135], v15, s[24:27], s49 offen offset:1024 sc1\n"
    "v_mfma_f32_16x16x32_bf16 v[192:195], a[104:107], v[104:107], v[192:195]\n"
    "v_mfma_f32_16x16x32_bf16 v[192:195], a[108:111], v[108:111], v[192:195]\n"
    "v_mfma_f32_16x16x32_bf16 v[204:207], a[124:127], v[108:111], v[204:207]\n"
    "v_mfma_f32_16x16x32_bf16 v[204:207], a[120:123], v[104:107], v[204:207]\n"
    "v_mfma_f32_16x16x32_bf16 v[204:207], a[116:119], v[100:103], v[204:207]\n"
    "v_mfma_f32_16x16x32_bf16 v[204:207], a[112:115], v[96:99], v[204:207]\n"
    "s_waitcnt vmcnt(14) lgkmcnt(0)\n"
    "s_barrier\n"
    "v_mfma_f32_16x16x32_bf16 v[160:163], a[0:3], a[176:179], v[160:163]\n"
    "ds_read_b64_tr_b16 a[64:65], v5 offset:16384\n"
    "v_mfma_f32_16x16x32_bf16 v[160:163], a[4:7], a[180:183], v[160:163]\n"
    "ds_read_b64_tr_b16 a[66:67], v5 offset:18432\n"
    "v_mfma_f32_16x16x32_bf16 v[160:163], a[8:11], a[184:187], v[160:163]\n"
    "buffer_load_dwordx4 v[136:139], v15, s[24:27], s50 offen offset:1024 sc1\n"
    "v_mfma_f32_16x16x32_bf16 v[160:163], a[12:15], a[188:191], v[160:163]\n"
    "ds_read_b64_tr_b16 a[68:69], v5 offset:20480\n"
    "v_mfma_f32_16x16x32_bf16 v[172:175], a[28:31], a[188:191], v[172:175]\n"
    "ds_read_b64_tr_b16 a[70:71], v5 offset:22528\n"
    "v_mfma_f32_16x16x32_bf16 v[172:175], a[24:27], a[184:187], v[172:175]\n"
    "ds_read_b64_tr_b16 a[72:73], v5 offset:24576\n"
    "v_mfma_f32_16x16x32_bf16 v[172:175], a[20:23], a[180:183], v[172:175]\n"
    "ds_read_b64_tr_b16 a[74:75], v5 offset:26624\n"
    "v_mfma_f32_16x16x32_bf16 v[172:175], a[16:19], a[176:179], v[172:175]\n"
    "ds_read_b64_tr_b16 a[76:77], v5 offset:28672\n"
    "v_mfma_f32_16x16x32_bf16 v[184:187], a[32:35], a[176:179], v[184:187]\n"
    "ds_read_b64_tr_b16 a[78:79], v5 offset:30720\n"
    "v_mfma_f32_16x16x32_bf16 v[184:187], a[36:39], a[180:183], v[184:187]\n"
    "buffer_load_dwordx4 v[140:143], v15, s[24:27], s51 offen offset:1024 sc1\n"
    "v_mfma_f32_16x16x32_bf16 v[184:187], a[40:43], a[184:187], v[184:187]\n"
    "ds_read_b64_tr_b16 a[80:81], v6 offset:16384\n"
    "v_mfma_f32_16x16x32_bf16 v[184:187], a[44:47], a[188:191], v[184:187]\n"
    "ds_read_b64_tr_b16 a[82:83], v6 offset:18432\n"
    "v_mfma_f32_16x16x32_bf16 v[196:199], a[60:63], a[188:191], v[196:199]\n"
    "ds_read_b64_tr_b16 a[84:85], v6 offset:20480\n"
    "v_mfma_f32_16x16x32_bf16 v[196:199], a[56:59], a[184:187], v[196:199]\n"
    "ds_read_b64_tr_b16 a[86:87], v6 offset:22528\n"
    "v_mfma_f32_16x16x32_bf16 v[196:199], a[52:55], a[180:183], v[196:199]\n"
    "ds_read_b64_tr_b16 a[88:89], v6 offset:24576\n"
    "v_mfma_f32_16x16x32_bf16 v[196:199], a[48:51], a[176:179], v[196:199]\n"
    "s_waitcnt vmcnt(12)\n"
    "ds_read_b64_tr_b16 a[90:91], v6 offset:26624\n"
    "v_mfma_f32_16x16x32_bf16 v[200:203], a[48:51], a[192:195], v[200:203]\n"
    "ds_read_b64_tr_b16 a[92:93], v6 offset:28672\n"
    "v_mfma_f32_16x16x32_bf16 v[200:203], a[52:55], a[196:199], v[200:203]\n"
    "ds_read_b64_tr_b16 a[94:95], v6 offset:30720\n"
    "v_mfma_f32_16x16x32_bf16 v[200:203], a[56:59], a[200:203], v[200:203]\n"
    "ds_read_b64_tr_b16 a[96:97], v7 offset:16384\n"
    "v_mfma_f32_16x16x32_bf16 v[200:203], a[60:63], a[204:207], v[200:203]\n"
    "ds_read_b64_tr_b16 a[98:99], v7 offset:18432\n"
    "v_mfma_f32_16x16x32_bf16 v[188:191], a[44:47], a[204:207], v[188:191]\n"
    "buffer_load_dwordx4 v[144:147], v15, s[24:27], s48 offen offset:2048 sc1\n"
    "v_cvt_pk_bf16_f32 v160, v160, v161\n"
    "v_mfma_f32_16x16x32_bf16 v[188:191], a[40:43], a[200:203], v[188:191]\n"
    "ds_read_b64_tr_b16 a[100:101], v7 offset:20480\n"
    "v_cvt_pk_bf16_f32 v161, v162, v163\n"
    "v_mfma_f32_16x16x32_bf16 v[188:191], a[36:39], a[196:199], v[188:191]\n"
    "ds_read_b64_tr_b16 a[102:103], v7 offset:22528\n"
    "v_cvt_pk_bf16_f32 v162, v172, v173\n"
    "v_mfma_f32_16x16x32_bf16 v[188:191], a[32:35], a[192:195], v[188:191]\n"
    "ds_read_b64_tr_b16 a[104:105], v7 offset:24576\n"
    "v_cvt_pk_bf16_f32 v163, v174, v175\n"
    "v_mfma_f32_16x16x32_bf16 v[176:179], a[16:19], a[192:195], v[176:179]\n"
    "ds_read_b64_tr_b16 a[106:107], v7 offset:26624\n"
    "v_cvt_pk_bf16_f32 v184, v184, v185\n"
    "v_mfma_f32_16x16x32_bf16 v[176:179], a[20:23], a[196:199], v[176:179]\n"
    "ds_read_b64_tr_b16 a[108:109], v7 offset:28672\n"
    "v_cvt_pk_bf16_f32 v185, v186, v187\n"
    "v_mfma_f32_16x16x32_bf16 v[176:179], a[24:27], a[200:203], v[176:179]\n"
    "ds_read_b64_tr_b16 a[110:111], v7 offset:30720\n"
    "v_cvt_pk_bf16_f32 v186, v196, v197\n"
    "v_mfma_f32_16x16x32_bf16 v[176:179], a[28:31], a[204:207], v[176:179]\n"
    "buffer_load_dwordx4 v[148:151], v15, s[24:27], s49 offen offset:2048 sc1\n"
    "v_cvt_pk_bf16_f32 v187, v198, v199\n"
    "v_mfma_f32_16x16x32_bf16 v[164:167], a[12:15], a[204:207], v[164:167]\n"
    "ds_read_b64_tr_b16 a[112:113], v8 offset:16384\n"
    "v_mfma_f32_16x16x32_bf16 v[164:167], a[8:11], a[200:203], v[164:167]\n"
    "ds_read_b64_tr_b16 a[114:115], v8 offset:18432\n"
    "v_mfma_f32_16x16x32_bf16 v[164:167], a[4:7], a[196:199], v[164:167]\n"
    "ds_read_b64_tr_b16 a[116:117], v8 offset:20480\n"
    "v_mfma_f32_16x16x32_bf16 v[164:167], a[0:3], a[192:195], v[164:167]\n"
    "s_waitcnt vmcnt(10)\n"
    "ds_read_b64_tr_b16 a[118:119], v8 offset:22528\n"
    "v_mfma_f32_16x16x32_bf16 v[168:171], a[0:3], a[208:211], v[168:171]\n"
    "ds_read_b64_tr_b16 a[120:121], v8 offset:24576\n"
    "v_mfma_f32_16x16x32_bf16 v[168:171], a[4:7], a[212:215], v[168:171]\n"
    "ds_read_b64_tr_b16 a[122:123], v8 offset:26624\n"
    "v_mfma_f32_16x16x32_bf16 v[168:171], a[8:11], a[216:219], v[168:171]\n"
    "buffer_load_dwordx4 v[152:155], v15, s[24:27], s50 offen offset:2048 sc1\n"
    "v_mfma_f32_16x16x32_bf16 v[168:171], a[12:15], a[220:223], v[168:171]\n"
    "ds_read_b64_tr_b16 a[124:125], v8 offset:28672\n"
    "v_mfma_f32_16x16x32_bf16 v[180:183], a[28:31], a[220:223], v[180:183]\n"
    "ds_read_b64_tr_b16 a[126:127], v8 offset:30720\n"
    "v_cvt_pk_bf16_f32 v164, v164, v165\n"
    "v_mfma_f32_16x16x32_bf16 v[180:183], a[24:27], a[216:219], v[180:183]\n"
    "v_cvt_pk_bf16_f32 v165, v166, v167\n"
    "v_cvt_pk_bf16_f32 v166, v176, v177\n"
    "v_mfma_f32_16x16x32_bf16 v[180:183], a[20:23], a[212:215], v[180:183]\n"
    "v_cvt_pk_bf16_f32 v167, v178, v179\n"
    "v_cvt_pk_bf16_f32 v188, v188, v189\n"
    "v_mfma_f32_16x16x32_bf16 v[180:183], a[16:19], a[208:211], v[180:183]\n"
    "v_cvt_pk_bf16_f32 v189, v190, v191\n"
    "v_cvt_pk_bf16_f32 v190, v200, v201\n"
    "v_mfma_f32_16x16x32_bf16 v[192:195], a[32:35], a[208:211], v[192:195]\n"
    "v_cvt_pk_bf16_f32 v191, v202, v203\n"
    "v_mfma_f32_16x16x32_bf16 v[192:195], a[36:39], a[212:215], v[192:195]\n"
    "buffer_load_dwordx4 v[156:159], v15, s[24:27], s51 offen offset:2048 sc1\n"
    "v_mfma_f32_16x16x32_bf16 v[192:195], a[40:43], a[216:219], v[192:195]\n"
    "v_mfma_f32_16x16x32_bf16 v[192:195], a[44:47], a[220:223], v[192:195]\n"
    "v_mfma_f32_16x16x32_bf16 v[204:207], a[60:63], a[220:223], v[204:207]\n"
    "v_mfma_f32_16x16x32_bf16 v[204:207], a[56:59], a[216:219], v[204:207]\n"
    "v_mfma_f32_16x16x32_bf16 v[204:207], a[52:55], a[212:215], v[204:207]\n"
    "v_mfma_f32_16x16x32_bf16 v[204:207], a[48:51], a[208:211], v[204:207]\n"
    "s_waitcnt lgkmcnt(0)\n"
    "v_mfma_f32_16x16x32_bf16 v[208:211], a[64:67], a[128:131], v[208:211]\n"
    "ds_read_b64_tr_b16 a[0:1], v5 offset:32768\n"
    "v_mfma_f32_16x16x32_bf16 v[208:211], a[68:71], a[132:135], v[208:211]\n"
    "ds_read_b64_tr_b16 a[2:3], v5 offset:34816\n"
    "v_mfma_f32_16x16x32_bf16 v[208:211], a[72:75], a[136:139], v[208:211]\n"
    "s_add_u32 s46, s55, 0\n"
    "buffer_store_dwordx4 v[160:163], v37, s[24:27], s46 offen offset:0 sc1\n"
    "v_mfma_f32_16x16x32_bf16 v[208:211], a[76:79], a[140:143], v[208:211]\n"
    "ds_read_b64_tr_b16 a[4:5], v5 offset:36864\n"
    "buffer_store_dwordx4 v[184:187], v37, s[24:27], s46 offen offset:1024 sc1\n"
    "v_mfma_f32_16x16x32_bf16 v[220:223], a[92:95], a[140:143], v[220:223]\n"
    "ds_read_b64_tr_b16 a[6:7], v5 offset:38912\n"
    "s_add_u32 s46, s55, 2048\n"
    "buffer_store_dwordx4 v[164:167], v37, s[24:27], s46 offen offset:0 sc1\n"
    "v_mfma_f32_16x16x32_bf16 v[220:223], a[88:91], a[136:139], v[220:223]\n"
    "ds_read_b64_tr_b16 a[8:9], v5 offset:40960\n"
    "buffer_store_dwordx4 v[188:191], v37, s[24:27], s46 offen offset:1024 sc1\n"
    "v_mfma_f32_16x16x32_bf16 v[220:223], a[84:87], a[132:135], v[220:223]\n"
    "ds_read_b64_tr_b16 a[10:11], v5 offset:43008\n"
    "v_cvt_pk_bf16_f32 v168, v168, v169\n"
    "v_cvt_pk_bf16_f32 v169, v170, v171\n"
    "v_cvt_pk_bf16_f32 v170, v180, v181\n"
    "v_cvt_pk_bf16_f32 v171, v182, v183\n"
    "v_cvt_pk_bf16_f32 v192, v192, v193\n"
    "v_cvt_pk_bf16_f32 v193, v194, v195\n"
    "v_cvt_pk_bf16_f32 v194, v204, v205\n"
    "v_cvt_pk_bf16_f32 v195, v206, v207\n"
    "s_add_u32 s46, s55, 4096\n"
    "buffer_store_dwordx4 v[168:171], v37, s[24:27], s46 offen offset:0 sc1\n"
    "v_mfma_f32_16x16x32_bf16 v[220:223], a[80:83], a[128:131], v[220:223]\n"
    "ds_read_b64_tr_b16 a[12:13], v5 offset:45056\n"
    "buffer_store_dwordx4 v[192:195], v37, s[24:27], s46 offen offset:1024 sc1\n"
    "v_mfma_f32_16x16x32_bf16 v[232:235], a[96:99], a[128:131], v[232:235]\n"
    "ds_read_b64_tr_b16 a[14:15], v5 offset:47104\n"
    "v_mfma_f32_16x16x32_bf16 v[232:235], a[100:103], a[132:135], v[232:235]\n"
    "v_mfma_f32_16x16x32_bf16 v[232:235], a[104:107], a[136:139], v[232:235]\n"
    "ds_read_b64_tr_b16 a[16:17], v6 offset:32768\n"
    "v_mfma_f32_16x16x32_bf16 v[232:235], a[108:111], a[140:143], v[232:235]\n"
    "ds_read_b64_tr_b16 a[18:19], v6 offset:34816\n"
    "v_mfma_f32_16x16x32_bf16 v[244:247], a[124:127], a[140:143], v[244:247]\n"
    "ds_read_b64_tr_b16 a[20:21], v6 offset:36864\n"
    "v_mfma_f32_16x16x32_bf16 v[244:247], a[120:123], a[136:139], v[244:247]\n"
    "ds_read_b64_tr_b16 a[22:23], v6 offset:38912\n"
    "v_mfma_f32_16x16x32_bf16 v[244:247], a[116:119], a[132:135], v[244:247]\n"
    "ds_read_b64_tr_b16 a[24:25], v6 offset:40960\n"
    "v_mfma_f32_16x16x32_bf16 v[244:247], a[112:115], a[128:131], v[244:247]\n"
    "ds_read_b64_tr_b16 a[26:27], v6 offset:43008\n"
    "v_mfma_f32_16x16x32_bf16 v[248:251], a[112:115], a[144:147], v[248:251]\n"
    "ds_read_b64_tr_b16 a[28:29], v6 offset:45056\n"
    "v_mfma_f32_16x16x32_bf16 v[248:251], a[116:119], a[148:151], v[248:251]\n"
    "ds_read_b64_tr_b16 a[30:31], v6 offset:47104\n"
    "v_mfma_f32_16x16x32_bf16 v[248:251], a[120:123], a[152:155], v[248:251]\n"
    "ds_read_b64_tr_b16 a[32:33], v7 offset:32768\n"
    "v_mfma_f32_16x16x32_bf16 v[248:251], a[124:127], a[156:159], v[248:251]\n"
    "ds_read_b64_tr_b16 a[34:35], v7 offset:34816\n"
    "v_mfma_f32_16x16x32_bf16 v[236:239], a[108:111], a[156:159], v[236:239]\n"
    "v_mfma_f32_16x16x32_bf16 v[236:239], a[104:107], a[152:155], v[236:239]\n"
    "ds_read_b64_tr_b16 a[36:37], v7 offset:36864\n"
    "v_mfma_f32_16x16x32_bf16 v[236:239], a[100:103], a[148:151], v[236:239]\n"
    "ds_read_b64_tr_b16 a[38:39], v7 offset:38912\n"
    "v_mfma_f32_16x16x32_bf16 v[236:239], a[96:99], a[144:147], v[236:239]\n"
    "ds_read_b64_tr_b16 a[40:41], v7 offset:40960\n"
    "v_mfma_f32_16x16x32_bf16 v[224:227], a[80:83], a[144:147], v[224:227]\n"
    "ds_read_b64_tr_b16 a[42:43], v7 offset:43008\n"
    "v_mfma_f32_16x16x32_bf16 v[224:227], a[84:87], a[148:151], v[224:227]\n"
    "ds_read_b64_tr_b16 a[44:45], v7 offset:45056\n"
    "v_mfma_f32_16x16x32_bf16 v[224:227], a[88:91], a[152:155], v[224:227]\n"
    "ds_read_b64_tr_b16 a[46:47], v7 offset:47104\n"
    "v_mfma_f32_16x16x32_bf16 v[224:227], a[92:95], a[156:159], v[224:227]\n"
    "v_mfma_f32_16x16x32_bf16 v[212:215], a[76:79], a[156:159], v[212:215]\n"
    "ds_read_b64_tr_b16 a[48:49], v8 offset:32768\n"
    "v_mfma_f32_16x16x32_bf16 v[212:215], a[72:75], a[152:155], v[212:215]\n"
    "ds_read_b64_tr_b16 a[50:51], v8 offset:34816\n"
    "v_mfma_f32_16x16x32_bf16 v[212:215], a[68:71], a[148:151], v[212:215]\n"
    "ds_read_b64_tr_b16 a[52:53], v8 offset:36864\n"
    "v_mfma_f32_16x16x32_bf16 v[212:215], a[64:67], a[144:147], v[212:215]\n"
    "ds_read_b64_tr_b16 a[54:55], v8 offset:38912\n"
    "v_mfma_f32_16x16x32_bf16 v[216:219], a[64:67], a[160:163], v[216:219]\n"
    "ds_read_b64_tr_b16 a[56:57], v8 offset:40960\n"
    "v_mfma_f32_16x16x32_bf16 v[216:219], a[68:71], a[164:167], v[216:219]\n"
    "ds_read_b64_tr_b16 a[58:59], v8 offset:43008\n"
    "v_mfma_f32_16x16x32_bf16 v[216:219], a[72:75], a[168:171], v[216:219]\n"
    "v_mfma_f32_16x16x32_bf16 v[216:219], a[76:79], a[172:175], v[216:219]\n"
    "ds_read_b64_tr_b16 a[60:61], v8 offset:45056\n"
    "v_mfma_f32_16x16x32_bf16 v[228:231], a[92:95], a[172:175], v[228:231]\n"
    "ds_read_b64_tr_b16 a[62:63], v8 offset:47104\n"
    "v_mfma_f32_16x16x32_bf16 v[228:231], a[88:91], a[168:171], v[228:231]\n"
    "v_mfma_f32_16x16x32_bf16 v[228:231], a[84:87], a[164:167], v[228:231]\n"
    "v_mfma_f32_16x16x32_bf16 v[228:231], a[80:83], a[160:163], v[228:231]\n"
    "v_mfma_f32_16x16x32_bf16 v[240:243], a[96:99], a[160:163], v[240:243]\n"
    "v_mfma_f32_16x16x32_bf16 v[240:243], a[100:103], a[164:167], v[240:243]\n"
    "v_mfma_f32_16x16x32_bf16 v[240:243], a[104:107], a[168:171], v[240:243]\n"
    "v_mfma_f32_16x16x32_bf16 v[240:243], a[108:111], a[172:175], v[240:243]\n"
    "v_mfma_f32_16x16x32_bf16 v[252:255], a[124:127], a[172:175], v[252:255]\n"
    "v_mfma_f32_16x16x32_bf16 v[252:255], a[120:123], a[168:171], v[252:255]\n"
    "v_mfma_f32_16x16x32_bf16 v[252:255], a[116:119], a[164:167], v[252:255]\n"
    "v_mfma_f32_16x16x32_bf16 v[252:255], a[112:115], a[160:163], v[252:255]\n"
    "s_waitcnt vmcnt(40) lgkmcnt(0)\n"
    "s_barrier\n"
    "v_mfma_f32_16x16x32_bf16 v[208:211], a[0:3], v[64:67], v[208:211]\n"
    "ds_read_b64_tr_b16 a[64:65], v5 offset:49152\n"
    "v_mfma_f32_16x16x32_bf16 v[208:211], a[4:7], v[68:71], v[208:211]\n"
    "ds_read_b64_tr_b16 a[66:67], v5 offset:51200\n"
    "v_mfma_f32_16x16x32_bf16 v[208:211], a[8:11], v[72:75], v[208:211]\n"
    "v_mfma_f32_16x16x32_bf16 v[208:211], a[12:15], v[76:79], v[208:211]\n"
    "ds_read_b64_tr_b16 a[68:69], v5 offset:53248\n"
    "v_mfma_f32_16x16x32_bf16 v[220:223], a[28:31], v[76:79], v[220:223]\n"
    "ds_read_b64_tr_b16 a[70:71], v5 offset:55296\n"
    "v_mfma_f32_16x16x32_bf16 v[220:223], a[24:27], v[72:75], v[220:223]\n"
    "ds_read_b64_tr_b16 a[72:73], v5 offset:57344\n"
    "v_mfma_f32_16x16x32_bf16 v[220:223], a[20:23], v[68:71], v[220:223]\n"
    "ds_read_b64_tr_b16 a[74:75], v5 offset:59392\n"
    "v_mfma_f32_16x16x32_bf16 v[220:223], a[16:19], v[64:67], v[220:223]\n"
    "ds_read_b64_tr_b16 a[76:77], v5 offset:61440\n"
    "v_mfma_f32_16x16x32_bf16 v[232:235], a[32:35], v[64:67], v[232:235]\n"
    "ds_read_b64_tr_b16 a[78:79], v5 offset:63488\n"
    "v_xor_b32 v5, 0x10000, v5\n"
    "v_mfma_f32_16x16x32_bf16 v[232:235], a[36:39], v[68:71], v[232:235]\n"
    "v_mfma_f32_16x16x32_bf16 v[232:235], a[40:43], v[72:75], v[232:235]\n"
    "ds_read_b64_tr_b16 a[80:81], v6 offset:49152\n"
    "v_mfma_f32_16x16x32_bf16 v[232:235], a[44:47], v[76:79], v[232:235]\n"
    "ds_read_b64_tr_b16 a[82:83], v6 offset:51200\n"
    "v_mfma_f32_16x16x32_bf16 v[244:247], a[60:63], v[76:79], v[244:247]\n"
    "ds_read_b64_tr_b16 a[84:85], v6 offset:53248\n"
    "v_mfma_f32_16x16x32_bf16 v[244:247], a[56:59], v[72:75], v[244:247]\n"
    "ds_read_b64_tr_b16 a[86:87], v6 offset:55296\n"
    "v_mfma_f32_16x16x32_bf16 v[244:247], a[52:55], v[68:71], v[244:247]\n"
    "ds_read_b64_tr_b16 a[88:89], v6 offset:57344\n"
    "v_mfma_f32_16x16x32_bf16 v[244:247], a[48:51], v[64:67], v[244:247]\n"
    "ds_read_b64_tr_b16 a[90:91], v6 offset:59392\n"
    "v_mfma_f32_16x16x32_bf16 v[248:251], a[48:51], v[80:83], v[248:251]\n"
    "ds_read_b64_tr_b16 a[92:93], v6 offset:61440\n"
    "v_mfma_f32_16x16x32_bf16 v[248:251], a[52:55], v[84:87], v[248:251]\n"
    "ds_read_b64_tr_b16 a[94:95], v6 offset:63488\n"
    "v_xor_b32 v6, 0x10000, v6\n"
    "v_mfma_f32_16x16x32_bf16 v[248:251], a[56:59], v[88:91], v[248:251]\n"
    "ds_read_b64_tr_b16 a[96:97], v7 offset:49152\n"
    "v_mfma_f32_16x16x32_bf16 v[248:251], a[60:63], v[92:95], v[248:251]\n"
    "ds_read_b64_tr_b16 a[98:99], v7 offset:51200\n"
    "v_mfma_f32_16x16x32_bf16 v[236:239], a[44:47], v[92:95], v[236:239]\n"
    "v_mfma_f32_16x16x32_bf16 v[236:239], a[40:43], v[88:91], v[236:239]\n"
    "ds_read_b64_tr_b16 a[100:101], v7 offset:53248\n"
    "v_mfma_f32_16x16x32_bf16 v[236:239], a[36:39], v[84:87], v[236:239]\n"
    "ds_read_b64_tr_b16 a[102:103], v7 offset:55296\n"
    "v_mfma_f32_16x16x32_bf16 v[236:239], a[32:35], v[80:83], v[236:239]\n"
    "ds_read_b64_tr_b16 a[104:105], v7 offset:57344\n"
    "v_mfma_f32_16x16x32_bf16 v[224:227], a[16:19], v[80:83], v[224:227]\n"
    "ds_read_b64_tr_b16 a[106:107], v7 offset:59392\n"
    "v_mfma_f32_16x16x32_bf16 v[224:227], a[20:23], v[84:87], v[224:227]\n"
    "ds_read_b64_tr_b16 a[108:109], v7 offset:61440\n"
    "v_mfma_f32_16x16x32_bf16 v[224:227], a[24:27], v[88:91], v[224:227]\n"
    "ds_read_b64_tr_b16 a[110:111], v7 offset:63488\n"
    "v_xor_b32 v7, 0x10000, v7\n"
    "v_mfma_f32_16x16x32_bf16 v[224:227], a[28:31], v[92:95], v[224:227]\n"
    "v_mfma_f32_16x16x32_bf16 v[212:215], a[12:15], v[92:95], v[212:215]\n"
    "ds_read_b64_tr_b16 a[112:113], v8 offset:49152\n"
    "v_mfma_f32_16x16x32_bf16 v[212:215], a[8:11], v[88:91], v[212:215]\n"
    "ds_read_b64_tr_b16 a[114:115], v8 offset:51200\n"
    "v_mfma_f32_16x16x32_bf16 v[212:215], a[4:7], v[84:87], v[212:215]\n"
    "ds_read_b64_tr_b16 a[116:117], v8 offset:53248\n"
    "v_mfma_f32_16x16x32_bf16 v[212:215], a[0:3], v[80:83], v[212:215]\n"
    "ds_read_b64_tr_b16 a[118:119], v8 offset:55296\n"
    "v_mfma_f32_16x16x32_bf16 v[216:219], a[0:3], v[96:99], v[216:219]\n"
    "ds_read_b64_tr_b16 a[120:121], v8 offset:57344\n"
    "v_mfma_f32_16x16x32_bf16 v[216:219], a[4:7], v[100:103], v[216:219]\n"
    "ds_read_b64_tr_b16 a[122:123], v8 offset:59392\n"
    "v_mfma_f32_16x16x32_bf16 v[216:219], a[8:11], v[104:107], v[216:219]\n"
    "v_mfma_f32_16x16x32_bf16 v[216:219], a[12:15], v[108:111], v[216:219]\n"
    "ds_read_b64_tr_b16 a[124:125], v8 offset:61440\n"
    "v_mfma_f32_16x16x32_bf16 v[228:231], a[28:31], v[108:111], v[228:231]\n"
    "ds_read_b64_tr_b16 a[126:127], v8 offset:63488\n"
    "v_xor_b32 v8, 0x10000, v8\n"
    "v_mfma_f32_16x16x32_bf16 v[228:231], a[24:27], v[104:107], v[228:231]\n"
    "v_mfma_f32_16x16x32_bf16 v[228:231], a[20:23], v[100:103], v[228:231]\n"
    "v_mfma_f32_16x16x32_bf16 v[228:231], a[16:19], v[96:99], v[228:231]\n"
    "v_mfma_f32_16x16x32_bf16 v[240:243], a[32:35], v[96:99], v[240:243]\n"
    "v_mfma_f32_16x16x32_bf16 v[240:243], a[36:39], v[100:103], v[240:243]\n"
    "v_mfma_f32_16x16x32_bf16 v[240:243], a[40:43], v[104:107], v[240:243]\n"
    "v_mfma_f32_16x16x32_bf16 v[240:243], a[44:47], v[108:111], v[240:243]\n"
    "v_mfma_f32_16x16x32_bf16 v[252:255], a[60:63], v[108:111], v[252:255]\n"
    "v_mfma_f32_16x16x32_bf16 v[252:255], a[56:59], v[104:107], v[252:255]\n"
    "v_mfma_f32_16x16x32_bf16 v[252:255], a[52:55], v[100:103], v[252:255]\n"
    "v_mfma_f32_16x16x32_bf16 v[252:255], a[48:51], v[96:99], v[252:255]\n"
    "s_waitcnt lgkmcnt(0)\n"
    "s_waitcnt vmcnt(0)\n"
    "s_xor_b32 s57, s56, 32\n"
    "v_mov_b64 v[62:63], s[62:63]\n"
    "s_mov_b64 exec, 1\n"
    "buffer_store_dwordx2 v[62:63], off, s[32:35], s57 sc1\n"
    "s_mov_b64 exec, -1\n"
    "s_and_b32 s46, %5, 1\n"
    "s_mul_i32 s46, s46, 90112\n"
    "s_add_u32 s55, s55, 45056\n"
    "s_sub_u32 s55, s55, s46\n"
    "v_mfma_f32_16x16x32_bf16 v[208:211], a[64:67], a[176:179], v[208:211]\n"
    "ds_read_b64_tr_b16 a[0:1], v5 offset:0\n"
    "v_readlane_b32 s38, v43, s52\n"
    "v_mfma_f32_16x16x32_bf16 v[208:211], a[68:71], a[180:183], v[208:211]\n"
    "s_add_u32 s52, s52, 1\n"
    "s_add_u32 s48, s38, 0\n"
    "s_add_u32 s49, s38, 11264\n"
    "s_add_u32 s50, s38, 22528\n"
    "s_add_u32 s51, s38, 33792\n"
    "ds_read_b64_tr_b16 a[2:3], v5 offset:2048\n"
    "buffer_load_dwordx4 a[128:131], v15, s[24:27], s48 offen offset:0 sc1\n"
    "v_mfma_f32_16x16x32_bf16 v[208:211], a[72:75], a[184:187], v[208:211]\n"
    "v_mfma_f32_16x16x32_bf16 v[208:211], a[76:79], a[188:191], v[208:211]\n"
    "ds_read_b64_tr_b16 a[4:5], v5 offset:4096\n"
    "v_mfma_f32_16x16x32_bf16 v[220:223], a[92:95], a[188:191], v[220:223]\n"
    "ds_read_b64_tr_b16 a[6:7], v5 offset:6144\n"
    "buffer_load_dwordx4 a[132:135], v15, s[24:27], s49 offen offset:0 sc1\n"
    "v_mfma_f32_16x16x32_bf16 v[220:223], a[88:91], a[184:187], v[220:223]\n"
    "ds_read_b64_tr_b16 a[8:9], v5 offset:8192\n"
    "v_mfma_f32_16x16x32_bf16 v[220:223], a[84:87], a[180:183], v[220:223]\n"
    "ds_read_b64_tr_b16 a[10:11], v5 offset:10240\n"
    "v_mfma_f32_16x16x32_bf16 v[220:223], a[80:83], a[176:179], v[220:223]\n"
    "ds_read_b64_tr_b16 a[12:13], v5 offset:12288\n"
    "v_mfma_f32_16x16x32_bf16 v[232:235], a[96:99], a[176:179], v[232:235]\n"
    "ds_read_b64_tr_b16 a[14:15], v5 offset:14336\n"
    "buffer_load_dwordx4 a[136:139], v15, s[24:27], s50 offen offset:0 sc1\n"
    "v_mfma_f32_16x16x32_bf16 v[232:235], a[100:103], a[180:183], v[232:235]\n"
    "v_mfma_f32_16x16x32_bf16 v[232:235], a[104:107], a[184:187], v[232:235]\n"
    "ds_read_b64_tr_b16 a[16:17], v6 offset:0\n"
    "v_mfma_f32_16x16x32_bf16 v[232:235], a[108:111], a[188:191], v[232:235]\n"
    "ds_read_b64_tr_b16 a[18:19], v6 offset:2048\n"
    "v_mfma_f32_16x16x32_bf16 v[244:247], a[124:127], a[188:191], v[244:247]\n"
    "ds_read_b64_tr_b16 a[20:21], v6 offset:4096\n"
    "buffer_load_dwordx4 a[140:143], v15, s[24:27], s51 offen offset:0 sc1\n"
    "v_mfma_f32_16x16x32_bf16 v[244:247], a[120:123], a[184:187], v[244:247]\n"
    "ds_read_b64_tr_b16 a[22:23], v6 offset:6144\n"
    "v_mfma_f32_16x16x32_bf16 v[244:247], a[116:119], a[180:183], v[244:247]\n"
    "ds_read_b64_tr_b16 a[24:25], v6 offset:8192\n"
    "v_mfma_f32_16x16x32_bf16 v[244:247], a[112:115], a[176:179], v[244:247]\n"
    "ds_read_b64_tr_b16 a[26:27], v6 offset:10240\n"
    "v_mfma_f32_16x16x32_bf16 v[248:251], a[112:115], a[192:195], v[248:251]\n"
    "ds_read_b64_tr_b16 a[28:29], v6 offset:12288\n"
    "buffer_load_dwordx4 a[144:147], v15, s[24:27], s48 offen offset:1024 sc1\n"
    "v_mfma_f32_16x16x32_bf16 v[248:251], a[116:119], a[196:199], v[248:251]\n"
    "ds_read_b64_tr_b16 a[30:31], v6 offset:14336\n"
    "v_mfma_f32_16x16x32_bf16 v[248:251], a[120:123], a[200:203], v[248:251]\n"
    "ds_read_b64_tr_b16 a[32:33], v7 offset:0\n"
    "v_mfma_f32_16x16x32_bf16 v[248:251], a[124:127], a[204:207], v[248:251]\n"
    "ds_read_b64_tr_b16 a[34:35], v7 offset:2048\n"
    "buffer_load_dwordx4 a[148:151], v15, s[24:27], s49 offen offset:1024 sc1\n"
    "v_mfma_f32_16x16x32_bf16 v[236:239], a[108:111], a[204:207], v[236:239]\n"
    "v_mfma_f32_16x16x32_bf16 v[236:239], a[104:107], a[200:203], v[236:239]\n"
    "ds_read_b64_tr_b16 a[36:37], v7 offset:4096\n"
    "v_mfma_f32_16x16x32_bf16 v[236:239], a[100:103], a[196:199], v[236:239]\n"
    "ds_read_b64_tr_b16 a[38:39], v7 offset:6144\n"
    "v_mfma_f32_16x16x32_bf16 v[236:239], a[96:99], a[192:195], v[236:239]\n"
    "ds_read_b64_tr_b16 a[40:41], v7 offset:8192\n"
    "buffer_load_dwordx4 a[152:155], v15, s[24:27], s50 offen offset:1024 sc1\n"
    "v_mfma_f32_16x16x32_bf16 v[224:227], a[80:83], a[192:195], v[224:227]\n"
    "ds_read_b64_tr_b16 a[42:43], v7 offset:10240\n"
    "v_mfma_f32_16x16x32_bf16 v[224:227], a[84:87], a[196:199], v[224:227]\n"
    "ds_read_b64_tr_b16 a[44:45], v7 offset:12288\n"
    "v_mfma_f32_16x16x32_bf16 v[224:227], a[88:91], a[200:203], v[224:227]\n"
    "ds_read_b64_tr_b16 a[46:47], v7 offset:14336\n"
    "v_mfma_f32_16x16x32_bf16 v[224:227], a[92:95], a[204:207], v[224:227]\n"
    "buffer_load_dwordx4 a[156:159], v15, s[24:27], s51 offen offset:1024 sc1\n"
    "v_mfma_f32_16x16x32_bf16 v[212:215], a[76:79], a[204:207], v[212:215]\n"
    "ds_read_b64_tr_b16 a[48:49], v8 offset:0\n"
    "v_mfma_f32_16x16x32_bf16 v[212:215], a[72:75], a[200:203], v[212:215]\n"
    "ds_read_b64_tr_b16 a[50:51], v8 offset:2048\n"
    "v_mfma_f32_16x16x32_bf16 v[212:215], a[68:71], a[196:199], v[212:215]\n"
    "ds_read_b64_tr_b16 a[52:53], v8 offset:4096\n"
    "v_mfma_f32_16x16x32_bf16 v[212:215], a[64:67], a[192:195], v[212:215]\n"
    "ds_read_b64_tr_b16 a[54:55], v8 offset:6144\n"
    "buffer_load_dwordx4 a[160:163], v15, s[24:27], s48 offen offset:2048 sc1\n"
    "v_mfma_f32_16x16x32_bf16 v[216:219], a[64:67], a[208:211], v[216:219]\n"
    "ds_read_b64_tr_b16 a[56:57], v8 offset:8192\n"
    "v_mfma_f32_16x16x32_bf16 v[216:219], a[68:71], a[212:215], v[216:219]\n"
    "ds_read_b64_tr_b16 a[58:59], v8 offset:10240\n"
    "v_mfma_f32_16x16x32_bf16 v[216:219], a[72:75], a[216:219], v[216:219]\n"
    "buffer_load_dwordx4 a[164:167], v15, s[24:27], s49 offen offset:2048 sc1\n"
    "v_mfma_f32_16x16x32_bf16 v[216:219], a[76:79], a[220:223], v[216:219]\n"
    "ds_read_b64_tr_b16 a[60:61], v8 offset:12288\n"
    "v_mfma_f32_16x16x32_bf16 v[228:231], a[92:95], a[220:223], v[228:231]\n"
    "ds_read_b64_tr_b16 a[62:63], v8 offset:14336\n"
    "v_mfma_f32_16x16x32_bf16 v[228:231], a[88:91], a[216:219], v[228:231]\n"
    "v_mfma_f32_16x16x32_bf16 v[228:231], a[84:87], a[212:215], v[228:231]\n"
    "buffer_load_dwordx4 a[168:171], v15, s[24:27], s50 offen offset:2048 sc1\n"
    "v_mfma_f32_16x16x32_bf16 v[228:231], a[80:83], a[208:211], v[228:231]\n"
    "v_mfma_f32_16x16x32_bf16 v[240:243], a[96:99], a[208:211], v[240:243]\n"
    "v_mfma_f32_16x16x32_bf16 v[240:243], a[100:103], a[212:215], v[240:243]\n"
    "v_mfma_f32_16x16x32_bf16 v[240:243], a[104:107], a[216:219], v[240:243]\n"
    "buffer_load_dwordx4 a[172:175], v15, s[24:27], s51 offen offset:2048 sc1\n"
    "v_mfma_f32_16x16x32_bf16 v[240:243], a[108:111], a[220:223], v[240:243]\n"
    "v_mfma_f32_16x16x32_bf16 v[252:255], a[124:127], a[220:223], v[252:255]\n"
    "v_mfma_f32_16x16x32_bf16 v[252:255], a[120:123], a[216:219], v[252:255]\n"
    "v_mfma_f32_16x16x32_bf16 v[252:255], a[116:119], a[212:215], v[252:255]\n"
    "v_mfma_f32_16x16x32_bf16 v[252:255], a[112:115], a[208:211], v[252:255]\n"
    "s_waitcnt vmcnt(27) lgkmcnt(0)\n"
    "s_barrier\n"
    "v_mfma_f32_16x16x32_bf16 v[208:211], a[0:3], v[112:115], v[208:211]\n"
    "ds_read_b64_tr_b16 a[64:65], v5 offset:16384\n"
    "v_readlane_b32 s38, v43, s52\n"
    "v_mfma_f32_16x16x32_bf16 v[208:211], a[4:7], v[116:119], v[208:211]\n"
    "s_add_u32 s52, s52, 1\n"
    "s_add_u32 s48, s38, 0\n"
    "s_add_u32 s49, s38, 11264\n"
    "s_add_u32 s50, s38, 22528\n"
    "s_add_u32 s51, s38, 33792\n"
    "ds_read_b64_tr_b16 a[66:67], v5 offset:18432\n"
    "buffer_load_dwordx4 v[64:67], v15, s[24:27], s48 offen offset:0 sc1\n"
    "v_mfma_f32_16x16x32_bf16 v[208:211], a[8:11], v[120:123], v[208:211]\n"
    "v_mfma_f32_16x16x32_bf16 v[208:211], a[12:15], v[124:127], v[208:211]\n"
    "ds_read_b64_tr_b16 a[68:69], v5 offset:20480\n"
    "v_mfma_f32_16x16x32_bf16 v[220:223], a[28:31], v[124:127], v[220:223]\n"
    "ds_read_b64_tr_b16 a[70:71], v5 offset:22528\n"
    "buffer_load_dwordx4 v[68:71], v15, s[24:27], s49 offen offset:0 sc1\n"
    "v_mfma_f32_16x16x32_bf16 v[220:223], a[24:27], v[120:123], v[220:223]\n"
    "ds_read_b64_tr_b16 a[72:73], v5 offset:24576\n"
    "v_mfma_f32_16x16x32_bf16 v[220:223], a[20:23], v[116:119], v[220:223]\n"
    "ds_read_b64_tr_b16 a[74:75], v5 offset:26624\n"
    "v_mfma_f32_16x16x32_bf16 v[220:223], a[16:19], v[112:115], v[220:223]\n"
    "ds_read_b64_tr_b16 a[76:77], v5 offset:28672\n"
    "v_mfma_f32_16x16x32_bf16 v[232:235], a[32:35], v[112:115], v[232:235]\n"
    "ds_read_b64_tr_b16 a[78:79], v5 offset:30720\n"
    "buffer_load_dwordx4 v[72:75], v15, s[24:27], s50 offen offset:0 sc1\n"
    "v_mfma_f32_16x16x32_bf16 v[232:235], a[36:39], v[116:119], v[232:235]\n"
    "v_mfma_f32_16x16x32_bf16 v[232:235], a[40:43], v[120:123], v[232:235]\n"
    "ds_read_b64_tr_b16 a[80:81], v6 offset:16384\n"
    "v_mfma_f32_16x16x32_bf16 v[232:235], a[44:47], v[124:127], v[232:235]\n"
    "ds_read_b64_tr_b16 a[82:83], v6 offset:18432\n"
    "v_mfma_f32_16x16x32_bf16 v[244:247], a[60:63], v[124:127], v[244:247]\n"
    "ds_read_b64_tr_b16 a[84:85], v6 offset:20480\n"
    "buffer_load_dwordx4 v[76:79], v15, s[24:27], s51 offen offset:0 sc1\n"
    "v_mfma_f32_16x16x32_bf16 v[244:247], a[56:59], v[120:123], v[244:247]\n"
    "ds_read_b64_tr_b16 a[86:87], v6 offset:22528\n"
    "v_mfma_f32_16x16x32_bf16 v[244:247], a[52:55], v[116:119], v[244:247]\n"
    "ds_read_b64_tr_b16 a[88:89], v6 offset:24576\n"
    "v_mfma_f32_16x16x32_bf16 v[244:247], a[48:51], v[112:115], v[244:247]\n"
    "s_waitcnt vmcnt(27)\n"
    "ds_read_b64_tr_b16 a[90:91], v6 offset:26624\n"
    "v_mfma_f32_16x16x32_bf16 v[248:251], a[48:51], v[128:131], v[248:251]\n"
    "ds_read_b64_tr_b16 a[92:93], v6 offset:28672\n"
    "buffer_load_dwordx4 v[80:83], v15, s[24:27], s48 offen offset:1024 sc1\n"
    "v_mfma_f32_16x16x32_bf16 v[248:251], a[52:55], v[132:135], v[248:251]\n"
    "ds_read_b64_tr_b16 a[94:95], v6 offset:30720\n"
    "v_mfma_f32_16x16x32_bf16 v[248:251], a[56:59], v[136:139], v[248:251]\n"
    "ds_read_b64_tr_b16 a[96:97], v7 offset:16384\n"
    "v_mfma_f32_16x16x32_bf16 v[248:251], a[60:63], v[140:143], v[248:251]\n"
    "ds_read_b64_tr_b16 a[98:99], v7 offset:18432\n"
    "buffer_load_dwordx4 v[84:87], v15, s[24:27], s49 offen offset:1024 sc1\n"
    "v_mfma_f32_16x16x32_bf16 v[236:239], a[44:47], v[140:143], v[236:239]\n"
    "v_mfma_f32_16x16x32_bf16 v[236:239], a[40:43], v[136:139], v[236:239]\n"
    "ds_read_b64_tr_b16 a[100:101], v7 offset:20480\n"
    "v_mfma_f32_16x16x32_bf16 v[236:239], a[36:39], v[132:135], v[236:239]\n"
    "ds_read_b64_tr_b16 a[102:103], v7 offset:22528\n"
    "v_mfma_f32_16x16x32_bf16 v[236:239], a[32:35], v[128:131], v[236:239]\n"
    "ds_read_b64_tr_b16 a[104:105], v7 offset:24576\n"
    "buffer_load_dwordx4 v[88:91], v15, s[24:27], s50 offen offset:1024 sc1\n"
    "v_mfma_f32_16x16x32_bf16 v[224:227], a[16:19], v[128:131], v[224:227]\n"
    "ds_read_b64_tr_b16 a[106:107], v7 offset:26624\n"
    "v_mfma_f32_16x16x32_bf16 v[224:227], a[20:23], v[132:135], v[224:227]\n"
    "ds_read_b64_tr_b16 a[108:109], v7 offset:28672\n"
    "v_mfma_f32_16x16x32_bf16 v[224:227], a[24:27], v[136:139], v[224:227]\n"
    "ds_read_b64_tr_b16 a[110:111], v7 offset:30720\n"
    "v_mfma_f32_16x16x32_bf16 v[224:227], a[28:31], v[140:143], v[224:227]\n"
    "buffer_load_dwordx4 v[92:95], v15, s[24:27], s51 offen offset:1024 sc1\n"
    "v_mfma_f32_16x16x32_bf16 v[212:215], a[12:15], v[140:143], v[212:215]\n"
    "ds_read_b64_tr_b16 a[112:113], v8 offset:16384\n"
    "v_mfma_f32_16x16x32_bf16 v[212:215], a[8:11], v[136:139], v[212:215]\n"
    "ds_read_b64_tr_b16 a[114:115], v8 offset:18432\n"
    "v_mfma_f32_16x16x32_bf16 v[212:215], a[4:7], v[132:135], v[212:215]\n"
    "ds_read_b64_tr_b16 a[116:117], v8 offset:20480\n"
    "v_mfma_f32_16x16x32_bf16 v[212:215], a[0:3], v[128:131], v[212:215]\n"
    "s_waitcnt vmcnt(27)\n"
    "ds_read_b64_tr_b16 a[118:119], v8 offset:22528\n"
    "buffer_load_dwordx4 v[96:99], v15, s[24:27], s48 offen offset:2048 sc1\n"
    "v_mfma_f32_16x16x32_bf16 v[216:219], a[0:3], v[144:147], v[216:219]\n"
    "ds_read_b64_tr_b16 a[120:121], v8 offset:24576\n"
    "v_mfma_f32_16x16x32_bf16 v[216:219], a[4:7], v[148:151], v[216:219]\n"
    "ds_read_b64_tr_b16 a[122:123], v8 offset:26624\n"
    "v_mfma_f32_16x16x32_bf16 v[216:219], a[8:11], v[152:155], v[216:219]\n"
    "buffer_load_dwordx4 v[100:103], v15, s[24:27], s49 offen offset:2048 sc1\n"
    "v_mfma_f32_16x16x32_bf16 v[216:219], a[12:15], v[156:159], v[216:219]\n"
    "ds_read_b64_tr_b16 a[124:125], v8 offset:28672\n"
    "v_mfma_f32_16x16x32_bf16 v[228:231], a[28:31], v[156:159], v[228:231]\n"
    "ds_read_b64_tr_b16 a[126:127], v8 offset:30720\n"
    "v_mfma_f32_16x16x32_bf16 v[228:231], a[24:27], v[152:155], v[228:231]\n"
    "v_mfma_f32_16x16x32_bf16 v[228:231], a[20:23], v[148:151], v[228:231]\n"
    "buffer_load_dwordx4 v[104:107], v15, s[24:27], s50 offen offset:2048 sc1\n"
    "v_mfma_f32_16x16x32_bf16 v[228:231], a[16:19], v[144:147], v[228:231]\n"
    "v_mfma_f32_16x16x32_bf16 v[240:243], a[32:35], v[144:147], v[240:243]\n"
    "v_mfma_f32_16x16x32_bf16 v[240:243], a[36:39], v[148:151], v[240:243]\n"
    "v_mfma_f32_16x16x32_bf16 v[240:243], a[40:43], v[152:155], v[240:243]\n"
    "buffer_load_dwordx4 v[108:111], v15, s[24:27], s51 offen offset:2048 sc1\n"
    "v_mfma_f32_16x16x32_bf16 v[240:243], a[44:47], v[156:159], v[240:243]\n"
    "v_mfma_f32_16x16x32_bf16 v[252:255], a[60:63], v[156:159], v[252:255]\n"
    "v_mfma_f32_16x16x32_bf16 v[252:255], a[56:59], v[152:155], v[252:255]\n"
    "v_mfma_f32_16x16x32_bf16 v[252:255], a[52:55], v[148:151], v[252:255]\n"
    "v_mfma_f32_16x16x32_bf16 v[252:255], a[48:51], v[144:147], v[252:255]\n"
    "buffer_load_dwordx2 v[40:41], off, s[32:35], s56 sc1\n"
    "s_waitcnt vmcnt(21) lgkmcnt(0)\n"
    "v_mfma_f32_16x16x32_bf16 v[208:211], a[64:67], a[128:131], v[208:211]\n"
    "ds_read_b64_tr_b16 a[0:1], v5 offset:32768\n"
    "v_readlane_b32 s38, v43, s52\n"
    "v_mfma_f32_16x16x32_bf16 v[208:211], a[68:71], a[132:135], v[208:211]\n"
    "s_add_u32 s52, s52, 1\n"
    "s_add_u32 s48, s38, 0\n"
    "s_add_u32 s49, s38, 11264\n"
    "s_add_u32 s50, s38, 22528\n"
    "s_add_u32 s51, s38, 33792\n"
    "ds_read_b64_tr_b16 a[2:3], v5 offset:34816\n"
    "buffer_load_dwordx4 a[176:179], v15, s[24:27], s48 offen offset:0 sc1\n"
    "v_mfma_f32_16x16x32_bf16 v[208:211], a[72:75], a[136:139], v[208:211]\n"
    "v_mfma_f32_16x16x32_bf16 v[208:211], a[76:79], a[140:143], v[208:211]\n"
    "ds_read_b64_tr_b16 a[4:5], v5 offset:36864\n"
    "v_mfma_f32_16x16x32_bf16 v[220:223], a[92:95], a[140:143], v[220:223]\n"
    "ds_read_b64_tr_b16 a[6:7], v5 offset:38912\n"
    "buffer_load_dwordx4 a[180:183], v15, s[24:27], s49 offen offset:0 sc1\n"
    "v_mfma_f32_16x16x32_bf16 v[220:223], a[88:91], a[136:139], v[220:223]\n"
    "ds_read_b64_tr_b16 a[8:9], v5 offset:40960\n"
    "v_mfma_f32_16x16x32_bf16 v[220:223], a[84:87], a[132:135], v[220:223]\n"
    "ds_read_b64_tr_b16 a[10:11], v5 offset:43008\n"
    "v_mfma_f32_16x16x32_bf16 v[220:223], a[80:83], a[128:131], v[220:223]\n"
    "ds_read_b64_tr_b16 a[12:13], v5 offset:45056\n"
    "v_mfma_f32_16x16x32_bf16 v[232:235], a[96:99], a[128:131], v[232:235]\n"
    "ds_read_b64_tr_b16 a[14:15], v5 offset:47104\n"
    "buffer_load_dwordx4 a[184:187], v15, s[24:27], s50 offen offset:0 sc1\n"
    "v_mfma_f32_16x16x32_bf16 v[232:235], a[100:103], a[132:135], v[232:235]\n"
    "v_mfma_f32_16x16x32_bf16 v[232:235], a[104:107], a[136:139], v[232:235]\n"
    "ds_read_b64_tr_b16 a[16:17], v6 offset:32768\n"
    "v_mfma_f32_16x16x32_bf16 v[232:235], a[108:111], a[140:143], v[232:235]\n"
    "ds_read_b64_tr_b16 a[18:19], v6 offset:34816\n"
    "v_mfma_f32_16x16x32_bf16 v[244:247], a[124:127], a[140:143], v[244:247]\n"
    "ds_read_b64_tr_b16 a[20:21], v6 offset:36864\n"
    "buffer_load_dwordx4 a[188:191], v15, s[24:27], s51 offen offset:0 sc1\n"
    "v_mfma_f32_16x16x32_bf16 v[244:247], a[120:123], a[136:139], v[244:247]\n"
    "ds_read_b64_tr_b16 a[22:23], v6 offset:38912\n"
    "v_mfma_f32_16x16x32_bf16 v[244:247], a[116:119], a[132:135], v[244:247]\n"
    "ds_read_b64_tr_b16 a[24:25], v6 offset:40960\n"
    "v_mfma_f32_16x16x32_bf16 v[244:247], a[112:115], a[128:131], v[244:247]\n"
    "s_waitcnt vmcnt(21)\n"
    "ds_read_b64_tr_b16 a[26:27], v6 offset:43008\n"
    "v_mfma_f32_16x16x32_bf16 v[248:251], a[112:115], a[144:147], v[248:251]\n"
    "ds_read_b64_tr_b16 a[28:29], v6 offset:45056\n"
    "buffer_load_dwordx4 a[192:195], v15, s[24:27], s48 offen offset:1024 sc1\n"
    "v_mfma_f32_16x16x32_bf16 v[248:251], a[116:119], a[148:151], v[248:251]\n"
    "ds_read_b64_tr_b16 a[30:31], v6 offset:47104\n"
    "v_mfma_f32_16x16x32_bf16 v[248:251], a[120:123], a[152:155], v[248:251]\n"
    "ds_read_b64_tr_b16 a[32:33], v7 offset:32768\n"
    "v_mfma_f32_16x16x32_bf16 v[248:251], a[124:127], a[156:159], v[248:251]\n"
    "ds_read_b64_tr_b16 a[34:35], v7 offset:34816\n"
    "buffer_load_dwordx4 a[196:199], v15, s[24:27], s49 offen offset:1024 sc1\n"
    "v_mfma_f32_16x16x32_bf16 v[236:239], a[108:111], a[156:159], v[236:239]\n"
    "v_mfma_f32_16x16x32_bf16 v[236:239], a[104:107], a[152:155], v[236:239]\n"
    "ds_read_b64_tr_b16 a[36:37], v7 offset:36864\n"
    "v_mfma_f32_16x16x32_bf16 v[236:239], a[100:103], a[148:151], v[236:239]\n"
    "ds_read_b64_tr_b16 a[38:39], v7 offset:38912\n"
    "v_mfma_f32_16x16x32_bf16 v[236:239], a[96:99], a[144:147], v[236:239]\n"
    "ds_read_b64_tr_b16 a[40:41], v7 offset:40960\n"
    "buffer_load_dwordx4 a[200:203], v15, s[24:27], s50 offen offset:1024 sc1\n"
    "v_mfma_f32_16x16x32_bf16 v[224:227], a[80:83], a[144:147], v[224:227]\n"
    "ds_read_b64_tr_b16 a[42:43], v7 offset:43008\n"
    "v_mfma_f32_16x16x32_bf16 v[224:227], a[84:87], a[148:151], v[224:227]\n"
    "ds_read_b64_tr_b16 a[44:45], v7 offset:45056\n"
    "v_mfma_f32_16x16x32_bf16 v[224:227], a[88:91], a[152:155], v[224:227]\n"
    "ds_read_b64_tr_b16 a[46:47], v7 offset:47104\n"
    "v_mfma_f32_16x16x32_bf16 v[224:227], a[92:95], a[156:159], v[224:227]\n"
    "buffer_load_dwordx4 a[204:207], v15, s[24:27], s51 offen offset:1024 sc1\n"
    "v_mfma_f32_16x16x32_bf16 v[212:215], a[76:79], a[156:159], v[212:215]\n"
    "ds_read_b64_tr_b16 a[48:49], v8 offset:32768\n"
    "v_mfma_f32_16x16x32_bf16 v[212:215], a[72:75], a[152:155], v[212:215]\n"
    "ds_read_b64_tr_b16 a[50:51], v8 offset:34816\n"
    "v_mfma_f32_16x16x32_bf16 v[212:215], a[68:71], a[148:151], v[212:215]\n"
    "ds_read_b64_tr_b16 a[52:53], v8 offset:36864\n"
    "v_mfma_f32_16x16x32_bf16 v[212:215], a[64:67], a[144:147], v[212:215]\n"
    "s_waitcnt vmcnt(21)\n"
    "ds_read_b64_tr_b16 a[54:55], v8 offset:38912\n"
    "buffer_load_dwordx4 a[208:211], v15, s[24:27], s48 offen offset:2048 sc1\n"
    "v_mfma_f32_16x16x32_bf16 v[216:219], a[64:67], a[160:163], v[216:219]\n"
    "ds_read_b64_tr_b16 a[56:57], v8 offset:40960\n"
    "v_mfma_f32_16x16x32_bf16 v[216:219], a[68:71], a[164:167], v[216:219]\n"
    "ds_read_b64_tr_b16 a[58:59], v8 offset:43008\n"
    "v_mfma_f32_16x16x32_bf16 v[216:219], a[72:75], a[168:171], v[216:219]\n"
    "buffer_load_dwordx4 a[212:215], v15, s[24:27], s49 offen offset:2048 sc1\n"
    "v_mfma_f32_16x16x32_bf16 v[216:219], a[76:79], a[172:175], v[216:219]\n"
    "ds_read_b64_tr_b16 a[60:61], v8 offset:45056\n"
    "v_mfma_f32_16x16x32_bf16 v[228:231], a[92:95], a[172:175], v[228:231]\n"
    "ds_read_b64_tr_b16 a[62:63], v8 offset:47104\n"
    "v_mfma_f32_16x16x32_bf16 v[228:231], a[88:91], a[168:171], v[228:231]\n"
    "v_mfma_f32_16x16x32_bf16 v[228:231], a[84:87], a[164:167], v[228:231]\n"
    "buffer_load_dwordx4 a[216:219], v15, s[24:27], s50 offen offset:2048 sc1\n"
    "v_mfma_f32_16x16x32_bf16 v[228:231], a[80:83], a[160:163], v[228:231]\n"
    "v_mfma_f32_16x16x32_bf16 v[240:243], a[96:99], a[160:163], v[240:243]\n"
    "s_waitcnt vmcnt(11)\n"
    "s_mov_b32 s57, 0x40000\n"
    ".L_fspCp_%=:\n"
    "v_cmp_ne_u64 vcc, s[62:63], v[40:41]\n"
    "s_cbranch_vccz .L_fokCp_%=\n"
    "s_sleep 4\n"
    "buffer_load_dwordx2 v[40:41], off, s[32:35], s56 sc1\n"
    "s_sub_u32 s57, s57, 1\n"
    "s_waitcnt vmcnt(0)\n"
    "s_cmp_lg_u32 s57, 0\n"
    "s_cbranch_scc1 .L_fspCp_%=\n"
    ".L_fokCp_%=:\n"
    "s_add_u32 s46, s55, 0\n"
    "buffer_load_dwordx4 v[172:175], v37, s[24:27], s46 offen offset:0 sc1\n"
    "v_mfma_f32_16x16x32_bf16 v[240:243], a[100:103], a[164:167], v[240:243]\n"
    "buffer_load_dwordx4 v[196:199], v37, s[24:27], s46 offen offset:1024 sc1\n"
    "v_mfma_f32_16x16x32_bf16 v[240:243], a[104:107], a[168:171], v[240:243]\n"
    "buffer_load_dwordx4 a[220:223], v15, s[24:27], s51 offen offset:2048 sc1\n"
    "s_add_u32 s46, s55, 2048\n"
    "buffer_load_dwordx4 v[176:179], v37, s[24:27], s46 offen offset:0 sc1\n"
    "v_mfma_f32_16x16x32_bf16 v[240:243], a[108:111], a[172:175], v[240:243]\n"
    "v_mfma_f32_16x16x32_bf16 v[252:255], a[124:127], a[172:175], v[252:255]\n"
    "buffer_load_dwordx4 v[200:203], v37, s[24:27], s46 offen offset:1024 sc1\n"
    "v_mfma_f32_16x16x32_bf16 v[252:255], a[120:123], a[168:171], v[252:255]\n"
    "s_add_u32 s46, s55, 4096\n"
    "buffer_load_dwordx4 v[180:183], v37, s[24:27], s46 offen offset:0 sc1\n"
    "v_mfma_f32_16x16x32_bf16 v[252:255], a[116:119], a[164:167], v[252:255]\n"
    "buffer_load_dwordx4 v[204:207], v37, s[24:27], s46 offen offset:1024 sc1\n"
    "v_mfma_f32_16x16x32_bf16 v[252:255], a[112:115], a[160:163], v[252:255]\n"
    "s_waitcnt vmcnt(27) lgkmcnt(0)\n"
    "s_barrier\n"
    "v_mfma_f32_16x16x32_bf16 v[208:211], a[0:3], v[64:67], v[208:211]\n"
    "ds_read_b64_tr_b16 a[64:65], v5 offset:49152\n"
    "v_and_b32 v44, 63, %7\n"
    "v_mfma_f32_16x16x32_bf16 v[208:211], a[4:7], v[68:71], v[208:211]\n"
    "ds_read_b64_tr_b16 a[66:67], v5 offset:51200\n"
    "v_lshrrev_b32 v45, 4, v44\n"
    "v_mfma_f32_16x16x32_bf16 v[208:211], a[8:11], v[72:75], v[208:211]\n"
    "v_and_b32 v46, 15, v44\n"
    "v_bfe_u32 v47, v44, 2, 2\n"
    "v_mfma_f32_16x16x32_bf16 v[208:211], a[12:15], v[76:79], v[208:211]\n"
    "ds_read_b64_tr_b16 a[68:69], v5 offset:53248\n"
    "v_and_b32 v48, 3, v44\n"
    "v_mfma_f32_16x16x32_bf16 v[220:223], a[28:31], v[76:79], v[220:223]\n"
    "ds_read_b64_tr_b16 a[70:71], v5 offset:55296\n"
    "v_and_b32 v49, 1, v45\n"
    "v_mfma_f32_16x16x32_bf16 v[220:223], a[24:27], v[72:75], v[220:223]\n"
    "ds_read_b64_tr_b16 a[72:73], v5 offset:57344\n"
    "v_lshl_or_b32 v49, v49, 2, v47\n"
    "v_mfma_f32_16x16x32_bf16 v[220:223], a[20:23], v[68:71], v[220:223]\n"
    "ds_read_b64_tr_b16 a[74:75], v5 offset:59392\n"
    "v_lshrrev_b32 v50, 3, v44\n"
    "v_mfma_f32_16x16x32_bf16 v[220:223], a[16:19], v[64:67], v[220:223]\n"
    "ds_read_b64_tr_b16 a[76:77], v5 offset:61440\n"
    "v_and_b32 v51, 7, v44\n"
    "s_lshl_b32 s61, s36, 13\n"
    "s_add_u32 s61, s61, 131072\n"
    "v_mfma_f32_16x16x32_bf16 v[232:235], a[32:35], v[64:67], v[232:235]\n"
    "ds_read_b64_tr_b16 a[78:79], v5 offset:63488\n"
    "v_xor_b32 v5, 0x10000, v5\n"
    "v_mfma_f32_16x16x32_bf16 v[232:235], a[36:39], v[68:71], v[232:235]\n"
    "v_lshrrev_b32 v52, 1, v45\n"
    "v_and_b32 v55, 7, v46\n"
    "v_mfma_f32_16x16x32_bf16 v[232:235], a[40:43], v[72:75], v[232:235]\n"
    "ds_read_b64_tr_b16 a[80:81], v6 offset:49152\n"
    "v_xor_b32 v52, v52, v55\n"
    "v_mfma_f32_16x16x32_bf16 v[232:235], a[44:47], v[76:79], v[232:235]\n"
    "ds_read_b64_tr_b16 a[82:83], v6 offset:51200\n"
    "v_lshlrev_b32 v52, 4, v52\n"
    "v_mfma_f32_16x16x32_bf16 v[244:247], a[60:63], v[76:79], v[244:247]\n"
    "ds_read_b64_tr_b16 a[84:85], v6 offset:53248\n"
    "v_and_b32 v55, 1, v45\n"
    "v_mfma_f32_16x16x32_bf16 v[244:247], a[56:59], v[72:75], v[244:247]\n"
    "ds_read_b64_tr_b16 a[86:87], v6 offset:55296\n"
    "v_lshl_add_u32 v52, v55, 3, v52\n"
    "v_mfma_f32_16x16x32_bf16 v[244:247], a[52:55], v[68:71], v[244:247]\n"
    "ds_read_b64_tr_b16 a[88:89], v6 offset:57344\n"
    "v_lshl_add_u32 v52, v46, 7, v52\n"
    "v_mfma_f32_16x16x32_bf16 v[244:247], a[48:51], v[64:67], v[244:247]\n"
    "s_waitcnt vmcnt(23)\n"
    "ds_read_b64_tr_b16 a[90:91], v6 offset:59392\n"
    "v_add_u32 v52, s61, v52\n"
    "v_mfma_f32_16x16x32_bf16 v[248:251], a[48:51], v[80:83], v[248:251]\n"
    "ds_read_b64_tr_b16 a[92:93], v6 offset:61440\n"
    "v_xor_b32 v53, v51, v50\n"
    "v_mfma_f32_16x16x32_bf16 v[248:251], a[52:55], v[84:87], v[248:251]\n"
    "ds_read_b64_tr_b16 a[94:95], v6 offset:63488\n"
    "v_xor_b32 v6, 0x10000, v6\n"
    "v_mfma_f32_16x16x32_bf16 v[248:251], a[56:59], v[88:91], v[248:251]\n"
    "ds_read_b64_tr_b16 a[96:97], v7 offset:49152\n"
    "v_lshlrev_b32 v53, 4, v53\n"
    "v_mfma_f32_16x16x32_bf16 v[248:251], a[60:63], v[92:95], v[248:251]\n"
    "ds_read_b64_tr_b16 a[98:99], v7 offset:51200\n"
    "v_lshl_add_u32 v53, v50, 7, v53\n"
    "v_mfma_f32_16x16x32_bf16 v[236:239], a[44:47], v[92:95], v[236:239]\n"
    "v_add_u32 v53, s61, v53\n"
    "v_lshlrev_b32 v54, 4, v51\n"
    "v_mfma_f32_16x16x32_bf16 v[236:239], a[40:43], v[88:91], v[236:239]\n"
    "ds_read_b64_tr_b16 a[100:101], v7 offset:53248\n"
    "v_lshl_add_u32 v54, v50, 12, v54\n"
    "v_mfma_f32_16x16x32_bf16 v[236:239], a[36:39], v[84:87], v[236:239]\n"
    "ds_read_b64_tr_b16 a[102:103], v7 offset:55296\n"
    "v_xor_b32 v17, 0, v52\n"
    "v_mfma_f32_16x16x32_bf16 v[236:239], a[32:35], v[80:83], v[236:239]\n"
    "ds_read_b64_tr_b16 a[104:105], v7 offset:57344\n"
    "v_xor_b32 v18, 32, v52\n"
    "v_mfma_f32_16x16x32_bf16 v[224:227], a[16:19], v[80:83], v[224:227]\n"
    "ds_read_b64_tr_b16 a[106:107], v7 offset:59392\n"
    "v_xor_b32 v19, 64, v52\n"
    "v_mfma_f32_16x16x32_bf16 v[224:227], a[20:23], v[84:87], v[224:227]\n"
    "ds_read_b64_tr_b16 a[108:109], v7 offset:61440\n"
    "v_xor_b32 v20, 96, v52\n"
    "v_mfma_f32_16x16x32_bf16 v[224:227], a[24:27], v[88:91], v[224:227]\n"
    "ds_read_b64_tr_b16 a[110:111], v7 offset:63488\n"
    "v_xor_b32 v7, 0x10000, v7\n"
    "v_mfma_f32_16x16x32_bf16 v[224:227], a[28:31], v[92:95], v[224:227]\n"
    "v_mfma_f32_16x16x32_bf16 v[212:215], a[12:15], v[92:95], v[212:215]\n"
    "ds_read_b64_tr_b16 a[112:113], v8 offset:49152\n"
    "v_mfma_f32_16x16x32_bf16 v[212:215], a[8:11], v[88:91], v[212:215]\n"
    "ds_read_b64_tr_b16 a[114:115], v8 offset:51200\n"
    "v_mfma_f32_16x16x32_bf16 v[212:215], a[4:7], v[84:87], v[212:215]\n"
    "ds_read_b64_tr_b16 a[116:117], v8 offset:53248\n"
    "v_mfma_f32_16x16x32_bf16 v[212:215], a[0:3], v[80:83], v[212:215]\n"
    "s_waitcnt vmcnt(19)\n"
    "ds_read_b64_tr_b16 a[118:119], v8 offset:55296\n"
    "v_mfma_f32_16x16x32_bf16 v[216:219], a[0:3], v[96:99], v[216:219]\n"
    "ds_read_b64_tr_b16 a[120:121], v8 offset:57344\n"
    "v_mfma_f32_16x16x32_bf16 v[216:219], a[4:7], v[100:103], v[216:219]\n"
    "ds_read_b64_tr_b16 a[122:123], v8 offset:59392\n"
    "v_mfma_f32_16x16x32_bf16 v[216:219], a[8:11], v[104:107], v[216:219]\n"
    "v_mfma_f32_16x16x32_bf16 v[216:219], a[12:15], v[108:111], v[216:219]\n"
    "ds_read_b64_tr_b16 a[124:125], v8 offset:61440\n"
    "v_mfma_f32_16x16x32_bf16 v[228:231], a[28:31], v[108:111], v[228:231]\n"
    "ds_read_b64_tr_b16 a[126:127], v8 offset:63488\n"
    "v_xor_b32 v8, 0x10000, v8\n"
    "v_mfma_f32_16x16x32_bf16 v[228:231], a[24:27], v[104:107], v[228:231]\n"
    "v_mfma_f32_16x16x32_bf16 v[228:231], a[20:23], v[100:103], v[228:231]\n"
    "v_mfma_f32_16x16x32_bf16 v[228:231], a[16:19], v[96:99], v[228:231]\n"
    "v_mfma_f32_16x16x32_bf16 v[240:243], a[32:35], v[96:99], v[240:243]\n"
    "v_mfma_f32_16x16x32_bf16 v[240:243], a[36:39], v[100:103], v[240:243]\n"
    "v_mfma_f32_16x16x32_bf16 v[240:243], a[40:43], v[104:107], v[240:243]\n"
    "v_mfma_f32_16x16x32_bf16 v[240:243], a[44:47], v[108:111], v[240:243]\n"
    "v_mfma_f32_16x16x32_bf16 v[252:255], a[60:63], v[108:111], v[252:255]\n"
    "v_mfma_f32_16x16x32_bf16 v[252:255], a[56:59], v[104:107], v[252:255]\n"
    "v_mfma_f32_16x16x32_bf16 v[252:255], a[52:55], v[100:103], v[252:255]\n"
    "v_mfma_f32_16x16x32_bf16 v[252:255], a[48:51], v[96:99], v[252:255]\n"
    "s_waitcnt vmcnt(14) lgkmcnt(0)\n"
    "s_mul_i32 s46, s36, 196608\n"
    "v_mfma_f32_16x16x32_bf16 v[208:211], a[64:67], a[176:179], v[208:211]\n"
    "v_mfma_f32_16x16x32_bf16 v[208:211], a[68:71], a[180:183], v[208:211]\n"
    "v_mfma_f32_16x16x32_bf16 v[208:211], a[72:75], a[184:187], v[208:211]\n"
    "v_mfma_f32_16x16x32_bf16 v[208:211], a[76:79], a[188:191], v[208:211]\n"
    "v_mfma_f32_16x16x32_bf16 v[220:223], a[80:83], a[176:179], v[220:223]\n"
    "v_mfma_f32_16x16x32_bf16 v[220:223], a[84:87], a[180:183], v[220:223]\n"
    "v_mfma_f32_16x16x32_bf16 v[220:223], a[88:91], a[184:187], v[220:223]\n"
    "v_mfma_f32_16x16x32_bf16 v[220:223], a[92:95], a[188:191], v[220:223]\n"
    "v_mfma_f32_16x16x32_bf16 v[232:235], a[96:99], a[176:179], v[232:235]\n"
    "v_mfma_f32_16x16x32_bf16 v[232:235], a[100:103], a[180:183], v[232:235]\n"
    "v_mfma_f32_16x16x32_bf16 v[232:235], a[104:107], a[184:187], v[232:235]\n"
    "v_mfma_f32_16x16x32_bf16 v[232:235], a[108:111], a[188:191], v[232:235]\n"
    "v_mfma_f32_16x16x32_bf16 v[244:247], a[112:115], a[176:179], v[244:247]\n"
    "v_mfma_f32_16x16x32_bf16 v[244:247], a[116:119], a[180:183], v[244:247]\n"
    "v_mfma_f32_16x16x32_bf16 v[244:247], a[120:123], a[184:187], v[244:247]\n"
    "v_mfma_f32_16x16x32_bf16 v[244:247], a[124:127], a[188:191], v[244:247]\n"
    "s_waitcnt vmcnt(0)\n"
    "v_mfma_f32_16x16x32_bf16 v[208:211], a[232:235], v[172:175], v[208:211]\n"
    "v_mfma_f32_16x16x32_bf16 v[220:223], a[236:239], v[172:175], v[220:223]\n"
    "v_mfma_f32_16x16x32_bf16 v[232:235], a[232:235], v[196:199], v[232:235]\n"
    "v_mfma_f32_16x16x32_bf16 v[244:247], a[236:239], v[196:199], v[244:247]\n"
    "v_mfma_f32_16x16x32_bf16 v[212:215], a[64:67], a[192:195], v[212:215]\n"
    "v_mfma_f32_16x16x32_bf16 v[212:215], a[68:71], a[196:199], v[212:215]\n"
    "v_mfma_f32_16x16x32_bf16 v[212:215], a[72:75], a[200:203], v[212:215]\n"
    "v_mfma_f32_16x16x32_bf16 v[212:215], a[76:79], a[204:207], v[212:215]\n"
    "v_mfma_f32_16x16x32_bf16 v[224:227], a[80:83], a[192:195], v[224:227]\n"
    "v_mfma_f32_16x16x32_bf16 v[224:227], a[84:87], a[196:199], v[224:227]\n"
    "v_cvt_pk_bf16_f32 v208, v208, v209\n"
    "v_cvt_pk_bf16_f32 v209, v210, v211\n"
    "v_mfma_f32_16x16x32_bf16 v[224:227], a[88:91], a[200:203], v[224:227]\n"
    "ds_write_b64 v17, v[208:209] offset:0\n"
    "v_cvt_pk_bf16_f32 v220, v220, v221\n"
    "v_mfma_f32_16x16x32_bf16 v[224:227], a[92:95], a[204:207], v[224:227]\n"
    "v_cvt_pk_bf16_f32 v221, v222, v223\n"
    "ds_write_b64 v18, v[220:221] offset:0\n"
    "v_mfma_f32_16x16x32_bf16 v[236:239], a[96:99], a[192:195], v[236:239]\n"
    "v_cvt_pk_bf16_f32 v232, v232, v233\n"
    "v_cvt_pk_bf16_f32 v233, v234, v235\n"
    "v_mfma_f32_16x16x32_bf16 v[236:239], a[100:103], a[196:199], v[236:239]\n"
    "ds_write_b64 v19, v[232:233] offset:0\n"
    "v_cvt_pk_bf16_f32 v244, v244, v245\n"
    "v_mfma_f32_16x16x32_bf16 v[236:239], a[104:107], a[200:203], v[236:239]\n"
    "v_cvt_pk_bf16_f32 v245, v246, v247\n"
    "ds_write_b64 v20, v[244:245] offset:0\n"
    "v_mfma_f32_16x16x32_bf16 v[236:239], a[108:111], a[204:207], v[236:239]\n"
    "ds_read_b128 v[112:115], v53 offset:0\n"
    "v_mfma_f32_16x16x32_bf16 v[248:251], a[112:115], a[192:195], v[248:251]\n"
    "ds_read_b128 v[116:119], v53 offset:1024\n"
    "v_mfma_f32_16x16x32_bf16 v[248:251], a[116:119], a[196:199], v[248:251]\n"
    "v_mfma_f32_16x16x32_bf16 v[248:251], a[120:123], a[200:203], v[248:251]\n"
    "v_mfma_f32_16x16x32_bf16 v[248:251], a[124:127], a[204:207], v[248:251]\n"
    "v_mfma_f32_16x16x32_bf16 v[212:215], a[232:235], v[176:179], v[212:215]\n"
    "v_mfma_f32_16x16x32_bf16 v[224:227], a[236:239], v[176:179], v[224:227]\n"
    "v_mfma_f32_16x16x32_bf16 v[236:239], a[232:235], v[200:203], v[236:239]\n"
    "v_mfma_f32_16x16x32_bf16 v[248:251], a[236:239], v[200:203], v[248:251]\n"
    "v_mfma_f32_16x16x32_bf16 v[216:219], a[64:67], a[208:211], v[216:219]\n"
    "s_waitcnt lgkmcnt(0)\n"
    "buffer_store_dwordx4 v[112:115], v54, s[8:11], s46 offen\n"
    "s_add_u32 s46, s46, 32768\n"
    "buffer_store_dwordx4 v[116:119], v54, s[8:11], s46 offen\n"
    "s_add_u32 s46, s46, 32768\n"
    "v_mfma_f32_16x16x32_bf16 v[216:219], a[68:71], a[212:215], v[216:219]\n"
    "v_mfma_f32_16x16x32_bf16 v[216:219], a[72:75], a[216:219], v[216:219]\n"
    "v_mfma_f32_16x16x32_bf16 v[216:219], a[76:79], a[220:223], v[216:219]\n"
    "v_mfma_f32_16x16x32_bf16 v[228:231], a[80:83], a[208:211], v[228:231]\n"
    "v_mfma_f32_16x16x32_bf16 v[228:231], a[84:87], a[212:215], v[228:231]\n"
    "v_cvt_pk_bf16_f32 v212, v212, v213\n"
    "v_cvt_pk_bf16_f32 v213, v214, v215\n"
    "v_mfma_f32_16x16x32_bf16 v[228:231], a[88:91], a[216:219], v[228:231]\n"
    "ds_write_b64 v17, v[212:213] offset:2048\n"
    "v_cvt_pk_bf16_f32 v224, v224, v225\n"
    "v_mfma_f32_16x16x32_bf16 v[228:231], a[92:95], a[220:223], v[228:231]\n"
    "v_cvt_pk_bf16_f32 v225, v226, v227\n"
    "ds_write_b64 v18, v[224:225] offset:2048\n"
    "v_mfma_f32_16x16x32_bf16 v[240:243], a[96:99], a[208:211], v[240:243]\n"
    "v_cvt_pk_bf16_f32 v236, v236, v237\n"
    "v_cvt_pk_bf16_f32 v237, v238, v239\n"
    "v_mfma_f32_16x16x32_bf16 v[240:243], a[100:103], a[212:215], v[240:243]\n"
    "ds_write_b64 v19, v[236:237] offset:2048\n"
    "v_cvt_pk_bf16_f32 v248, v248, v249\n"
    "v_mfma_f32_16x16x32_bf16 v[240:243], a[104:107], a[216:219], v[240:243]\n"
    "v_cvt_pk_bf16_f32 v249, v250, v251\n"
    "ds_write_b64 v20, v[248:249] offset:2048\n"
    "v_mfma_f32_16x16x32_bf16 v[240:243], a[108:111], a[220:223], v[240:243]\n"
    "ds_read_b128 v[120:123], v53 offset:2048\n"
    "v_mfma_f32_16x16x32_bf16 v[252:255], a[112:115], a[208:211], v[252:255]\n"
    "ds_read_b128 v[124:127], v53 offset:3072\n"
    "v_mfma_f32_16x16x32_bf16 v[252:255], a[116:119], a[212:215], v[252:255]\n"
    "v_mfma_f32_16x16x32_bf16 v[252:255], a[120:123], a[216:219], v[252:255]\n"
    "v_mfma_f32_16x16x32_bf16 v[252:255], a[124:127], a[220:223], v[252:255]\n"
    "v_mfma_f32_16x16x32_bf16 v[216:219], a[232:235], v[180:183], v[216:219]\n"
    "v_mfma_f32_16x16x32_bf16 v[228:231], a[236:239], v[180:183], v[228:231]\n"
    "v_mfma_f32_16x16x32_bf16 v[240:243], a[232:235], v[204:207], v[240:243]\n"
    "v_mfma_f32_16x16x32_bf16 v[252:255], a[236:239], v[204:207], v[252:255]\n"
    "s_waitcnt lgkmcnt(0)\n"
    "buffer_store_dwordx4 v[120:123], v54, s[8:11], s46 offen\n"
    "s_add_u32 s46, s46, 32768\n"
    "buffer_store_dwordx4 v[124:127], v54, s[8:11], s46 offen\n"
    "s_add_u32 s46, s46, 32768\n"
    "s_nop 3\n"
    "v_cvt_pk_bf16_f32 v216, v216, v217\n"
    "v_cvt_pk_bf16_f32 v217, v218, v219\n"
    "ds_write_b64 v17, v[216:217] offset:4096\n"
    "v_cvt_pk_bf16_f32 v228, v228, v229\n"
    "v_cvt_pk_bf16_f32 v229, v230, v231\n"
    "ds_write_b64 v18, v[228:229] offset:4096\n"
    "v_cvt_pk_bf16_f32 v240, v240, v241\n"
    "v_cvt_pk_bf16_f32 v241, v242, v243\n"
    "ds_write_b64 v19, v[240:241] offset:4096\n"
    "v_cvt_pk_bf16_f32 v252, v252, v253\n"
    "v_cvt_pk_bf16_f32 v253, v254, v255\n"
    "ds_write_b64 v20, v[252:253] offset:4096\n"
    "ds_read_b128 v[128:131], v53 offset:4096\n"
    "ds_read_b128 v[132:135], v53 offset:5120\n"
    "s_waitcnt lgkmcnt(0)\n"
    "buffer_store_dwordx4 v[128:131], v54, s[8:11], s46 offen\n"
    "s_add_u32 s46, s46, 32768\n"
    "buffer_store_dwordx4 v[132:135], v54, s[8:11], s46 offen\n"
    "s_add_u32 s46, s46, 32768\n"
    "s_waitcnt vmcnt(0)\n"
    "s_branch .L_end_%=\n"
    ".L_fbC_%=:\n"
    "s_waitcnt vmcnt(0) lgkmcnt(0)\n"
    "s_mov_b32 s44, m0\n"
    "s_mov_b32 m0, s61\n"
    "v_and_b32 v52, 63, %7\n"
    "v_lshrrev_b32 v53, 4, v52\n"
    "v_and_b32 v52, 15, v52\n"
    "v_lshlrev_b32 v52, 8, v52\n"
    "v_lshl_add_u32 v52, v53, 4, v52\n"
    "v_and_b32 v56, 0xfff, v25\n"
    "v_add_u32 v56, s61, v56\n"
    "v_and_b32 v57, 0xfff, v26\n"
    "v_add_u32 v57, s61, v57\n"
    "v_and_b32 v58, 0xfff, v27\n"
    "v_add_u32 v58, s61, v58\n"
    "v_and_b32 v59, 0xfff, v28\n"
    "v_add_u32 v59, s61, v59\n"
    "v_and_b32 v60, 0xfff, v29\n"
    "v_add_u32 v60, s61, v60\n"
    "v_and_b32 v61, 0xfff, v30\n"
    "v_add_u32 v61, s61, v61\n"
    "v_and_b32 v62, 0xfff, v31\n"
    "v_add_u32 v62, s61, v62\n"
    "v_and_b32 v63, 0xfff, v32\n"
    "v_add_u32 v63, s61, v63\n"
    "s_mul_i32 s42, s55, s60\n"
    "s_add_u32 s46, s42, 0x20000\n"
    "v_mov_b32 v232, 0xff800000\n"
    "v_mov_b32 v236, 0xff800000\n"
    "v_mov_b32 v240, 0xff800000\n"
    ".L_fb1C_%=:\n"
    "s_add_u32 s57, s42, 0\n"
    "buffer_load_dwordx4 a[64:67], v52, s[16:19], s57 offen offset:0\n"
    "buffer_load_dwordx4 a[68:71], v52, s[16:19], s57 offen offset:64\n"
    "buffer_load_dwordx4 a[72:75], v52, s[16:19], s57 offen offset:128\n"
    "buffer_load_dwordx4 a[76:79], v52, s[16:19], s57 offen offset:192\n"
    "s_add_u32 s57, s42, 4096\n"
    "buffer_load_dwordx4 a[80:83], v52, s[16:19], s57 offen offset:0\n"
    "buffer_load_dwordx4 a[84:87], v52, s[16:19], s57 offen offset:64\n"
    "buffer_load_dwordx4 a[88:91], v52, s[16:19], s57 offen offset:128\n"
    "buffer_load_dwordx4 a[92:95], v52, s[16:19], s57 offen offset:192\n"
    "s_waitcnt vmcnt(0)\n"
    "v_mfma_f32_16x16x32_bf16 v[160:163], a[64:67], a[176:179], 0\n"
    "v_mfma_f32_16x16x32_bf16 v[160:163], a[68:71], a[180:183], v[160:163]\n"
    "v_mfma_f32_16x16x32_bf16 v[160:163], a[72:75], a[184:187], v[160:163]\n"
    "v_mfma_f32_16x16x32_bf16 v[160:163], a[76:79], a[188:191], v[160:163]\n"
    "v_mfma_f32_16x16x32_bf16 v[184:187], a[64:67], a[192:195], 0\n"
    "v_mfma_f32_16x16x32_bf16 v[184:187], a[68:71], a[196:199], v[184:187]\n"
    "v_mfma_f32_16x16x32_bf16 v[184:187], a[72:75], a[200:203], v[184:187]\n"
    "v_mfma_f32_16x16x32_bf16 v[184:187], a[76:79], a[204:207], v[184:187]\n"
    "v_mfma_f32_16x16x32_bf16 v[208:211], a[64:67], a[208:211], 0\n"
    "v_mfma_f32_16x16x32_bf16 v[208:211], a[68:71], a[212:215], v[208:211]\n"
    "v_mfma_f32_16x16x32_bf16 v[208:211], a[72:75], a[216:219], v[208:211]\n"
    "v_mfma_f32_16x16x32_bf16 v[208:211], a[76:79], a[220:223], v[208:211]\n"
    "v_mfma_f32_16x16x32_bf16 v[176:179], a[80:83], a[176:179], 0\n"
    "v_mfma_f32_16x16x32_bf16 v[176:179], a[84:87], a[180:183], v[176:179]\n"
    "v_mfma_f32_16x16x32_bf16 v[176:179], a[88:91], a[184:187], v[176:179]\n"
    "v_mfma_f32_16x16x32_bf16 v[176:179], a[92:95], a[188:191], v[176:179]\n"
    "v_mfma_f32_16x16x32_bf16 v[200:203], a[80:83], a[192:195], 0\n"
    "v_mfma_f32_16x16x32_bf16 v[200:203], a[84:87], a[196:199], v[200:203]\n"
    "v_mfma_f32_16x16x32_bf16 v[200:203], a[88:91], a[200:203], v[200:203]\n"
    "v_mfma_f32_16x16x32_bf16 v[200:203], a[92:95], a[204:207], v[200:203]\n"
    "v_mfma_f32_16x16x32_bf16 v[224:227], a[80:83], a[208:211], 0\n"
    "v_mfma_f32_16x16x32_bf16 v[224:227], a[84:87], a[212:215], v[224:227]\n"
    "v_mfma_f32_16x16x32_bf16 v[224:227], a[88:91], a[216:219], v[224:227]\n"
    "v_mfma_f32_16x16x32_bf16 v[224:227], a[92:95], a[220:223], v[224:227]\n"
    "s_nop 7\n"
    "v_max3_f32 v232, v232, v160, v161\n"
    "v_max3_f32 v232, v232, v162, v163\n"
    "v_max3_f32 v232, v232, v176, v177\n"
    "v_max3_f32 v232, v232, v178, v179\n"
    "v_max3_f32 v236, v236, v184, v185\n"
    "v_max3_f32 v236, v236, v186, v187\n"
    "v_max3_f32 v236, v236, v200, v201\n"
    "v_max3_f32 v236, v236, v202, v203\n"
    "v_max3_f32 v240, v240, v208, v209\n"
    "v_max3_f32 v240, v240, v210, v211\n"
    "v_max3_f32 v240, v240, v224, v225\n"
    "v_max3_f32 v240, v240, v226, v227\n"
    "s_add_u32 s42, s42, 8192\n"
    "s_cmp_lg_u32 s42, s46\n"
    "s_cbranch_scc1 .L_fb1C_%=\n"
    "v_mov_b32 v44, v232\n"
    "v_mov_b32 v46, v236\n"
    "v_mov_b32 v48, v240\n"
    "v_mov_b32 v45, v44\n"
    "v_mov_b32 v47, v46\n"
    "v_mov_b32 v49, v48\n"
    "s_nop 1\n"
    "v_permlane32_swap_b32 v44, v45\n"
    "v_permlane32_swap_b32 v46, v47\n"
    "v_permlane32_swap_b32 v48, v49\n"
    "v_max_f32 v44, v44, v45\n"
    "v_max_f32 v46, v46, v47\n"
    "v_max_f32 v48, v48, v49\n"
    "v_mov_b32 v45, v44\n"
    "v_mov_b32 v47, v46\n"
    "v_mov_b32 v49, v48\n"
    "s_nop 1\n"
    "v_permlane16_swap_b32 v44, v45\n"
    "v_permlane16_swap_b32 v46, v47\n"
    "v_permlane16_swap_b32 v48, v49\n"
    "v_max_f32 v44, v44, v45\n"
    "v_max_f32 v46, v46, v47\n"
    "v_max_f32 v48, v48, v49\n"
    "v_sub_f32 v232, 0, v44\n"
    "v_sub_f32 v233, 0, v44\n"
    "v_sub_f32 v234, 0, v44\n"
    "v_sub_f32 v235, 0, v44\n"
    "v_sub_f32 v236, 0, v46\n"
    "v_sub_f32 v237, 0, v46\n"
    "v_sub_f32 v238, 0, v46\n"
    "v_sub_f32 v239, 0, v46\n"
    "v_sub_f32 v240, 0, v48\n"
    "v_sub_f32 v241, 0, v48\n"
    "v_sub_f32 v242, 0, v48\n"
    "v_sub_f32 v243, 0, v48\n"
    "v_mfma_f32_32x32x16_bf16 v[64:79], a[228:231], a[228:231], 0\n"
    "v_mfma_f32_32x32x16_bf16 v[80:95], a[228:231], a[228:231], 0\n"
    "v_mfma_f32_32x32x16_bf16 v[96:111], a[228:231], a[228:231], 0\n"
    "v_mfma_f32_32x32x16_bf16 v[112:127], a[228:231], a[228:231], 0\n"
    "v_mfma_f32_32x32x16_bf16 v[128:143], a[228:231], a[228:231], 0\n"
    "v_mfma_f32_32x32x16_bf16 v[144:159], a[228:231], a[228:231], 0\n"
    "v_mov_b32 v244, 0\n"
    "v_mov_b32 v245, 0\n"
    "v_mov_b32 v246, 0\n"
    "v_mov_b32 v247, 0\n"
    "v_mov_b32 v248, 0\n"
    "v_mov_b32 v249, 0\n"
    "v_mov_b32 v250, 0\n"
    "v_mov_b32 v251, 0\n"
    "v_mov_b32 v252, 0\n"
    "v_mov_b32 v253, 0\n"
    "v_mov_b32 v254, 0\n"
    "v_mov_b32 v255, 0\n"
    "s_sub_u32 s42, s42, 0x20000\n"
    ".L_fb2C_%=:\n"
    "s_add_u32 s57, s42, 0\n"
    "buffer_load_dwordx4 a[64:67], v52, s[16:19], s57 offen offset:0\n"
    "buffer_load_dwordx4 a[68:71], v52, s[16:19], s57 offen offset:64\n"
    "buffer_load_dwordx4 a[72:75], v52, s[16:19], s57 offen offset:128\n"
    "buffer_load_dwordx4 a[76:79], v52, s[16:19], s57 offen offset:192\n"
    "s_add_u32 s57, s42, 4096\n"
    "buffer_load_dwordx4 a[80:83], v52, s[16:19], s57 offen offset:0\n"
    "buffer_load_dwordx4 a[84:87], v52, s[16:19], s57 offen offset:64\n"
    "buffer_load_dwordx4 a[88:91], v52, s[16:19], s57 offen offset:128\n"
    "buffer_load_dwordx4 a[92:95], v52, s[16:19], s57 offen offset:192\n"
    "s_waitcnt lgkmcnt(0)\n"
    "s_add_u32 s57, s42, 0\n"
    "buffer_load_dwordx4 v13, s[20:23], s57 offen lds\n"
    "buffer_load_dwordx4 v14, s[20:23], s57 offen offset:1024 lds\n"
    "buffer_load_dwordx4 v13, s[20:23], s57 offen offset:2048 lds\n"
    "buffer_load_dwordx4 v14, s[20:23], s57 offen offset:3072 lds\n"
    "s_waitcnt vmcnt(0)\n"
    "ds_read_b64_tr_b16 a[96:97], v56\n"
    "ds_read_b64_tr_b16 a[100:101], v57\n"
    "ds_read_b64_tr_b16 a[104:105], v58\n"
    "ds_read_b64_tr_b16 a[108:109], v59\n"
    "ds_read_b64_tr_b16 a[112:113], v60\n"
    "ds_read_b64_tr_b16 a[116:117], v61\n"
    "ds_read_b64_tr_b16 a[120:121], v62\n"
    "ds_read_b64_tr_b16 a[124:125], v63\n"
    "s_waitcnt lgkmcnt(0)\n"
    "s_add_u32 s57, s42, 4096\n"
    "buffer_load_dwordx4 v13, s[20:23], s57 offen lds\n"
    "buffer_load_dwordx4 v14, s[20:23], s57 offen offset:1024 lds\n"
    "buffer_load_dwordx4 v13, s[20:23], s57 offen offset:2048 lds\n"
    "buffer_load_dwordx4 v14, s[20:23], s57 offen offset:3072 lds\n"
    "s_waitcnt vmcnt(0)\n"
    "ds_read_b64_tr_b16 a[98:99], v56\n"
    "ds_read_b64_tr_b16 a[102:103], v57\n"
    "ds_read_b64_tr_b16 a[106:107], v58\n"
    "ds_read_b64_tr_b16 a[110:111], v59\n"
    "ds_read_b64_tr_b16 a[114:115], v60\n"
    "ds_read_b64_tr_b16 a[118:119], v61\n"
    "ds_read_b64_tr_b16 a[122:123], v62\n"
    "ds_read_b64_tr_b16 a[126:127], v63\n"
    "v_mfma_f32_16x16x32_bf16 v[160:163], a[64:67], a[176:179], v[232:235]\n"
    "v_mfma_f32_16x16x32_bf16 v[160:163], a[68:71], a[180:183], v[160:163]\n"
    "v_mfma_f32_16x16x32_bf16 v[160:163], a[72:75], a[184:187], v[160:163]\n"
    "v_mfma_f32_16x16x32_bf16 v[160:163], a[76:79], a[188:191], v[160:163]\n"
    "v_mfma_f32_16x16x32_bf16 v[184:187], a[64:67], a[192:195], v[236:239]\n"
    "v_mfma_f32_16x16x32_bf16 v[184:187], a[68:71], a[196:199], v[184:187]\n"
    "v_mfma_f32_16x16x32_bf16 v[184:187], a[72:75], a[200:203], v[184:187]\n"
    "v_mfma_f32_16x16x32_bf16 v[184:187], a[76:79], a[204:207], v[184:187]\n"
    "v_mfma_f32_16x16x32_bf16 v[208:211], a[64:67], a[208:211], v[240:243]\n"
    "v_mfma_f32_16x16x32_bf16 v[208:211], a[68:71], a[212:215], v[208:211]\n"
    "v_mfma_f32_16x16x32_bf16 v[208:211], a[72:75], a[216:219], v[208:211]\n"
    "v_mfma_f32_16x16x32_bf16 v[208:211], a[76:79], a[220:223], v[208:211]\n"
    "v_mfma_f32_16x16x32_bf16 v[176:179], a[80:83], a[176:179], v[232:235]\n"
    "v_mfma_f32_16x16x32_bf16 v[176:179], a[84:87], a[180:183], v[176:179]\n"
    "v_mfma_f32_16x16x32_bf16 v[176:179], a[88:91], a[184:187], v[176:179]\n"
    "v_mfma_f32_16x16x32_bf16 v[176:179], a[92:95], a[188:191], v[176:179]\n"
    "v_mfma_f32_16x16x32_bf16 v[200:203], a[80:83], a[192:195], v[236:239]\n"
    "v_mfma_f32_16x16x32_bf16 v[200:203], a[84:87], a[196:199], v[200:203]\n"
    "v_mfma_f32_16x16x32_bf16 v[200:203], a[88:91], a[200:203], v[200:203]\n"
    "v_mfma_f32_16x16x32_bf16 v[200:203], a[92:95], a[204:207], v[200:203]\n"
    "v_mfma_f32_16x16x32_bf16 v[224:227], a[80:83], a[208:211], v[240:243]\n"
    "v_mfma_f32_16x16x32_bf16 v[224:227], a[84:87], a[212:215], v[224:227]\n"
    "v_mfma_f32_16x16x32_bf16 v[224:227], a[88:91], a[216:219], v[224:227]\n"
    "v_mfma_f32_16x16x32_bf16 v[224:227], a[92:95], a[220:223], v[224:227]\n"
    "s_nop 7\n"
    "v_exp_f32 v160, v160\n"
    "v_exp_f32 v161, v161\n"
    "v_exp_f32 v162, v162\n"
    "v_exp_f32 v163, v163\n"
    "v_exp_f32 v176, v176\n"
    "v_exp_f32 v177, v177\n"
    "v_exp_f32 v178, v178\n"
    "v_exp_f32 v179, v179\n"
    "v_exp_f32 v184, v184\n"
    "v_exp_f32 v185, v185\n"
    "v_exp_f32 v186, v186\n"
    "v_exp_f32 v187, v187\n"
    "v_exp_f32 v200, v200\n"
    "v_exp_f32 v201, v201\n"
    "v_exp_f32 v202, v202\n"
    "v_exp_f32 v203, v203\n"
    "v_exp_f32 v208, v208\n"
    "v_exp_f32 v209, v209\n"
    "v_exp_f32 v210, v210\n"
    "v_exp_f32 v211, v211\n"
    "v_exp_f32 v224, v224\n"
    "v_exp_f32 v225, v225\n"
    "v_exp_f32 v226, v226\n"
    "v_exp_f32 v227, v227\n"
    "s_nop 0\n"
    "v_cvt_pk_bf16_f32 v160, v160, v161\n"
    "v_cvt_pk_bf16_f32 v161, v162, v163\n"
    "v_cvt_pk_bf16_f32 v162, v176, v177\n"
    "v_cvt_pk_bf16_f32 v163, v178, v179\n"
    "v_cvt_pk_bf16_f32 v184, v184, v185\n"
    "v_cvt_pk_bf16_f32 v185, v186, v187\n"
    "v_cvt_pk_bf16_f32 v186, v200, v201\n"
    "v_cvt_pk_bf16_f32 v187, v202, v203\n"
    "v_cvt_pk_bf16_f32 v208, v208, v209\n"
    "v_cvt_pk_bf16_f32 v209, v210, v211\n"
    "v_cvt_pk_bf16_f32 v210, v224, v225\n"
    "v_cvt_pk_bf16_f32 v211, v226, v227\n"
    "s_waitcnt lgkmcnt(0)\n"
    "s_nop 0\n"
    "v_mfma_f32_16x16x32_bf16 v[64:67], a[96:99], v[160:163], v[64:67]\n"
    "v_mfma_f32_16x16x32_bf16 v[68:71], a[96:99], v[184:187], v[68:71]\n"
    "v_mfma_f32_16x16x32_bf16 v[72:75], a[96:99], v[208:211], v[72:75]\n"
    "v_mfma_f32_16x16x32_bf16 v[76:79], a[100:103], v[160:163], v[76:79]\n"
    "v_mfma_f32_16x16x32_bf16 v[80:83], a[100:103], v[184:187], v[80:83]\n"
    "v_mfma_f32_16x16x32_bf16 v[84:87], a[100:103], v[208:211], v[84:87]\n"
    "v_mfma_f32_16x16x32_bf16 v[88:91], a[104:107], v[160:163], v[88:91]\n"
    "v_mfma_f32_16x16x32_bf16 v[92:95], a[104:107], v[184:187], v[92:95]\n"
    "v_mfma_f32_16x16x32_bf16 v[96:99], a[104:107], v[208:211], v[96:99]\n"
    "v_mfma_f32_16x16x32_bf16 v[100:103], a[108:111], v[160:163], v[100:103]\n"
    "v_mfma_f32_16x16x32_bf16 v[104:107], a[108:111], v[184:187], v[104:107]\n"
    "v_mfma_f32_16x16x32_bf16 v[108:111], a[108:111], v[208:211], v[108:111]\n"
    "v_mfma_f32_16x16x32_bf16 v[112:115], a[112:115], v[160:163], v[112:115]\n"
    "v_mfma_f32_16x16x32_bf16 v[116:119], a[112:115], v[184:187], v[116:119]\n"
    "v_mfma_f32_16x16x32_bf16 v[120:123], a[112:115], v[208:211], v[120:123]\n"
    "v_mfma_f32_16x16x32_bf16 v[124:127], a[116:119], v[160:163], v[124:127]\n"
    "v_mfma_f32_16x16x32_bf16 v[128:131], a[116:119], v[184:187], v[128:131]\n"
    "v_mfma_f32_16x16x32_bf16 v[132:135], a[116:119], v[208:211], v[132:135]\n"
    "v_mfma_f32_16x16x32_bf16 v[136:139], a[120:123], v[160:163], v[136:139]\n"
    "v_mfma_f32_16x16x32_bf16 v[140:143], a[120:123], v[184:187], v[140:143]\n"
    "v_mfma_f32_16x16x32_bf16 v[144:147], a[120:123], v[208:211], v[144:147]\n"
    "v_mfma_f32_16x16x32_bf16 v[148:151], a[124:127], v[160:163], v[148:151]\n"
    "v_mfma_f32_16x16x32_bf16 v[152:155], a[124:127], v[184:187], v[152:155]\n"
    "v_mfma_f32_16x16x32_bf16 v[156:159], a[124:127], v[208:211], v[156:159]\n"
    "v_mfma_f32_16x16x32_bf16 v[244:247], a[224:227], v[160:163], v[244:247]\n"
    "v_mfma_f32_16x16x32_bf16 v[248:251], a[224:227], v[184:187], v[248:251]\n"
    "v_mfma_f32_16x16x32_bf16 v[252:255], a[224:227], v[208:211], v[252:255]\n"
    "s_add_u32 s42, s42, 8192\n"
    "s_cmp_lg_u32 s42, s46\n"
    "s_cbranch_scc1 .L_fb2C_%=\n"
    "s_nop 7\n"
    "s_mov_b32 m0, s44\n"
    "s_cmp_eq_u32 s55, 0\n"
    "s_cbranch_scc1 .L_fbretC0_%=\n"
    "s_branch .L_fbretC1_%=\n"
    ".L_end_%=:\n"
    :: "s"(px), "s"(pw), "s"(pk), "s"(pv), "s"(pf), "s"(j), "s"(idx), "v"(threadIdx.x), "s"(pwarm), "s"(pwo), "s"(pout), "s"(((b + idx) & 3u) | (wline << 8))
    : "memory", "vcc", "scc", "v1", "v2", "v3", "v4", "v5", "v6", "v7", "v8", "v9", "v10", "v11", "v12", "v13", "v14", "v15", "v16", "v17", "v18", "v19", "v20", "v21", "v22", "v23", "v24", "v25", "v26", "v27", "v28", "v29", "v30", "v31", "v32", "v33", "v34", "v35", "v36", "v37", "v38", "v39", "v40", "v41", "v42", "v43", "v44", "v45", "v46", "v47", "v48", "v49", "v50", "v51", "v52", "v53", "v54", "v55", "v56", "v57", "v58", "v59", "v60", "v61", "v62", "v63", "v64", "v65", "v66", "v67", "v68", "v69", "v70", "v71", "v72", "v73", "v74", "v75", "v76", "v77", "v78", "v79", "v80", "v81", "v82", "v83", "v84", "v85", "v86", "v87", "v88", "v89", "v90", "v91", "v92", "v93", "v94", "v95", "v96", "v97", "v98", "v99", "v100", "v101", "v102", "v103", "v104", "v105", "v106", "v107", "v108", "v109", "v110", "v111", "v112", "v113", "v114", "v115", "v116", "v117", "v118", "v119", "v120", "v121", "v122", "v123", "v124", "v125", "v126", "v127", "v128", "v129", "v130", "v131", "v132", "v133", "v134", "v135", "v136", "v137", "v138", "v139", "v140", "v141", "v142", "v143", "v144", "v145", "v146", "v147", "v148", "v149", "v150", "v151", "v152", "v153", "v154", "v155", "v156", "v157", "v158", "v159", "v160", "v161", "v162", "v163", "v164", "v165", "v166", "v167", "v168", "v169", "v170", "v171", "v172", "v173", "v174", "v175", "v176", "v177", "v178", "v179", "v180", "v181", "v182", "v183", "v184", "v185", "v186", "v187", "v188", "v189", "v190", "v191", "v192", "v193", "v194", "v195", "v196", "v197", "v198", "v199", "v200", "v201", "v202", "v203", "v204", "v205", "v206", "v207", "v208", "v209", "v210", "v211", "v212", "v213", "v214", "v215", "v216", "v217", "v218", "v219", "v220", "v221", "v222", "v223", "v224", "v225", "v226", "v227", "v228", "v229", "v230", "v231", "v232", "v233", "v234", "v235", "v236", "v237", "v238", "v239", "v240", "v241", "v242", "v243", "v244", "v245", "v246", "v247", "v248", "v249", "v250", "v251", "v252", "v253", "v254", "v255", "a0", "a1", "a2", "a3", "a4", "a5", "a6", "a7", "a8", "a9", "a10", "a11", "a12", "a13", "a14", "a15", "a16", "a17", "a18", "a19", "a20", "a21", "a22", "a23", "a24", "a25", "a26", "a27", "a28", "a29", "a30", "a31", "a32", "a33", "a34", "a35", "a36", "a37", "a38", "a39", "a40", "a41", "a42", "a43", "a44", "a45", "a46", "a47", "a48", "a49", "a50", "a51", "a52", "a53", "a54", "a55", "a56", "a57", "a58", "a59", "a60", "a61", "a62", "a63", "a64", "a65", "a66", "a67", "a68", "a69", "a70", "a71", "a72", "a73", "a74", "a75", "a76", "a77", "a78", "a79", "a80", "a81", "a82", "a83", "a84", "a85", "a86", "a87", "a88", "a89", "a90", "a91", "a92", "a93", "a94", "a95", "a96", "a97", "a98", "a99", "a100", "a101", "a102", "a103", "a104", "a105", "a106", "a107", "a108", "a109", "a110", "a111", "a112", "a113", "a114", "a115", "a116", "a117", "a118", "a119", "a120", "a121", "a122", "a123", "a124", "a125", "a126", "a127", "a128", "a129", "a130", "a131", "a132", "a133", "a134", "a135", "a136", "a137", "a138", "a139", "a140", "a141", "a142", "a143", "a144", "a145", "a146", "a147", "a148", "a149", "a150", "a151", "a152", "a153", "a154", "a155", "a156", "a157", "a158", "a159", "a160", "a161", "a162", "a163", "a164", "a165", "a166", "a167", "a168", "a169", "a170", "a171", "a172", "a173", "a174", "a175", "a176", "a177", "a178", "a179", "a180", "a181", "a182", "a183", "a184", "a185", "a186", "a187", "a188", "a189", "a190", "a191", "a192", "a193", "a194", "a195", "a196", "a197", "a198", "a199", "a200", "a201", "a202", "a203", "a204", "a205", "a206", "a207", "a208", "a209", "a210", "a211", "a212", "a213", "a214", "a215", "a216", "a217", "a218", "a219", "a220", "a221", "a222", "a223", "a224", "a225", "a226", "a227", "a228", "a229", "a230", "a231", "a232", "a233", "a234", "a235", "a236", "a237", "a238", "a239", "a240", "a241", "a242", "a243", "a244", "a245", "a246", "a247", "a248", "a249", "a250", "a251", "a252", "a253", "a254", "a255", "s8", "s9", "s10", "s11", "s12", "s13", "s14", "s15", "s16", "s17", "s18", "s19", "s20", "s21", "s22", "s23", "s24", "s25", "s26", "s27", "s28", "s29", "s30", "s31", "s33", "s34", "s35", "s36", "s37", "s38", "s39", "s40", "s41", "s42", "s43", "s44", "s45", "s46", "s47", "s48", "s49", "s50", "s51", "s52", "s53", "s54", "s55", "s56", "s57", "s58", "s59", "s60", "s61", "s62", "s63");
}


extern "C" void kernel_launch(void* const* d_in, const int* in_sizes, int n_in, void* d_out, int out_size, void* d_ws, size_t ws_size,
                              hipStream_t stream) {

  fused_attn_block<<<256, 256, 0, stream>>>((const char*)d_in[0], (const char*)d_in[1], (const char*)d_in[2], (const char*)d_in[3], (const char*)d_in[4],
                                            (const char*)d_in[5], (const char*)d_in[6], (const char*)d_in[7], (const char*)d_in[8], (const char*)d_in[9],
                                            (char*)d_out, (char*)d_ws);

}
```
